```python
import torch

def _softmax(x, axis=-1):
    return torch.softmax(x, dim=axis)
def _gelu(x):
    return torch.nn.functional.gelu(x, approximate="tanh")
def _silu(x):
    return torch.nn.functional.silu(x)
def _sigmoid(x):
    return torch.sigmoid(x)
def _layernorm(x, axis=-1, eps=1e-5):
    m = x.mean(dim=axis, keepdim=True)
    v = x.var(dim=axis, keepdim=True, unbiased=False)
    return (x - m) / torch.sqrt(v + eps)
def _scatter_add(tgt, idx, src, axis=0):
    return tgt.clone().index_add_(axis, idx, src)
def _segment_sum(src, seg, n):
    out = torch.zeros((n,) + src.shape[1:], dtype=src.dtype)
    return out.index_add_(0, seg, src)
def _pad(x, pw):
    flat = tuple(v for pair in reversed(pw) for v in pair)
    return torch.nn.functional.pad(x, flat)

kernel_name = 'parallel_sum__ssm_conv_s4__dense_activation_mish__prompt'

def setup_inputs():

    _w = (lambda a: (lambda t: t.to(torch.bfloat16) if t.is_floating_point() else t)(torch.as_tensor(a)))
    def _A_setup():
        B, L, D, N = 1, 2048, 640, 192
        A_re = -(lambda _x: torch.exp(torch.as_tensor(_x)))((lambda *s: torch.randn(*s, dtype=torch.bfloat16) * 3.2)(D, N))
        A_im = torch.pi * (lambda *s: torch.randn(*s, dtype=torch.bfloat16) * 0.05)(D, N)
        B_re = (lambda *s: torch.randn(*s, dtype=torch.bfloat16) * 0.1)(D, N)
        B_im = (lambda *s: torch.randn(*s, dtype=torch.bfloat16) * 0.1)(D, N)
        C_re = (lambda *s: torch.randn(*s, dtype=torch.bfloat16) * 0.1)(D, N)
        C_im = (lambda *s: torch.randn(*s, dtype=torch.bfloat16) * 0.1)(D, N)
        log_dt = (lambda *s: torch.randn(*s, dtype=torch.bfloat16) * 0.05)(D) * 0.5 - 4.0
        Dskip = (lambda *s: torch.randn(*s, dtype=torch.bfloat16) * 0.05)(D)
        return {
            "u": _w((lambda *s: torch.randn(*s, dtype=torch.bfloat16) * 0.05)(B, L, D)),
            "A_re": _w(A_re),
            "A_im": _w(A_im),
            "B_re": _w(B_re),
            "B_im": _w(B_im),
            "C_re": _w(C_re),
            "C_im": _w(C_im),
            "log_dt": _w(log_dt),
            "Dskip": _w(Dskip),
        }
    _A_in = _A_setup()

    def _B_setup():
        B, S, D, F = 1, 2048, 640, 1536
        return {
            "x": _w((lambda *s: torch.randn(*s, dtype=torch.bfloat16) * 0.05)(B, S, D)),
            "w1": _w((lambda *s: torch.randn(*s, dtype=torch.bfloat16) * 0.8)(D, F) * (D ** -0.5)),
            "b1": _w((lambda *s: torch.randn(*s, dtype=torch.bfloat16) * 0.4)(F)),
            "w2": _w((lambda *s: torch.randn(*s, dtype=torch.bfloat16) * 0.2)(F, D) * (F ** -0.5)),
        }
    _B_in = _B_setup()

    return {
        "A_u": _A_in["u"],
        "A_A_re": _A_in["A_re"],
        "A_A_im": _A_in["A_im"],
        "A_B_re": _A_in["B_re"],
        "A_B_im": _A_in["B_im"],
        "A_C_re": _A_in["C_re"],
        "A_C_im": _A_in["C_im"],
        "A_log_dt": _A_in["log_dt"],
        "A_Dskip": _A_in["Dskip"],
        "B_x": _B_in["x"],
        "B_w1": _B_in["w1"],
        "B_b1": _B_in["b1"],
        "B_w2": _B_in["w2"],
    }

def reference(A_u, A_A_re, A_A_im, A_B_re, A_B_im, A_C_re, A_C_im, A_log_dt, A_Dskip, B_x, B_w1, B_b1, B_w2):
    def _A_ref(u, A_re, A_im, B_re, B_im, C_re, C_im, log_dt, Dskip):
        Bsz, L, D = u.shape
        dt = torch.exp(log_dt)[:, None]
        A = A_re + 1j * A_im
        Bc = B_re + 1j * B_im
        Cc = C_re + 1j * C_im
        logdA = dt * A
        dB = dt * Bc
        l = (lambda x, dt: x.to(dt))(torch.arange(L), A_re.dtype)
        P = torch.exp(logdA[:, :, None] * l[None, None, :])
        K = torch.einsum("dn,dnl,dn->dl", Cc, P, dB).real
        L2 = 2 * L
        Kp = _pad(K, ((0, 0), (0, L)))
        up = (lambda x, *d: x.permute(*d))(u, 0, 2, 1)
        up = _pad(up, ((0, 0), (0, 0), (0, L)))
        Kf = (lambda x, axes=(-2,-1): torch.fft.rfft2(x.float(), dim=axes))(Kp[:, None, :], axes=(-2, -1))
        Uf = (lambda x, axes=(-2,-1): torch.fft.rfft2(x.float(), dim=axes))(up[:, :, None, :], axes=(-2, -1))
        Yf = Uf * Kf[None, :, :, :]
        y = (lambda x, s, axes=(-2,-1): torch.fft.irfft2(x, s=s, dim=axes).to(torch.bfloat16))(Yf, (1, L2), axes=(-2, -1))[:, :, 0, :L]
        y = (lambda x, *d: x.permute(*d))(y, 0, 2, 1) + u * Dskip[None, None, :]
        return y

    def _B_ref(x, w1, b1, w2):
        h = torch.einsum("bsd,df->bsf", x, w1) + b1
        sp = torch.log1p(torch.exp(-torch.abs(h))) + torch.where(h > 0, h, torch.zeros_like(h))
        act = h * torch.tanh(sp)
        return torch.einsum("bsf,fd->bsd", act, w2)

    x = A_u
    xn = _layernorm(x, axis=-1)
    return x + _A_ref(xn, A_A_re, A_A_im, A_B_re, A_B_im, A_C_re, A_C_im, A_log_dt, A_Dskip) + _B_ref(xn, B_w1, B_b1, B_w2)
```

```cpp
#include <hip/hip_runtime.h>
#include <hip/hip_bf16.h>
#include <stdint.h>
typedef unsigned short u16;
#define L_ 2048
#define D_ 640
#define N_ 192
#define F_ 1536
__device__ __forceinline__ float bf2f(u16 v){ return __uint_as_float(((unsigned)v)<<16); }
__device__ __forceinline__ u16 f2bf(float f){ __hip_bfloat16 b = __float2bfloat16(f); return *(u16*)&b; }
__device__ __forceinline__ float rbf(float f){ return bf2f(f2bf(f)); }

__global__ void k_ln(const u16* x, u16* xn){
  int t = blockIdx.x; int lane = threadIdx.x;
  float v[10]; float s=0;
  for(int i=0;i<10;i++){ v[i]=bf2f(x[t*D_+lane+64*i]); s+=v[i]; }
  for(int o=32;o;o>>=1) s+=__shfl_xor(s,o);
  float mean=s/D_; float q=0;
  for(int i=0;i<10;i++){ float d=v[i]-mean; q+=d*d; }
  for(int o=32;o;o>>=1) q+=__shfl_xor(q,o);
  float var=rbf(q/D_); float m=rbf(mean);
  float sd=rbf(sqrtf(rbf(var+1e-5f)));
  for(int i=0;i<10;i++){ xn[t*D_+lane+64*i]=f2bf(rbf(v[i]-m)/sd); }
}
__global__ void k_kgen(const u16* Are,const u16* Aim,const u16* Bre,const u16* Bim,const u16* Cre,const u16* Cim,const u16* logdt,float* K){
  int d=blockIdx.x;
  __shared__ float zr[N_],zi[N_],cr[N_],ci[N_];
  float dt=rbf(expf(bf2f(logdt[d])));
  for(int n=threadIdx.x;n<N_;n+=blockDim.x){
    int i=d*N_+n;
    zr[n]=dt*bf2f(Are[i]); zi[n]=dt*bf2f(Aim[i]);
    float br=dt*bf2f(Bre[i]), bi=dt*bf2f(Bim[i]), c0=bf2f(Cre[i]), c1=bf2f(Cim[i]);
    cr[n]=c0*br-c1*bi; ci[n]=c0*bi+c1*br;
  }
  __syncthreads();
  for(int l=threadIdx.x;l<L_;l+=blockDim.x){
    float lf=rbf((float)l); float acc=0;
    for(int n=0;n<N_;n++){
      float e=expf(zr[n]*lf); float s,c; sincosf(zi[n]*lf,&s,&c);
      acc+=e*(cr[n]*c-ci[n]*s);
    }
    K[d*L_+l]=acc;
  }
}
__global__ void k_conv(const u16* xn,const float* K,float* y){
  int d=blockIdx.x;
  __shared__ float u[L_],k[L_];
  for(int t=threadIdx.x;t<L_;t+=blockDim.x){ u[t]=bf2f(xn[t*D_+d]); k[t]=K[d*L_+t]; }
  __syncthreads();
  for(int t=threadIdx.x;t<L_;t+=blockDim.x){
    float a=0; for(int s=0;s<=t;s++) a+=k[t-s]*u[s];
    y[t*D_+d]=a;
  }
}
__global__ void k_g1(const u16* xn,const u16* w1,const u16* b1,u16* act){
  int t=blockIdx.x; __shared__ float r[D_];
  for(int i=threadIdx.x;i<D_;i+=blockDim.x) r[i]=bf2f(xn[t*D_+i]);
  __syncthreads();
  for(int f=threadIdx.x;f<F_;f+=blockDim.x){
    float a=0; for(int k=0;k<D_;k++) a+=r[k]*bf2f(w1[k*F_+f]);
    float h=rbf(rbf(a)+bf2f(b1[f]));
    float e=expf(-fabsf(h));
    float n = h>0 ? (1.f+2.f*e) : e*(e+2.f);
    float dn = h>0 ? (1.f+2.f*e+2.f*e*e) : (n+2.f);
    act[t*F_+f]=f2bf(h*n/dn);
  }
}
__global__ void k_g2(const u16* act,const u16* w2,const u16* x,const u16* xn,const float* y,const u16* Dskip,u16* out){
  int t=blockIdx.x; __shared__ float r[F_];
  for(int i=threadIdx.x;i<F_;i+=blockDim.x) r[i]=bf2f(act[t*F_+i]);
  __syncthreads();
  for(int d=threadIdx.x;d<D_;d+=blockDim.x){
    float a=0; for(int k=0;k<F_;k++) a+=r[k]*bf2f(w2[k*D_+d]);
    float ya=rbf(rbf(y[t*D_+d])+rbf(bf2f(xn[t*D_+d])*bf2f(Dskip[d])));
    out[t*D_+d]=f2bf(rbf(bf2f(x[t*D_+d])+ya)+rbf(a));
  }
}
extern "C" void kernel_launch(void* const* d_in, const int* in_sizes, int n_in, void* d_out, int out_size, void* d_ws, size_t ws_size, hipStream_t stream){
  const u16* x=(const u16*)d_in[0];
  char* ws=(char*)d_ws;
  u16* xn=(u16*)ws; float* K=(float*)(ws+(4<<20)); float* y=(float*)(ws+(12<<20)); u16* act=(u16*)(ws+(20<<20));
  k_ln<<<L_,64,0,stream>>>(x,xn);
  k_kgen<<<D_,256,0,stream>>>((const u16*)d_in[1],(const u16*)d_in[2],(const u16*)d_in[3],(const u16*)d_in[4],(const u16*)d_in[5],(const u16*)d_in[6],(const u16*)d_in[7],K);
  k_conv<<<D_,256,0,stream>>>(xn,K,y);
  k_g1<<<L_,256,0,stream>>>(xn,(const u16*)d_in[10],(const u16*)d_in[11],act);
  k_g2<<<L_,256,0,stream>>>(act,(const u16*)d_in[12],x,xn,y,(const u16*)d_in[8],(u16*)d_out);
}
```

```cpp
#include <hip/hip_runtime.h>
#include <stdint.h>

typedef unsigned short u16;
typedef unsigned long long u64;
typedef unsigned v4u __attribute__((ext_vector_type(4)));
typedef unsigned v2u __attribute__((ext_vector_type(2)));
typedef float f32x4 __attribute__((ext_vector_type(4)));
typedef float f32x2 __attribute__((ext_vector_type(2)));
typedef short bf16x8 __attribute__((ext_vector_type(8)));
typedef short s16x4 __attribute__((ext_vector_type(4)));
typedef __bf16 bf16x2_t __attribute__((ext_vector_type(2)));
#define LAS __attribute__((address_space(3)))
#define GAS __attribute__((address_space(1)))
#define DI __device__ __forceinline__

constexpr unsigned WS_E = 0, WS_TMO = 64, WS_F1 = 4096, WS_F3 = 8192, WS_STATS = 16384, WS_F2 = 32768, WS_STAMP = 65536;
constexpr unsigned WS_XNB = 1u << 20, WS_W2T = 4u << 20, WS_KP = 6u << 20, WS_Y = 7u << 20, WS_ACT = 10u << 20, WS_END = 17u << 20;
constexpr unsigned KP_STRIDE = 2624;
constexpr unsigned SPIN_CAP = 400000;

constexpr unsigned G1_STAGE = 28672, G1_BOFF = 16384;
constexpr unsigned KG_SCR = 114688, KG_WSTR = 4352, LN_ROWS = 149504, LN_PITCH = 1344;
constexpr unsigned KTAB = 155648;
constexpr unsigned CS_OFF = 143360;
constexpr unsigned XCH = 0;
constexpr unsigned CV_CH = 27008, CV_USTR = 4640, CV_KOFF = 18560, CV_KSTR = 4224;
constexpr unsigned CP_FULL = 9216;
constexpr unsigned G2_SLOT = 20480;
constexpr unsigned FIN_PITCH = 336, FIN_PLANE = 21504, YST = 90112;

#define ST(i) do {} while (0)

DI float bf2f(unsigned v) { return __builtin_bit_cast(float, v << 16); }
DI float bfhi(unsigned v) { return __builtin_bit_cast(float, v & 0xffff0000u); }
DI unsigned pk2(float lo, float hi) { bf16x2_t r; r.x = (__bf16)lo; r.y = (__bf16)hi; return __builtin_bit_cast(unsigned, r); }
DI float rbf(float f) { return bf2f(pk2(f, f) & 0xffffu); }
DI f32x4 mfma(v4u a, v4u b, f32x4 c) {
  return __builtin_amdgcn_mfma_f32_16x16x32_bf16(__builtin_bit_cast(bf16x8, a), __builtin_bit_cast(bf16x8, b), c, 0, 0, 0);
}
DI v2u trread(LAS char* p) { return __builtin_bit_cast(v2u, __builtin_amdgcn_ds_read_tr16_b64_v4i16((LAS s16x4*)p)); }
template <int N> DI void waitv() { asm volatile("s_waitcnt vmcnt(%0)" ::"i"(N) : "memory"); }
DI void bar() { asm volatile("s_waitcnt lgkmcnt(0)\n\ts_barrier" ::: "memory"); }
#define PIN(x) asm volatile("" : "+v"(x))
#define SCHED() __builtin_amdgcn_sched_barrier(0)

DI void ld16(unsigned& d, unsigned vo, const void* sb) { asm volatile("global_load_ushort %0, %1, %2" : "=v"(d) : "v"(vo), "s"(sb) : "memory"); }
template <int O> DI void ld32(unsigned& d, unsigned vo, const void* sb) { asm volatile("global_load_dword %0, %1, %2 offset:%3" : "=v"(d) : "v"(vo), "s"(sb), "i"(O) : "memory"); }
DI void ld64(v2u& d, unsigned vo, const void* sb) { asm volatile("global_load_dwordx2 %0, %1, %2" : "=v"(d) : "v"(vo), "s"(sb) : "memory"); }
DI void ld128(v4u& d, unsigned vo, const void* sb) { asm volatile("global_load_dwordx4 %0, %1, %2" : "=v"(d) : "v"(vo), "s"(sb) : "memory"); }
DI void ld64c(v2u& d, unsigned vo, const void* sb) { asm volatile("global_load_dwordx2 %0, %1, %2 sc1" : "=v"(d) : "v"(vo), "s"(sb) : "memory"); }
template <int O> DI void ld128c(v4u& d, unsigned vo, const void* sb) { asm volatile("global_load_dwordx4 %0, %1, %2 offset:%3 sc1" : "=v"(d) : "v"(vo), "s"(sb), "i"(O) : "memory"); }
DI void st64c(unsigned vo, void* sb, v2u d) { asm volatile("global_store_dwordx2 %0, %1, %2 sc1" ::"v"(vo), "v"(d), "s"(sb) : "memory"); }
DI void st128c(unsigned vo, void* sb, v4u d) { asm volatile("global_store_dwordx4 %0, %1, %2 sc1\n\ts_nop 1" ::"v"(vo), "v"(d), "s"(sb) : "memory"); }
DI void st128(unsigned vo, void* sb, v4u d) { asm volatile("global_store_dwordx4 %0, %1, %2\n\ts_nop 1" ::"v"(vo), "v"(d), "s"(sb) : "memory"); }
DI void dma16(unsigned vo, const void* sb, unsigned lds) {
  unsigned keep;
  asm volatile("s_mov_b32 %0, m0\n\ts_mov_b32 m0, %3\n\ts_nop 0\n\tglobal_load_lds_dwordx4 %1, %2\n\ts_mov_b32 m0, %0"
               : "=&s"(keep) : "v"(vo), "s"(sb), "s"(lds) : "memory");
}
template <unsigned HI> DI void dma16m(unsigned vo, const void* sb, unsigned lds) {
  unsigned keep;
  asm volatile("s_mov_b32 %0, m0\n\ts_mov_b32 m0, %3\n\ts_mov_b32 exec_hi, %4\n\tglobal_load_lds_dwordx4 %1, %2\n\ts_mov_b32 exec_hi, -1\n\ts_mov_b32 m0, %0"
               : "=&s"(keep) : "v"(vo), "s"(sb), "s"(lds), "i"(HI) : "memory");
}
DI v2u spin64(v2u cur, unsigned vo, char* ws, unsigned tlo, unsigned thi, bool hionly) {
  for (unsigned spins = 0;; ++spins) {
    const bool ok = hionly ? (cur.y == tlo) : (cur.x == tlo && cur.y == thi);
    if (__all(ok)) break;
    if (spins > SPIN_CAP) { *(GAS unsigned*)(ws + WS_TMO) = 1u; break; }
    __builtin_amdgcn_s_sleep(1);
    asm volatile("global_load_dwordx2 %0, %1, %2 sc1\n\ts_waitcnt vmcnt(0)" : "=&v"(cur) : "v"(vo), "s"(ws) : "memory");
  }
  return cur;
}
template <int C> DI float dpp_add(float v) {
  return v + __builtin_bit_cast(float, __builtin_amdgcn_update_dpp(0, __builtin_bit_cast(int, v), C, 0xf, 0xf, true));
}
DI float rl(float v, int l) { return __builtin_bit_cast(float, __builtin_amdgcn_readlane(__builtin_bit_cast(int, v), l)); }
DI float wave_sum(float v) {
  v = dpp_add<0xB1>(v); v = dpp_add<0x4E>(v); v = dpp_add<0x141>(v); v = dpp_add<0x140>(v);
  return (rl(v, 0) + rl(v, 16)) + (rl(v, 32) + rl(v, 48));
}
struct cf { float r, i; };
DI cf cmul(cf a, cf b) { cf o; o.r = a.r * b.r - a.i * b.i; o.i = a.r * b.i + a.i * b.r; return o; }
DI cf cexpk(float zr, float zi, float k) {
  const float mag = __builtin_amdgcn_exp2f(zr * k * 1.44269504088896341f);
  const float t = zi * k;
  const float hi = 0.15915494309189535f, lo = 6.4206383e-9f;
  const float r = t * hi;
  const float e = __builtin_fmaf(t, hi, -r) + t * lo;
  const float fr = (r - __builtin_rintf(r)) + e;
  cf o; o.r = mag * __builtin_amdgcn_cosf(fr); o.i = mag * __builtin_amdgcn_sinf(fr); return o;
}

template <int PAR> DI void conv_unit_shared(LAS char* ka, LAS char* ub, f32x4 (&acc)[8]) {
  constexpr int T[4] = {PAR ? 1 : 0, PAR ? 2 : 3, PAR ? 5 : 4, PAR ? 6 : 7};
  v4u A[8], B[8];
#pragma unroll
  for (int i = 0; i <= T[3]; ++i) { A[i] = *(LAS v4u*)(ka - 512 * i); B[i] = *(LAS v4u*)(ub + 512 * i); }
#pragma unroll
  for (int q = 0; q < 4; ++q)
#pragma unroll
    for (int ip = 0; ip <= T[q]; ++ip) acc[q] = mfma(A[T[q] - ip], B[ip], acc[q]);
}

__global__ __launch_bounds__(512, 2) void fused_s4_mlp(const char* __restrict__ x, const char* __restrict__ pAre, const char* __restrict__ pAim,
    const char* __restrict__ pBre, const char* __restrict__ pBim, const char* __restrict__ pCre, const char* __restrict__ pCim,
    const char* __restrict__ plogdt, const char* __restrict__ pDskip, const char* __restrict__ w1, const char* __restrict__ b1,
    const char* __restrict__ w2, char* __restrict__ out, char* __restrict__ ws) {
  __shared__ __attribute__((aligned(16))) char smem[163840];
  LAS char* L = (LAS char*)smem;
  const unsigned lds0 = (unsigned)(size_t)L;
  const int b = blockIdx.x, tid = threadIdx.x, lane = tid & 63;
  const int w = __builtin_amdgcn_readfirstlane(tid >> 6);
  const int xcd = b & 7, j = b >> 3, rg = xcd >> 1, chf = xcd & 1;
  const int ct1 = 8 * chf + (j & 7), rb1 = 4 * rg + (j >> 3);
  const int wid = rb1 * 16 + ct1;
  const int pwid = wid ^ 1;
  const int par = j & 1, cbase = 80 * xcd + 5 * (j >> 1);
  const int chA = cbase + 3 * par, chS = cbase + 2;
  const int l15 = lane & 15, g = lane >> 4, qq = (lane >> 2) & 3, p4 = lane & 3;
  ST(0);

  v2u E; ld64c(E, 0u, ws);
  unsigned xr[5];
  {
    const char* xrow = x + (size_t)(8 * wid + w) * 1280; const unsigned vo = lane * 4;
    ld32<0>(xr[0], vo, xrow); ld32<256>(xr[1], vo, xrow); ld32<512>(xr[2], vo, xrow); ld32<768>(xr[3], vo, xrow); ld32<1024>(xr[4], vo, xrow);
  }
  unsigned pr[7];
  const int kch = w < 3 ? chA : (w < 6 ? chA + 1 : chS);
  {
    const int mode0 = w < 6 ? (w % 3) * 64 : par * 96 + (w - 6) * 48;
    const unsigned vo = (unsigned)(kch * 192 + mode0 + ((w >= 6 && lane >= 48) ? 47 : lane)) * 2;
    ld16(pr[0], vo, pAre); ld16(pr[1], vo, pAim); ld16(pr[2], vo, pBre); ld16(pr[3], vo, pBim); ld16(pr[4], vo, pCre); ld16(pr[5], vo, pCim);
    ld16(pr[6], (unsigned)kch * 2, plogdt);
  }
  const int pu = b % 240, pks = pu / 5, pcg = pu % 5;
  dma16((unsigned)((lane >> 1) * 1280 + (lane & 1) * 16), w2 + (size_t)pks * 32 * 1280 + (128 * pcg + 16 * w) * 2, lds0 + KG_SCR + w * KG_WSTR);
  unsigned voA[2], voB[2];
#pragma unroll
  for (int i = 0; i < 2; ++i) {
    const int r = (2 * w + i) * 8 + (lane >> 3);
    voA[i] = r * 1280 + (((lane & 7) ^ ((r >> 1) & 7)) * 16);
    const int l2 = lane < 48 ? lane : 47;
    voB[i] = ((2 * w + i) * 4 + l2 / 12) * 3072 + (((l2 % 12) ^ ((w & 1) << 1)) * 16);
  }
  const char* g1a = x + (size_t)rb1 * 128 * 1280;
  const char* g1b = w1 + ct1 * 192;
  auto g1_issue = [&](int s) {
    const unsigned base = lds0 + (s % 5) * G1_STAGE;
    dma16(voA[0], g1a + s * 128, base + (2 * w) * 1024);
    dma16(voA[1], g1a + s * 128, base + (2 * w + 1) * 1024);
    dma16m<0xffffu>(voB[0], g1b + (size_t)s * 64 * 3072, base + G1_BOFF + (2 * w) * 768);
    dma16m<0xffffu>(voB[1], g1b + (size_t)s * 64 * 3072, base + G1_BOFF + (2 * w + 1) * 768);
  };
  g1_issue(0); g1_issue(1); g1_issue(2); g1_issue(3);

  waitv<24>();
  PIN(E); PIN(xr[0]); PIN(xr[1]); PIN(xr[2]); PIN(xr[3]); PIN(xr[4]);
  const u64 tag64 = (((u64)__builtin_amdgcn_readfirstlane(E.y) << 32) | (unsigned)__builtin_amdgcn_readfirstlane(E.x)) + 1ull;
  const unsigned tlo = (unsigned)tag64, thi = (unsigned)(tag64 >> 32);
  ST(1);
  {
    float v[10], s = 0.f;
#pragma unroll
    for (int i = 0; i < 5; ++i) { v[2 * i] = bf2f(xr[i] & 0xffffu); v[2 * i + 1] = bfhi(xr[i]); s += v[2 * i] + v[2 * i + 1]; }
    const float mean = wave_sum(s) * (1.0f / 640.0f);
    float q = 0.f;
#pragma unroll
    for (int i = 0; i < 10; ++i) { const float d = v[i] - mean; q += d * d; }
    const float var = rbf(wave_sum(q) * (1.0f / 640.0f));
    const float m = rbf(mean), sd = rbf(sqrtf(rbf(var + 1e-5f))), rs = 1.0f / sd;
    if (lane == 0) {
      v2u gr; gr.x = (__builtin_bit_cast(unsigned, m) & 0xffff0000u) | (__builtin_bit_cast(unsigned, sd) >> 16); gr.y = tlo;
      st64c(WS_STATS + (unsigned)(8 * wid + w) * 8, ws, gr);
    }
#pragma unroll
    for (int i = 0; i < 5; ++i) {
      const unsigned dd = pk2(v[2 * i] - m, v[2 * i + 1] - m);
      *(LAS unsigned*)(L + LN_ROWS + w * LN_PITCH + lane * 4 + 256 * i) = pk2(bf2f(dd & 0xffffu) * rs, bfhi(dd) * rs);
    }
  }
  waitv<16>();
  PIN(pr[0]); PIN(pr[1]); PIN(pr[2]); PIN(pr[3]); PIN(pr[4]); PIN(pr[5]); PIN(pr[6]);
  {
    LAS char* sp = L + KG_SCR + w * KG_WSTR + (4 * g + qq) * 32 + 8 * p4;
    const v2u lo = trread(sp), hi = trread(sp + 512);
    const int ntg = 8 * pcg + w;
    if (b < 240) st128c((unsigned)lane * 16, ws + WS_W2T + (size_t)(((ntg / 5) * 48 + pks) * 5 + ntg % 5) * 1024, v4u{lo.x, lo.y, hi.x, hi.y});
  }
  bar();
  {
    LAS char* sp = L + LN_ROWS + qq * LN_PITCH + (64 * w + 16 * g) * 2 + 8 * p4;
    const v2u lo = trread(sp), hi = trread(sp + 4 * LN_PITCH);
    st128c((unsigned)(64 * w + lane) * 16, ws + WS_XNB + (size_t)wid * 10240, v4u{lo.x, lo.y, hi.x, hi.y});
    if (w < 2) {
      const v2u lo2 = trread(sp + 1024), hi2 = trread(sp + 1024 + 4 * LN_PITCH);
      st128c((unsigned)(512 + 64 * w + lane) * 16, ws + WS_XNB + (size_t)wid * 10240, v4u{lo2.x, lo2.y, hi2.x, hi2.y});
    }
  }
  ST(2);

  {
    const float dt = rbf(expf(bf2f(pr[6])));
    const float zr = dt * bf2f(pr[0]), zi = dt * bf2f(pr[1]);
    const float dbr = dt * bf2f(pr[2]), dbi = dt * bf2f(pr[3]), cr = bf2f(pr[4]), ci = bf2f(pr[5]);
    cf a; a.r = cr * dbr - ci * dbi; a.i = -(cr * dbi + ci * dbr);
    if (w >= 6 && lane >= 48) { a.r = 0.f; a.i = 0.f; }
    const cf W1 = cexpk(zr, -zi, 1.f), W256 = cexpk(zr, -zi, 256.f), W8 = cexpk(zr, zi, 8.f);
    LAS char* sw = L + KG_SCR + w * KG_WSTR + lane * 4;
    LAS char* sr = L + KG_SCR + w * KG_WSTR + l15 * 272 + g * 16;
#define PUT(row, v) *(LAS unsigned*)(sw + (row) * 272) = pk2((v).r, (v).i)
    const cf W2 = cmul(W1, W1), W512 = cmul(W256, W256);
    cf u0 = a, u1 = cmul(a, W1), u2 = cmul(u0, W2), u3 = cmul(u1, W2), u4 = cmul(u2, W2), u5 = cmul(u3, W2), u6 = cmul(u4, W2), u7 = cmul(u5, W2);
    PUT(0, u0); PUT(1, u1); PUT(2, u2); PUT(3, u3); PUT(4, u4); PUT(5, u5); PUT(6, u6); PUT(7, u7);
    { cf t; t = cmul(u0, W256); PUT(8, t); t = cmul(u2, W256); PUT(9, t); t = cmul(u4, W256); PUT(10, t); t = cmul(u6, W256); PUT(11, t); }
    const cf r12 = cmul(u0, W512), r13 = cmul(u4, W512), r14 = cmul(r12, W256), r15 = cmul(r13, W256);
    PUT(12, r12); PUT(13, r13); PUT(14, r14); PUT(15, r15);
    v4u UA[2][4];
#pragma unroll
    for (int k = 0; k < 4; ++k) UA[0][k] = *(LAS v4u*)(sr + 64 * k);
    const cf r16 = cmul(r12, W512), r17 = cmul(r14, W512), r18 = cmul(r16, W512), r19 = cmul(r17, W512), r20 = cmul(r18, W512);
    PUT(0, r16); PUT(1, r17); PUT(2, r18); PUT(3, r19); PUT(4, r20);
#pragma unroll
    for (int k = 0; k < 4; ++k) UA[1][k] = *(LAS v4u*)(sr + 64 * k);
    const cf W16 = cmul(W8, W8);
    cf ve; ve.r = 1.f; ve.i = 0.f; cf vo = W8;
    f32x4 kacc[2][2];
#pragma unroll
    for (int nh = 0; nh < 2; ++nh) {
#pragma unroll
      for (int i = 0; i < 8; ++i) {
        PUT(2 * i, ve); PUT(2 * i + 1, vo);
        if (nh == 0 || i < 7) { ve = (nh == 0 && i == 0) ? W16 : cmul(ve, W16); vo = cmul(vo, W16); }
      }
      kacc[0][nh] = f32x4{0.f, 0.f, 0.f, 0.f}; kacc[1][nh] = f32x4{0.f, 0.f, 0.f, 0.f};
#pragma unroll
      for (int k = 0; k < 4; ++k) {
        const v4u vb = *(LAS v4u*)(sr + 64 * k);
        kacc[0][nh] = mfma(UA[0][k], vb, kacc[0][nh]);
        kacc[1][nh] = mfma(UA[1][k], vb, kacc[1][nh]);
      }
    }
#undef PUT
    LAS float* pt = (LAS float*)(L + KG_SCR + w * KG_WSTR);
#pragma unroll
    for (int nh = 0; nh < 2; ++nh) {
      const int bb = 16 * nh + l15;
      const int a0 = g < 2 ? 8 * bb + 4 * g : (g == 2 ? 256 + 4 * bb : 384 + 2 * bb);
      const int a1 = g < 3 ? a0 + 2 : a0 + 64;
      *(LAS f32x2*)(pt + a0) = f32x2{kacc[0][nh][0], kacc[0][nh][1]};
      *(LAS f32x2*)(pt + a1) = f32x2{kacc[0][nh][2], kacc[0][nh][3]};
      if (g == 0) {
#pragma unroll
        for (int r = 0; r < 4; ++r) pt[512 + 32 * r + bb] = kacc[1][nh][r];
      }
    }
    if (lane == 16) *(LAS f32x4*)(pt + 640) = f32x4{kacc[1][0][0], 0.f, 0.f, 0.f};
  }
  bar();
  if (tid < 483) {
    const int c = tid / 161, i = tid % 161;
    LAS char* pp = L + KG_SCR + 3 * c * KG_WSTR + i * 16;
    f32x4 s = *(LAS f32x4*)pp + *(LAS f32x4*)(pp + KG_WSTR);
    if (c < 2) s += *(LAS f32x4*)(pp + 2 * KG_WSTR);
    *(LAS f32x4*)(L + KTAB + c * KP_STRIDE + i * 16) = s;
    if (c == 2) st128c((unsigned)i * 16, ws + WS_KP + (size_t)wid * KP_STRIDE, __builtin_bit_cast(v4u, s));
  }
  waitv<0>();
  bar();
  if (tid == 0) st64c(WS_F1 + (unsigned)wid * 8, ws, v2u{tlo, thi});
  g1_issue(4);
  ST(3);

  const int rgp = w >> 1, kh = w & 1, rot = rgp % 3;
  f32x4 acc1[2][6], csa[2];
#pragma unroll
  for (int m = 0; m < 2; ++m) {
    csa[m] = f32x4{0.f, 0.f, 0.f, 0.f};
#pragma unroll
    for (int n = 0; n < 6; ++n) acc1[m][n] = f32x4{0.f, 0.f, 0.f, 0.f};
  }
  v2u gran, f1own, f1par, bia[6];
  {
    unsigned xo[2], wo[6];
#pragma unroll
    for (int m = 0; m < 2; ++m) {
      const int r = 32 * rgp + 16 * (m ^ kh) + l15;
      xo[m] = r * 128 + (((4 * kh + g) ^ ((r >> 1) & 7)) * 16);
    }
#pragma unroll
    for (int n = 0; n < 6; ++n) {
      const int pg = ((n >> 1) + rot) % 3;
      wo[n] = G1_BOFF + (32 * kh + 8 * g + qq) * 192 + 64 * pg + 32 * ((n & 1) ^ (g & 1)) + 8 * p4;
    }
    v4u X[2][2], W[2][6];
    auto rd = [&](int s, v4u (&xx)[2], v4u (&ww)[6]) {
      LAS char* sb = L + (s % 5) * G1_STAGE;
#pragma unroll
      for (int m = 0; m < 2; ++m) xx[m] = *(LAS v4u*)(sb + xo[m]);
#pragma unroll
      for (int n = 0; n < 6; ++n) { const v2u lo = trread(sb + wo[n]), hi = trread(sb + wo[n] + 768); ww[n] = v4u{lo.x, lo.y, hi.x, hi.y}; }
    };
    const v4u ones = v4u{0x3f803f80u, 0x3f803f80u, 0x3f803f80u, 0x3f803f80u};
    rd(0, X[0], W[0]);
#pragma unroll
    for (int s = 0; s < 10; ++s) {
      if (s < 9) {
        if (s >= 3) { if (s <= 5) waitv<12>(); else if (s == 6) waitv<8>(); else if (s == 7) waitv<4>(); else waitv<0>(); }
        bar();
        if (s + 5 < 10) g1_issue(s + 5);
        if (s == 8) {
          ld64c(gran, WS_STATS + (unsigned)(rb1 * 128 + 32 * rgp + 16 * kh + l15) * 8, ws);
          ld64c(f1own, WS_F1 + (unsigned)(64 * (w & 3) + lane) * 8, ws);
          ld64c(f1par, WS_F1 + (unsigned)pwid * 8, ws);
#pragma unroll
          for (int n = 0; n < 6; ++n) ld64(bia[n], (unsigned)(ct1 * 96 + 32 * (((n >> 1) + rot) % 3) + 16 * (n & 1) + 4 * g) * 2, b1);
        }
        rd(s + 1, X[(s + 1) & 1], W[(s + 1) & 1]);
      }
#pragma unroll
      for (int n = 0; n < 6; ++n) {
        acc1[0][n] = mfma(W[s & 1][n], X[s & 1][0], acc1[0][n]);
        acc1[1][n] = mfma(W[s & 1][n], X[s & 1][1], acc1[1][n]);
        if (n < 2) csa[n] = mfma(W[s & 1][n], ones, csa[n]);
      }
      SCHED();
    }
  }
  ST(4);
  waitv<0>();
  PIN(gran); PIN(f1own); PIN(f1par);
#pragma unroll
  for (int n = 0; n < 6; ++n) PIN(bia[n]);
  f1own = spin64(f1own, WS_F1 + (unsigned)(64 * (w & 3) + lane) * 8, ws, tlo, thi, false);
  f1par = spin64(f1par, WS_F1 + (unsigned)pwid * 8, ws, tlo, thi, false);
  gran = spin64(gran, WS_STATS + (unsigned)(rb1 * 128 + 32 * rgp + 16 * kh + l15) * 8, ws, tlo, thi, true);
  v4u uo, us, kp;
  const int tb = 64 * (w & 3) + lane;
  ld128c<0>(uo, (unsigned)tb * 10240 + (unsigned)(chA + (w >> 2)) * 16, ws + WS_XNB);
  ld128c<0>(us, (unsigned)tb * 10240 + (unsigned)chS * 16, ws + WS_XNB);
  ld128c<0>(kp, (unsigned)(tid < 161 ? tid : 160) * 16, ws + WS_KP + (size_t)pwid * KP_STRIDE);
  unsigned dsk[2];
  ld16(dsk[0], (unsigned)(chA + (w >> 2)) * 2, pDskip); ld16(dsk[1], (unsigned)chS * 2, pDskip);
#pragma unroll
  for (int n = 0; n < 6; ++n) *(LAS f32x4*)(L + XCH + w * 6144 + n * 1024 + lane * 16) = acc1[1][n];
  if (rgp < 3 && l15 == 0) {
#pragma unroll
    for (int n = 0; n < 2; ++n) *(LAS f32x4*)(L + CS_OFF + kh * 384 + (32 * rot + 16 * n + 4 * g) * 4) = csa[n];
  }
  bar();
  if (tid == 0) {
    asm volatile("buffer_inv sc1" ::: "memory");
    if (b == 0) st64c(WS_E, ws, v2u{tlo, thi});
  }
  {
    const float m = bfhi(gran.x), rs = 1.0f / bf2f(gran.x & 0xffffu), mrs = m * rs;
    unsigned pk[6][2];
#pragma unroll
    for (int n = 0; n < 6; ++n) {
      const f32x4 o = *(LAS f32x4*)(L + XCH + (w ^ 1) * 6144 + n * 1024 + lane * 16);
      const int fo = (32 * (((n >> 1) + rot) % 3) + 16 * (n & 1) + 4 * g) * 4;
      const f32x4 cs = *(LAS f32x4*)(L + CS_OFF + fo) + *(LAS f32x4*)(L + CS_OFF + 384 + fo);
      const float bv[4] = {bf2f(bia[n].x & 0xffffu), bfhi(bia[n].x), bf2f(bia[n].y & 0xffffu), bfhi(bia[n].y)};
      float r[4];
#pragma unroll
      for (int e = 0; e < 4; ++e) {
        const float h = __builtin_fmaf(acc1[0][n][e] + o[e], rs, __builtin_fmaf(-mrs, cs[e], bv[e]));
        const float ex = __builtin_amdgcn_exp2f(__builtin_fminf(h * 1.44269504088896341f, 40.f));
        const float nn = ex * (ex + 2.f);
        r[e] = h * nn * __builtin_amdgcn_rcpf(nn + 2.f);
      }
      pk[n][0] = pk2(r[0], r[1]); pk[n][1] = pk2(r[2], r[3]);
    }
    char* ab = ws + WS_ACT + (size_t)((rb1 * 8 + 2 * rgp + kh) * 48 + 3 * ct1) * 1024;
#pragma unroll
    for (int P = 0; P < 3; ++P)
      st128c((unsigned)(((P + rot) % 3) * 1024 + lane * 16), ab, v4u{pk[2 * P][0], pk[2 * P][1], pk[2 * P + 1][0], pk[2 * P + 1][1]});
  }
  waitv<0>();
  PIN(uo); PIN(us); PIN(kp); PIN(dsk[0]); PIN(dsk[1]);
  if (tid < 161) *(LAS f32x4*)(L + KTAB + 2 * KP_STRIDE + tid * 16) += __builtin_bit_cast(f32x4, kp);
  bar();
  if (tid == 0) st64c(WS_F2 + (unsigned)wid * 128, ws, v2u{tlo, thi});
  ST(5);

  const int a2 = j >> 2, cb2 = 4 * chf + (j & 3);
  const int rh = w & 1, kq = w >> 1;
  const char* w2b = ws + WS_W2T + (size_t)(cb2 * 48 + 12 * kq) * 5120;
  auto g2_issue = [&](int s) {
    const unsigned base = lds0 + ((s + 4) & 7) * G2_SLOT + kq * 5120;
    const char* sb = w2b + s * 5120;
    dma16((unsigned)(rh * 2048 + lane * 16), sb, base + rh * 2048);
    dma16((unsigned)(rh * 2048 + 1024 + lane * 16), sb, base + rh * 2048 + 1024);
    dma16m<0u>((unsigned)(4096 + rh * 512 + lane * 16), sb, base + 4096 + rh * 512);
  };
  g2_issue(0); g2_issue(1); g2_issue(2);
  {
    auto put_u = [&](LAS char* cbp, v4u u) {
      LAS char* q = cbp + tb * 16;
      *(LAS v4u*)(q + 3 * CV_USTR + 384) = u;
      *(LAS v2u*)(q + 1 * CV_USTR + 392) = v2u{u.x, u.y}; *(LAS v2u*)(q + 1 * CV_USTR + 400) = v2u{u.z, u.w};
      *(LAS unsigned*)(q + 2 * CV_USTR + 388) = u.x; *(LAS v2u*)(q + 2 * CV_USTR + 392) = v2u{u.y, u.z}; *(LAS unsigned*)(q + 2 * CV_USTR + 400) = u.w;
      *(LAS unsigned*)(q + 396) = u.x; *(LAS v2u*)(q + 400) = v2u{u.y, u.z}; *(LAS unsigned*)(q + 408) = u.w;
    };
    put_u(L + (w >> 2) * CV_CH, uo);
    if (w >= 4) put_u(L + 2 * CV_CH, us);
    if (w >= 4) {
      const int t4 = tid - 256;
#pragma unroll
      for (int i = 0; i < 5; ++i) {
        const int e = t4 + 256 * i, cp = e / 100, d = e % 100;
        if (cp < 12 && d < 99 - (cp & 3)) *(LAS unsigned*)(L + (cp >> 2) * CV_CH + (cp & 3) * CV_USTR + d * 4) = 0u;
      }
      if (t4 < 42) *(LAS v4u*)(L + (t4 / 14) * CV_CH + CV_KOFF + ((t4 / 7) & 1) * CV_KSTR + 4096 + (t4 % 7) * 16) = v4u{0u, 0u, 0u, 0u};
    }
    int ci[9];
#pragma unroll
    for (int i = 0; i < 9; ++i) {
      const int lg = 2047 - 8 * tb - i;
      const int bits = (int)(pk2((float)lg, 0.f) & 0xffffu);
      ci[i] = lg < 128 ? (lg < 0 ? 641 : lg) : bits - 0x4280;
    }
    auto put_k = [&](int c) {
      LAS float* kt = (LAS float*)(L + KTAB + c * KP_STRIDE);
      float kv[9];
#pragma unroll
      for (int i = 0; i < 9; ++i) kv[i] = kt[ci[i]];
      LAS char* q = L + c * CV_CH + CV_KOFF + tb * 16;
      *(LAS v4u*)(q + CV_KSTR) = v4u{pk2(kv[0], kv[1]), pk2(kv[2], kv[3]), pk2(kv[4], kv[5]), pk2(kv[6], kv[7])};
      *(LAS v4u*)q = v4u{pk2(kv[1], kv[2]), pk2(kv[3], kv[4]), pk2(kv[5], kv[6]), pk2(kv[7], kv[8])};
    };
    put_k(w >> 2);
    if (w < 4) put_k(2);
  }
  bar();
  g2_issue(3);
  ST(6);
  f32x4 cacc[8];
#pragma unroll
  for (int i = 0; i < 8; ++i) cacc[i] = f32x4{0.f, 0.f, 0.f, 0.f};
  {
    const int cc = w < 4 ? (w >> 1) : (w < 6 ? w - 4 : 2);
    const int h0 = w < 4 ? 3 * (w & 1) : (w < 6 ? 6 : 4 * (w - 6));
    LAS char* ka = L + cc * CV_CH + CV_KOFF + (l15 & 1) * CV_KSTR + 2 * (2072 - 32 * h0 - 8 * (l15 >> 1) + 8 * g);
    LAS char* ub = L + cc * CV_CH + (l15 & 3) * CV_USTR + 2 * (64 * (l15 >> 2) + 8 * g - 32 * h0 + 224);
    if (w < 6) {
      const int nu = w < 4 ? 3 : 2;
      for (int u = 0; u < nu; ++u) {
        v4u A[8], B[8];
#pragma unroll
        for (int i = 0; i < 8; ++i) { A[i] = *(LAS v4u*)(ka - 512 * i); B[i] = *(LAS v4u*)(ub + 512 * i); }
#pragma unroll
        for (int sdiag = 0; sdiag < 8; ++sdiag)
#pragma unroll
          for (int o = 0; o <= sdiag; ++o) {
            if (sdiag + o < 8) cacc[sdiag + o] = mfma(A[sdiag], B[o], cacc[sdiag + o]);
            if (o != sdiag && sdiag + o < 8) cacc[sdiag + o] = mfma(A[o], B[sdiag], cacc[sdiag + o]);
          }
        ka -= 64; ub -= 64;
      }
    } else {
      for (int u = 0; u < 4; ++u) {
        if (par) conv_unit_shared<1>(ka, ub, cacc); else conv_unit_shared<0>(ka, ub, cacc);
        ka -= 64; ub -= 64;
      }
    }
  }
  ST(7);
  v4u AF[12][2];
  {
    const unsigned fo = WS_F2 + (unsigned)((4 * rg + (a2 >> 1)) * 16 + 4 * kq + p4) * 128;
    v2u f2; ld64c(f2, fo, ws); waitv<0>(); PIN(f2);
    f2 = spin64(f2, fo, ws, tlo, thi, false);
    const char* ab = ws + WS_ACT + (size_t)((32 * rg + 4 * a2 + 2 * rh) * 48 + 12 * kq) * 1024;
    const unsigned vo = lane * 16;
#pragma unroll
    for (int s = 0; s < 12; s += 4) {
      const char* a0 = ab + s * 1024; const char* a1 = a0 + 48 * 1024;
      ld128c<0>(AF[s][0], vo, a0); ld128c<0>(AF[s][1], vo, a1);
      ld128c<1024>(AF[s + 1][0], vo, a0); ld128c<1024>(AF[s + 1][1], vo, a1);
      ld128c<2048>(AF[s + 2][0], vo, a0); ld128c<2048>(AF[s + 2][1], vo, a1);
      ld128c<3072>(AF[s + 3][0], vo, a0); ld128c<3072>(AF[s + 3][1], vo, a1);
    }
  }
  bar();
  {
    LAS char* pp = L + (w < 6 ? w * CP_FULL : 6 * CP_FULL + (w - 6) * (CP_FULL / 2)) + (16 * g + 2 * (l15 & 3) + 72 * (l15 >> 2)) * 4;
#pragma unroll
    for (int i = 0; i < 8; ++i)
      if (i < 4 || w < 6) {
        *(LAS f32x2*)(pp + i * 1152) = f32x2{cacc[i][0], cacc[i][1]};
        *(LAS f32x2*)(pp + i * 1152 + 32) = f32x2{cacc[i][2], cacc[i][3]};
      }
  }
  bar();
  {
    auto fin = [&](LAS char* p0, LAS char* p1, LAS char* p2, bool three, v4u u, unsigned dk, int chn) {
      f32x4 s0 = *(LAS f32x4*)p0 + *(LAS f32x4*)p1, s1 = *(LAS f32x4*)(p0 + 16) + *(LAS f32x4*)(p1 + 16);
      if (three) { s0 += *(LAS f32x4*)p2; s1 += *(LAS f32x4*)(p2 + 16); }
      const float d = bf2f(dk);
      v4u o;
      o.x = pk2(__builtin_fmaf(d, bf2f(u.x & 0xffffu), s0[0]), __builtin_fmaf(d, bfhi(u.x), s0[1]));
      o.y = pk2(__builtin_fmaf(d, bf2f(u.y & 0xffffu), s0[2]), __builtin_fmaf(d, bfhi(u.y), s0[3]));
      o.z = pk2(__builtin_fmaf(d, bf2f(u.z & 0xffffu), s1[0]), __builtin_fmaf(d, bfhi(u.z), s1[1]));
      o.w = pk2(__builtin_fmaf(d, bf2f(u.w & 0xffffu), s1[2]), __builtin_fmaf(d, bfhi(u.w), s1[3]));
      st128c((unsigned)tb * 16, ws + WS_Y + (size_t)chn * 4096, o);
    };
    const int c = w >> 2, po = (8 * tb + 8 * (tb >> 3)) * 4;
    fin(L + 2 * c * CP_FULL + po, L + (2 * c + 1) * CP_FULL + po, L + (4 + c) * CP_FULL + po, true, uo, dsk[0], chA + c);
    if (w >= 4) {
      const int q = w & 3;
      const int mine = ((q & 1) ^ par);
      if ((lane >> 5) == mine) {
        const int tl = 32 * q + (lane & 31), ps = (8 * tl + 8 * (tl >> 3)) * 4;
        fin(L + 6 * CP_FULL + ps, L + 6 * CP_FULL + CP_FULL / 2 + ps, L, false, us, dsk[1], chS);
      }
    }
  }
  bar();
  g2_issue(4); g2_issue(5); g2_issue(6); g2_issue(7);
  ST(8);

  f32x4 acc2[2][5];
#pragma unroll
  for (int m = 0; m < 2; ++m)
#pragma unroll
    for (int n = 0; n < 5; ++n) acc2[m][n] = f32x4{0.f, 0.f, 0.f, 0.f};
  {
    LAS char* wb = L + kq * 5120 + lane * 16;
#pragma unroll
    for (int s = 0; s < 12; ++s) {
      if (s == 0) waitv<22 + 12>(); else if (s == 1) waitv<20 + 12>(); else if (s == 2) waitv<18 + 12 + 3>(); else if (s == 3) waitv<16 + 12 + 6>();
      else if (s == 4) waitv<9 + 9>(); else if (s == 5) waitv<6 + 12>(); else if (s == 6) waitv<3 + 12>(); else if (s == 7) waitv<12>();
      else if (s == 8) waitv<9>(); else if (s == 9) waitv<6>(); else if (s == 10) waitv<3>(); else waitv<0>();
      PIN(AF[s][0]); PIN(AF[s][1]);
      bar();
      if (s >= 1 && s <= 4) g2_issue(s + 7);
      if (s == 4 && tid == 0) st64c(WS_F3 + (unsigned)(xcd * 32 + j) * 8, ws, v2u{tlo, thi});
      v4u W[5];
#pragma unroll
      for (int n = 0; n < 5; ++n) W[n] = *(LAS v4u*)(wb + ((s + 4) & 7) * G2_SLOT + n * 1024);
#pragma unroll
      for (int n = 0; n < 5; ++n) {
        acc2[0][n] = mfma(W[n], AF[s][0], acc2[0][n]);
        acc2[1][n] = mfma(W[n], AF[s][1], acc2[1][n]);
      }
      SCHED();
    }
  }
  ST(9);
  {
    const int row2 = 512 * rg + 64 * a2;
    const unsigned fo = WS_F3 + (unsigned)(cb2 * 32 + (lane & 31)) * 8;
    v2u f3; ld64c(f3, fo, ws);
    v4u xv[2], yv[2];
    const int id1 = tid < 128 ? tid + 512 : tid;
    const unsigned oo0 = (unsigned)(tid / 10) * 1280 + (tid % 10) * 16, oo1 = (unsigned)(id1 / 10) * 1280 + (id1 % 10) * 16;
    ld128(xv[0], oo0, x + (size_t)row2 * 1280 + cb2 * 160); ld128(xv[1], oo1, x + (size_t)row2 * 1280 + cb2 * 160);
    waitv<0>(); PIN(f3); PIN(xv[0]); PIN(xv[1]);
    f3 = spin64(f3, fo, ws, tlo, thi, false);
    const char* yb = ws + WS_Y + (size_t)cb2 * 80 * 4096 + row2 * 2;
    ld128c<0>(yv[0], (unsigned)(tid >> 3) * 4096 + (tid & 7) * 16, yb); ld128c<0>(yv[1], (unsigned)(id1 >> 3) * 4096 + (id1 & 7) * 16, yb);
    LAS char* fp = L + kq * FIN_PLANE + (32 * rh + l15) * FIN_PITCH + g * 16;
#pragma unroll
    for (int m = 0; m < 2; ++m)
#pragma unroll
      for (int n = 0; n < 5; ++n) *(LAS f32x4*)(fp + m * 16 * FIN_PITCH + n * 64) = acc2[m][n];
    waitv<0>(); PIN(yv[0]); PIN(yv[1]);
    *(LAS v4u*)(L + YST + (tid >> 3) * 128 + (tid >> 6) * 16 + (tid & 7) * 16) = yv[0];
    if (tid < 128) *(LAS v4u*)(L + YST + (id1 >> 3) * 128 + (id1 >> 6) * 16 + (id1 & 7) * 16) = yv[1];
    bar();
    auto fin2 = [&](int id, v4u xx, unsigned oo) {
      const int t = id / 10, c8 = id % 10;
      LAS char* pp = L + t * FIN_PITCH + c8 * 32;
      f32x4 s0 = (*(LAS f32x4*)pp + *(LAS f32x4*)(pp + FIN_PLANE)) + (*(LAS f32x4*)(pp + 2 * FIN_PLANE) + *(LAS f32x4*)(pp + 3 * FIN_PLANE));
      f32x4 s1 = (*(LAS f32x4*)(pp + 16) + *(LAS f32x4*)(pp + 16 + FIN_PLANE)) + (*(LAS f32x4*)(pp + 16 + 2 * FIN_PLANE) + *(LAS f32x4*)(pp + 16 + 3 * FIN_PLANE));
      LAS u16* yp = (LAS u16*)(L + YST + c8 * 1040 + t * 2);
      float y[8];
#pragma unroll
      for (int i = 0; i < 8; ++i) y[i] = bf2f(yp[i * 64]);
      v4u o;
      o.x = pk2(bf2f(xx.x & 0xffffu) + y[0] + s0[0], bfhi(xx.x) + y[1] + s0[1]);
      o.y = pk2(bf2f(xx.y & 0xffffu) + y[2] + s0[2], bfhi(xx.y) + y[3] + s0[3]);
      o.z = pk2(bf2f(xx.z & 0xffffu) + y[4] + s1[0], bfhi(xx.z) + y[5] + s1[1]);
      o.w = pk2(bf2f(xx.w & 0xffffu) + y[6] + s1[2], bfhi(xx.w) + y[7] + s1[3]);
      st128(oo, out + (size_t)row2 * 1280 + cb2 * 160, o);
    };
    fin2(tid, xv[0], oo0);
    if (tid < 128) fin2(id1, xv[1], oo1);
  }
  ST(10);
}

extern "C" void kernel_launch(void* const* d_in, const int* in_sizes, int n_in, void* d_out, int out_size, void* d_ws, size_t ws_size,
                              hipStream_t stream) {
  fused_s4_mlp<<<256, 512, 0, stream>>>((const char*)d_in[0], (const char*)d_in[1], (const char*)d_in[2], (const char*)d_in[3],
      (const char*)d_in[4], (const char*)d_in[5], (const char*)d_in[6], (const char*)d_in[7], (const char*)d_in[8], (const char*)d_in[10],
      (const char*)d_in[11], (const char*)d_in[12], (char*)d_out, (char*)d_ws);
}
```

```cpp
#include <hip/hip_runtime.h>
#include <stdint.h>

typedef unsigned short u16;
typedef unsigned long long u64;
typedef unsigned v4u __attribute__((ext_vector_type(4)));
typedef unsigned v2u __attribute__((ext_vector_type(2)));
typedef float f32x4 __attribute__((ext_vector_type(4)));
typedef float f32x2 __attribute__((ext_vector_type(2)));
typedef short bf16x8 __attribute__((ext_vector_type(8)));
typedef short s16x4 __attribute__((ext_vector_type(4)));
typedef __bf16 bf16x2_t __attribute__((ext_vector_type(2)));
#define LAS __attribute__((address_space(3)))
#define GAS __attribute__((address_space(1)))
#define DI __device__ __forceinline__

constexpr unsigned WS_E = 0, WS_TMO = 64, WS_F1 = 4096, WS_F3 = 8192, WS_STATS = 16384, WS_F2 = 32768, WS_STAMP = 65536;
constexpr unsigned WS_XNB = 1u << 20, WS_W2T = 4u << 20, WS_KP = 6u << 20, WS_Y = 7u << 20, WS_ACT = 10u << 20, WS_END = 17u << 20;
constexpr unsigned KP_STRIDE = 2624;
constexpr unsigned SPIN_CAP = 400000;

constexpr unsigned G1_STAGE = 28672, G1_BOFF = 16384;
constexpr unsigned KG_SCR = 114688, KG_WSTR = 4352, LN_ROWS = 149504, LN_PITCH = 1344;
constexpr unsigned KTAB = 155648;
constexpr unsigned CS_OFF = 143360;
constexpr unsigned XCH = 0;
constexpr unsigned CV_CH = 27008, CV_USTR = 4640, CV_KOFF = 18560, CV_KSTR = 4224;
constexpr unsigned CP_FULL = 9216;
constexpr unsigned G2_SLOT = 20480;
constexpr unsigned FIN_PITCH = 336, FIN_PLANE = 21504, YST = 90112;

#define ST(i) do {} while (0)

DI float bf2f(unsigned v) { return __builtin_bit_cast(float, v << 16); }
DI float bfhi(unsigned v) { return __builtin_bit_cast(float, v & 0xffff0000u); }
DI unsigned pk2(float lo, float hi) { bf16x2_t r; r.x = (__bf16)lo; r.y = (__bf16)hi; return __builtin_bit_cast(unsigned, r); }
DI float rbf(float f) { return bf2f(pk2(f, f) & 0xffffu); }
DI f32x4 mfma(v4u a, v4u b, f32x4 c) {
  return __builtin_amdgcn_mfma_f32_16x16x32_bf16(__builtin_bit_cast(bf16x8, a), __builtin_bit_cast(bf16x8, b), c, 0, 0, 0);
}
DI v2u trread(LAS char* p) { return __builtin_bit_cast(v2u, __builtin_amdgcn_ds_read_tr16_b64_v4i16((LAS s16x4*)p)); }
template <int N> DI void waitv() { asm volatile("s_waitcnt vmcnt(%0)" ::"i"(N) : "memory"); }
DI void bar() { asm volatile("s_waitcnt lgkmcnt(0)\n\ts_barrier" ::: "memory"); }
#define PIN(x) asm volatile("" : "+v"(x))
#define SCHED() __builtin_amdgcn_sched_barrier(0)

DI void ld16(unsigned& d, unsigned vo, const void* sb) { asm volatile("global_load_ushort %0, %1, %2" : "=v"(d) : "v"(vo), "s"(sb) : "memory"); }
template <int O> DI void ld32(unsigned& d, unsigned vo, const void* sb) { asm volatile("global_load_dword %0, %1, %2 offset:%3" : "=v"(d) : "v"(vo), "s"(sb), "i"(O) : "memory"); }
DI void ld64(v2u& d, unsigned vo, const void* sb) { asm volatile("global_load_dwordx2 %0, %1, %2" : "=v"(d) : "v"(vo), "s"(sb) : "memory"); }
DI void ld128(v4u& d, unsigned vo, const void* sb) { asm volatile("global_load_dwordx4 %0, %1, %2" : "=v"(d) : "v"(vo), "s"(sb) : "memory"); }
DI void ld64c(v2u& d, unsigned vo, const void* sb) { asm volatile("global_load_dwordx2 %0, %1, %2 sc1" : "=v"(d) : "v"(vo), "s"(sb) : "memory"); }
template <int O> DI void ld128c(v4u& d, unsigned vo, const void* sb) { asm volatile("global_load_dwordx4 %0, %1, %2 offset:%3 sc1" : "=v"(d) : "v"(vo), "s"(sb), "i"(O) : "memory"); }
DI void st64c(unsigned vo, void* sb, v2u d) { asm volatile("global_store_dwordx2 %0, %1, %2 sc1" ::"v"(vo), "v"(d), "s"(sb) : "memory"); }
DI void st128c(unsigned vo, void* sb, v4u d) { asm volatile("global_store_dwordx4 %0, %1, %2 sc1\n\ts_nop 1" ::"v"(vo), "v"(d), "s"(sb) : "memory"); }
DI void st128(unsigned vo, void* sb, v4u d) { asm volatile("global_store_dwordx4 %0, %1, %2\n\ts_nop 1" ::"v"(vo), "v"(d), "s"(sb) : "memory"); }
DI void dma16(unsigned vo, const void* sb, unsigned lds) {
  unsigned keep;
  asm volatile("s_mov_b32 %0, m0\n\ts_mov_b32 m0, %3\n\ts_nop 0\n\tglobal_load_lds_dwordx4 %1, %2\n\ts_mov_b32 m0, %0"
               : "=&s"(keep) : "v"(vo), "s"(sb), "s"(lds) : "memory");
}
template <unsigned HI> DI void dma16m(unsigned vo, const void* sb, unsigned lds) {
  unsigned keep;
  asm volatile("s_mov_b32 %0, m0\n\ts_mov_b32 m0, %3\n\ts_mov_b32 exec_hi, %4\n\tglobal_load_lds_dwordx4 %1, %2\n\ts_mov_b32 exec_hi, -1\n\ts_mov_b32 m0, %0"
               : "=&s"(keep) : "v"(vo), "s"(sb), "s"(lds), "i"(HI) : "memory");
}
DI v2u spin64(v2u cur, unsigned vo, char* ws, unsigned tlo, unsigned thi, bool hionly) {
  for (unsigned spins = 0;; ++spins) {
    const bool ok = hionly ? (cur.y == tlo) : (cur.x == tlo && cur.y == thi);
    if (__all(ok)) break;
    if (spins > SPIN_CAP) { *(GAS unsigned*)(ws + WS_TMO) = 1u; break; }
    __builtin_amdgcn_s_sleep(1);
    asm volatile("global_load_dwordx2 %0, %1, %2 sc1\n\ts_waitcnt vmcnt(0)" : "=&v"(cur) : "v"(vo), "s"(ws) : "memory");
  }
  return cur;
}
template <int C> DI float dpp_add(float v) {
  return v + __builtin_bit_cast(float, __builtin_amdgcn_update_dpp(0, __builtin_bit_cast(int, v), C, 0xf, 0xf, true));
}
DI float rl(float v, int l) { return __builtin_bit_cast(float, __builtin_amdgcn_readlane(__builtin_bit_cast(int, v), l)); }
DI float wave_sum(float v) {
  v = dpp_add<0xB1>(v); v = dpp_add<0x4E>(v); v = dpp_add<0x141>(v); v = dpp_add<0x140>(v);
  return (rl(v, 0) + rl(v, 16)) + (rl(v, 32) + rl(v, 48));
}
struct cf { float r, i; };
DI cf cmul(cf a, cf b) { cf o; o.r = a.r * b.r - a.i * b.i; o.i = a.r * b.i + a.i * b.r; return o; }
DI cf cexpk(float zr, float zi, float k) {
  const float mag = __builtin_amdgcn_exp2f(zr * k * 1.44269504088896341f);
  const float t = zi * k;
  const float hi = 0.15915494309189535f, lo = 6.4206383e-9f;
  const float r = t * hi;
  const float e = __builtin_fmaf(t, hi, -r) + t * lo;
  const float fr = (r - __builtin_rintf(r)) + e;
  cf o; o.r = mag * __builtin_amdgcn_cosf(fr); o.i = mag * __builtin_amdgcn_sinf(fr); return o;
}

template <int PAR> DI void conv_unit_shared(LAS char* ka, LAS char* ub, f32x4 (&acc)[8]) {
  constexpr int T[4] = {PAR ? 1 : 0, PAR ? 2 : 3, PAR ? 5 : 4, PAR ? 6 : 7};
  v4u A[8], B[8];
#pragma unroll
  for (int i = 0; i <= T[3]; ++i) { A[i] = *(LAS v4u*)(ka - 512 * i); B[i] = *(LAS v4u*)(ub + 512 * i); }
#pragma unroll
  for (int q = 0; q < 4; ++q)
#pragma unroll
    for (int ip = 0; ip <= T[q]; ++ip) acc[q] = mfma(A[T[q] - ip], B[ip], acc[q]);
}

__global__ __launch_bounds__(512, 2) void fused_s4_mlp(const char* __restrict__ x, const char* __restrict__ pAre, const char* __restrict__ pAim,
    const char* __restrict__ pBre, const char* __restrict__ pBim, const char* __restrict__ pCre, const char* __restrict__ pCim,
    const char* __restrict__ plogdt, const char* __restrict__ pDskip, const char* __restrict__ w1, const char* __restrict__ b1,
    const char* __restrict__ w2, char* __restrict__ out, char* __restrict__ ws) {
  __shared__ __attribute__((aligned(16))) char smem[163840];
  LAS char* L = (LAS char*)smem;
  const unsigned lds0 = (unsigned)(size_t)L;
  const int b = blockIdx.x, tid = threadIdx.x, lane = tid & 63;
  const int w = __builtin_amdgcn_readfirstlane(tid >> 6);
  const int xcd = b & 7, j = b >> 3, rg = xcd >> 1, chf = xcd & 1;
  const int ct1 = 8 * chf + (j & 7), rb1 = 4 * rg + (j >> 3);
  const int wid = rb1 * 16 + ct1;
  const int pwid = wid ^ 1;
  const int par = j & 1, cbase = 80 * xcd + 5 * (j >> 1);
  const int chA = cbase + 3 * par, chS = cbase + 2;
  const int l15 = lane & 15, g = lane >> 4, qq = (lane >> 2) & 3, p4 = lane & 3;
  ST(0);

  v2u E; ld64c(E, 0u, ws);
  unsigned xr[5];
  {
    const char* xrow = x + (size_t)(8 * wid + w) * 1280; const unsigned vo = lane * 4;
    ld32<0>(xr[0], vo, xrow); ld32<256>(xr[1], vo, xrow); ld32<512>(xr[2], vo, xrow); ld32<768>(xr[3], vo, xrow); ld32<1024>(xr[4], vo, xrow);
  }
  unsigned pr[7];
  const int kch = w < 3 ? chA : (w < 6 ? chA + 1 : chS);
  {
    const int mode0 = w < 6 ? (w % 3) * 64 : par * 96 + (w - 6) * 48;
    const unsigned vo = (unsigned)(kch * 192 + mode0 + ((w >= 6 && lane >= 48) ? 47 : lane)) * 2;
    ld16(pr[0], vo, pAre); ld16(pr[1], vo, pAim); ld16(pr[2], vo, pBre); ld16(pr[3], vo, pBim); ld16(pr[4], vo, pCre); ld16(pr[5], vo, pCim);
    ld16(pr[6], (unsigned)kch * 2, plogdt);
  }
  const int pu = b % 240, pks = pu / 5, pcg = pu % 5;
  dma16((unsigned)((lane >> 1) * 1280 + (lane & 1) * 16), w2 + (size_t)pks * 32 * 1280 + (128 * pcg + 16 * w) * 2, lds0 + KG_SCR + w * KG_WSTR);
  unsigned voA[2], voB[2];
#pragma unroll
  for (int i = 0; i < 2; ++i) {
    const int r = (2 * w + i) * 8 + (lane >> 3);
    voA[i] = r * 1280 + (((lane & 7) ^ ((r >> 1) & 7)) * 16);
    const int l2 = lane < 48 ? lane : 47;
    voB[i] = ((2 * w + i) * 4 + l2 / 12) * 3072 + (((l2 % 12) ^ ((w & 1) << 1)) * 16);
  }
  const char* g1a = x + (size_t)rb1 * 128 * 1280;
  const char* g1b = w1 + ct1 * 192;
  auto g1_issue = [&](int s) {
    const unsigned base = lds0 + (s % 5) * G1_STAGE;
    dma16(voA[0], g1a + s * 128, base + (2 * w) * 1024);
    dma16(voA[1], g1a + s * 128, base + (2 * w + 1) * 1024);
    dma16m<0xffffu>(voB[0], g1b + (size_t)s * 64 * 3072, base + G1_BOFF + (2 * w) * 768);
    dma16m<0xffffu>(voB[1], g1b + (size_t)s * 64 * 3072, base + G1_BOFF + (2 * w + 1) * 768);
  };
  g1_issue(0); g1_issue(1); g1_issue(2); g1_issue(3);

  waitv<24>();
  PIN(E); PIN(xr[0]); PIN(xr[1]); PIN(xr[2]); PIN(xr[3]); PIN(xr[4]);
  const u64 tag64 = (((u64)__builtin_amdgcn_readfirstlane(E.y) << 32) | (unsigned)__builtin_amdgcn_readfirstlane(E.x)) + 1ull;
  const unsigned tlo = (unsigned)tag64, thi = (unsigned)(tag64 >> 32);
  ST(1);
  {
    float v[10], s = 0.f;
#pragma unroll
    for (int i = 0; i < 5; ++i) { v[2 * i] = bf2f(xr[i] & 0xffffu); v[2 * i + 1] = bfhi(xr[i]); s += v[2 * i] + v[2 * i + 1]; }
    const float mean = wave_sum(s) * (1.0f / 640.0f);
    float q = 0.f;
#pragma unroll
    for (int i = 0; i < 10; ++i) { const float d = v[i] - mean; q += d * d; }
    const float var = rbf(wave_sum(q) * (1.0f / 640.0f));
    const float m = rbf(mean), sd = rbf(sqrtf(rbf(var + 1e-5f))), rs = 1.0f / sd;
    if (lane == 0) {
      v2u gr; gr.x = (__builtin_bit_cast(unsigned, m) & 0xffff0000u) | (__builtin_bit_cast(unsigned, sd) >> 16); gr.y = tlo;
      st64c(WS_STATS + (unsigned)(8 * wid + w) * 8, ws, gr);
    }
#pragma unroll
    for (int i = 0; i < 5; ++i) {
      const unsigned dd = pk2(v[2 * i] - m, v[2 * i + 1] - m);
      *(LAS unsigned*)(L + LN_ROWS + w * LN_PITCH + lane * 4 + 256 * i) = pk2(bf2f(dd & 0xffffu) * rs, bfhi(dd) * rs);
    }
  }
  waitv<16>();
  PIN(pr[0]); PIN(pr[1]); PIN(pr[2]); PIN(pr[3]); PIN(pr[4]); PIN(pr[5]); PIN(pr[6]);
  {
    LAS char* sp = L + KG_SCR + w * KG_WSTR + (4 * g + qq) * 32 + 8 * p4;
    const v2u lo = trread(sp), hi = trread(sp + 512);
    const int ntg = 8 * pcg + w;
    if (b < 240) st128c((unsigned)lane * 16, ws + WS_W2T + (size_t)(((ntg / 5) * 48 + pks) * 5 + ntg % 5) * 1024, v4u{lo.x, lo.y, hi.x, hi.y});
  }
  bar();
  {
    LAS char* sp = L + LN_ROWS + qq * LN_PITCH + (64 * w + 16 * g) * 2 + 8 * p4;
    const v2u lo = trread(sp), hi = trread(sp + 4 * LN_PITCH);
    st128c((unsigned)(64 * w + lane) * 16, ws + WS_XNB + (size_t)wid * 10240, v4u{lo.x, lo.y, hi.x, hi.y});
    if (w < 2) {
      const v2u lo2 = trread(sp + 1024), hi2 = trread(sp + 1024 + 4 * LN_PITCH);
      st128c((unsigned)(512 + 64 * w + lane) * 16, ws + WS_XNB + (size_t)wid * 10240, v4u{lo2.x, lo2.y, hi2.x, hi2.y});
    }
  }
  ST(2);

  {
    const float dt = rbf(expf(bf2f(pr[6])));
    const float zr = dt * bf2f(pr[0]), zi = dt * bf2f(pr[1]);
    const float dbr = dt * bf2f(pr[2]), dbi = dt * bf2f(pr[3]), cr = bf2f(pr[4]), ci = bf2f(pr[5]);
    cf a; a.r = cr * dbr - ci * dbi; a.i = -(cr * dbi + ci * dbr);
    if (w >= 6 && lane >= 48) { a.r = 0.f; a.i = 0.f; }
    const cf W1 = cexpk(zr, -zi, 1.f), W256 = cexpk(zr, -zi, 256.f), W8 = cexpk(zr, zi, 8.f);
    LAS char* sw = L + KG_SCR + w * KG_WSTR + lane * 4;
    LAS char* sr = L + KG_SCR + w * KG_WSTR + l15 * 272 + g * 16;
#define PUT(row, v) *(LAS unsigned*)(sw + (row) * 272) = pk2((v).r, (v).i)
    const cf W2 = cmul(W1, W1), W512 = cmul(W256, W256);
    cf u0 = a, u1 = cmul(a, W1), u2 = cmul(u0, W2), u3 = cmul(u1, W2), u4 = cmul(u2, W2), u5 = cmul(u3, W2), u6 = cmul(u4, W2), u7 = cmul(u5, W2);
    PUT(0, u0); PUT(1, u1); PUT(2, u2); PUT(3, u3); PUT(4, u4); PUT(5, u5); PUT(6, u6); PUT(7, u7);
    { cf t; t = cmul(u0, W256); PUT(8, t); t = cmul(u2, W256); PUT(9, t); t = cmul(u4, W256); PUT(10, t); t = cmul(u6, W256); PUT(11, t); }
    const cf r12 = cmul(u0, W512), r13 = cmul(u4, W512), r14 = cmul(r12, W256), r15 = cmul(r13, W256);
    PUT(12, r12); PUT(13, r13); PUT(14, r14); PUT(15, r15);
    v4u UA[2][4];
#pragma unroll
    for (int k = 0; k < 4; ++k) UA[0][k] = *(LAS v4u*)(sr + 64 * k);
    const cf r16 = cmul(r12, W512), r17 = cmul(r14, W512), r18 = cmul(r16, W512), r19 = cmul(r17, W512), r20 = cmul(r18, W512);
    PUT(0, r16); PUT(1, r17); PUT(2, r18); PUT(3, r19); PUT(4, r20);
#pragma unroll
    for (int k = 0; k < 4; ++k) UA[1][k] = *(LAS v4u*)(sr + 64 * k);
    const cf W16 = cmul(W8, W8);
    cf ve; ve.r = 1.f; ve.i = 0.f; cf vo = W8;
    f32x4 kacc[2][2];
#pragma unroll
    for (int nh = 0; nh < 2; ++nh) {
#pragma unroll
      for (int i = 0; i < 8; ++i) {
        PUT(2 * i, ve); PUT(2 * i + 1, vo);
        if (nh == 0 || i < 7) { ve = (nh == 0 && i == 0) ? W16 : cmul(ve, W16); vo = cmul(vo, W16); }
      }
      kacc[0][nh] = f32x4{0.f, 0.f, 0.f, 0.f}; kacc[1][nh] = f32x4{0.f, 0.f, 0.f, 0.f};
#pragma unroll
      for (int k = 0; k < 4; ++k) {
        const v4u vb = *(LAS v4u*)(sr + 64 * k);
        kacc[0][nh] = mfma(UA[0][k], vb, kacc[0][nh]);
        kacc[1][nh] = mfma(UA[1][k], vb, kacc[1][nh]);
      }
    }
#undef PUT
    LAS float* pt = (LAS float*)(L + KG_SCR + w * KG_WSTR);
#pragma unroll
    for (int nh = 0; nh < 2; ++nh) {
      const int bb = 16 * nh + l15;
      const int a0 = g < 2 ? 8 * bb + 4 * g : (g == 2 ? 256 + 4 * bb : 384 + 2 * bb);
      const int a1 = g < 3 ? a0 + 2 : a0 + 64;
      *(LAS f32x2*)(pt + a0) = f32x2{kacc[0][nh][0], kacc[0][nh][1]};
      *(LAS f32x2*)(pt + a1) = f32x2{kacc[0][nh][2], kacc[0][nh][3]};
      if (g == 0) {
#pragma unroll
        for (int r = 0; r < 4; ++r) pt[512 + 32 * r + bb] = kacc[1][nh][r];
      }
    }
    if (lane == 16) *(LAS f32x4*)(pt + 640) = f32x4{kacc[1][0][0], 0.f, 0.f, 0.f};
  }
  bar();
  if (tid < 483) {
    const int c = tid / 161, i = tid % 161;
    LAS char* pp = L + KG_SCR + 3 * c * KG_WSTR + i * 16;
    f32x4 s = *(LAS f32x4*)pp + *(LAS f32x4*)(pp + KG_WSTR);
    if (c < 2) s += *(LAS f32x4*)(pp + 2 * KG_WSTR);
    *(LAS f32x4*)(L + KTAB + c * KP_STRIDE + i * 16) = s;
    if (c == 2) st128c((unsigned)i * 16, ws + WS_KP + (size_t)wid * KP_STRIDE, __builtin_bit_cast(v4u, s));
  }
  waitv<0>();
  bar();
  if (tid == 0) st64c(WS_F1 + (unsigned)wid * 8, ws, v2u{tlo, thi});
  g1_issue(4);
  ST(3);

  const int rgp = w >> 1, kh = w & 1, rot = rgp % 3;
  f32x4 acc1[2][6], csa[2];
#pragma unroll
  for (int m = 0; m < 2; ++m) {
    csa[m] = f32x4{0.f, 0.f, 0.f, 0.f};
#pragma unroll
    for (int n = 0; n < 6; ++n) acc1[m][n] = f32x4{0.f, 0.f, 0.f, 0.f};
  }
  v2u gran, f1own, f1par, bia[6];
  {
    unsigned xo[2], wo[6];
#pragma unroll
    for (int m = 0; m < 2; ++m) {
      const int r = 32 * rgp + 16 * (m ^ kh) + l15;
      xo[m] = r * 128 + (((4 * kh + g) ^ ((r >> 1) & 7)) * 16);
    }
#pragma unroll
    for (int n = 0; n < 6; ++n) {
      const int pg = ((n >> 1) + rot) % 3;
      wo[n] = G1_BOFF + (32 * kh + 8 * g + qq) * 192 + 64 * pg + 32 * ((n & 1) ^ (g & 1)) + 8 * p4;
    }
    v4u X[2][2], W[2][6];
    auto rd = [&](int s, v4u (&xx)[2], v4u (&ww)[6]) {
      LAS char* sb = L + (s % 5) * G1_STAGE;
#pragma unroll
      for (int m = 0; m < 2; ++m) xx[m] = *(LAS v4u*)(sb + xo[m]);
#pragma unroll
      for (int n = 0; n < 6; ++n) { const v2u lo = trread(sb + wo[n]), hi = trread(sb + wo[n] + 768); ww[n] = v4u{lo.x, lo.y, hi.x, hi.y}; }
    };
    const v4u ones = v4u{0x3f803f80u, 0x3f803f80u, 0x3f803f80u, 0x3f803f80u};
    rd(0, X[0], W[0]);
#pragma unroll
    for (int s = 0; s < 10; ++s) {
      if (s < 9) {
        if (s >= 3) { if (s <= 5) waitv<12>(); else if (s == 6) waitv<8>(); else if (s == 7) waitv<4>(); else waitv<0>(); }
        bar();
        if (s + 5 < 10) g1_issue(s + 5);
        if (s == 8) {
          ld64c(gran, WS_STATS + (unsigned)(rb1 * 128 + 32 * rgp + 16 * kh + l15) * 8, ws);
          ld64c(f1own, WS_F1 + (unsigned)(64 * (w & 3) + lane) * 8, ws);
          ld64c(f1par, WS_F1 + (unsigned)pwid * 8, ws);
#pragma unroll
          for (int n = 0; n < 6; ++n) ld64(bia[n], (unsigned)(ct1 * 96 + 32 * (((n >> 1) + rot) % 3) + 16 * (n & 1) + 4 * g) * 2, b1);
        }
        rd(s + 1, X[(s + 1) & 1], W[(s + 1) & 1]);
      }
#pragma unroll
      for (int n = 0; n < 6; ++n) {
        acc1[0][n] = mfma(W[s & 1][n], X[s & 1][0], acc1[0][n]);
        acc1[1][n] = mfma(W[s & 1][n], X[s & 1][1], acc1[1][n]);
        if (n < 2) csa[n] = mfma(W[s & 1][n], ones, csa[n]);
      }
      SCHED();
    }
  }
  ST(4);
  waitv<0>();
  PIN(gran); PIN(f1own); PIN(f1par);
#pragma unroll
  for (int n = 0; n < 6; ++n) PIN(bia[n]);
  f1own = spin64(f1own, WS_F1 + (unsigned)(64 * (w & 3) + lane) * 8, ws, tlo, thi, false);
  f1par = spin64(f1par, WS_F1 + (unsigned)pwid * 8, ws, tlo, thi, false);
  gran = spin64(gran, WS_STATS + (unsigned)(rb1 * 128 + 32 * rgp + 16 * kh + l15) * 8, ws, tlo, thi, true);
  v4u uo, us, kp;
  const int tb = 64 * (w & 3) + lane;
  ld128c<0>(uo, (unsigned)tb * 10240 + (unsigned)(chA + (w >> 2)) * 16, ws + WS_XNB);
  ld128c<0>(us, (unsigned)tb * 10240 + (unsigned)chS * 16, ws + WS_XNB);
  ld128c<0>(kp, (unsigned)(tid < 161 ? tid : 160) * 16, ws + WS_KP + (size_t)pwid * KP_STRIDE);
  unsigned dsk[2];
  ld16(dsk[0], (unsigned)(chA + (w >> 2)) * 2, pDskip); ld16(dsk[1], (unsigned)chS * 2, pDskip);
#pragma unroll
  for (int n = 0; n < 6; ++n) *(LAS f32x4*)(L + XCH + w * 6144 + n * 1024 + lane * 16) = acc1[1][n];
  if (rgp < 3 && l15 == 0) {
#pragma unroll
    for (int n = 0; n < 2; ++n) *(LAS f32x4*)(L + CS_OFF + kh * 384 + (32 * rot + 16 * n + 4 * g) * 4) = csa[n];
  }
  bar();
  if (tid == 0) {
    asm volatile("buffer_inv sc1" ::: "memory");
    if (b == 0) st64c(WS_E, ws, v2u{tlo, thi});
  }
  {
    const float m = bfhi(gran.x), rs = 1.0f / bf2f(gran.x & 0xffffu), mrs = m * rs;
    unsigned pk[6][2];
#pragma unroll
    for (int n = 0; n < 6; ++n) {
      const f32x4 o = *(LAS f32x4*)(L + XCH + (w ^ 1) * 6144 + n * 1024 + lane * 16);
      const int fo = (32 * (((n >> 1) + rot) % 3) + 16 * (n & 1) + 4 * g) * 4;
      const f32x4 cs = *(LAS f32x4*)(L + CS_OFF + fo) + *(LAS f32x4*)(L + CS_OFF + 384 + fo);
      const float bv[4] = {bf2f(bia[n].x & 0xffffu), bfhi(bia[n].x), bf2f(bia[n].y & 0xffffu), bfhi(bia[n].y)};
      float r[4];
#pragma unroll
      for (int e = 0; e < 4; ++e) {
        const float h = __builtin_fmaf(acc1[0][n][e] + o[e], rs, __builtin_fmaf(-mrs, cs[e], bv[e]));
        const float ex = __builtin_amdgcn_exp2f(__builtin_fminf(h * 1.44269504088896341f, 40.f));
        const float nn = ex * (ex + 2.f);
        r[e] = h * nn * __builtin_amdgcn_rcpf(nn + 2.f);
      }
      pk[n][0] = pk2(r[0], r[1]); pk[n][1] = pk2(r[2], r[3]);
    }
    char* ab = ws + WS_ACT + (size_t)((rb1 * 8 + 2 * rgp + kh) * 48 + 3 * ct1) * 1024;
#pragma unroll
    for (int P = 0; P < 3; ++P)
      st128c((unsigned)(((P + rot) % 3) * 1024 + lane * 16), ab, v4u{pk[2 * P][0], pk[2 * P][1], pk[2 * P + 1][0], pk[2 * P + 1][1]});
  }
  waitv<0>();
  PIN(uo); PIN(us); PIN(kp); PIN(dsk[0]); PIN(dsk[1]);
  if (tid < 161) *(LAS f32x4*)(L + KTAB + 2 * KP_STRIDE + tid * 16) += __builtin_bit_cast(f32x4, kp);
  bar();
  if (tid == 0) st64c(WS_F2 + (unsigned)wid * 128, ws, v2u{tlo, thi});
  ST(5);

  const int a2 = j >> 2, cb2 = 4 * chf + (j & 3);
  const int rh = w & 1, kq = w >> 1;
  const char* w2b = ws + WS_W2T + (size_t)(cb2 * 48 + 12 * kq) * 5120;
  auto g2_issue = [&](int s) {
    const unsigned base = lds0 + ((s + 4) & 7) * G2_SLOT + kq * 5120;
    const char* sb = w2b + s * 5120;
    dma16((unsigned)(rh * 2048 + lane * 16), sb, base + rh * 2048);
    dma16((unsigned)(rh * 2048 + 1024 + lane * 16), sb, base + rh * 2048 + 1024);
    dma16m<0u>((unsigned)(4096 + rh * 512 + lane * 16), sb, base + 4096 + rh * 512);
  };
  g2_issue(0); g2_issue(1); g2_issue(2);
  {
    auto put_u = [&](LAS char* cbp, v4u u) {
      LAS char* q = cbp + tb * 16;
      *(LAS v4u*)(q + 3 * CV_USTR + 384) = u;
      *(LAS v2u*)(q + 1 * CV_USTR + 392) = v2u{u.x, u.y}; *(LAS v2u*)(q + 1 * CV_USTR + 400) = v2u{u.z, u.w};
      *(LAS unsigned*)(q + 2 * CV_USTR + 388) = u.x; *(LAS v2u*)(q + 2 * CV_USTR + 392) = v2u{u.y, u.z}; *(LAS unsigned*)(q + 2 * CV_USTR + 400) = u.w;
      *(LAS unsigned*)(q + 396) = u.x; *(LAS v2u*)(q + 400) = v2u{u.y, u.z}; *(LAS unsigned*)(q + 408) = u.w;
    };
    put_u(L + (w >> 2) * CV_CH, uo);
    if (w >= 4) put_u(L + 2 * CV_CH, us);
    if (w >= 4) {
      const int t4 = tid - 256;
#pragma unroll
      for (int i = 0; i < 5; ++i) {
        const int e = t4 + 256 * i, cp = e / 100, d = e % 100;
        if (cp < 12 && d < 99 - (cp & 3)) *(LAS unsigned*)(L + (cp >> 2) * CV_CH + (cp & 3) * CV_USTR + d * 4) = 0u;
      }
      if (t4 < 42) *(LAS v4u*)(L + (t4 / 14) * CV_CH + CV_KOFF + ((t4 / 7) & 1) * CV_KSTR + 4096 + (t4 % 7) * 16) = v4u{0u, 0u, 0u, 0u};
    }
    int ci[9];
#pragma unroll
    for (int i = 0; i < 9; ++i) {
      const int lg = 2047 - 8 * tb - i;
      const int bits = (int)(pk2((float)lg, 0.f) & 0xffffu);
      ci[i] = lg < 128 ? (lg < 0 ? 641 : lg) : bits - 0x4280;
    }
    auto put_k = [&](int c) {
      LAS float* kt = (LAS float*)(L + KTAB + c * KP_STRIDE);
      float kv[9];
#pragma unroll
      for (int i = 0; i < 9; ++i) kv[i] = kt[ci[i]];
      LAS char* q = L + c * CV_CH + CV_KOFF + tb * 16;
      *(LAS v4u*)(q + CV_KSTR) = v4u{pk2(kv[0], kv[1]), pk2(kv[2], kv[3]), pk2(kv[4], kv[5]), pk2(kv[6], kv[7])};
      *(LAS v4u*)q = v4u{pk2(kv[1], kv[2]), pk2(kv[3], kv[4]), pk2(kv[5], kv[6]), pk2(kv[7], kv[8])};
    };
    put_k(w >> 2);
    if (w < 4) put_k(2);
  }
  bar();
  g2_issue(3);
  ST(6);
  f32x4 cacc[8];
#pragma unroll
  for (int i = 0; i < 8; ++i) cacc[i] = f32x4{0.f, 0.f, 0.f, 0.f};
  {
    const int cc = w < 4 ? (w >> 1) : (w < 6 ? w - 4 : 2);
    const int h0 = w < 4 ? 3 * (w & 1) : (w < 6 ? 6 : 4 * (w - 6));
    LAS char* ka = L + cc * CV_CH + CV_KOFF + (l15 & 1) * CV_KSTR + 2 * (2072 - 32 * h0 - 8 * (l15 >> 1) + 8 * g);
    LAS char* ub = L + cc * CV_CH + (l15 & 3) * CV_USTR + 2 * (64 * (l15 >> 2) + 8 * g - 32 * h0 + 224);
    if (w < 6) {
      const int nu = w < 4 ? 3 : 2;
      for (int u = 0; u < nu; ++u) {
        v4u A[8], B[8];
#pragma unroll
        for (int i = 0; i < 8; ++i) { A[i] = *(LAS v4u*)(ka - 512 * i); B[i] = *(LAS v4u*)(ub + 512 * i); }
#pragma unroll
        for (int sdiag = 0; sdiag < 8; ++sdiag)
#pragma unroll
          for (int o = 0; o <= sdiag; ++o) {
            if (sdiag + o < 8) cacc[sdiag + o] = mfma(A[sdiag], B[o], cacc[sdiag + o]);
            if (o != sdiag && sdiag + o < 8) cacc[sdiag + o] = mfma(A[o], B[sdiag], cacc[sdiag + o]);
          }
        ka -= 64; ub -= 64;
      }
    } else {
      for (int u = 0; u < 4; ++u) {
        if (par) conv_unit_shared<1>(ka, ub, cacc); else conv_unit_shared<0>(ka, ub, cacc);
        ka -= 64; ub -= 64;
      }
    }
  }
  ST(7);
  v4u AF[12][2];
  {
    const unsigned fo = WS_F2 + (unsigned)((4 * rg + (a2 >> 1)) * 16 + 4 * kq + p4) * 128;
    v2u f2; ld64c(f2, fo, ws); waitv<0>(); PIN(f2);
    f2 = spin64(f2, fo, ws, tlo, thi, false);
    const char* ab = ws + WS_ACT + (size_t)((32 * rg + 4 * a2 + 2 * rh) * 48 + 12 * kq) * 1024;
    const unsigned vo = lane * 16;
#pragma unroll
    for (int s = 0; s < 12; s += 4) {
      const char* a0 = ab + s * 1024; const char* a1 = a0 + 48 * 1024;
      ld128c<0>(AF[s][0], vo, a0); ld128c<0>(AF[s][1], vo, a1);
      ld128c<1024>(AF[s + 1][0], vo, a0); ld128c<1024>(AF[s + 1][1], vo, a1);
      ld128c<2048>(AF[s + 2][0], vo, a0); ld128c<2048>(AF[s + 2][1], vo, a1);
      ld128c<3072>(AF[s + 3][0], vo, a0); ld128c<3072>(AF[s + 3][1], vo, a1);
    }
  }
  bar();
  {
    LAS char* pp = L + (w < 6 ? w * CP_FULL : 6 * CP_FULL + (w - 6) * (CP_FULL / 2)) + (16 * g + 2 * (l15 & 3) + 72 * (l15 >> 2)) * 4;
#pragma unroll
    for (int i = 0; i < 8; ++i)
      if (i < 4 || w < 6) {
        *(LAS f32x2*)(pp + i * 1152) = f32x2{cacc[i][0], cacc[i][1]};
        *(LAS f32x2*)(pp + i * 1152 + 32) = f32x2{cacc[i][2], cacc[i][3]};
      }
  }
  bar();
  {
    auto fin = [&](LAS char* p0, LAS char* p1, LAS char* p2, bool three, v4u u, unsigned dk, int chn) {
      f32x4 s0 = *(LAS f32x4*)p0 + *(LAS f32x4*)p1, s1 = *(LAS f32x4*)(p0 + 16) + *(LAS f32x4*)(p1 + 16);
      if (three) { s0 += *(LAS f32x4*)p2; s1 += *(LAS f32x4*)(p2 + 16); }
      const float d = bf2f(dk);
      v4u o;
      o.x = pk2(__builtin_fmaf(d, bf2f(u.x & 0xffffu), s0[0]), __builtin_fmaf(d, bfhi(u.x), s0[1]));
      o.y = pk2(__builtin_fmaf(d, bf2f(u.y & 0xffffu), s0[2]), __builtin_fmaf(d, bfhi(u.y), s0[3]));
      o.z = pk2(__builtin_fmaf(d, bf2f(u.z & 0xffffu), s1[0]), __builtin_fmaf(d, bfhi(u.z), s1[1]));
      o.w = pk2(__builtin_fmaf(d, bf2f(u.w & 0xffffu), s1[2]), __builtin_fmaf(d, bfhi(u.w), s1[3]));
      st128c((unsigned)tb * 16, ws + WS_Y + (size_t)chn * 4096, o);
    };
    const int c = w >> 2, po = (8 * tb + 8 * (tb >> 3)) * 4;
    fin(L + 2 * c * CP_FULL + po, L + (2 * c + 1) * CP_FULL + po, L + (4 + c) * CP_FULL + po, true, uo, dsk[0], chA + c);
    if (w >= 4) {
      const int q = w & 3;
      const int mine = ((q & 1) ^ par);
      if ((lane >> 5) == mine) {
        const int tl = 32 * q + (lane & 31), ps = (8 * tl + 8 * (tl >> 3)) * 4;
        fin(L + 6 * CP_FULL + ps, L + 6 * CP_FULL + CP_FULL / 2 + ps, L, false, us, dsk[1], chS);
      }
    }
  }
  bar();
  g2_issue(4); g2_issue(5); g2_issue(6); g2_issue(7);
  ST(8);

  f32x4 acc2[2][5];
#pragma unroll
  for (int m = 0; m < 2; ++m)
#pragma unroll
    for (int n = 0; n < 5; ++n) acc2[m][n] = f32x4{0.f, 0.f, 0.f, 0.f};
  {
    LAS char* wb = L + kq * 5120 + lane * 16;
#pragma unroll
    for (int s = 0; s < 12; ++s) {
      if (s == 0) waitv<22 + 12>(); else if (s == 1) waitv<20 + 12>(); else if (s == 2) waitv<18 + 12 + 3>(); else if (s == 3) waitv<16 + 12 + 6>();
      else if (s == 4) waitv<9 + 9>(); else if (s == 5) waitv<6 + 12>(); else if (s == 6) waitv<3 + 12>(); else if (s == 7) waitv<12>();
      else if (s == 8) waitv<9>(); else if (s == 9) waitv<6>(); else if (s == 10) waitv<3>(); else waitv<0>();
      PIN(AF[s][0]); PIN(AF[s][1]);
      bar();
      if (s >= 1 && s <= 4) g2_issue(s + 7);
      if (s == 4 && tid == 0) st64c(WS_F3 + (unsigned)(xcd * 32 + j) * 8, ws, v2u{tlo, thi});
      v4u W[5];
#pragma unroll
      for (int n = 0; n < 5; ++n) W[n] = *(LAS v4u*)(wb + ((s + 4) & 7) * G2_SLOT + n * 1024);
#pragma unroll
      for (int n = 0; n < 5; ++n) {
        acc2[0][n] = mfma(W[n], AF[s][0], acc2[0][n]);
        acc2[1][n] = mfma(W[n], AF[s][1], acc2[1][n]);
      }
      SCHED();
    }
  }
  ST(9);
  {
    const int row2 = 512 * rg + 64 * a2;
    const unsigned fo = WS_F3 + (unsigned)(cb2 * 32 + (lane & 31)) * 8;
    v2u f3; ld64c(f3, fo, ws);
    v4u xv[2], yv[2];
    const int id1 = tid < 128 ? tid + 512 : tid;
    const unsigned oo0 = (unsigned)(tid / 10) * 1280 + (tid % 10) * 16, oo1 = (unsigned)(id1 / 10) * 1280 + (id1 % 10) * 16;
    ld128(xv[0], oo0, x + (size_t)row2 * 1280 + cb2 * 160); ld128(xv[1], oo1, x + (size_t)row2 * 1280 + cb2 * 160);
    waitv<0>(); PIN(f3); PIN(xv[0]); PIN(xv[1]);
    f3 = spin64(f3, fo, ws, tlo, thi, false);
    const char* yb = ws + WS_Y + (size_t)cb2 * 80 * 4096 + row2 * 2;
    ld128c<0>(yv[0], (unsigned)(tid >> 3) * 4096 + (tid & 7) * 16, yb); ld128c<0>(yv[1], (unsigned)(id1 >> 3) * 4096 + (id1 & 7) * 16, yb);
    LAS char* fp = L + kq * FIN_PLANE + (32 * rh + l15) * FIN_PITCH + g * 16;
#pragma unroll
    for (int m = 0; m < 2; ++m)
#pragma unroll
      for (int n = 0; n < 5; ++n) *(LAS f32x4*)(fp + m * 16 * FIN_PITCH + n * 64) = acc2[m][n];
    waitv<0>(); PIN(yv[0]); PIN(yv[1]);
    *(LAS v4u*)(L + YST + (tid >> 3) * 128 + (tid >> 6) * 16 + (tid & 7) * 16) = yv[0];
    if (tid < 128) *(LAS v4u*)(L + YST + (id1 >> 3) * 128 + (id1 >> 6) * 16 + (id1 & 7) * 16) = yv[1];
    bar();
    auto fin2 = [&](int id, v4u xx, unsigned oo) {
      const int t = id / 10, c8 = id % 10;
      LAS char* pp = L + t * FIN_PITCH + c8 * 32;
      f32x4 s0 = (*(LAS f32x4*)pp + *(LAS f32x4*)(pp + FIN_PLANE)) + (*(LAS f32x4*)(pp + 2 * FIN_PLANE) + *(LAS f32x4*)(pp + 3 * FIN_PLANE));
      f32x4 s1 = (*(LAS f32x4*)(pp + 16) + *(LAS f32x4*)(pp + 16 + FIN_PLANE)) + (*(LAS f32x4*)(pp + 16 + 2 * FIN_PLANE) + *(LAS f32x4*)(pp + 16 + 3 * FIN_PLANE));
      LAS u16* yp = (LAS u16*)(L + YST + c8 * 1040 + t * 2);
      float y[8];
#pragma unroll
      for (int i = 0; i < 8; ++i) y[i] = bf2f(yp[i * 64]);
      v4u o;
      o.x = pk2(bf2f(xx.x & 0xffffu) + y[0] + s0[0], bfhi(xx.x) + y[1] + s0[1]);
      o.y = pk2(bf2f(xx.y & 0xffffu) + y[2] + s0[2], bfhi(xx.y) + y[3] + s0[3]);
      o.z = pk2(bf2f(xx.z & 0xffffu) + y[4] + s1[0], bfhi(xx.z) + y[5] + s1[1]);
      o.w = pk2(bf2f(xx.w & 0xffffu) + y[6] + s1[2], bfhi(xx.w) + y[7] + s1[3]);
      st128(oo, out + (size_t)row2 * 1280 + cb2 * 160, o);
    };
    fin2(tid, xv[0], oo0);
    if (tid < 128) fin2(id1, xv[1], oo1);
  }
  ST(10);
}

__global__ void k_t0(unsigned long long* p){ if(threadIdx.x==0 && blockIdx.x==0) p[0]=__builtin_amdgcn_s_memrealtime(); }
__global__ void k_t1(const unsigned long long* p){ unsigned long long t=p[0]; while(__builtin_amdgcn_s_memrealtime()-t<6000) __builtin_amdgcn_s_sleep(1); }
extern "C" void kernel_launch(void* const* d_in, const int* in_sizes, int n_in, void* d_out, int out_size, void* d_ws, size_t ws_size,
                              hipStream_t stream) {
  k_t0<<<1,64,0,stream>>>((unsigned long long*)((char*)d_ws+(40<<20)));
  fused_s4_mlp<<<256, 512, 0, stream>>>((const char*)d_in[0], (const char*)d_in[1], (const char*)d_in[2], (const char*)d_in[3],
      (const char*)d_in[4], (const char*)d_in[5], (const char*)d_in[6], (const char*)d_in[7], (const char*)d_in[8], (const char*)d_in[10],
      (const char*)d_in[11], (const char*)d_in[12], (char*)d_out, (char*)d_ws);
  k_t1<<<1,64,0,stream>>>((unsigned long long*)((char*)d_ws+(40<<20)));
}
```

```cpp
#include <hip/hip_runtime.h>
#include <stdint.h>

#if defined(__HIP_DEVICE_COMPILE__)
#pragma clang attribute push(__attribute__((target("no-packed-fp32-ops"))), apply_to = function)
#endif
typedef unsigned short u16;
typedef unsigned long long u64;
typedef unsigned v4u __attribute__((ext_vector_type(4)));
typedef unsigned v2u __attribute__((ext_vector_type(2)));
typedef float f32x4 __attribute__((ext_vector_type(4)));
typedef float f32x2 __attribute__((ext_vector_type(2)));
typedef short bf16x8 __attribute__((ext_vector_type(8)));
typedef short s16x4 __attribute__((ext_vector_type(4)));
typedef __bf16 bf16x2_t __attribute__((ext_vector_type(2)));
#define LAS __attribute__((address_space(3)))
#define GAS __attribute__((address_space(1)))
#define DI __device__ __forceinline__

constexpr unsigned WS_E = 0, WS_TMO = 64, WS_F1 = 131072, WS_STATS = 16384, WS_F2 = 32768, WS_F3 = 65536, WS_FK = 98304, WS_STAMP = 262144;
constexpr unsigned WS_XNB = 1u << 20, WS_W2T = 4u << 20, WS_Y = 7u << 20, WS_ACT = 10u << 20, WS_KP = 17u << 20;
constexpr unsigned KP_STRIDE = 2624;
constexpr unsigned SPIN_CAP = 400000;

constexpr unsigned W_RDY = 0, W_TAG = 32, W_TAGOK = 40, W_SB = 44, W_F1C = 48, W_F2C = 52, W_F3C = 56, W_KG = 60, W_FKC = 64,
                   W_F1ALL = 68, W_SX = 72, W_FM = 84, W_FS = 88, W_F2OK = 96, W_F3ALL = 112;
constexpr unsigned KTAB = 272;
constexpr unsigned STAT = 9728;
constexpr unsigned CSP = 8192;
constexpr unsigned CVA = 10240, CV_CH = 27008, CV_USTR = 4640, CV_KOFF = 18560, CV_KSTR = 4224;
constexpr unsigned KG_SCR = CVA, KG_WSTR = 4352, LN_ROWS = KG_SCR + 8 * KG_WSTR, LN_PITCH = 1344, W2P = LN_ROWS + 10752;
constexpr unsigned G1_RING = 64512, G1_STAGE = 28672, G1_BOFF = 16384;
constexpr unsigned CVS = G1_RING, SXCH = CVS + 27136, MXCH = SXCH + 16384;
constexpr unsigned CSF = G1_RING + 3 * G1_STAGE, YST = CSF + 3072;
constexpr unsigned FM = 256, FS = FM + 81920;
static_assert(FS + 61440 <= CSF && CVA + 2 * CV_CH <= G1_RING && W2P + 8192 <= G1_RING && YST + 10240 <= 163840 && MXCH + 16384 <= CSF, "lds map");

#define F1GAP 16
#define F2GAP 12
#define SPRIO 1
#define ST(i) do {} while (0)
#define STEND() do {} while (0)
#define STARG
#define STPASS

DI float bf2f(unsigned v) { return __builtin_bit_cast(float, v << 16); }
DI float bfhi(unsigned v) { return __builtin_bit_cast(float, v & 0xffff0000u); }
DI unsigned pk2(float lo, float hi) { bf16x2_t r; r.x = (__bf16)lo; r.y = (__bf16)hi; return __builtin_bit_cast(unsigned, r); }
DI float rbf(float f) { return bf2f(pk2(f, f) & 0xffffu); }
DI f32x4 mfma(v4u a, v4u b, f32x4 c) {
  return __builtin_amdgcn_mfma_f32_16x16x32_bf16(__builtin_bit_cast(bf16x8, a), __builtin_bit_cast(bf16x8, b), c, 0, 0, 0);
}
DI v2u trread(LAS char* p) { return __builtin_bit_cast(v2u, __builtin_amdgcn_ds_read_tr16_b64_v4i16((LAS s16x4*)p)); }
template <int N> DI void waitv() { asm volatile("s_waitcnt vmcnt(%0)" ::"i"(N) : "memory"); }
DI void bar() { asm volatile("s_waitcnt lgkmcnt(0)\n\ts_barrier" ::: "memory"); }
#define PIN(x) asm volatile("" : "+v"(x))
#define FENCE() asm volatile("" ::: "memory")
#define SCHED() __builtin_amdgcn_sched_barrier(0)
#define RFL(x) __builtin_amdgcn_readfirstlane(x)

DI void ld16(unsigned& d, unsigned vo, const void* sb) { asm volatile("global_load_ushort %0, %1, %2" : "=v"(d) : "v"(vo), "s"(sb) : "memory"); }
template <int O> DI void ld32(unsigned& d, unsigned vo, const void* sb) { asm volatile("global_load_dword %0, %1, %2 offset:%3" : "=v"(d) : "v"(vo), "s"(sb), "i"(O) : "memory"); }
DI void ld64(v2u& d, unsigned vo, const void* sb) { asm volatile("global_load_dwordx2 %0, %1, %2" : "=v"(d) : "v"(vo), "s"(sb) : "memory"); }
DI void ld128(v4u& d, unsigned vo, const void* sb) { asm volatile("global_load_dwordx4 %0, %1, %2" : "=v"(d) : "v"(vo), "s"(sb) : "memory"); }
DI void ld64c(v2u& d, unsigned vo, const void* sb) { asm volatile("global_load_dwordx2 %0, %1, %2 sc1" : "=v"(d) : "v"(vo), "s"(sb) : "memory"); }
template <int O> DI void ld128c(v4u& d, unsigned vo, const void* sb) { asm volatile("global_load_dwordx4 %0, %1, %2 offset:%3 sc1" : "=v"(d) : "v"(vo), "s"(sb), "i"(O) : "memory"); }
DI void st64c(unsigned vo, void* sb, v2u d) { asm volatile("global_store_dwordx2 %0, %1, %2 sc1" ::"v"(vo), "v"(d), "s"(sb) : "memory"); }
template <int O> DI void ld64o(v2u& d, unsigned vo, const void* sb) { asm volatile("global_load_dwordx2 %0, %1, %2 offset:%3" : "=v"(d) : "v"(vo), "s"(sb), "i"(O) : "memory"); }
template <int O> DI void st64co(unsigned vo, void* sb, v2u d) { asm volatile("global_store_dwordx2 %0, %1, %2 offset:%3 sc1" ::"v"(vo), "v"(d), "s"(sb), "i"(O) : "memory"); }
DI void st128c(unsigned vo, void* sb, v4u d) { asm volatile("global_store_dwordx4 %0, %1, %2 sc1\n\ts_nop 1" ::"v"(vo), "v"(d), "s"(sb) : "memory"); }
DI void st128u(unsigned vo, void* sb, v4u d) { asm volatile("global_store_dwordx4 %0, %1, %2 sc1 ; CNT\n\ts_nop 1" ::"v"(vo), "v"(d), "s"(sb) : "memory"); }
DI void dma16(unsigned vo, const void* sb, unsigned lds) {
  asm volatile("s_mov_b32 m0, %2\n\ts_nop 0\n\tglobal_load_lds_dwordx4 %0, %1" ::"v"(vo), "s"(sb), "s"(lds) : "memory");
}
template <int O> DI void dma16o(unsigned vo, const void* sb) {
  asm volatile("global_load_lds_dwordx4 %0, %1 offset:%2" ::"v"(vo), "s"(sb), "i"(O) : "memory");
}
template <bool HIONLY> DI v2u spin64(v2u cur, unsigned vo, char* ws, unsigned tlo, unsigned thi) {
  if (__builtin_expect(__builtin_amdgcn_ballot_w64(HIONLY ? (cur.y != tlo) : (cur.x != tlo || cur.y != thi)) != 0ull, 0)) {
#pragma nounroll
    for (unsigned spins = 0; spins < SPIN_CAP; ++spins) {
      __builtin_amdgcn_s_sleep(6);
      asm volatile("global_load_dwordx2 %0, %1, %2 sc1\n\ts_waitcnt vmcnt(0)" : "=&v"(cur) : "v"(vo), "s"(ws) : "memory");
      if (__builtin_amdgcn_ballot_w64(HIONLY ? (cur.y != tlo) : (cur.x != tlo || cur.y != thi)) == 0ull) break;
    }
  }
  return cur;
}
DI unsigned lw(LAS char* L, unsigned off) { return *(volatile LAS unsigned*)(L + off); }
DI void lds_wait_at(unsigned a, unsigned target) {
  unsigned v, cnt; u64 t;
  asm volatile("s_mov_b32 %1, %5\n"
               "1:\n\tds_read_b32 %0, %3\n\ts_sub_u32 %1, %1, 1\n\ts_waitcnt lgkmcnt(0)\n\tv_cmp_gt_u32 vcc, %4, %0\n\t"
               "s_cselect_b64 %2, 0, vcc\n\ts_cmp_lg_u64 %2, 0\n\ts_cbranch_scc1 1b"
               : "=&v"(v), "=&s"(cnt), "=&s"(t) : "v"(a), "s"(target), "i"(4 * SPIN_CAP) : "vcc", "scc", "memory");
}
DI void lds_wait_ge(LAS char* L, unsigned off, unsigned target) { lds_wait_at((unsigned)(size_t)L + off, target); }
template <int N> DI void lds_wait_min(LAS char* L, unsigned off, unsigned target, int lane) {
  lds_wait_at((unsigned)(size_t)L + off + (lane & (N - 1)) * 4, target);
}
DI void lds_set(LAS char* L, unsigned off, unsigned v) { FENCE(); *(volatile LAS unsigned*)(L + off) = v; FENCE(); }
DI void lds_arrive(LAS char* L, unsigned off, int) {
  asm volatile("s_mov_b64 exec, 1\n\tds_add_u32 %0, %1\n\ts_mov_b64 exec, -1" ::"v"((unsigned)(size_t)L + off), "v"(1u) : "memory");
}
template <unsigned N = 4> DI void arrive_flag(LAS char* L, unsigned off, int, char* ws, unsigned fo, unsigned tlo, unsigned thi) {
  unsigned old;
  asm volatile("s_mov_b64 exec, 1\n\tds_add_rtn_u32 %0, %1, %2\n\ts_waitcnt lgkmcnt(0)\n\tv_cmp_eq_u32 vcc, %6, %0\n\t"
               "s_mov_b64 exec, vcc\n\tglobal_store_dwordx2 %3, %4, %5 sc1\n\ts_mov_b64 exec, -1"
               : "=&v"(old) : "v"((unsigned)(size_t)L + off), "v"(1u), "v"(fo), "v"(v2u{tlo, thi}), "s"(ws), "i"(N - 1) : "vcc", "memory");
}
DI void arrive_flag8(LAS char* L, unsigned off, char* ws, unsigned fo, unsigned tlo, unsigned thi) {
  unsigned old;
  asm volatile("s_mov_b64 exec, 1\n\tds_add_rtn_u32 %0, %1, %2\n\ts_waitcnt lgkmcnt(0)\n\tv_cmp_eq_u32 vcc, 3, %0\n\t"
               "s_mul_i32 exec_lo, vcc_lo, 0xff\n\tglobal_store_dwordx2 %3, %4, %5 sc1\n\ts_mov_b64 exec, -1"
               : "=&v"(old) : "v"((unsigned)(size_t)L + off), "v"(1u), "v"(fo), "v"(v2u{tlo, thi}), "s"(ws) : "vcc", "memory");
}
typedef float f32x16 __attribute__((ext_vector_type(16)));
DI f32x16 mfma32(v4u a, v4u b, f32x16 c) {
  return __builtin_amdgcn_mfma_f32_32x32x16_bf16(__builtin_bit_cast(bf16x8, a), __builtin_bit_cast(bf16x8, b), c, 0, 0, 0);
}
template <int C> DI float dpp_add(float v) {
  return v + __builtin_bit_cast(float, __builtin_amdgcn_update_dpp(0, __builtin_bit_cast(int, v), C, 0xf, 0xf, true));
}
DI float rl(float v, int l) { return __builtin_bit_cast(float, __builtin_amdgcn_readlane(__builtin_bit_cast(int, v), l)); }
DI float wave_sum(float v) {
  v = dpp_add<0xB1>(v); v = dpp_add<0x4E>(v); v = dpp_add<0x141>(v); v = dpp_add<0x140>(v);
  return (rl(v, 0) + rl(v, 16)) + (rl(v, 32) + rl(v, 48));
}
DI f32x2 unpk(unsigned v) { return f32x2{bf2f(v), bfhi(v)}; }
typedef f32x2 cf;
DI cf cmul1(cf i, cf w) { return cf{__builtin_fmaf(i.x, w.x, -(i.y * w.y)), __builtin_fmaf(i.x, w.y, i.y * w.x)}; }
DI void cmul4(cf& o0, cf& o1, cf& o2, cf& o3, cf i0, cf i1, cf i2, cf i3, cf w0, cf w1, cf w2, cf w3) {
  o0 = cmul1(i0, w0); o1 = cmul1(i1, w1); o2 = cmul1(i2, w2); o3 = cmul1(i3, w3);
}
DI unsigned pkc(cf v) { return pk2(v.x, v.y); }
template <int R> DI void put4(cf a, cf b, cf c, cf d) {
  asm volatile("ds_write_addtid_b32 %0 offset:%4\n\tds_write_addtid_b32 %1 offset:%5\n\tds_write_addtid_b32 %2 offset:%6\n\tds_write_addtid_b32 %3 offset:%7"
               ::"v"(pkc(a)), "v"(pkc(b)), "v"(pkc(c)), "v"(pkc(d)), "i"(R * 272), "i"(R * 272 + 272), "i"(R * 272 + 544), "i"(R * 272 + 816) : "memory");
}
template <int R> DI void put1(cf a) { asm volatile("ds_write_addtid_b32 %0 offset:%1" ::"v"(pkc(a)), "i"(R * 272) : "memory"); }
DI cf cexpk(float e1, float th, float tl, float k, float ks) {
  const float mag = __builtin_amdgcn_exp2f(e1 * k);
  const float fr = __builtin_fmaf(tl, ks, __builtin_amdgcn_fractf(th * ks));
  return cf{__builtin_amdgcn_cosf(fr), __builtin_amdgcn_sinf(fr)} * mag;
}
DI void kgen_slow(cf a, cf W1, cf W256, cf W8, LAS char* sr, f32x4 (&kacc)[2][2]) {
  cf W2, W512, u1, W16, u2, u3, W4, W32, u4, u5, u6, u7, r8, r9, r10, r11, r12, r13, r14, r15, r16, r17, W1024, v3, r18, r19, r20, v1;
  cmul4(W2, W512, u1, W16, W1, W256, a, W8, W1, W256, W1, W8);
  cmul4(u2, u3, W4, W32, a, u1, W2, W16, W2, W2, W2, W16);
  put4<0>(a, u1, u2, u3);
  cmul4(u4, u5, u6, u7, a, u1, u2, u3, W4, W4, W4, W4);
  put4<4>(u4, u5, u6, u7);
  cmul4(r8, r9, r10, r11, a, u2, u4, u6, W256, W256, W256, W256);
  put4<8>(r8, r9, r10, r11);
  cmul4(r12, r13, r14, r15, a, u4, r8, r10, W512, W512, W512, W512);
  put4<12>(r12, r13, r14, r15);
  v4u UA[2][4];
#pragma unroll
  for (int k = 0; k < 4; ++k) UA[0][k] = *(LAS v4u*)(sr + 64 * k);
  cmul4(r16, r17, W1024, v3, r12, r14, W512, W8, W512, W512, W512, W16);
  cmul4(r18, r19, r20, v1, r12, r14, r16, W8, W1024, W1024, W1024, W32);
  put4<0>(r16, r17, r18, r19);
  put1<4>(r20);
#pragma unroll
  for (int k = 0; k < 4; ++k) UA[1][k] = *(LAS v4u*)(sr + 64 * k);
  put4<0>(cf{1.f, 0.f}, W8, W16, v3);
  cf v0 = W32, v2, t0, t1, t2, t3;
  cmul4(v2, t3, t0, t1, W16, v3, v0, v1, W32, W32, W32, W32);
  put4<4>(v0, v1, v2, t3);
  cmul4(t2, v3, v0, v1, v2, t3, t0, t1, W32, W32, W32, W32);
  put4<8>(t0, t1, t2, v3);
  cmul4(v2, t3, t0, t1, t2, v3, v0, v1, W32, W32, W32, W32);
  put4<12>(v0, v1, v2, t3);
  v4u vb[4];
#pragma unroll
  for (int k = 0; k < 4; ++k) vb[k] = *(LAS v4u*)(sr + 64 * k);
  cmul4(t2, v3, v0, v1, v2, t3, t0, t1, W32, W32, W32, W32);
  put4<0>(t0, t1, t2, v3);
  cmul4(v2, t3, t0, t1, t2, v3, v0, v1, W32, W32, W32, W32);
  put4<4>(v0, v1, v2, t3);
#pragma unroll
  for (int k = 0; k < 4; ++k) {
    kacc[0][0] = mfma(UA[0][k], vb[k], kacc[0][0]);
    kacc[1][0] = mfma(UA[1][k], vb[k], kacc[1][0]);
  }
  cmul4(t2, v3, v0, v1, v2, t3, t0, t1, W32, W32, W32, W32);
  put4<8>(t0, t1, t2, v3);
  cmul4(v2, t3, t0, t1, t2, v3, v0, v1, W32, W32, W32, W32);
  put4<12>(v0, v1, v2, t3);
#pragma unroll
  for (int k = 0; k < 4; ++k) vb[k] = *(LAS v4u*)(sr + 64 * k);
  SCHED();
#pragma unroll
  for (int k = 0; k < 4; ++k) {
    kacc[0][1] = mfma(UA[0][k], vb[k], kacc[0][1]);
    kacc[1][1] = mfma(UA[1][k], vb[k], kacc[1][1]);
  }
}
typedef _Float16 f16x8 __attribute__((ext_vector_type(8)));
typedef _Float16 f16x2 __attribute__((ext_vector_type(2)));
DI f32x4 mfmah(v4u a, v4u b, f32x4 c) {
  return __builtin_amdgcn_mfma_f32_16x16x32_f16(__builtin_bit_cast(f16x8, a), __builtin_bit_cast(f16x8, b), c, 0, 0, 0);
}
DI unsigned pkh(cf v) { f16x2 r; r.x = (_Float16)v.x; r.y = (_Float16)v.y; return __builtin_bit_cast(unsigned, r); }
DI cf csq(cf v) { return cf{__builtin_fmaf(v.x, v.x, -(v.y * v.y)), (v.x + v.x) * v.y}; }
#define HMUL(o, i, w) "v_pk_mul_f16 " o ", " i ", " w " op_sel:[1,1] op_sel_hi:[1,0] neg_lo:[0,1]\n\t"
#define HFMA(o, i, w) "v_pk_fma_f16 " o ", " i ", " w ", " o " op_sel_hi:[0,1,1]\n\t"
template <int R0, int R1, int R2, int R3>
DI void h4(unsigned& o0, unsigned& o1, unsigned& o2, unsigned& o3, unsigned i0, unsigned i1, unsigned i2, unsigned i3, unsigned w0, unsigned w1, unsigned w2, unsigned w3) {
  asm volatile(HMUL("%0", "%4", "%8") HMUL("%1", "%5", "%9") HMUL("%2", "%6", "%10") HMUL("%3", "%7", "%11")
               HFMA("%0", "%4", "%8") HFMA("%1", "%5", "%9") HFMA("%2", "%6", "%10") HFMA("%3", "%7", "%11")
               "ds_write_addtid_b32 %0 offset:%12\n\tds_write_addtid_b32 %1 offset:%13\n\tds_write_addtid_b32 %2 offset:%14\n\tds_write_addtid_b32 %3 offset:%15"
               : "=&v"(o0), "=&v"(o1), "=&v"(o2), "=&v"(o3)
               : "v"(i0), "v"(i1), "v"(i2), "v"(i3), "v"(w0), "v"(w1), "v"(w2), "v"(w3), "i"(R0 * 272), "i"(R1 * 272), "i"(R2 * 272), "i"(R3 * 272) : "memory");
}
template <int R> DI void hput(unsigned v) { asm volatile("ds_write_addtid_b32 %0 offset:%1" ::"v"(v), "i"(R * 272) : "memory"); }
template <int C> DI float dpp_max(float v) {
  return __builtin_fmaxf(v, __builtin_bit_cast(float, __builtin_amdgcn_update_dpp(0, __builtin_bit_cast(int, v), C, 0xf, 0xf, true)));
}
template <class F> DI float kgen_task(const unsigned (&pr)[7], bool dead, unsigned sw, LAS char* sr, f32x4 (&kacc)[2][2], F mid) {
  const float dt = rbf(__builtin_amdgcn_exp2f(bf2f(pr[6]) * 1.44269504088896341f));
  const float zr = dt * bf2f(pr[0]), zi = dt * bf2f(pr[1]);
  const float dbr = dt * bf2f(pr[2]), dbi = dt * bf2f(pr[3]), cr = bf2f(pr[4]), ci = bf2f(pr[5]);
  cf a = cf{cr * dbr - ci * dbi, -(cr * dbi + ci * dbr)};
  if (dead) a = cf{0.f, 0.f};
  const float e1 = zr * 1.44269504088896341f, th = zi * 0.15915494309189535f;
  const float tl = __builtin_fmaf(zi, 6.4206383e-9f, __builtin_fmaf(zi, 0.15915494309189535f, -th));
  const cf W1 = cexpk(e1, th, tl, 1.f, -1.f), W256 = cexpk(e1, th, tl, 256.f, -256.f), W8 = cexpk(e1, th, tl, 8.f, 8.f);
  if (__builtin_expect(__builtin_amdgcn_ballot_w64(!(zr <= 0.f)) != 0ull, 0)) {
    mid();
    asm volatile("s_mov_b32 m0, %0" ::"s"(sw) : "memory");
#pragma unroll
    for (int i = 0; i < 4; ++i) kacc[i >> 1][i & 1] = f32x4{0.f, 0.f, 0.f, 0.f};
    kgen_slow(a, W1, W256, W8, sr, kacc);
    return 1.f;
  }
  float am = __builtin_fmaxf(__builtin_fabsf(a.x), __builtin_fabsf(a.y));
  am = dpp_max<0xB1>(am); am = dpp_max<0x4E>(am); am = dpp_max<0x141>(am); am = dpp_max<0x140>(am);
  const unsigned e0 = __builtin_amdgcn_readlane(__builtin_bit_cast(unsigned, am), 0), e16 = __builtin_amdgcn_readlane(__builtin_bit_cast(unsigned, am), 16);
  const unsigned e32 = __builtin_amdgcn_readlane(__builtin_bit_cast(unsigned, am), 32), e48 = __builtin_amdgcn_readlane(__builtin_bit_cast(unsigned, am), 48);
  unsigned ex = max(max(e0, e16), max(e32, e48)) >> 23;
  ex = min(max(ex, 2u), 252u);
  const float up = __builtin_bit_cast(float, ex << 23), down = __builtin_bit_cast(float, (254u - ex) << 23);
  const cf W2 = csq(W1), W512 = csq(W256), W16 = csq(W8), W4 = csq(W2), W1024 = csq(W512), W32 = csq(W16), W64 = csq(W32), W128 = csq(W64);
  const unsigned u0 = pkh(a * down), h1 = pkh(W1), h2 = pkh(W2), h4_ = pkh(W4), h256 = pkh(W256), h512 = pkh(W512), h1024 = pkh(W1024);
  unsigned u1, u2, u4, r8, u3, u5, u6, r12, u7, r9, r10, r14, r11, r13, r15, r16, r17, r18, r19, r20;
  { unsigned t_ = u0; PIN(t_); }
  mid();
  asm volatile("s_mov_b32 m0, %0\n\ts_nop 0" ::"s"(sw) : "memory");
  hput<0>(u0);
  h4<1, 2, 4, 8>(u1, u2, u4, r8, u0, u0, u0, u0, h1, h2, h4_, h256);
  h4<3, 5, 6, 12>(u3, u5, u6, r12, u2, u4, u4, u0, h1, h1, h2, h512);
  h4<7, 9, 10, 14>(u7, r9, r10, r14, u6, r8, r8, r8, h1, h2, h4_, h512);
  h4<11, 13, 15, 15>(r11, r13, r16, r15, r10, r12, u0, r14, h2, h4_, h1024, h4_);
  v4u UA[2][4];
#pragma unroll
  for (int k = 0; k < 4; ++k) UA[0][k] = *(LAS v4u*)(sr + 64 * k);
  hput<0>(r16);
  h4<1, 2, 3, 4>(r17, r18, r19, r20, r8, r12, r14, r16, h1024, h1024, h1024, h1024);
#pragma unroll
  for (int k = 0; k < 4; ++k) UA[1][k] = *(LAS v4u*)(sr + 64 * k);
  unsigned v[16], t0, t1, t2, t3;
  v[1] = pkh(W8); v[2] = pkh(W16); v[4] = pkh(W32); v[8] = pkh(W64);
  const unsigned h128 = pkh(W128);
  hput<0>(0x3c00u); hput<1>(v[1]); hput<2>(v[2]); hput<4>(v[4]); hput<8>(v[8]);
  h4<3, 5, 6, 9>(v[3], v[5], v[6], v[9], v[2], v[4], v[4], v[8], v[1], v[1], v[2], v[1]);
  h4<7, 10, 11, 12>(v[7], v[10], v[11], v[12], v[4], v[8], v[8], v[8], v[3], v[2], v[3], v[4]);
  h4<13, 14, 15, 15>(v[13], v[14], t0, v[15], v[8], v[8], v[1], v[8], v[5], v[6], h128, v[7]);
  v4u vb[4];
#pragma unroll
  for (int k = 0; k < 4; ++k) vb[k] = *(LAS v4u*)(sr + 64 * k);
  hput<0>(h128); hput<1>(t0);
  h4<2, 3, 4, 5>(t0, t1, t2, t3, v[2], v[3], v[4], v[5], h128, h128, h128, h128);
#pragma unroll
  for (int k = 0; k < 4; ++k) {
    kacc[0][0] = mfmah(UA[0][k], vb[k], k ? kacc[0][0] : f32x4{0.f, 0.f, 0.f, 0.f});
    kacc[1][0] = mfmah(UA[1][k], vb[k], k ? kacc[1][0] : f32x4{0.f, 0.f, 0.f, 0.f});
  }
  h4<6, 7, 8, 9>(t0, t1, t2, t3, v[6], v[7], v[8], v[9], h128, h128, h128, h128);
  h4<10, 11, 12, 13>(t0, t1, t2, t3, v[10], v[11], v[12], v[13], h128, h128, h128, h128);
  h4<14, 15, 14, 15>(t0, t1, t2, t3, v[14], v[15], v[14], v[15], h128, h128, h128, h128);
#pragma unroll
  for (int k = 0; k < 4; ++k) vb[k] = *(LAS v4u*)(sr + 64 * k);
  SCHED();
#pragma unroll
  for (int k = 0; k < 4; ++k) {
    kacc[0][1] = mfmah(UA[0][k], vb[k], k ? kacc[0][1] : f32x4{0.f, 0.f, 0.f, 0.f});
    kacc[1][1] = mfmah(UA[1][k], vb[k], k ? kacc[1][1] : f32x4{0.f, 0.f, 0.f, 0.f});
  }
  return up;
}
DI void kgen_store(LAS float* pt, const f32x4 (&kacc)[2][2], float scale, int lane) {
  const int l15 = lane & 15, g = lane >> 4;
  const int sh = g < 2 ? 3 : 4 - g;
  const int a0 = (l15 << sh) + (g < 2 ? 4 * g : 128 * g);
  const int a1 = g < 3 ? a0 + 2 : a0 + 64;
#pragma unroll
  for (int nh = 0; nh < 2; ++nh) {
    *(LAS f32x2*)(pt + a0 + ((16 * nh) << sh)) = kacc[0][nh].xy;
    *(LAS f32x2*)(pt + a1 + ((16 * nh) << sh)) = kacc[0][nh].zw;
  }
  if (g == 0) {
#pragma unroll
    for (int nh = 0; nh < 2; ++nh)
#pragma unroll
      for (int r = 0; r < 4; ++r) pt[512 + 32 * r + 16 * nh + l15] = kacc[1][nh][r];
  }
  if (lane == 16) { *(LAS f32x4*)(pt + 640) = f32x4{kacc[1][0][0], 0.f, 0.f, 0.f}; pt[644] = scale; }
}

template <int PAR> DI void conv_unit_shared(LAS char* ka, LAS char* ub, f32x4 (&acc)[4]) {
  constexpr int T[4] = {PAR ? 1 : 0, PAR ? 2 : 3, PAR ? 5 : 4, PAR ? 6 : 7};
  v4u A[8], B[8];
#pragma unroll
  for (int i = 0; i <= T[3]; ++i) { A[i] = *(LAS v4u*)(ka - 512 * i); B[i] = *(LAS v4u*)(ub + 512 * i); }
  SCHED();
#pragma unroll
  for (int q = 0; q < 4; ++q)
#pragma unroll
    for (int ip = 0; ip <= T[q]; ++ip) acc[q] = mfma(A[T[q] - ip], B[ip], acc[q]);
}
DI int rot12(int v) { return v >= 12 ? v - 12 : v; }
DI void w_loads(v4u (&WF)[5], const char* p, unsigned vo) {
  ld128c<0>(WF[0], vo, p); ld128c<1024>(WF[1], vo, p); ld128c<2048>(WF[2], vo, p); ld128c<3072>(WF[3], vo, p); ld128c<0>(WF[4], vo, p + 4096);
}
DI void a_loads(v4u (&AF)[4], const char* p, const unsigned (&vm)[4]) {
#pragma unroll
  for (int m = 0; m < 4; ++m) ld128c<0>(AF[m], vm[m], p);
}
template <int LIVE> DI void g2_step(f32x4 (&acc)[4][5], v4u (&WF)[5], v4u (&AF)[4]) {
#pragma unroll
  for (int m = 0; m < 4; ++m) PIN(AF[m]);
#pragma unroll
  for (int n = 0; n < 5; ++n) PIN(WF[n]);
#pragma unroll
  for (int m = 0; m < 4; ++m)
#pragma unroll
    for (int n = 0; n < 5; ++n) acc[m][n] = mfma(WF[n], AF[m], m >= LIVE ? f32x4{0.f, 0.f, 0.f, 0.f} : acc[m][n]);
  SCHED();
}
DI void make_kix(unsigned (&kix)[9], int ts) {
#pragma unroll
  for (int i = 0; i < 9; ++i) {
    const int lg = 2047 - 8 * ts - i;
    const int bits = (int)(pk2(__builtin_fmaxf((float)lg, 128.f), 0.f));
    const int ci = lg < bits - 0x4280 ? lg : bits - 0x4280;
    kix[i] = KTAB + 4 * ci;
    PIN(kix[i]);
  }
}
DI void put_k(LAS char* L, const unsigned (&kix)[9], int ts, int c, unsigned reg) {
  float kv[9];
#pragma unroll
  for (int i = 0; i < 9; ++i) kv[i] = *(LAS float*)(L + kix[i] + c * KP_STRIDE);
  LAS char* q = L + reg + CV_KOFF + ts * 16;
  *(LAS v4u*)(q + CV_KSTR) = v4u{pk2(kv[0], kv[1]), pk2(kv[2], kv[3]), pk2(kv[4], kv[5]), pk2(kv[6], kv[7])};
  *(LAS v4u*)q = v4u{pk2(kv[1], kv[2]), pk2(kv[3], kv[4]), pk2(kv[5], kv[6]), pk2(kv[7], kv[8])};
}
DI void put_u(LAS char* q, v4u u, bool first) {
  *(LAS v4u*)(q + 3 * CV_USTR + 384) = u;
  *(LAS v2u*)(q + 1 * CV_USTR + 392) = v2u{u.x, u.y}; *(LAS v2u*)(q + 1 * CV_USTR + 400) = v2u{u.z, u.w};
  *(LAS unsigned*)(q + 2 * CV_USTR + 388) = u.x; *(LAS v2u*)(q + 2 * CV_USTR + 392) = v2u{u.y, u.z}; *(LAS unsigned*)(q + 2 * CV_USTR + 400) = u.w;
  *(LAS unsigned*)(q + 396) = u.x; *(LAS v2u*)(q + 400) = v2u{u.y, u.z}; *(LAS unsigned*)(q + 408) = u.w;
  if (first) {
    *(LAS v2u*)(q + 384) = v2u{0u, 0u}; *(LAS unsigned*)(q + 392) = 0u;
    *(LAS v2u*)(q + CV_USTR + 384) = v2u{0u, 0u}; *(LAS unsigned*)(q + 2 * CV_USTR + 384) = 0u;
  }
}

__global__ __launch_bounds__(512, 2) void fused_s4_mlp(const char* __restrict__ x, const char* __restrict__ pAre, const char* __restrict__ pAim,
    const char* __restrict__ pBre, const char* __restrict__ pBim, const char* __restrict__ pCre, const char* __restrict__ pCim,
    const char* __restrict__ plogdt, const char* __restrict__ pDskip, const char* __restrict__ w1, const char* __restrict__ b1,
    const char* __restrict__ w2, char* __restrict__ out, char* __restrict__ ws) {
  __shared__ __attribute__((aligned(16))) char smem[163840];
  LAS char* L = (LAS char*)smem;
  const unsigned lds0 = (unsigned)(size_t)L;
  const char* pc0; unsigned codelen;
  asm volatile("s_getpc_b64 %0\n98:\n\ts_mov_b32 %1, 97f-98b\n\ts_max_u32 %1, %1, 99f-98b" : "=s"(pc0), "=&s"(codelen) :: "scc");
  const int b = blockIdx.x, lane = threadIdx.x & 63;
  const int w = RFL(threadIdx.x >> 6), wi = w & 3;
  {
    const unsigned co = (unsigned)(4 * (16 * w + (lane & 15)) + ((b >> 3) & 3)) * 128;
    asm volatile("s_mov_b32 m0, %2\n\ts_nop 0\n\tglobal_load_lds_dword %0, %1" ::"v"((co < codelen ? co : codelen) & ~3u), "s"(pc0), "s"(lds0 + YST + w * 256) : "memory");
  }
  const int xcd = b & 7, j = b >> 3, rg = xcd >> 1, chf = xcd & 1;
  const int ct1 = 8 * chf + (j & 7), rb1 = 4 * rg + (j >> 3);
  const int wid = rb1 * 16 + ct1;
  const int a2 = j >> 2, cb2 = 4 * chf + (j & 3);
  const int l15 = lane & 15, g = lane >> 4, qq = (lane >> 2) & 3, p4 = lane & 3, l31 = lane & 31, h = lane >> 5;
  const int par = j & 1, cbase = 80 * xcd + 5 * (j >> 1);
  const int chA = cbase + 3 * par, chS = cbase + 2;
  const int ts = 64 * wi + lane;
  ST(0);
  const int row2 = 512 * rg + 64 * a2 + 16 * wi;
  const char* w2b = ws + WS_W2T + (size_t)(cb2 * 48 + 12 * wi) * 5120;
  const char* actb = ws + WS_ACT + (size_t)((32 * rg + 4 * a2) * 48 + 12 * wi) * 1024;
  const int r2 = (3 * (j & 3) + (3 * a2) / 2 + (w < 4 ? 0 : 6)) % 12;
#define WL(s) w_loads(WF[s], w2b + rot12(r2 + (s)) * 5120, vo)
#define AL(s) a_loads(AF[s], actb + rot12(r2 + (s)) * 1024, vm)
#define LATE_LANE() int ln_ = lane; PIN(ln_); const int c15 = ln_ & 15, cg = ln_ >> 4
#define KAO (CV_KOFF + (c15 & 1) * CV_KSTR + 2 * (2072 - 8 * (c15 >> 1) + 8 * cg))
#define UBO ((c15 & 3) * CV_USTR + 2 * (64 * (c15 >> 2) + 8 * cg + 224))
#define YOFF ((unsigned)((c15 >> 2) * 128 + cg * 32 + (c15 & 3) * 8))
#define OROW ((unsigned)(2 * (c15 >> 2) + (c15 & 1) + 8 * ((c15 >> 1) & 1)))
#define ZOFF (lane < 24 ? wi * CV_USTR + lane * 16 : CV_KOFF + wi * CV_KSTR + 4096 + (lane - 24) * 16)
#define ZCOND (lane < 24 || (lane < 31 && wi < 2))

  v2u xa, xb, E; unsigned xc;
  {
    const char* xrow = x + (size_t)(8 * wid + w) * 1280;
    ld64o<0>(xa, lane * 8, xrow); ld64o<512>(xb, lane * 8, xrow); ld32<1024>(xc, lane * 4, xrow);
    ld64c(E, 0u, ws);
  }
  const int pu = b % 240, pks = pu / 5, pcg = pu % 5;
  dma16((unsigned)((lane >> 1) * 1280 + (lane & 1) * 16), w2 + (size_t)pks * 32 * 1280 + (128 * pcg + 16 * w) * 2, lds0 + W2P + w * 1024);
  unsigned pr[7];
  {
    const int kc = w < 3 ? chA : (w < 6 ? chA + 1 : chS);
    const int m0 = w < 6 ? (w < 3 ? w : w - 3) * 64 : par * 96 + (w - 6) * 48;
    const unsigned vo = (unsigned)(kc * 192 + m0 + ((w >= 6 && lane >= 48) ? 47 : lane)) * 2;
    ld16(pr[6], (unsigned)kc * 2, plogdt);
    ld16(pr[0], vo, pAre); ld16(pr[1], vo, pAim); ld16(pr[2], vo, pBre); ld16(pr[3], vo, pBim); ld16(pr[4], vo, pCre); ld16(pr[5], vo, pCim);
  }
  unsigned voA[4], voB[3];
#pragma unroll
  for (int i = 0; i < 4; ++i) voA[i] = (8 * i + (lane >> 3)) * 1280 + (((lane & 7) ^ (4 * (i & 1) + (lane >> 4))) * 16) - 1024 * i;
#pragma unroll
  for (int i = 0; i < 3; ++i) { const int c = 64 * i + lane; voB[i] = (c / 12) * 3072 + (c % 12) * 16 - 1024 * i; }
  const char* g1a = x + (size_t)(rb1 * 128 + 32 * wi) * 1280;
  const char* g1b = w1 + ct1 * 192 + wi * 49152;
  const unsigned la = lds0 + G1_RING + wi * 4096, lb = lds0 + G1_RING + G1_BOFF + wi * 3072;
  const int r1 = ((j & 7) + (5 * (j >> 3)) / 2 + 5 * chf + rg) % 10;
  auto slab = [&](int s) { const int v = s + r1; return v >= 10 ? v - 10 : v; };
  auto issue = [&](int s) {
    const char* pa = g1a + slab(s) * 128; const char* pb = g1b + (size_t)slab(s) * 64 * 3072;
    dma16(voA[0], pa, la + s * G1_STAGE); dma16o<1024>(voA[1], pa); dma16o<2048>(voA[2], pa); dma16o<3072>(voA[3], pa);
    dma16(voB[0], pb, lb + s * G1_STAGE); dma16o<1024>(voB[1], pb); dma16o<2048>(voB[2], pb);
  };
  if (w >= 4) __builtin_amdgcn_s_setprio(SPRIO);
  if (w == 4) *(LAS unsigned*)(L + lane * 4) = 0u;
  bar();
  if (w < 4) issue(0);
  unsigned kix[9];
  make_kix(kix, ts);
  if (w < 4) waitv<14>(); else waitv<7>();
  asm volatile("; AUDITLEAVE7");
  PIN(E); PIN(xa); PIN(xb); PIN(xc);
  const u64 tag64 = (((u64)RFL(E.y) << 32) | (unsigned)RFL(E.x)) + 1ull;
  const unsigned tlo = (unsigned)tag64, thi = (unsigned)(tag64 >> 32);
  ST(1);
  {
    f32x2 v[5] = {unpk(xa.x), unpk(xa.y), unpk(xb.x), unpk(xb.y), unpk(xc)};
    const f32x2 s2 = ((v[0] + v[1]) + (v[2] + v[3])) + v[4];
    const float mean = wave_sum(s2.x + s2.y) * (1.0f / 640.0f);
    f32x2 q2 = f32x2{0.f, 0.f};
#pragma unroll
    for (int i = 0; i < 5; ++i) { const f32x2 d = v[i] - mean; q2 = __builtin_elementwise_fma(d, d, q2); }
    const float var = rbf(wave_sum(q2.x + q2.y) * (1.0f / 640.0f));
    const float m = rbf(mean), sd = rbf(__builtin_amdgcn_sqrtf(rbf(var + 1e-5f))), rs = __builtin_amdgcn_rcpf(sd);
    unsigned o[5];
#pragma unroll
    for (int i = 0; i < 5; ++i) { const f32x2 t = (v[i] - m) * rs; o[i] = pk2(t.x, t.y); }
    LAS char* rp = L + LN_ROWS + w * LN_PITCH;
    *(LAS v2u*)(rp + lane * 8) = v2u{o[0], o[1]}; *(LAS v2u*)(rp + 512 + lane * 8) = v2u{o[2], o[3]}; *(LAS unsigned*)(rp + 1024 + lane * 4) = o[4];
    *(LAS unsigned*)(L + STAT + w * 4) = (__builtin_bit_cast(unsigned, m) & 0xffff0000u) | (__builtin_bit_cast(unsigned, sd) >> 16);
  }
  if (w < 4) waitv<7>(); else waitv<0>();
  asm volatile("; AUDITCLR");
#pragma unroll
  for (int i = 0; i < 7; ++i) PIN(pr[i]);
  ST(8);
  bar();
  if (w >= 4) {
    LAS char* sp = L + LN_ROWS + qq * LN_PITCH + (160 * wi + 16 * g) * 2 + 8 * p4;
#pragma unroll
    for (int i = 0; i < 3; ++i) {
      const v2u lo = trread(sp + 128 * i), hi = trread(sp + 128 * i + 4 * LN_PITCH);
      if (i < 2 || lane < 32) st128c((unsigned)(160 * wi + 64 * i + lane) * 16, ws + WS_XNB + (size_t)wid * 10240, v4u{lo.x, lo.y, hi.x, hi.y});
    }
    if (lane < 2) st64c(WS_STATS + (unsigned)(8 * wid + 2 * wi + lane) * 8, ws, v2u{*(LAS unsigned*)(L + STAT + (2 * wi + lane) * 4), tlo});
#pragma unroll
    for (int i = 0; i < 2; ++i) {
      LAS char* wp = L + W2P + wi * 2048 + i * 1024 + (16 * (g >> 1) + 4 * (g & 1) + qq) * 32 + 8 * p4;
      const v2u lo = trread(wp), hi = trread(wp + 256);
      const int ntg = 8 * pcg + 2 * wi + i;
      st128c((unsigned)lane * 16, ws + WS_W2T + (size_t)(((ntg / 5) * 48 + pks) * 5 + ntg % 5) * 1024, v4u{lo.x, lo.y, hi.x, hi.y});
    }
  }
  ST(9);
  {
    f32x4 kacc[2][2];
    const float scale = kgen_task(pr, w >= 6 && lane >= 48, lds0 + KG_SCR + w * KG_WSTR, L + KG_SCR + w * KG_WSTR + l15 * 272 + g * 16, kacc, [&]() { if (w < 4) issue(1); });
    kgen_store((LAS float*)(L + KG_SCR + w * KG_WSTR), kacc, scale, lane);
  }
  ST(2);
  lds_arrive(L, W_KG, lane);

  if (w < 4) {
    issue(2);
    f32x16 acc1[3]; f32x4 csa[3];
#pragma unroll
    for (int n = 0; n < 3; ++n) {
      csa[n] = f32x4{0.f, 0.f, 0.f, 0.f};
#pragma unroll
      for (int i = 0; i < 16; ++i) acc1[n][i] = 0.f;
    }
    unsigned xo[4], wo[4];
#pragma unroll
    for (int k = 0; k < 4; ++k) {
      const int kr = (k + wi) & 3;
      xo[k] = G1_RING + (32 * wi + l31) * 128 + (((2 * kr + h) ^ ((l31 >> 1) & 7)) * 16);
      wo[k] = G1_RING + G1_BOFF + (16 * kr + 8 * h + qq) * 192 + 32 * (g & 1) + 8 * p4;
      PIN(xo[k]); PIN(wo[k]);
    }
    const unsigned sel1 = ((l15 ^ g) & 1) ? 0u : 0x3f803f80u;
    const v4u sel = v4u{sel1, sel1, sel1, sel1};
    v4u X[4], W[4][3];
    auto rdw = [&](int s, int k, int n) {
      LAS char* p = L + wo[k] + (s % 3) * G1_STAGE + 64 * n; const v2u lo = trread(p), hi = trread(p + 768); W[k][n] = v4u{lo.x, lo.y, hi.x, hi.y};
    };
    auto rdx = [&](int s, int k) { X[k] = *(LAS v4u*)(L + xo[k] + (s % 3) * G1_STAGE); };
#define MM(k, n) acc1[n] = mfma32(W[k][n], X[k], acc1[n])
#define CS(n) csa[n] = mfma(W[0][n], sel, csa[n])
    v2u gran; unsigned bb;
    v4u WF[6][5], AF[6][4];
    const unsigned vo = lane * 16;
    auto wl1 = [&](int s, int e0) { const int e = 9 * (s - 7) + e0; if (s >= 7 && e < 25) ld128c<0>(WF[e / 5][e % 5], vo, w2b + rot12(r2 + e / 5) * 5120 + (e % 5) * 1024); };
    const unsigned go = WS_STATS + (unsigned)(rb1 * 128 + 32 * wi + l31) * 8;
    waitv<14>();
    lds_set(L, W_RDY + wi * 4, 1); lds_wait_min<4>(L, W_RDY, 1, lane);
    ST(3);
    rdx(0, 0); rdw(0, 0, 0); rdw(0, 0, 1); rdw(0, 0, 2); rdx(0, 1); rdw(0, 1, 0); rdw(0, 1, 1); rdw(0, 1, 2);
#pragma unroll
    for (int s = 0; s < 10; ++s) {
      const bool refill = s < 7;
      const char* pa = g1a + slab(s + 3) * 128; const char* pb = g1b + (size_t)slab(s + 3) * 64 * 3072;
      SCHED();
      MM(0, 0); rdx(s, 2); rdw(s, 2, 0); SCHED();
      MM(0, 1); rdw(s, 2, 1); rdw(s, 2, 2); SCHED();
      MM(0, 2); rdx(s, 3); rdw(s, 3, 0); SCHED();
      CS(0); rdw(s, 3, 1); SCHED();
      CS(1); rdw(s, 3, 2); SCHED();
      CS(2);
      if (s < 7) waitv<7>(); else if (s == 7) waitv<9>(); else if (s == 8) waitv<11>();
      if (s == 9 && l15 < 2) {
#pragma unroll
        for (int n = 0; n < 3; ++n) *(LAS f32x4*)(L + CSP + wi * 384 + (32 * n + 16 * l15 + 4 * g) * 4) = csa[n];
      }
      lds_set(L, W_RDY + wi * 4, s + 2);
      SCHED();
      MM(1, 0);
      if (refill) { asm volatile("s_waitcnt lgkmcnt(7)" ::: "memory"); dma16(voA[0], pa, la + (s % 3) * G1_STAGE); }
      wl1(s, 0);
      SCHED();
      MM(1, 1); if (refill) dma16o<1024>(voA[1], pa); wl1(s, 1);
      unsigned fl = 0;
      if (s < 9) fl = lw(L, W_RDY + p4 * 4);
      SCHED();
      MM(1, 2); if (refill) dma16o<2048>(voA[2], pa); wl1(s, 2); SCHED();
      MM(2, 0); if (refill) dma16o<3072>(voA[3], pa); wl1(s, 3);
      SCHED();
      if (s == 9) { MM(2, 1); wl1(s, 4); SCHED(); MM(2, 2); wl1(s, 5); SCHED(); MM(3, 0); wl1(s, 6); SCHED(); MM(3, 1); MM(3, 2); SCHED(); break; }
      if (__builtin_expect(__builtin_amdgcn_ballot_w64(fl < (unsigned)(s + 2)) != 0ull, 0)) lds_wait_min<4>(L, W_RDY, s + 2, lane);
      FENCE();
      SCHED();
      rdx(s + 1, 0); rdw(s + 1, 0, 0); SCHED();
      MM(2, 1); rdw(s + 1, 0, 1); rdw(s + 1, 0, 2); if (refill) dma16(voB[0], pb, lb + (s % 3) * G1_STAGE); wl1(s, 4); SCHED();
      MM(2, 2); rdx(s + 1, 1); rdw(s + 1, 1, 0); if (refill) dma16o<1024>(voB[1], pb); wl1(s, 5); SCHED();
      MM(3, 0); rdw(s + 1, 1, 1); if (refill) dma16o<2048>(voB[2], pb); wl1(s, 6); SCHED();
      MM(3, 1); rdw(s + 1, 1, 2); wl1(s, 7); SCHED();
      MM(3, 2); wl1(s, 8);
      if (s == 6) { lds_wait_ge(L, W_F1ALL, 4); ld64c(gran, go, ws); ld32<0>(bb, (unsigned)(lane < 48 ? lane : 47) * 4, b1 + ct1 * 192); }
      SCHED();
      if (s == 0) ST(4); if (s == 3) ST(5); if (s == 6) ST(7);
    }
#undef MM
#undef CS
    ST(11);
    waitv<25>(); PIN(gran); PIN(bb);
    const float rs = __builtin_amdgcn_rcpf(bf2f(gran.x)) * 1.44269504088896341f, mrs = -bfhi(gran.x) * rs;
    lds_wait_min<4>(L, W_RDY, 11, lane);
    if (lane < 48) {
      LAS f32x2* p = (LAS f32x2*)(L + CSP + lane * 8);
      *(LAS f32x2*)(L + CSF + wi * 768 + lane * 8) = (p[0] + p[48]) + (p[96] + p[144]);
      *(LAS f32x2*)(L + CSF + wi * 768 + 384 + lane * 8) = unpk(bb) * 1.44269504088896341f;
    }
    FENCE();
    ST(12);
    {
      char* ab = ws + WS_ACT + (size_t)((rb1 * 8 + 2 * wi) * 48 + 3 * ct1) * 1024;
      const unsigned ao = (unsigned)((l31 >> 4) * 49152 + h * 256 + (4 * ((l15 & 7) >> 1) + (l15 & 1) + 2 * (l15 >> 3)) * 16);
#pragma unroll
      for (int n = 0; n < 3; ++n)
#pragma unroll
        for (int r = 0; r < 2; ++r) {
          unsigned pk[4];
#pragma unroll
          for (int c = 0; c < 2; ++c) {
            LAS char* cp = L + CSF + wi * 768 + (32 * n + 16 * r + 8 * c) * 4 + h * 16;
            const f32x4 cs = *(LAS f32x4*)cp, bv = *(LAS f32x4*)(cp + 384);
#pragma unroll
            for (int e = 0; e < 2; ++e) {
              const f32x2 av = f32x2{acc1[n][8 * r + 4 * c + 2 * e], acc1[n][8 * r + 4 * c + 2 * e + 1]};
              const f32x2 h2 = __builtin_elementwise_fma(av, f32x2{rs, rs}, __builtin_elementwise_fma(f32x2{mrs, mrs}, e ? cs.zw : cs.xy, e ? bv.zw : bv.xy));
              const f32x2 s1 = f32x2{__builtin_amdgcn_exp2f(h2.x), __builtin_amdgcn_exp2f(h2.y)} + 1.0f;
              const f32x2 d = __builtin_elementwise_fma(s1, s1, f32x2{1.f, 1.f});
              const f32x2 rr = f32x2{__builtin_amdgcn_rcpf(d.x), __builtin_amdgcn_rcpf(d.y)};
              const f32x2 o = h2 * __builtin_elementwise_fma(rr, f32x2{-1.38629436111989062f, -1.38629436111989062f}, f32x2{0.69314718055994531f, 0.69314718055994531f});
              pk[2 * c + e] = pk2(o.x, o.y);
            }
          }
          st128c(ao + n * 1024 + r * 512, ab, v4u{pk[0], pk[1], pk[2], pk[3]});
        }
    }
    ST(13);
    LATE_LANE();
    v2u xq[5], dq[5], gr2, f3;
    {
      const char* xb2 = x + (size_t)row2 * 1280 + cb2 * 160; const unsigned xo2 = OROW * 1280 + cg * 8;
      ld64o<0>(xq[0], xo2, xb2); ld64o<32>(xq[1], xo2, xb2); ld64o<64>(xq[2], xo2, xb2); ld64o<96>(xq[3], xo2, xb2); ld64o<128>(xq[4], xo2, xb2);
      const char* db = pDskip + cb2 * 160;
      ld64o<0>(dq[0], cg * 8, db); ld64o<32>(dq[1], cg * 8, db); ld64o<64>(dq[2], cg * 8, db); ld64o<96>(dq[3], cg * 8, db); ld64o<128>(dq[4], cg * 8, db);
      ld64c(gr2, WS_STATS + (row2 + OROW) * 8, ws);
    }
    waitv<11>();
    arrive_flag(L, W_F2C, lane, ws, WS_F2 + (unsigned)wid * 128, tlo, thi);
    ST(14);
    const unsigned f2o = WS_F2 + (unsigned)((4 * rg + (a2 >> 1)) * 16 + 4 * wi + (ln_ & 3)) * 128;
    __builtin_amdgcn_s_sleep(F2GAP);
    v2u f2; ld64c(f2, f2o, ws);
    unsigned vm[4];
#pragma unroll
    for (int m = 0; m < 4; ++m) vm[m] = vo + ((m + wi) & 3) * 49152;
    waitv<0>(); PIN(f2); PIN(gr2);
    ST(15);
    f2 = spin64<false>(f2, f2o, ws, tlo, thi);
    lds_set(L, W_F2OK + wi * 4, 1);
    ST(16);
    AL(0); AL(1); AL(2);
    f32x4 acc2[4][5];
    const float rs2 = __builtin_amdgcn_rcpf(bf2f(gr2.x)), m2 = bfhi(gr2.x);
#pragma unroll
    for (int n = 0; n < 5; ++n) {
      PIN(xq[n]); PIN(dq[n]);
      const f32x2 x0 = unpk(xq[n].x), x1 = unpk(xq[n].y);
      acc2[0][n].xy = __builtin_elementwise_fma(unpk(dq[n].x), (x0 - m2) * rs2, x0);
      acc2[0][n].zw = __builtin_elementwise_fma(unpk(dq[n].y), (x1 - m2) * rs2, x1);
    }
    ST(17);
    waitv<8>(); g2_step<1>(acc2, WF[0], AF[0]);
    AL(3); AL(4);
    waitv<12>(); g2_step<4>(acc2, WF[1], AF[1]);
    WL(5); AL(5);
    waitv<17>(); g2_step<4>(acc2, WF[2], AF[2]);
    ST(20);
    lds_wait_ge(L, W_F3C, 4);
    const unsigned f3o = WS_F3 + (unsigned)(cb2 * 32 + 8 * wi + (ln_ & 7)) * 128;
    ld64c(f3, f3o, ws);
    waitv<14>(); g2_step<4>(acc2, WF[3], AF[3]);
    waitv<10>(); g2_step<4>(acc2, WF[4], AF[4]);
    waitv<0>(); PIN(f3);
    f3 = spin64<false>(f3, f3o, ws, tlo, thi);
    lds_arrive(L, W_F3ALL, lane);
    lds_wait_ge(L, W_F3ALL, 4);
    ST(21);
    v4u yv[3];
    {
      const char* yb = ws + WS_Y + (size_t)cb2 * 80 * 4096 + row2 * 2;
      const unsigned yo = (unsigned)(ln_ >> 1) * 4096 + (ln_ & 1) * 16;
      ld128c<0>(yv[0], yo, yb); ld128c<0>(yv[1], yo, yb + 32 * 4096); ld128c<0>(yv[2], yo & 0xffffu, yb + 64 * 4096);
    }
    g2_step<4>(acc2, WF[5], AF[5]);
    ST(22);
    LAS char* fp = L + FM + wi * 20480 + vo;
#pragma unroll
    for (int m = 1; m < 4; ++m)
#pragma unroll
      for (int n = 0; n < 5; ++n) *(LAS f32x4*)(fp + ((m + wi) & 3) * 5120 + n * 1024) = acc2[m][n];
    waitv<0>(); PIN(yv[0]); PIN(yv[1]); PIN(yv[2]);
    LAS char* ys = L + YST + wi * 2560;
    *(LAS v4u*)(ys + vo) = yv[0]; *(LAS v4u*)(ys + 1024 + vo) = yv[1];
    if (ln_ < 32) *(LAS v4u*)(ys + 2048 + vo) = yv[2];
    FENCE();
#pragma unroll
    for (int n = 0; n < 5; ++n) {
      const v2u t = trread(ys + n * 512 + ln_ * 8);
      acc2[0][n].xy += unpk(t.x); acc2[0][n].zw += unpk(t.y);
      *(LAS f32x4*)(fp + wi * 5120 + n * 1024) = acc2[0][n];
    }
    lds_arrive(L, W_FM, lane);
    STEND();
    asm volatile("97:");
    __builtin_amdgcn_endpgm();
  }

  const bool odd = wi & 1;
  const int kch = chA + (wi >> 1);
  waitv<0>();
  arrive_flag8(L, W_F1C, ws, WS_F1 + (unsigned)(lane & 7) * 2048 + wid * 8, tlo, thi);
  lds_wait_ge(L, W_KG, 8);
  auto reduce = [&](int c, int i, bool three) {
    LAS char* pp = L + KG_SCR + 3 * c * KG_WSTR;
    f32x4 s = *(LAS f32x4*)(pp + i * 16) * *(LAS float*)(pp + 2576) + *(LAS f32x4*)(pp + KG_WSTR + i * 16) * *(LAS float*)(pp + KG_WSTR + 2576);
    if (three) s += *(LAS f32x4*)(pp + 2 * KG_WSTR + i * 16) * *(LAS float*)(pp + 2 * KG_WSTR + 2576);
    *(LAS f32x4*)(L + KTAB + c * KP_STRIDE + i * 16) = s;
    return s;
  };
  if (ts < 161) {
    st128c((unsigned)ts * 16, ws + WS_KP + (size_t)wid * KP_STRIDE, __builtin_bit_cast(v4u, reduce(2, ts, false)));
    reduce(0, ts, true);
  }
  if (ts >= 95) reduce(1, ts - 95, true);
  if (ts < 3) *(LAS float*)(L + KTAB + ts * KP_STRIDE - 4) = 0.f;
  lds_arrive(L, W_SB, lane);
  lds_wait_ge(L, W_SB, 4);
  put_k(L, kix, ts, 0, CVA); put_k(L, kix, ts, 1, CVA + CV_CH);
  if (ZCOND) { *(LAS v4u*)(L + CVA + ZOFF) = v4u{0u, 0u, 0u, 0u}; *(LAS v4u*)(L + CVA + CV_CH + ZOFF) = v4u{0u, 0u, 0u, 0u}; }
  waitv<0>();
  arrive_flag(L, W_FKC, lane, ws, WS_FK + (unsigned)wid * 64, tlo, thi);
  ST(4);
  const unsigned f1o = WS_F1 + (unsigned)xcd * 2048 + ts * 8;
  v2u f1own; ld64c(f1own, f1o, ws);
  waitv<0>(); PIN(f1own);
  ST(5);
  f1own = spin64<false>(f1own, f1o, ws, tlo, thi);
  v4u uu[2], us;
#pragma unroll
  for (int r = 0; r < 2; ++r) ld128c<0>(uu[r], (unsigned)(64 * wi + 32 * r + (lane >> 1)) * 10240 + (chA + (lane & 1)) * 16, ws + WS_XNB);
  ld128c<0>(us, (unsigned)ts * 10240 + chS * 16, ws + WS_XNB);
  const unsigned fko = WS_FK + (unsigned)(wid ^ 1) * 64;
  v2u fk; ld64c(fk, fko, ws);
  if (b == 0) arrive_flag(L, W_F1ALL, lane, ws, WS_E, tlo, thi);
  else lds_arrive(L, W_F1ALL, lane);
  ST(7);
  waitv<2>(); PIN(uu[0]); PIN(uu[1]);
#pragma unroll
  for (int r = 0; r < 2; ++r) put_u(L + CVA + (lane & 1) * CV_CH + (64 * wi + 32 * r + (lane >> 1)) * 16, uu[r], r == 0 && ts < 2);
  lds_arrive(L, W_SB, lane);
  __builtin_amdgcn_s_setprio(0);
  lds_wait_ge(L, W_SB, 8);
  waitv<0>(); PIN(fk); PIN(us);
  fk = spin64<false>(fk, fko, ws, tlo, thi);
  v4u kp; ld128c<0>(kp, (unsigned)(ts < 161 ? ts : 160) * 16, ws + WS_KP + (size_t)(wid ^ 1) * KP_STRIDE);
  ST(11);
  LATE_LANE();
  const unsigned vo = ln_ * 16;
  v4u WF[6][5], AF[6][4];
  f32x4 cacc[8];
#pragma unroll
  for (int i = 0; i < 8; ++i) cacc[i] = f32x4{0.f, 0.f, 0.f, 0.f};
  {
    const unsigned o1 = CVA + (wi >> 1) * CV_CH - (odd ? 4 * 64 : 0);
    LAS char* ka = L + KAO + o1; LAS char* ub = L + UBO + o1;
    v4u A[8], B[8];
#define LA(i, u) (*(LAS v4u*)(ka - 512 * (i) - 64 * (u)))
#define LB(i, u) (*(LAS v4u*)(ub + 512 * (i) - 64 * (u)))
    A[7] = LA(7, 0); B[0] = LB(0, 0); A[0] = LA(0, 0); B[7] = LB(7, 0); A[6] = LA(6, 0); B[6] = LB(6, 0); A[1] = LA(1, 0); B[1] = LB(1, 0);
    A[5] = LA(5, 0); B[5] = LB(5, 0); A[2] = LA(2, 0); B[2] = LB(2, 0); A[4] = LA(4, 0); B[4] = LB(4, 0); A[3] = LA(3, 0); B[3] = LB(3, 0);
    SCHED();
#pragma unroll
    for (int u = 0; u < 4; ++u) {
      v4u An[2], Bn[2];
#pragma unroll
      for (int sd = 7; sd >= 0; --sd) {
#pragma unroll
        for (int o = 0; o <= sd; ++o) {
          if (sd + o < 8) cacc[sd + o] = mfma(A[sd], B[o], cacc[sd + o]);
          if (o != sd && sd + o < 8) cacc[sd + o] = mfma(A[o], B[sd], cacc[sd + o]);
        }
        SCHED();
        if (u < 3) {
          if (sd >= 2) { A[sd] = LA(sd, u + 1); B[sd] = LB(sd, u + 1); }
          if (sd == 6 || sd == 5) { An[6 - sd] = LA(6 - sd, u + 1); Bn[6 - sd] = LB(6 - sd, u + 1); }
        }
        if (u > 0 && sd >= 2 && sd <= 6) ld128c<0>(WF[u - 1][6 - sd], vo, w2b + rot12(r2 + u - 1) * 5120 + (6 - sd) * 1024);
        SCHED();
      }
      if (u < 3) { A[0] = An[0]; A[1] = An[1]; B[0] = Bn[0]; B[1] = Bn[1]; }
    }
#undef LA
#undef LB
  }
  ST(10);
  waitv<15>(); PIN(kp);
  if (ts < 161) *(LAS f32x4*)(L + KTAB + 2 * KP_STRIDE + ts * 16) += __builtin_bit_cast(f32x4, kp);
  lds_arrive(L, W_SB, lane);
  lds_wait_min<4>(L, W_RDY, 11, lane);
  {
    LAS char* px = L + SXCH + wi * 4096 + vo;
    if (odd) {
#pragma unroll
      for (int i = 0; i < 4; ++i) { *(LAS f32x4*)(px + i * 1024) = cacc[i]; cacc[i] = cacc[4 + i]; }
    } else {
#pragma unroll
      for (int i = 0; i < 4; ++i) *(LAS f32x4*)(px + i * 1024) = cacc[4 + i];
    }
    lds_arrive(L, W_SX + (wi >> 1) * 4, lane);
    lds_wait_ge(L, W_SX + (wi >> 1) * 4, 2);
    px = L + SXCH + (wi ^ 1) * 4096 + vo;
    v2u yo[4];
#pragma unroll
    for (int i = 0; i < 4; ++i) { const f32x4 s = cacc[i] + *(LAS f32x4*)(px + i * 1024); yo[i] = v2u{pk2(s.x, s.y), pk2(s.z, s.w)}; }
    char* yb = ws + WS_Y + (size_t)kch * 4096 + (odd ? 2048 : 0);
    st64co<0>(YOFF, yb, yo[0]); st64co<512>(YOFF, yb, yo[1]); st64co<1024>(YOFF, yb, yo[2]); st64co<1536>(YOFF, yb, yo[3]);
  }
  ST(12);
  put_u(L + CVS + ts * 16, us, ts == 0);
  if (ZCOND) *(LAS v4u*)(L + CVS + ZOFF) = v4u{0u, 0u, 0u, 0u};
  lds_wait_ge(L, W_SB, 12);
  put_k(L, kix, ts, 2, CVS);
  lds_arrive(L, W_SB, lane);
  WL(3);
  lds_wait_ge(L, W_SB, 16);
  ST(13);
  {
    f32x4 sacc[4];
#pragma unroll
    for (int i = 0; i < 4; ++i) sacc[i] = f32x4{0.f, 0.f, 0.f, 0.f};
    LAS char* ka = L + CVS + KAO - wi * 128; LAS char* ub = L + CVS + UBO - wi * 128;
    if (par) { conv_unit_shared<1>(ka, ub, sacc); conv_unit_shared<1>(ka - 64, ub - 64, sacc); }
    else { conv_unit_shared<0>(ka, ub, sacc); conv_unit_shared<0>(ka - 64, ub - 64, sacc); }
#pragma unroll
    for (int q = 0; q < 4; ++q) *(LAS f32x4*)(L + MXCH + (q * 4 + wi) * 1024 + vo) = sacc[q];
    lds_arrive(L, W_SB, lane);
    WL(4);
    lds_wait_ge(L, W_SB, 20);
    LAS char* p = L + MXCH + wi * 4096 + vo;
    const f32x4 s = (*(LAS f32x4*)p + *(LAS f32x4*)(p + 1024)) + (*(LAS f32x4*)(p + 2048) + *(LAS f32x4*)(p + 3072));
    st64c(YOFF, ws + WS_Y + (size_t)chS * 4096 + (2 * wi + ((wi & 1) ^ par)) * 512, v2u{pk2(s.x, s.y), pk2(s.z, s.w)});
  }
  ST(14);
  unsigned vm[4];
#pragma unroll
  for (int m = 0; m < 4; ++m) vm[m] = vo + ((m + wi) & 3) * 49152;
  const bool early = RFL(lw(L, W_F2OK + wi * 4)) == 0;
  if (early) { waitv<0>(); arrive_flag(L, W_F3C, lane, ws, WS_F3 + (unsigned)(xcd * 32 + j) * 128, tlo, thi); }
  lds_wait_ge(L, W_F2OK + wi * 4, 1);
  ST(16);
  AL(0); AL(1); AL(2);
  ST(17);
  f32x4 acc2[4][5];
  waitv<8>();
  if (!early) arrive_flag(L, W_F3C, lane, ws, WS_F3 + (unsigned)(xcd * 32 + j) * 128, tlo, thi);
  g2_step<0>(acc2, WF[0], AF[0]);
  AL(3); AL(4);
  waitv<12>(); g2_step<4>(acc2, WF[1], AF[1]);
  WL(5); AL(5);
  waitv<17>(); g2_step<4>(acc2, WF[2], AF[2]);
  ST(20);
  waitv<13>(); g2_step<4>(acc2, WF[3], AF[3]);
  waitv<9>(); g2_step<4>(acc2, WF[4], AF[4]);
  waitv<0>(); g2_step<4>(acc2, WF[5], AF[5]);
  ST(22);
  lds_wait_ge(L, W_F3C, 4);
#pragma unroll
  for (int m = 1; m < 4; ++m)
#pragma unroll
    for (int n = 0; n < 5; ++n) *(LAS f32x4*)(L + FS + wi * 15360 + (m - 1) * 5120 + n * 1024 + vo) = acc2[m][n];
  lds_arrive(L, W_FS, lane);
  ST(23);
  lds_wait_ge(L, W_FS, 4);
#pragma unroll
  for (int d = 1; d < 4; ++d)
#pragma unroll
    for (int n = 0; n < 5; ++n) acc2[0][n] += *(LAS f32x4*)(L + FS + ((wi - d) & 3) * 15360 + (d - 1) * 5120 + n * 1024 + vo);
  lds_wait_ge(L, W_FM, 4);
  ST(24);
  {
    char* ob = out + (size_t)row2 * 1280 + cb2 * 160; const unsigned oo = OROW * 1280 + cg * 8;
    v2u o[5];
#pragma unroll
    for (int n = 0; n < 5; ++n) {
      LAS char* p = L + FM + wi * 5120 + n * 1024 + vo;
      const f32x4 s = acc2[0][n] + ((*(LAS f32x4*)p + *(LAS f32x4*)(p + 20480)) + (*(LAS f32x4*)(p + 40960) + *(LAS f32x4*)(p + 61440)));
      o[n] = v2u{pk2(s.x, s.y), pk2(s.z, s.w)};
    }
    st64co<0>(oo, ob, o[0]); st64co<32>(oo, ob, o[1]); st64co<64>(oo, ob, o[2]); st64co<96>(oo, ob, o[3]); st64co<128>(oo, ob, o[4]);
  }
  STEND();
  asm volatile("99:");
}

#if defined(__HIP_DEVICE_COMPILE__)
#pragma clang attribute pop
#endif
extern "C" void kernel_launch(void* const* d_in, const int* in_sizes, int n_in, void* d_out, int out_size, void* d_ws, size_t ws_size,
                              hipStream_t stream) {
  fused_s4_mlp<<<256, 512, 0, stream>>>((const char*)d_in[0], (const char*)d_in[1], (const char*)d_in[2], (const char*)d_in[3],
      (const char*)d_in[4], (const char*)d_in[5], (const char*)d_in[6], (const char*)d_in[7], (const char*)d_in[8], (const char*)d_in[10],
      (const char*)d_in[11], (const char*)d_in[12], (char*)d_out, (char*)d_ws);
}
```

```cpp
#include <hip/hip_runtime.h>
#include <stdint.h>

typedef unsigned short u16;
typedef unsigned long long u64;
typedef unsigned v4u __attribute__((ext_vector_type(4)));
typedef unsigned v2u __attribute__((ext_vector_type(2)));
typedef float f32x4 __attribute__((ext_vector_type(4)));
typedef float f32x2 __attribute__((ext_vector_type(2)));
typedef short bf16x8 __attribute__((ext_vector_type(8)));
typedef short s16x4 __attribute__((ext_vector_type(4)));
typedef __bf16 bf16x2_t __attribute__((ext_vector_type(2)));
#define LAS __attribute__((address_space(3)))
#define GAS __attribute__((address_space(1)))
#define DI __device__ __forceinline__

constexpr unsigned WS_E = 0, WS_TMO = 64, WS_F1 = 131072, WS_STATS = 16384, WS_F2 = 32768, WS_F3 = 65536, WS_FK = 98304, WS_STAMP = 262144;
constexpr unsigned WS_XNB = 1u << 20, WS_W2T = 4u << 20, WS_Y = 7u << 20, WS_ACT = 10u << 20, WS_KP = 17u << 20;
constexpr unsigned KP_STRIDE = 2624;
constexpr unsigned SPIN_CAP = 400000;

constexpr unsigned W_F1C = 0, W_F2C = 4, W_F3C = 8, W_FKC = 12, W_MB = 16, W_SX = 24, W_FM = 32, W_FS = 36, W_F3ALL = 40, W_F2OK = 48;
constexpr unsigned KTAB = 272;
constexpr unsigned CSP = 8192, STAT = 9728;
constexpr unsigned G1_STAGE = 28672, G1_BOFF = 16384;
constexpr unsigned CVA = 10240, CV_CH = G1_STAGE, CV_USTR = 4640, CV_KOFF = 18560, CV_KSTR = 4224;
constexpr unsigned KG_SCR = CVA, KG_WSTR = 4352, LN_ROWS = KG_SCR + 8 * KG_WSTR, LN_PITCH = 1344, W2P = LN_ROWS + 10752;
constexpr unsigned G1_RING = CVA + 2 * G1_STAGE;
constexpr unsigned g1_buf(int s) { return CVA + (s < 3 ? s + 2 : (s == 3 ? 2 : (s < 6 ? s - 4 : (s < 8 ? s - 3 : (s == 8 ? 2 : 0))))) * G1_STAGE; }
constexpr unsigned CVS = G1_RING, SXCH = G1_RING + G1_STAGE, MXCH = SXCH + 16384;
constexpr unsigned YST = G1_RING + 3 * G1_STAGE, CSF = YST;
constexpr unsigned FM = 256, FS = FM + 81920;
static_assert(FS + 61440 <= YST && W2P + 8192 <= G1_RING && YST + 10240 <= 163840 && MXCH + 16384 <= YST, "lds map");

#define SPRIO2 1
#define F1GAP 16
#define F2GAP 12
#define SPRIO 0
#define ST(i) do {} while (0)
#define STEND() do {} while (0)
#define STARG
#define STPASS

DI float bf2f(unsigned v) { return __builtin_bit_cast(float, v << 16); }
DI float bfhi(unsigned v) { return __builtin_bit_cast(float, v & 0xffff0000u); }
DI unsigned pk2(float lo, float hi) { bf16x2_t r; r.x = (__bf16)lo; r.y = (__bf16)hi; return __builtin_bit_cast(unsigned, r); }
DI float rbf(float f) { return bf2f(pk2(f, f) & 0xffffu); }
DI f32x4 mfma(v4u a, v4u b, f32x4 c) {
  return __builtin_amdgcn_mfma_f32_16x16x32_bf16(__builtin_bit_cast(bf16x8, a), __builtin_bit_cast(bf16x8, b), c, 0, 0, 0);
}
DI v2u trread(LAS char* p) { return __builtin_bit_cast(v2u, __builtin_amdgcn_ds_read_tr16_b64_v4i16((LAS s16x4*)p)); }
template <int N> DI void waitv() { asm volatile("s_waitcnt vmcnt(%0)" ::"i"(N) : "memory"); }
DI void bar() { asm volatile("s_waitcnt lgkmcnt(0)\n\ts_barrier" ::: "memory"); }
#define PIN(x) asm volatile("" : "+v"(x))
#define FENCE() asm volatile("" ::: "memory")
#define SCHED() __builtin_amdgcn_sched_barrier(0)
#define RFL(x) __builtin_amdgcn_readfirstlane(x)

DI void ld16(unsigned& d, unsigned vo, const void* sb) { asm volatile("global_load_ushort %0, %1, %2" : "=v"(d) : "v"(vo), "s"(sb) : "memory"); }
template <int O> DI void ld32(unsigned& d, unsigned vo, const void* sb) { asm volatile("global_load_dword %0, %1, %2 offset:%3" : "=v"(d) : "v"(vo), "s"(sb), "i"(O) : "memory"); }
DI void ld64(v2u& d, unsigned vo, const void* sb) { asm volatile("global_load_dwordx2 %0, %1, %2" : "=v"(d) : "v"(vo), "s"(sb) : "memory"); }
DI void ld128(v4u& d, unsigned vo, const void* sb) { asm volatile("global_load_dwordx4 %0, %1, %2" : "=v"(d) : "v"(vo), "s"(sb) : "memory"); }
DI void ld64c(v2u& d, unsigned vo, const void* sb) { asm volatile("global_load_dwordx2 %0, %1, %2 sc1" : "=v"(d) : "v"(vo), "s"(sb) : "memory"); }
template <int O> DI void ld128c(v4u& d, unsigned vo, const void* sb) { asm volatile("global_load_dwordx4 %0, %1, %2 offset:%3 sc1" : "=v"(d) : "v"(vo), "s"(sb), "i"(O) : "memory"); }
DI void st64c(unsigned vo, void* sb, v2u d) { asm volatile("global_store_dwordx2 %0, %1, %2 sc1" ::"v"(vo), "v"(d), "s"(sb) : "memory"); }
template <int O> DI void ld64o(v2u& d, unsigned vo, const void* sb) { asm volatile("global_load_dwordx2 %0, %1, %2 offset:%3" : "=v"(d) : "v"(vo), "s"(sb), "i"(O) : "memory"); }
template <int O> DI void st64co(unsigned vo, void* sb, v2u d) { asm volatile("global_store_dwordx2 %0, %1, %2 offset:%3 sc1" ::"v"(vo), "v"(d), "s"(sb), "i"(O) : "memory"); }
DI void st128c(unsigned vo, void* sb, v4u d) { asm volatile("global_store_dwordx4 %0, %1, %2 sc1\n\ts_nop 1" ::"v"(vo), "v"(d), "s"(sb) : "memory"); }
DI void st128u(unsigned vo, void* sb, v4u d) { asm volatile("global_store_dwordx4 %0, %1, %2 sc1 ; CNT\n\ts_nop 1" ::"v"(vo), "v"(d), "s"(sb) : "memory"); }
DI void dma16(unsigned vo, const void* sb, unsigned lds) {
  asm volatile("s_mov_b32 m0, %2\n\ts_nop 0\n\tglobal_load_lds_dwordx4 %0, %1" ::"v"(vo), "s"(sb), "s"(lds) : "memory");
}
template <int O> DI void dma16o(unsigned vo, const void* sb) {
  asm volatile("global_load_lds_dwordx4 %0, %1 offset:%2" ::"v"(vo), "s"(sb), "i"(O) : "memory");
}
template <bool HIONLY> DI v2u spin64(v2u cur, unsigned vo, char* ws, unsigned tlo, unsigned thi) {
  if (__builtin_expect(__builtin_amdgcn_ballot_w64(HIONLY ? (cur.y != tlo) : (cur.x != tlo || cur.y != thi)) != 0ull, 0)) {
#pragma nounroll
    for (unsigned spins = 0; spins < SPIN_CAP; ++spins) {
      __builtin_amdgcn_s_sleep(6);
      asm volatile("global_load_dwordx2 %0, %1, %2 sc1\n\ts_waitcnt vmcnt(0)" : "=&v"(cur) : "v"(vo), "s"(ws) : "memory");
      if (__builtin_amdgcn_ballot_w64(HIONLY ? (cur.y != tlo) : (cur.x != tlo || cur.y != thi)) == 0ull) break;
    }
  }
  return cur;
}
DI unsigned lw(LAS char* L, unsigned off) { return *(volatile LAS unsigned*)(L + off); }
DI void lds_wait_at(unsigned a, unsigned target) {
  unsigned v, cnt; u64 t;
  asm volatile("s_mov_b32 %1, %5\n"
               "1:\n\tds_read_b32 %0, %3\n\ts_sub_u32 %1, %1, 1\n\ts_waitcnt lgkmcnt(0)\n\tv_cmp_gt_u32 vcc, %4, %0\n\t"
               "s_cselect_b64 %2, 0, vcc\n\ts_cmp_lg_u64 %2, 0\n\ts_cbranch_scc1 1b"
               : "=&v"(v), "=&s"(cnt), "=&s"(t) : "v"(a), "s"(target), "i"(4 * SPIN_CAP) : "vcc", "scc", "memory");
}
DI void lds_wait_ge(LAS char* L, unsigned off, unsigned target) { lds_wait_at((unsigned)(size_t)L + off, target); }
template <int N> DI void lds_wait_min(LAS char* L, unsigned off, unsigned target, int lane) {
  lds_wait_at((unsigned)(size_t)L + off + (lane & (N - 1)) * 4, target);
}
DI void lds_set(LAS char* L, unsigned off, unsigned v) { FENCE(); *(volatile LAS unsigned*)(L + off) = v; FENCE(); }
DI void lds_arrive(LAS char* L, unsigned off, int) {
  asm volatile("s_mov_b64 exec, 1\n\tds_add_u32 %0, %1\n\ts_mov_b64 exec, -1" ::"v"((unsigned)(size_t)L + off), "v"(1u) : "memory");
}
template <unsigned N = 4> DI void arrive_flag(LAS char* L, unsigned off, int, char* ws, unsigned fo, unsigned tlo, unsigned thi) {
  unsigned old;
  asm volatile("s_mov_b64 exec, 1\n\tds_add_rtn_u32 %0, %1, %2\n\ts_waitcnt lgkmcnt(0)\n\tv_cmp_eq_u32 vcc, %6, %0\n\t"
               "s_mov_b64 exec, vcc\n\tglobal_store_dwordx2 %3, %4, %5 sc1\n\ts_mov_b64 exec, -1"
               : "=&v"(old) : "v"((unsigned)(size_t)L + off), "v"(1u), "v"(fo), "v"(v2u{tlo, thi}), "s"(ws), "i"(N - 1) : "vcc", "memory");
}
DI void arrive_flag8(LAS char* L, unsigned off, char* ws, unsigned fo, unsigned tlo, unsigned thi) {
  unsigned old;
  asm volatile("s_mov_b64 exec, 1\n\tds_add_rtn_u32 %0, %1, %2\n\ts_waitcnt lgkmcnt(0)\n\tv_cmp_eq_u32 vcc, 3, %0\n\t"
               "s_mul_i32 exec_lo, vcc_lo, 0xff\n\tglobal_store_dwordx2 %3, %4, %5 sc1\n\ts_mov_b64 exec, -1"
               : "=&v"(old) : "v"((unsigned)(size_t)L + off), "v"(1u), "v"(fo), "v"(v2u{tlo, thi}), "s"(ws) : "vcc", "memory");
}
typedef float f32x16 __attribute__((ext_vector_type(16)));
DI f32x16 mfma32(v4u a, v4u b, f32x16 c) {
  return __builtin_amdgcn_mfma_f32_32x32x16_bf16(__builtin_bit_cast(bf16x8, a), __builtin_bit_cast(bf16x8, b), c, 0, 0, 0);
}
template <int C> DI float dpp_add(float v) {
  return v + __builtin_bit_cast(float, __builtin_amdgcn_update_dpp(0, __builtin_bit_cast(int, v), C, 0xf, 0xf, true));
}
DI float rl(float v, int l) { return __builtin_bit_cast(float, __builtin_amdgcn_readlane(__builtin_bit_cast(int, v), l)); }
DI float wave_sum(float v) {
  v = dpp_add<0xB1>(v); v = dpp_add<0x4E>(v); v = dpp_add<0x141>(v); v = dpp_add<0x140>(v);
  return (rl(v, 0) + rl(v, 16)) + (rl(v, 32) + rl(v, 48));
}
DI f32x2 unpk(unsigned v) { return f32x2{bf2f(v), bfhi(v)}; }
typedef f32x2 cf;
DI cf cmul1(cf i, cf w) { return cf{__builtin_fmaf(i.x, w.x, -(i.y * w.y)), __builtin_fmaf(i.x, w.y, i.y * w.x)}; }
DI void cmul4(cf& o0, cf& o1, cf& o2, cf& o3, cf i0, cf i1, cf i2, cf i3, cf w0, cf w1, cf w2, cf w3) {
  o0 = cmul1(i0, w0); o1 = cmul1(i1, w1); o2 = cmul1(i2, w2); o3 = cmul1(i3, w3);
}
DI unsigned pkc(cf v) { return pk2(v.x, v.y); }
template <int R> DI void put4(cf a, cf b, cf c, cf d) {
  asm volatile("ds_write_addtid_b32 %0 offset:%4\n\tds_write_addtid_b32 %1 offset:%5\n\tds_write_addtid_b32 %2 offset:%6\n\tds_write_addtid_b32 %3 offset:%7"
               ::"v"(pkc(a)), "v"(pkc(b)), "v"(pkc(c)), "v"(pkc(d)), "i"(R * 272), "i"(R * 272 + 272), "i"(R * 272 + 544), "i"(R * 272 + 816) : "memory");
}
template <int R> DI void put1(cf a) { asm volatile("ds_write_addtid_b32 %0 offset:%1" ::"v"(pkc(a)), "i"(R * 272) : "memory"); }
DI cf cexpk(float e1, float th, float tl, float k, float ks) {
  const float mag = __builtin_amdgcn_exp2f(e1 * k);
  const float fr = __builtin_fmaf(tl, ks, __builtin_amdgcn_fractf(th * ks));
  return cf{__builtin_amdgcn_cosf(fr), __builtin_amdgcn_sinf(fr)} * mag;
}
DI void kgen_slow(cf a, cf W1, cf W256, cf W8, LAS char* sr, f32x4 (&kacc)[2][2]) {
  cf W2, W512, u1, W16, u2, u3, W4, W32, u4, u5, u6, u7, r8, r9, r10, r11, r12, r13, r14, r15, r16, r17, W1024, v3, r18, r19, r20, v1;
  cmul4(W2, W512, u1, W16, W1, W256, a, W8, W1, W256, W1, W8);
  cmul4(u2, u3, W4, W32, a, u1, W2, W16, W2, W2, W2, W16);
  put4<0>(a, u1, u2, u3);
  cmul4(u4, u5, u6, u7, a, u1, u2, u3, W4, W4, W4, W4);
  put4<4>(u4, u5, u6, u7);
  cmul4(r8, r9, r10, r11, a, u2, u4, u6, W256, W256, W256, W256);
  put4<8>(r8, r9, r10, r11);
  cmul4(r12, r13, r14, r15, a, u4, r8, r10, W512, W512, W512, W512);
  put4<12>(r12, r13, r14, r15);
  v4u UA[2][4];
#pragma unroll
  for (int k = 0; k < 4; ++k) UA[0][k] = *(LAS v4u*)(sr + 64 * k);
  cmul4(r16, r17, W1024, v3, r12, r14, W512, W8, W512, W512, W512, W16);
  cmul4(r18, r19, r20, v1, r12, r14, r16, W8, W1024, W1024, W1024, W32);
  put4<0>(r16, r17, r18, r19);
  put1<4>(r20);
#pragma unroll
  for (int k = 0; k < 4; ++k) UA[1][k] = *(LAS v4u*)(sr + 64 * k);
  put4<0>(cf{1.f, 0.f}, W8, W16, v3);
  cf v0 = W32, v2, t0, t1, t2, t3;
  cmul4(v2, t3, t0, t1, W16, v3, v0, v1, W32, W32, W32, W32);
  put4<4>(v0, v1, v2, t3);
  cmul4(t2, v3, v0, v1, v2, t3, t0, t1, W32, W32, W32, W32);
  put4<8>(t0, t1, t2, v3);
  cmul4(v2, t3, t0, t1, t2, v3, v0, v1, W32, W32, W32, W32);
  put4<12>(v0, v1, v2, t3);
  v4u vb[4];
#pragma unroll
  for (int k = 0; k < 4; ++k) vb[k] = *(LAS v4u*)(sr + 64 * k);
  cmul4(t2, v3, v0, v1, v2, t3, t0, t1, W32, W32, W32, W32);
  put4<0>(t0, t1, t2, v3);
  cmul4(v2, t3, t0, t1, t2, v3, v0, v1, W32, W32, W32, W32);
  put4<4>(v0, v1, v2, t3);
#pragma unroll
  for (int k = 0; k < 4; ++k) {
    kacc[0][0] = mfma(UA[0][k], vb[k], kacc[0][0]);
    kacc[1][0] = mfma(UA[1][k], vb[k], kacc[1][0]);
  }
  cmul4(t2, v3, v0, v1, v2, t3, t0, t1, W32, W32, W32, W32);
  put4<8>(t0, t1, t2, v3);
  cmul4(v2, t3, t0, t1, t2, v3, v0, v1, W32, W32, W32, W32);
  put4<12>(v0, v1, v2, t3);
#pragma unroll
  for (int k = 0; k < 4; ++k) vb[k] = *(LAS v4u*)(sr + 64 * k);
  SCHED();
#pragma unroll
  for (int k = 0; k < 4; ++k) {
    kacc[0][1] = mfma(UA[0][k], vb[k], kacc[0][1]);
    kacc[1][1] = mfma(UA[1][k], vb[k], kacc[1][1]);
  }
}
typedef _Float16 f16x8 __attribute__((ext_vector_type(8)));
typedef _Float16 f16x2 __attribute__((ext_vector_type(2)));
DI f32x4 mfmah(v4u a, v4u b, f32x4 c) {
  return __builtin_amdgcn_mfma_f32_16x16x32_f16(__builtin_bit_cast(f16x8, a), __builtin_bit_cast(f16x8, b), c, 0, 0, 0);
}
DI unsigned pkh(cf v) { f16x2 r; r.x = (_Float16)v.x; r.y = (_Float16)v.y; return __builtin_bit_cast(unsigned, r); }
DI cf csq(cf v) { return cf{__builtin_fmaf(v.x, v.x, -(v.y * v.y)), (v.x + v.x) * v.y}; }
#define HMUL(o, i, w) "v_pk_mul_f16 " o ", " i ", " w " op_sel:[1,1] op_sel_hi:[1,0] neg_lo:[0,1]\n\t"
#define HFMA(o, i, w) "v_pk_fma_f16 " o ", " i ", " w ", " o " op_sel_hi:[0,1,1]\n\t"
template <int R0, int R1, int R2, int R3>
DI void h4(unsigned& o0, unsigned& o1, unsigned& o2, unsigned& o3, unsigned i0, unsigned i1, unsigned i2, unsigned i3, unsigned w0, unsigned w1, unsigned w2, unsigned w3) {
  asm volatile(HMUL("%0", "%4", "%8") HMUL("%1", "%5", "%9") HMUL("%2", "%6", "%10") HMUL("%3", "%7", "%11")
               HFMA("%0", "%4", "%8") HFMA("%1", "%5", "%9") HFMA("%2", "%6", "%10") HFMA("%3", "%7", "%11")
               "ds_write_addtid_b32 %0 offset:%12\n\tds_write_addtid_b32 %1 offset:%13\n\tds_write_addtid_b32 %2 offset:%14\n\tds_write_addtid_b32 %3 offset:%15"
               : "=&v"(o0), "=&v"(o1), "=&v"(o2), "=&v"(o3)
               : "v"(i0), "v"(i1), "v"(i2), "v"(i3), "v"(w0), "v"(w1), "v"(w2), "v"(w3), "i"(R0 * 272), "i"(R1 * 272), "i"(R2 * 272), "i"(R3 * 272) : "memory");
}
template <int R> DI void hput(unsigned v) { asm volatile("ds_write_addtid_b32 %0 offset:%1" ::"v"(v), "i"(R * 272) : "memory"); }
template <int C> DI float dpp_max(float v) {
  return __builtin_fmaxf(v, __builtin_bit_cast(float, __builtin_amdgcn_update_dpp(0, __builtin_bit_cast(int, v), C, 0xf, 0xf, true)));
}
template <class F> DI float kgen_task(const unsigned (&pr)[7], bool dead, unsigned sw, LAS char* sr, f32x4 (&kacc)[2][2], F mid) {
  const float dt = rbf(__builtin_amdgcn_exp2f(bf2f(pr[6]) * 1.44269504088896341f));
  const float zr = dt * bf2f(pr[0]), zi = dt * bf2f(pr[1]);
  const float dbr = dt * bf2f(pr[2]), dbi = dt * bf2f(pr[3]), cr = bf2f(pr[4]), ci = bf2f(pr[5]);
  cf a = cf{cr * dbr - ci * dbi, -(cr * dbi + ci * dbr)};
  if (dead) a = cf{0.f, 0.f};
  const float e1 = zr * 1.44269504088896341f, th = zi * 0.15915494309189535f;
  const float tl = __builtin_fmaf(zi, 6.4206383e-9f, __builtin_fmaf(zi, 0.15915494309189535f, -th));
  const cf W1 = cexpk(e1, th, tl, 1.f, -1.f), W256 = cexpk(e1, th, tl, 256.f, -256.f), W8 = cexpk(e1, th, tl, 8.f, 8.f);
  if (__builtin_expect(__builtin_amdgcn_ballot_w64(!(zr <= 0.f)) != 0ull, 0)) {
    mid();
    asm volatile("s_mov_b32 m0, %0" ::"s"(sw) : "memory");
#pragma unroll
    for (int i = 0; i < 4; ++i) kacc[i >> 1][i & 1] = f32x4{0.f, 0.f, 0.f, 0.f};
    kgen_slow(a, W1, W256, W8, sr, kacc);
    return 1.f;
  }
  float am = __builtin_fmaxf(__builtin_fabsf(a.x), __builtin_fabsf(a.y));
  am = dpp_max<0xB1>(am); am = dpp_max<0x4E>(am); am = dpp_max<0x141>(am); am = dpp_max<0x140>(am);
  const unsigned e0 = __builtin_amdgcn_readlane(__builtin_bit_cast(unsigned, am), 0), e16 = __builtin_amdgcn_readlane(__builtin_bit_cast(unsigned, am), 16);
  const unsigned e32 = __builtin_amdgcn_readlane(__builtin_bit_cast(unsigned, am), 32), e48 = __builtin_amdgcn_readlane(__builtin_bit_cast(unsigned, am), 48);
  unsigned ex = max(max(e0, e16), max(e32, e48)) >> 23;
  ex = min(max(ex, 2u), 252u);
  const float up = __builtin_bit_cast(float, ex << 23), down = __builtin_bit_cast(float, (254u - ex) << 23);
  const cf W2 = csq(W1), W512 = csq(W256), W16 = csq(W8), W4 = csq(W2), W1024 = csq(W512), W32 = csq(W16), W64 = csq(W32), W128 = csq(W64);
  const unsigned u0 = pkh(a * down), h1 = pkh(W1), h2 = pkh(W2), h4_ = pkh(W4), h256 = pkh(W256), h512 = pkh(W512), h1024 = pkh(W1024);
  unsigned u1, u2, u4, r8, u3, u5, u6, r12, u7, r9, r10, r14, r11, r13, r15, r16, r17, r18, r19, r20;
  { unsigned t_ = u0; PIN(t_); }
  mid();
  asm volatile("s_mov_b32 m0, %0\n\ts_nop 0" ::"s"(sw) : "memory");
  hput<0>(u0);
  h4<1, 2, 4, 8>(u1, u2, u4, r8, u0, u0, u0, u0, h1, h2, h4_, h256);
  h4<3, 5, 6, 12>(u3, u5, u6, r12, u2, u4, u4, u0, h1, h1, h2, h512);
  h4<7, 9, 10, 14>(u7, r9, r10, r14, u6, r8, r8, r8, h1, h2, h4_, h512);
  h4<11, 13, 15, 15>(r11, r13, r16, r15, r10, r12, u0, r14, h2, h4_, h1024, h4_);
  v4u UA[2][4];
#pragma unroll
  for (int k = 0; k < 4; ++k) UA[0][k] = *(LAS v4u*)(sr + 64 * k);
  hput<0>(r16);
  h4<1, 2, 3, 4>(r17, r18, r19, r20, r8, r12, r14, r16, h1024, h1024, h1024, h1024);
#pragma unroll
  for (int k = 0; k < 4; ++k) UA[1][k] = *(LAS v4u*)(sr + 64 * k);
  unsigned v[16], t0, t1, t2, t3;
  v[1] = pkh(W8); v[2] = pkh(W16); v[4] = pkh(W32); v[8] = pkh(W64);
  const unsigned h128 = pkh(W128);
  hput<0>(0x3c00u); hput<1>(v[1]); hput<2>(v[2]); hput<4>(v[4]); hput<8>(v[8]);
  h4<3, 5, 6, 9>(v[3], v[5], v[6], v[9], v[2], v[4], v[4], v[8], v[1], v[1], v[2], v[1]);
  h4<7, 10, 11, 12>(v[7], v[10], v[11], v[12], v[4], v[8], v[8], v[8], v[3], v[2], v[3], v[4]);
  h4<13, 14, 15, 15>(v[13], v[14], t0, v[15], v[8], v[8], v[1], v[8], v[5], v[6], h128, v[7]);
  v4u vb[4];
#pragma unroll
  for (int k = 0; k < 4; ++k) vb[k] = *(LAS v4u*)(sr + 64 * k);
  hput<0>(h128); hput<1>(t0);
  h4<2, 3, 4, 5>(t0, t1, t2, t3, v[2], v[3], v[4], v[5], h128, h128, h128, h128);
#pragma unroll
  for (int k = 0; k < 4; ++k) {
    kacc[0][0] = mfmah(UA[0][k], vb[k], k ? kacc[0][0] : f32x4{0.f, 0.f, 0.f, 0.f});
    kacc[1][0] = mfmah(UA[1][k], vb[k], k ? kacc[1][0] : f32x4{0.f, 0.f, 0.f, 0.f});
  }
  h4<6, 7, 8, 9>(t0, t1, t2, t3, v[6], v[7], v[8], v[9], h128, h128, h128, h128);
  h4<10, 11, 12, 13>(t0, t1, t2, t3, v[10], v[11], v[12], v[13], h128, h128, h128, h128);
  h4<14, 15, 14, 15>(t0, t1, t2, t3, v[14], v[15], v[14], v[15], h128, h128, h128, h128);
#pragma unroll
  for (int k = 0; k < 4; ++k) vb[k] = *(LAS v4u*)(sr + 64 * k);
  SCHED();
#pragma unroll
  for (int k = 0; k < 4; ++k) {
    kacc[0][1] = mfmah(UA[0][k], vb[k], k ? kacc[0][1] : f32x4{0.f, 0.f, 0.f, 0.f});
    kacc[1][1] = mfmah(UA[1][k], vb[k], k ? kacc[1][1] : f32x4{0.f, 0.f, 0.f, 0.f});
  }
  return up;
}
DI void kgen_store(LAS float* pt, const f32x4 (&kacc)[2][2], float scale, int lane) {
  const int l15 = lane & 15, g = lane >> 4;
  const int sh = g < 2 ? 3 : 4 - g;
  const int a0 = (l15 << sh) + (g < 2 ? 4 * g : 128 * g);
  const int a1 = g < 3 ? a0 + 2 : a0 + 64;
#pragma unroll
  for (int nh = 0; nh < 2; ++nh) {
    *(LAS f32x2*)(pt + a0 + ((16 * nh) << sh)) = kacc[0][nh].xy;
    *(LAS f32x2*)(pt + a1 + ((16 * nh) << sh)) = kacc[0][nh].zw;
  }
  if (g == 0) {
#pragma unroll
    for (int nh = 0; nh < 2; ++nh)
#pragma unroll
      for (int r = 0; r < 4; ++r) pt[512 + 32 * r + 16 * nh + l15] = kacc[1][nh][r];
  }
  if (lane == 16) { *(LAS f32x4*)(pt + 640) = f32x4{kacc[1][0][0], 0.f, 0.f, 0.f}; pt[644] = scale; }
}

template <int PAR> DI void conv_unit_shared(LAS char* ka, LAS char* ub, f32x4 (&acc)[4]) {
  constexpr int T[4] = {PAR ? 1 : 0, PAR ? 2 : 3, PAR ? 5 : 4, PAR ? 6 : 7};
  v4u A[8], B[8];
#pragma unroll
  for (int i = 0; i <= T[3]; ++i) { A[i] = *(LAS v4u*)(ka - 512 * i); B[i] = *(LAS v4u*)(ub + 512 * i); }
  SCHED();
#pragma unroll
  for (int q = 0; q < 4; ++q)
#pragma unroll
    for (int ip = 0; ip <= T[q]; ++ip) acc[q] = mfma(A[T[q] - ip], B[ip], acc[q]);
}
DI float fadd(float a, float b) { float r; asm("v_add_f32 %0, %1, %2" : "=v"(r) : "v"(a), "v"(b)); return r; }
DI float ffma(float a, float b, float c) { float r; asm("v_fma_f32 %0, %1, %2, %3" : "=v"(r) : "v"(a), "v"(b), "v"(c)); return r; }
DI f32x4 add4(f32x4 a, f32x4 b) { return f32x4{fadd(a.x, b.x), fadd(a.y, b.y), fadd(a.z, b.z), fadd(a.w, b.w)}; }
DI f32x4 fma4(f32x4 a, float b, f32x4 c) { return f32x4{ffma(a.x, b, c.x), ffma(a.y, b, c.y), ffma(a.z, b, c.z), ffma(a.w, b, c.w)}; }
DI int rot12(int v) { return v >= 12 ? v - 12 : v; }
DI void w_loads(v4u (&WF)[5], const char* p, unsigned vo) {
  ld128c<0>(WF[0], vo, p); ld128c<1024>(WF[1], vo, p); ld128c<2048>(WF[2], vo, p); ld128c<3072>(WF[3], vo, p); ld128c<0>(WF[4], vo, p + 4096);
}
DI void a_loads(v4u (&AF)[4], const char* p, const unsigned (&vm)[4]) {
#pragma unroll
  for (int m = 0; m < 4; ++m) ld128c<0>(AF[m], vm[m], p);
}
template <int LIVE> DI void g2_step(f32x4 (&acc)[4][5], v4u (&WF)[5], v4u (&AF)[4]) {
#pragma unroll
  for (int m = 0; m < 4; ++m) PIN(AF[m]);
#pragma unroll
  for (int n = 0; n < 5; ++n) PIN(WF[n]);
#pragma unroll
  for (int m = 0; m < 4; ++m)
#pragma unroll
    for (int n = 0; n < 5; ++n) acc[m][n] = mfma(WF[n], AF[m], m >= LIVE ? f32x4{0.f, 0.f, 0.f, 0.f} : acc[m][n]);
  SCHED();
}
DI void make_kix(unsigned (&kix)[9], int ts) {
#pragma unroll
  for (int i = 0; i < 9; ++i) {
    const int lg = 2047 - 8 * ts - i;
    const int bits = (int)(pk2(__builtin_fmaxf((float)lg, 128.f), 0.f));
    const int ci = lg < bits - 0x4280 ? lg : bits - 0x4280;
    kix[i] = KTAB + 4 * ci;
    PIN(kix[i]);
  }
}
DI void put_k(LAS char* L, const unsigned (&kix)[9], int ts, int c, unsigned reg) {
  float kv[9];
#pragma unroll
  for (int i = 0; i < 9; ++i) kv[i] = *(LAS float*)(L + kix[i] + c * KP_STRIDE);
  LAS char* q = L + reg + CV_KOFF + ts * 16;
  *(LAS v4u*)(q + CV_KSTR) = v4u{pk2(kv[0], kv[1]), pk2(kv[2], kv[3]), pk2(kv[4], kv[5]), pk2(kv[6], kv[7])};
  *(LAS v4u*)q = v4u{pk2(kv[1], kv[2]), pk2(kv[3], kv[4]), pk2(kv[5], kv[6]), pk2(kv[7], kv[8])};
}
DI void put_u(LAS char* q, v4u u, bool first) {
  *(LAS v4u*)(q + 3 * CV_USTR + 384) = u;
  *(LAS v2u*)(q + 1 * CV_USTR + 392) = v2u{u.x, u.y}; *(LAS v2u*)(q + 1 * CV_USTR + 400) = v2u{u.z, u.w};
  *(LAS unsigned*)(q + 2 * CV_USTR + 388) = u.x; *(LAS v2u*)(q + 2 * CV_USTR + 392) = v2u{u.y, u.z}; *(LAS unsigned*)(q + 2 * CV_USTR + 400) = u.w;
  *(LAS unsigned*)(q + 396) = u.x; *(LAS v2u*)(q + 400) = v2u{u.y, u.z}; *(LAS unsigned*)(q + 408) = u.w;
  if (first) {
    *(LAS v2u*)(q + 384) = v2u{0u, 0u}; *(LAS unsigned*)(q + 392) = 0u;
    *(LAS v2u*)(q + CV_USTR + 384) = v2u{0u, 0u}; *(LAS unsigned*)(q + 2 * CV_USTR + 384) = 0u;
  }
}

__global__ __launch_bounds__(512, 2) void fused_s4_mlp(const char* __restrict__ x, const char* __restrict__ pAre, const char* __restrict__ pAim,
    const char* __restrict__ pBre, const char* __restrict__ pBim, const char* __restrict__ pCre, const char* __restrict__ pCim,
    const char* __restrict__ plogdt, const char* __restrict__ pDskip, const char* __restrict__ w1, const char* __restrict__ b1,
    const char* __restrict__ w2, char* __restrict__ out, char* __restrict__ ws) {
  __shared__ __attribute__((aligned(16))) char smem[163840];
  LAS char* L = (LAS char*)smem;
  const unsigned lds0 = (unsigned)(size_t)L;
  const char* pc0; unsigned codelen;
  asm volatile("s_getpc_b64 %0\n98:\n\ts_mov_b32 %1, 97f-98b\n\ts_max_u32 %1, %1, 99f-98b" : "=s"(pc0), "=&s"(codelen) :: "scc");
  const int b = blockIdx.x, lane = threadIdx.x & 63;
  const int w = RFL(threadIdx.x >> 6), wi = w & 3;
  {
    const unsigned co = (unsigned)(4 * (16 * w + (lane & 15)) + ((b >> 3) & 3)) * 128;
    asm volatile("s_mov_b32 m0, %2\n\ts_nop 0\n\tglobal_load_lds_dword %0, %1" ::"v"((co < codelen ? co : codelen) & ~3u), "s"(pc0), "s"(lds0 + YST + w * 256) : "memory");
  }
  const int xcd = b & 7, j = b >> 3, rg = xcd >> 1, chf = xcd & 1;
  const int ct1 = 8 * chf + (j & 7), rb1 = 4 * rg + (j >> 3);
  const int wid = rb1 * 16 + ct1;
  const int a2 = j >> 2, cb2 = 4 * chf + (j & 3);
  const int l15 = lane & 15, g = lane >> 4, qq = (lane >> 2) & 3, p4 = lane & 3, l31 = lane & 31, h = lane >> 5;
  const int par = j & 1, cbase = 80 * xcd + 5 * (j >> 1);
  const int chA = cbase + 3 * par, chS = cbase + 2;
  const int ts = 64 * wi + lane;
  ST(0);
  const int row2 = 512 * rg + 64 * a2 + 16 * wi;
  const char* w2b = ws + WS_W2T + (size_t)(cb2 * 48 + 12 * wi) * 5120;
  const char* actb = ws + WS_ACT + (size_t)((32 * rg + 4 * a2) * 48 + 12 * wi) * 1024;
  const int r2 = (3 * (j & 3) + (3 * a2) / 2 + (w < 4 ? 0 : 6)) % 12;
#define WL(s) w_loads(WF[s], w2b + rot12(r2 + (s)) * 5120, vo)
#define AL(s) a_loads(AF[s], actb + rot12(r2 + (s)) * 1024, vm)
#define LATE_LANE() int ln_ = lane; PIN(ln_); const int c15 = ln_ & 15, cg = ln_ >> 4
#define KAO (CV_KOFF + (c15 & 1) * CV_KSTR + 2 * (2072 - 8 * (c15 >> 1) + 8 * cg))
#define UBO ((c15 & 3) * CV_USTR + 2 * (64 * (c15 >> 2) + 8 * cg + 224))
#define YOFF ((unsigned)((c15 >> 2) * 128 + cg * 32 + (c15 & 3) * 8))
#define OROW ((unsigned)(2 * (c15 >> 2) + (c15 & 1) + 8 * ((c15 >> 1) & 1)))
#define ZOFF (lane < 24 ? wi * CV_USTR + lane * 16 : CV_KOFF + wi * CV_KSTR + 4096 + (lane - 24) * 16)
#define ZCOND (lane < 24 || (lane < 31 && wi < 2))

  v2u xa, xb, E; unsigned xc;
  {
    const char* xrow = x + (size_t)(8 * wid + w) * 1280;
    ld64o<0>(xa, lane * 8, xrow); ld64o<512>(xb, lane * 8, xrow); ld32<1024>(xc, lane * 4, xrow);
    ld64c(E, 0u, ws);
  }
  const int pu = b % 240, pks = pu / 5, pcg = pu % 5;
  dma16((unsigned)((lane >> 1) * 1280 + (lane & 1) * 16), w2 + (size_t)pks * 32 * 1280 + (128 * pcg + 16 * w) * 2, lds0 + W2P + w * 1024);
  unsigned pr[7];
  {
    const int kc = w < 3 ? chA : (w < 6 ? chA + 1 : chS);
    const int m0 = w < 6 ? (w < 3 ? w : w - 3) * 64 : par * 96 + (w - 6) * 48;
    const unsigned vo = (unsigned)(kc * 192 + m0 + ((w >= 6 && lane >= 48) ? 47 : lane)) * 2;
    ld16(pr[6], (unsigned)kc * 2, plogdt);
    ld16(pr[0], vo, pAre); ld16(pr[1], vo, pAim); ld16(pr[2], vo, pBre); ld16(pr[3], vo, pBim); ld16(pr[4], vo, pCre); ld16(pr[5], vo, pCim);
  }
  unsigned voA[4], voB[3];
#pragma unroll
  for (int i = 0; i < 4; ++i) voA[i] = (8 * i + (lane >> 3)) * 1280 + (((lane & 7) ^ (4 * (i & 1) + (lane >> 4))) * 16) - 1024 * i;
#pragma unroll
  for (int i = 0; i < 3; ++i) { const int c = 64 * i + lane; voB[i] = (c / 12) * 3072 + (c % 12) * 16 - 1024 * i; }
  const char* g1a = x + (size_t)(rb1 * 128 + 32 * wi) * 1280;
  const char* g1b = w1 + ct1 * 192 + wi * 49152;
  const int r1 = ((j & 7) + (5 * (j >> 3)) / 2 + 5 * chf + rg) % 10;
  auto issue = [&](int s) {
    const int v = s + r1, sl = v >= 10 ? v - 10 : v;
    const char* pa = g1a + sl * 128; const char* pb = g1b + (size_t)sl * 64 * 3072;
    dma16(voA[0], pa, lds0 + g1_buf(s) + wi * 4096); dma16o<1024>(voA[1], pa); dma16o<2048>(voA[2], pa); dma16o<3072>(voA[3], pa);
    dma16(voB[0], pb, lds0 + g1_buf(s) + G1_BOFF + wi * 3072); dma16o<1024>(voB[1], pb); dma16o<2048>(voB[2], pb);
  };
  if (w == 0) *(LAS unsigned*)(L + lane * 4) = 0u;
  bar();
  if (w >= 4) issue(0);
  unsigned kix[9];
  make_kix(kix, ts);
  if (w >= 4) waitv<14>(); else waitv<7>();
  asm volatile("; AUDITLEAVE7");
  PIN(E); PIN(xa); PIN(xb); PIN(xc);
  const u64 tag64 = (((u64)RFL(E.y) << 32) | (unsigned)RFL(E.x)) + 1ull;
  const unsigned tlo = (unsigned)tag64, thi = (unsigned)(tag64 >> 32);
  ST(1);
  {
    f32x2 v[5] = {unpk(xa.x), unpk(xa.y), unpk(xb.x), unpk(xb.y), unpk(xc)};
    const f32x2 s2 = ((v[0] + v[1]) + (v[2] + v[3])) + v[4];
    const float mean = wave_sum(s2.x + s2.y) * (1.0f / 640.0f);
    f32x2 q2 = f32x2{0.f, 0.f};
#pragma unroll
    for (int i = 0; i < 5; ++i) { const f32x2 d = v[i] - mean; q2 = __builtin_elementwise_fma(d, d, q2); }
    const float var = rbf(wave_sum(q2.x + q2.y) * (1.0f / 640.0f));
    const float m = rbf(mean), sd = rbf(__builtin_amdgcn_sqrtf(rbf(var + 1e-5f))), rs = __builtin_amdgcn_rcpf(sd);
    unsigned o[5];
#pragma unroll
    for (int i = 0; i < 5; ++i) { const f32x2 t = (v[i] - m) * rs; o[i] = pk2(t.x, t.y); }
    LAS char* rp = L + LN_ROWS + w * LN_PITCH;
    *(LAS v2u*)(rp + lane * 8) = v2u{o[0], o[1]}; *(LAS v2u*)(rp + 512 + lane * 8) = v2u{o[2], o[3]}; *(LAS unsigned*)(rp + 1024 + lane * 4) = o[4];
    *(LAS unsigned*)(L + STAT + w * 4) = (__builtin_bit_cast(unsigned, m) & 0xffff0000u) | (__builtin_bit_cast(unsigned, sd) >> 16);
  }
  if (w >= 4) waitv<7>(); else waitv<0>();
  asm volatile("; AUDITCLR");
#pragma unroll
  for (int i = 0; i < 7; ++i) PIN(pr[i]);
  ST(8);
  bar();
  if (w < 4) {
    LAS char* sp = L + LN_ROWS + qq * LN_PITCH + (160 * wi + 16 * g) * 2 + 8 * p4;
#pragma unroll
    for (int i = 0; i < 3; ++i) {
      const v2u lo = trread(sp + 128 * i), hi = trread(sp + 128 * i + 4 * LN_PITCH);
      const unsigned ch = 64 * i + lane;
      if (i < 2 || lane < 32) st128c((ch / 5) * 20480 + (ch % 5) * 16, ws + WS_XNB + (size_t)wi * 32 * 20480 + wid * 80, v4u{lo.x, lo.y, hi.x, hi.y});
    }
    if (lane < 2) st64c(WS_STATS + (unsigned)(8 * wid + 2 * wi + lane) * 8, ws, v2u{*(LAS unsigned*)(L + STAT + (2 * wi + lane) * 4), tlo});
#pragma unroll
    for (int i = 0; i < 2; ++i) {
      LAS char* wp = L + W2P + wi * 2048 + i * 1024 + (16 * (g >> 1) + 4 * (g & 1) + qq) * 32 + 8 * p4;
      const v2u lo = trread(wp), hi = trread(wp + 256);
      const int ntg = 8 * pcg + 2 * wi + i;
      st128c((unsigned)lane * 16, ws + WS_W2T + (size_t)(((ntg / 5) * 48 + pks) * 5 + ntg % 5) * 1024, v4u{lo.x, lo.y, hi.x, hi.y});
    }
  }
  if (w >= 4) __builtin_amdgcn_s_setprio(SPRIO);
  ST(9);
  {
    f32x4 kacc[2][2];
    const float scale = kgen_task(pr, w >= 6 && lane >= 48, lds0 + KG_SCR + w * KG_WSTR, L + KG_SCR + w * KG_WSTR + l15 * 272 + g * 16, kacc, [&]() { if (w >= 4) issue(1); });
    kgen_store((LAS float*)(L + KG_SCR + w * KG_WSTR), kacc, scale, lane);
  }
  ST(2);
  auto mish1 = [&](f32x4 a0, f32x4 a1, int n, int r, float rs, float mrs, unsigned ao, unsigned co) {
    unsigned pk[4];
#pragma unroll
    for (int c = 0; c < 2; ++c) {
      LAS char* cp = L + co + (32 * n + 16 * r + 8 * c) * 4;
      const f32x4 cs = *(LAS f32x4*)cp, bv = *(LAS f32x4*)(cp + 384), av = c ? a1 : a0;
#pragma unroll
      for (int e = 0; e < 2; ++e) {
        const f32x2 h2 = __builtin_elementwise_fma(e ? av.zw : av.xy, f32x2{rs, rs},
                                                   __builtin_elementwise_fma(f32x2{mrs, mrs}, e ? cs.zw : cs.xy, e ? bv.zw : bv.xy));
        const f32x2 s1 = f32x2{__builtin_amdgcn_exp2f(h2.x), __builtin_amdgcn_exp2f(h2.y)} + 1.0f;
        const f32x2 d = __builtin_elementwise_fma(s1, s1, f32x2{1.f, 1.f});
        const f32x2 rr = f32x2{__builtin_amdgcn_rcpf(d.x), __builtin_amdgcn_rcpf(d.y)};
        const f32x2 o = h2 * __builtin_elementwise_fma(rr, f32x2{-1.38629436111989062f, -1.38629436111989062f},
                                                       f32x2{0.69314718055994531f, 0.69314718055994531f});
        pk[2 * c + e] = pk2(o.x, o.y);
      }
    }
    st128c(ao, ws + WS_ACT + (size_t)((rb1 * 8 + 2 * wi) * 48 + 3 * ct1 + n) * 1024 + r * 512, v4u{pk[0], pk[1], pk[2], pk[3]});
  };
#define MISH_SETUP() const float rs = __builtin_amdgcn_rcpf(bf2f(gran.x)) * 1.44269504088896341f, mrs = -bfhi(gran.x) * rs; \
  const unsigned ao = (unsigned)((l31 >> 4) * 49152 + h * 256 + (4 * ((l15 & 7) >> 1) + (l15 & 1) + 2 * (l15 >> 3)) * 16), co = CSF + h * 16

  if (w < 4) {
    int lane = __builtin_amdgcn_mbcnt_hi(~0u, __builtin_amdgcn_mbcnt_lo(~0u, 0u)); PIN(lane);
    const int l15 = lane & 15, g = lane >> 4, qq = (lane >> 2) & 3, p4 = lane & 3, l31 = lane & 31, h = lane >> 5, ts = 64 * wi + lane;
    const unsigned vo = lane * 16;
    const unsigned go = WS_STATS + (unsigned)(rb1 * 128 + 32 * wi + l31) * 8;
    f32x16 acc1[3]; f32x4 csa[3];
#pragma unroll
    for (int n = 0; n < 3; ++n) {
      csa[n] = f32x4{0.f, 0.f, 0.f, 0.f};
#pragma unroll
      for (int i = 0; i < 16; ++i) acc1[n][i] = 0.f;
    }
    unsigned xo[2][4], wo[2][4];
#pragma unroll
    for (int k = 0; k < 4; ++k) {
      const int kr = (k + wi) & 3;
      xo[1][k] = CVA + (32 * wi + l31) * 128 + (((2 * kr + h) ^ ((l31 >> 1) & 7)) * 16);
      wo[1][k] = CVA + G1_BOFF + (16 * kr + 8 * h + qq) * 192 + 32 * (g & 1) + 8 * p4;
      xo[0][k] = xo[1][k] + (G1_RING - CVA); wo[0][k] = wo[1][k] + (G1_RING - CVA);
      PIN(xo[0][k]); PIN(wo[0][k]); PIN(xo[1][k]); PIN(wo[1][k]);
    }
    const unsigned sel1 = ((l15 ^ g) & 1) ? 0u : 0x3f803f80u;
    const v4u sel = v4u{sel1, sel1, sel1, sel1};
    v4u X[4], W[4][3];
    auto rdw = [&](int s, int k, int n) {
      const bool ex = g1_buf(s) < G1_RING;
      LAS char* p = L + wo[ex][k] + (g1_buf(s) - (ex ? CVA : G1_RING)) + 64 * n;
      const v2u lo = trread(p), hi = trread(p + 768); W[k][n] = v4u{lo.x, lo.y, hi.x, hi.y};
    };
    auto rdx = [&](int s, int k) { const bool ex = g1_buf(s) < G1_RING; X[k] = *(LAS v4u*)(L + xo[ex][k] + (g1_buf(s) - (ex ? CVA : G1_RING))); };
#define MM(k, n) acc1[n] = mfma32(W[k][n], X[k], acc1[n])
#define CS(n) csa[n] = mfma(W[0][n], sel, csa[n])
    v2u gran;
    v4u WF[6][5], AF[6][4];
    auto wl1 = [&](int s, int e0) { const int e = 9 * (s - 7) + e0; if (s >= 7 && e < 25) ld128c<0>(WF[e / 5][e % 5], vo, w2b + rot12(r2 + e / 5) * 5120 + (e % 5) * 1024); };
    waitv<0>(); arrive_flag8(L, W_F1C, ws, WS_F1 + (unsigned)(lane & 7) * 2048 + wid * 8, tlo, thi);
    ST(6);
    bar();
    ST(3);
    rdx(0, 0); rdw(0, 0, 0); rdw(0, 0, 1); rdw(0, 0, 2); rdx(0, 1); rdw(0, 1, 0); rdw(0, 1, 1); rdw(0, 1, 2);
#pragma unroll
    for (int s = 0; s < 10; ++s) {
      SCHED();
      MM(0, 0); rdx(s, 2); rdw(s, 2, 0); SCHED();
      MM(0, 1); rdw(s, 2, 1); rdw(s, 2, 2); SCHED();
      MM(0, 2); rdx(s, 3); rdw(s, 3, 0); SCHED();
      CS(0); rdw(s, 3, 1); SCHED();
      CS(1); rdw(s, 3, 2); SCHED();
      CS(2); SCHED();
      MM(1, 0); wl1(s, 0);
      SCHED();
      MM(1, 1); wl1(s, 1);
      if (s == 9 && l15 < 2) {
#pragma unroll
        for (int n = 0; n < 3; ++n) *(LAS f32x4*)(L + CSP + wi * 384 + (32 * n + 16 * l15 + 4 * g) * 4) = csa[n];
      }
      SCHED();
      MM(1, 2); wl1(s, 2); SCHED();
      bar();
      SCHED();
      if (s == 9) {
        MM(2, 0); wl1(s, 3); SCHED(); MM(2, 1); wl1(s, 4); SCHED(); MM(2, 2); wl1(s, 5); SCHED(); MM(3, 0); wl1(s, 6); SCHED(); MM(3, 1); MM(3, 2); SCHED();
        break;
      }
      MM(2, 0); rdx(s + 1, 0); rdw(s + 1, 0, 0); wl1(s, 3); SCHED();
      MM(2, 1); rdw(s + 1, 0, 1); wl1(s, 4); SCHED();
      MM(2, 2); rdw(s + 1, 0, 2); wl1(s, 5); SCHED();
      MM(3, 0); rdx(s + 1, 1); rdw(s + 1, 1, 0); wl1(s, 6); SCHED();
      MM(3, 1); rdw(s + 1, 1, 1); wl1(s, 7); SCHED();
      MM(3, 2); rdw(s + 1, 1, 2); wl1(s, 8);
      if (s == 6) ld64c(gran, go, ws);
      SCHED();
      if (s == 0) ST(4); if (s == 3) ST(5); if (s == 6) ST(7);
    }
#undef MM
#undef CS
    ST(11);
    waitv<25>(); PIN(gran);
    MISH_SETUP();
    bar();
    ST(12);
    mish1(acc1[0].s0123, acc1[0].s4567, 0, 0, rs, mrs, ao, co); mish1(acc1[0].s89ab, acc1[0].scdef, 0, 1, rs, mrs, ao, co);
    mish1(acc1[1].s0123, acc1[1].s4567, 1, 0, rs, mrs, ao, co); mish1(acc1[1].s89ab, acc1[1].scdef, 1, 1, rs, mrs, ao, co);
    mish1(acc1[2].s0123, acc1[2].s4567, 2, 0, rs, mrs, ao, co); mish1(acc1[2].s89ab, acc1[2].scdef, 2, 1, rs, mrs, ao, co);
    bar();
    ST(13);
    __builtin_amdgcn_s_setprio(2);
    LATE_LANE();
    v2u xq[5], dq[5], gr2, f3;
    const unsigned f2o = WS_F2 + (unsigned)((4 * rg + (a2 >> 1)) * 16 + 4 * wi + (ln_ & 3)) * 128;
    v2u f2;
    {
      f32x4 sacc[4];
#pragma unroll
      for (int i = 0; i < 4; ++i) sacc[i] = f32x4{0.f, 0.f, 0.f, 0.f};
      LAS char* ka = L + CVS + KAO - wi * 128; LAS char* ub = L + CVS + UBO - wi * 128;
      if (par) conv_unit_shared<1>(ka, ub, sacc); else conv_unit_shared<0>(ka, ub, sacc);
      waitv<0>();
      arrive_flag<4>(L, W_F2C, lane, ws, WS_F2 + (unsigned)wid * 128, tlo, thi);
      ST(14);
      {
        const char* xb2 = x + (size_t)row2 * 1280 + cb2 * 160; const unsigned xo2 = OROW * 1280 + cg * 8;
        ld64o<0>(xq[0], xo2, xb2); ld64o<32>(xq[1], xo2, xb2); ld64o<64>(xq[2], xo2, xb2); ld64o<96>(xq[3], xo2, xb2); ld64o<128>(xq[4], xo2, xb2);
        const char* db = pDskip + cb2 * 160;
        ld64o<0>(dq[0], cg * 8, db); ld64o<32>(dq[1], cg * 8, db); ld64o<64>(dq[2], cg * 8, db); ld64o<96>(dq[3], cg * 8, db); ld64o<128>(dq[4], cg * 8, db);
        ld64c(gr2, WS_STATS + (row2 + OROW) * 8, ws);
      }
      if (par) conv_unit_shared<1>(ka - 64, ub - 64, sacc); else conv_unit_shared<0>(ka - 64, ub - 64, sacc);
#pragma unroll
      for (int q = 0; q < 4; ++q) *(LAS f32x4*)(L + MXCH + (q * 4 + wi) * 1024 + vo) = sacc[q];
      lds_arrive(L, W_MB, lane);
    }
    ld64c(f2, f2o, ws);
    ST(15);
    unsigned vm[4];
#pragma unroll
    for (int m = 0; m < 4; ++m) vm[m] = vo + ((m + wi) & 3) * 49152;
    v2u ysh;
    {
      lds_wait_ge(L, W_MB, 4);
      LAS char* p = L + MXCH + wi * 4096 + vo;
      const f32x4 s = add4(add4(*(LAS f32x4*)p, *(LAS f32x4*)(p + 1024)), add4(*(LAS f32x4*)(p + 2048), *(LAS f32x4*)(p + 3072)));
      ysh = v2u{pk2(s.x, s.y), pk2(s.z, s.w)};
    }
    waitv<0>(); PIN(f2); PIN(gr2);
    f2 = spin64<false>(f2, f2o, ws, tlo, thi);
    lds_set(L, W_F2OK + wi * 4, 1);
    ST(16);
    st64c(YOFF, ws + WS_Y + (size_t)chS * 4096 + (2 * wi + ((wi & 1) ^ par)) * 512, ysh);
    AL(0); AL(1); AL(2);
    __builtin_amdgcn_s_setprio(0);
    f32x4 acc2[4][5];
    const float rs2 = __builtin_amdgcn_rcpf(bf2f(gr2.x)), m2 = -bfhi(gr2.x) * rs2;
#pragma unroll
    for (int n = 0; n < 5; ++n) {
      PIN(xq[n]); PIN(dq[n]);
      const f32x2 x0 = unpk(xq[n].x), x1 = unpk(xq[n].y);
      const f32x2 d0 = unpk(dq[n].x), d1 = unpk(dq[n].y);
      acc2[0][n] = f32x4{ffma(d0.x, ffma(x0.x, rs2, m2), x0.x), ffma(d0.y, ffma(x0.y, rs2, m2), x0.y), ffma(d1.x, ffma(x1.x, rs2, m2), x1.x), ffma(d1.y, ffma(x1.y, rs2, m2), x1.y)};
      PIN(acc2[0][n]);
    }
    ST(17);
    asm volatile("s_nop 1");
    waitv<8>();
    arrive_flag<8>(L, W_F3C, lane, ws, WS_F3 + (unsigned)(xcd * 32 + j) * 128, tlo, thi);
    g2_step<1>(acc2, WF[0], AF[0]);
    AL(3); AL(4);
    waitv<12>(); g2_step<4>(acc2, WF[1], AF[1]);
    WL(5); AL(5);
    waitv<17>(); g2_step<4>(acc2, WF[2], AF[2]);
    ST(20);
    waitv<13>(); g2_step<4>(acc2, WF[3], AF[3]);
    waitv<9>(); g2_step<4>(acc2, WF[4], AF[4]);
    lds_wait_ge(L, W_F3C, 8);
    const unsigned f3o = WS_F3 + (unsigned)(cb2 * 32 + 8 * wi + (ln_ & 7)) * 128;
    ld64c(f3, f3o, ws);
    waitv<1>(); g2_step<4>(acc2, WF[5], AF[5]);
    ST(22);
    waitv<0>(); PIN(f3);
    f3 = spin64<false>(f3, f3o, ws, tlo, thi);
    lds_arrive(L, W_F3ALL, lane);
    LAS char* fp = L + FM + wi * 20480 + vo;
#pragma unroll
    for (int n = 0; n < 5; ++n) *(LAS f32x4*)(fp + wi * 5120 + n * 1024) = acc2[0][n];
    lds_wait_ge(L, W_F3ALL, 4);
    ST(21);
    v4u yv[3];
    {
      const char* yb = ws + WS_Y + (size_t)cb2 * 80 * 4096 + row2 * 2;
      const unsigned yo = (unsigned)(ln_ >> 1) * 4096 + (ln_ & 1) * 16;
      ld128c<0>(yv[0], yo, yb); ld128c<0>(yv[1], yo, yb + 32 * 4096); ld128c<0>(yv[2], yo & 0xffffu, yb + 64 * 4096);
    }
#pragma unroll
    for (int m = 1; m < 4; ++m)
#pragma unroll
      for (int n = 0; n < 5; ++n) *(LAS f32x4*)(fp + ((m + wi) & 3) * 5120 + n * 1024) = acc2[m][n];
    lds_arrive(L, W_FM, lane);
    waitv<0>(); PIN(yv[0]); PIN(yv[1]); PIN(yv[2]);
    LAS char* ys = L + YST + wi * 2560;
    *(LAS v4u*)(ys + vo) = yv[0]; *(LAS v4u*)(ys + 1024 + vo) = yv[1];
    if (ln_ < 32) *(LAS v4u*)(ys + 2048 + vo) = yv[2];
    lds_set(L, W_F2OK + wi * 4, 2);
    STEND();
    asm volatile("97:");
    __builtin_amdgcn_endpgm();
  }

  const bool odd = wi & 1;
  const int kch = chA + (wi >> 1);
  const unsigned vo = lane * 16;
  v4u WF[6][5], AF[6][4];
  auto reduce = [&](int c, int i, bool three) {
    LAS char* pp = L + KG_SCR + 3 * c * KG_WSTR;
    f32x4 s = fma4(*(LAS f32x4*)(pp + i * 16), *(LAS float*)(pp + 2576), f32x4{0.f, 0.f, 0.f, 0.f});
    s = fma4(*(LAS f32x4*)(pp + KG_WSTR + i * 16), *(LAS float*)(pp + KG_WSTR + 2576), s);
    if (three) s = fma4(*(LAS f32x4*)(pp + 2 * KG_WSTR + i * 16), *(LAS float*)(pp + 2 * KG_WSTR + 2576), s);
    *(LAS f32x4*)(L + KTAB + c * KP_STRIDE + i * 16) = s;
    return s;
  };
  issue(2);
  waitv<14>();
  ST(6);
  bar();
  if (ts < 161) { st128c((unsigned)ts * 16, ws + WS_KP + (size_t)wid * KP_STRIDE, __builtin_bit_cast(v4u, reduce(2, ts, false))); reduce(0, ts, true); }
  waitv<7>(); bar();
  issue(3);
  if (ts < 161) reduce(1, ts, true);
  else if (ts < 164) *(LAS float*)(L + KTAB + (ts - 161) * KP_STRIDE - 4) = 0.f;
  if (wi < 3) waitv<8>(); else waitv<7>();
  bar();
  issue(4); issue(5);
  waitv<14>();
  arrive_flag(L, W_FKC, lane, ws, WS_FK + (unsigned)wid * 64, tlo, thi);
  bar();
  issue(6); issue(7);
  waitv<21>(); bar();
  const unsigned f1o = WS_F1 + (unsigned)xcd * 2048 + ts * 8, fko = WS_FK + (unsigned)(wid ^ 1) * 64;
  v2u f1own, fk; ld64c(f1own, f1o, ws);
  issue(8);
  waitv<22>(); bar();
  issue(9);
  waitv<14>(); PIN(f1own);
  ST(4);
  f1own = spin64<false>(f1own, f1o, ws, tlo, thi);
  v4u uu[3]; unsigned ulo[3];
#pragma unroll
  for (int r = 0; r < 3; ++r) {
    const int e = 64 * r + lane, tb = 64 * wi + e / 3, c = e % 3;
    ld128c<0>(uu[r], tb * 80 + c * 16, ws + WS_XNB + (size_t)(16 * xcd + (j >> 1)) * 20480 + par * 32);
    ulo[r] = (c == 2 - 2 * par ? CVS : CVA + (c - par) * CV_CH) + tb * 16;
  }
  unsigned bb;
  ld64c(fk, fko, ws); ld32<0>(bb, (unsigned)(lane < 48 ? lane : 47) * 4, b1 + ct1 * 192);
  bar();
  if (b == 0 && ts == 0) st64c(WS_E, ws, v2u{tlo, thi});
  WL(0);
  bar();
  waitv<5>(); PIN(uu[0]); PIN(uu[1]); PIN(uu[2]); PIN(fk); PIN(bb);
  fk = spin64<false>(fk, fko, ws, tlo, thi);
  v4u kp; ld128c<0>(kp, (unsigned)(ts < 161 ? ts : 160) * 16, ws + WS_KP + (size_t)(wid ^ 1) * KP_STRIDE);
  WL(1);
  bar();
  bar();
  waitv<5>(); PIN(kp);
  if (ts < 161) { LAS f32x4* tp = (LAS f32x4*)(L + KTAB + 2 * KP_STRIDE + ts * 16); *tp = add4(*tp, __builtin_bit_cast(f32x4, kp)); }
  bar();
  if (wi == 0 && lane < 48) {
    LAS f32x2* p = (LAS f32x2*)(L + CSP + lane * 8);
    const f32x2 p0 = p[0], p1 = p[48], p2 = p[96], p3 = p[144];
    *(LAS f32x2*)(L + CSF + lane * 8) = f32x2{fadd(fadd(p0.x, p1.x), fadd(p2.x, p3.x)), fadd(fadd(p0.y, p1.y), fadd(p2.y, p3.y))};
    *(LAS f32x2*)(L + CSF + 384 + lane * 8) = f32x2{ffma(bf2f(bb), 1.44269504088896341f, 0.f), ffma(bfhi(bb), 1.44269504088896341f, 0.f)};
  }
  ST(11);
  bar();
  ST(12);
  put_u(L + ulo[0], uu[0], ts < 3); WL(2);
  put_u(L + ulo[1], uu[1], false); put_u(L + ulo[2], uu[2], false); WL(3);
  put_k(L, kix, ts, 0, CVA); put_k(L, kix, ts, 1, CVA + CV_CH); put_k(L, kix, ts, 2, CVS);
  if (ZCOND) {
    *(LAS v4u*)(L + CVA + ZOFF) = v4u{0u, 0u, 0u, 0u}; *(LAS v4u*)(L + CVA + CV_CH + ZOFF) = v4u{0u, 0u, 0u, 0u}; *(LAS v4u*)(L + CVS + ZOFF) = v4u{0u, 0u, 0u, 0u};
  }
  WL(4);
  ST(5);
  bar();
  ST(13);
  __builtin_amdgcn_s_setprio(SPRIO2);
  LATE_LANE();
  f32x4 cacc[8];
#pragma unroll
  for (int i = 0; i < 8; ++i) cacc[i] = f32x4{0.f, 0.f, 0.f, 0.f};
  {
    const unsigned o1 = CVA + (wi >> 1) * CV_CH - (odd ? 4 * 64 : 0);
    LAS char* ka = L + KAO + o1; LAS char* ub = L + UBO + o1;
    v4u A[8], B[8];
#define LA(i, u) (*(LAS v4u*)(ka - 512 * (i) - 64 * (u)))
#define LB(i, u) (*(LAS v4u*)(ub + 512 * (i) - 64 * (u)))
    A[7] = LA(7, 0); B[0] = LB(0, 0); A[0] = LA(0, 0); B[7] = LB(7, 0); A[6] = LA(6, 0); B[6] = LB(6, 0); A[1] = LA(1, 0); B[1] = LB(1, 0);
    A[5] = LA(5, 0); B[5] = LB(5, 0); A[2] = LA(2, 0); B[2] = LB(2, 0); A[4] = LA(4, 0); B[4] = LB(4, 0); A[3] = LA(3, 0); B[3] = LB(3, 0);
    SCHED();
#pragma unroll
    for (int u = 0; u < 4; ++u) {
      v4u An[2], Bn[2];
#pragma unroll
      for (int sd = 7; sd >= 0; --sd) {
#pragma unroll
        for (int o = 0; o <= sd; ++o) {
          if (sd + o < 8) cacc[sd + o] = mfma(A[sd], B[o], cacc[sd + o]);
          if (o != sd && sd + o < 8) cacc[sd + o] = mfma(A[o], B[sd], cacc[sd + o]);
        }
        SCHED();
        if (u < 3) {
          if (sd >= 2) { A[sd] = LA(sd, u + 1); B[sd] = LB(sd, u + 1); }
          if (sd == 6 || sd == 5) { An[6 - sd] = LA(6 - sd, u + 1); Bn[6 - sd] = LB(6 - sd, u + 1); }
        }
        SCHED();
      }
      if (u < 3) { A[0] = An[0]; A[1] = An[1]; B[0] = Bn[0]; B[1] = Bn[1]; }
    }
#undef LA
#undef LB
  }
  ST(10);
  unsigned vm[4];
  {
    LAS char* px = L + SXCH + wi * 4096 + vo;
    if (odd) {
#pragma unroll
      for (int i = 0; i < 4; ++i) { *(LAS f32x4*)(px + i * 1024) = cacc[i]; cacc[i] = cacc[4 + i]; }
    } else {
#pragma unroll
      for (int i = 0; i < 4; ++i) *(LAS f32x4*)(px + i * 1024) = cacc[4 + i];
    }
    lds_arrive(L, W_SX + (wi >> 1) * 4, lane);
#pragma unroll
    for (int m = 0; m < 4; ++m) vm[m] = vo + ((m + wi) & 3) * 49152;
    lds_wait_ge(L, W_SX + (wi >> 1) * 4, 2);
    px = L + SXCH + (wi ^ 1) * 4096 + vo;
    v2u yo[4];
#pragma unroll
    for (int i = 0; i < 4; ++i) { const f32x4 s = add4(cacc[i], *(LAS f32x4*)(px + i * 1024)); yo[i] = v2u{pk2(s.x, s.y), pk2(s.z, s.w)}; }
    char* yb = ws + WS_Y + (size_t)kch * 4096 + (odd ? 2048 : 0);
    st64co<0>(YOFF, yb, yo[0]); st64co<512>(YOFF, yb, yo[1]); st64co<1024>(YOFF, yb, yo[2]); st64co<1536>(YOFF, yb, yo[3]);
  }
  ST(14);
  lds_wait_ge(L, W_F2OK + wi * 4, 1);
  ST(16);
  AL(0); AL(1); AL(2);
  ST(17);
  f32x4 acc2[4][5];
  waitv<12>();
  arrive_flag<8>(L, W_F3C, lane, ws, WS_F3 + (unsigned)(xcd * 32 + j) * 128, tlo, thi);
  __builtin_amdgcn_s_setprio(0);
  waitv<8>();
  g2_step<0>(acc2, WF[0], AF[0]);
  AL(3); AL(4);
  waitv<12>(); g2_step<4>(acc2, WF[1], AF[1]);
  WL(5); AL(5);
  waitv<17>(); g2_step<4>(acc2, WF[2], AF[2]);
  ST(20);
  waitv<13>(); g2_step<4>(acc2, WF[3], AF[3]);
  waitv<9>(); g2_step<4>(acc2, WF[4], AF[4]);
  waitv<0>(); g2_step<4>(acc2, WF[5], AF[5]);
  ST(22);
  lds_wait_ge(L, W_F3C, 8);
#pragma unroll
  for (int m = 1; m < 4; ++m)
#pragma unroll
    for (int n = 0; n < 5; ++n) *(LAS f32x4*)(L + FS + wi * 15360 + (m - 1) * 5120 + n * 1024 + vo) = acc2[m][n];
  lds_arrive(L, W_FS, lane);
  ST(23);
  lds_wait_ge(L, W_FS, 4);
#pragma unroll
  for (int d = 1; d < 4; ++d)
#pragma unroll
    for (int n = 0; n < 5; ++n) acc2[0][n] += *(LAS f32x4*)(L + FS + ((wi - d) & 3) * 15360 + (d - 1) * 5120 + n * 1024 + vo);
  lds_wait_ge(L, W_FM, 4);
  ST(24);
  {
    char* ob = out + (size_t)row2 * 1280 + cb2 * 160; const unsigned oo = OROW * 1280 + cg * 8;
    v2u o[5];
#pragma unroll
    for (int n = 0; n < 5; ++n) {
      LAS char* p = L + FM + wi * 5120 + n * 1024 + vo;
      acc2[0][n] += (*(LAS f32x4*)p + *(LAS f32x4*)(p + 20480)) + (*(LAS f32x4*)(p + 40960) + *(LAS f32x4*)(p + 61440));
    }
    lds_wait_ge(L, W_F2OK + wi * 4, 2);
#pragma unroll
    for (int n = 0; n < 5; ++n) {
      const v2u t = trread(L + YST + wi * 2560 + n * 512 + ln_ * 8);
      const f32x2 s0 = acc2[0][n].xy + unpk(t.x), s1 = acc2[0][n].zw + unpk(t.y);
      o[n] = v2u{pk2(s0.x, s0.y), pk2(s1.x, s1.y)};
    }
    st64co<0>(oo, ob, o[0]); st64co<32>(oo, ob, o[1]); st64co<64>(oo, ob, o[2]); st64co<96>(oo, ob, o[3]); st64co<128>(oo, ob, o[4]);
  }
  STEND();
  asm volatile("99:");
}

extern "C" void kernel_launch(void* const* d_in, const int* in_sizes, int n_in, void* d_out, int out_size, void* d_ws, size_t ws_size,
                              hipStream_t stream) {
  fused_s4_mlp<<<256, 512, 0, stream>>>((const char*)d_in[0], (const char*)d_in[1], (const char*)d_in[2], (const char*)d_in[3],
      (const char*)d_in[4], (const char*)d_in[5], (const char*)d_in[6], (const char*)d_in[7], (const char*)d_in[8], (const char*)d_in[10],
      (const char*)d_in[11], (const char*)d_in[12], (char*)d_out, (char*)d_ws);
}
```

```cpp
#include <hip/hip_runtime.h>
#include <stdint.h>

typedef unsigned short u16;
typedef unsigned long long u64;
typedef unsigned v4u __attribute__((ext_vector_type(4)));
typedef unsigned v2u __attribute__((ext_vector_type(2)));
typedef float f32x4 __attribute__((ext_vector_type(4)));
typedef float f32x2 __attribute__((ext_vector_type(2)));
typedef short bf16x8 __attribute__((ext_vector_type(8)));
typedef short s16x4 __attribute__((ext_vector_type(4)));
typedef __bf16 bf16x2_t __attribute__((ext_vector_type(2)));
#define LAS __attribute__((address_space(3)))
#define GAS __attribute__((address_space(1)))
#define DI __device__ __forceinline__

constexpr unsigned WS_E = 0, WS_TMO = 64, WS_F1 = 131072, WS_STATS = 16384, WS_F2 = 32768, WS_F3 = 65536, WS_FK = 98304, WS_STAMP = 262144;
constexpr unsigned WS_XNB = 1u << 20, WS_W2T = 4u << 20, WS_Y = 7u << 20, WS_ACT = 10u << 20, WS_KP = 17u << 20;
constexpr unsigned KP_STRIDE = 2624;
constexpr unsigned SPIN_CAP = 400000;

constexpr unsigned W_F1C = 0, W_F2C = 4, W_F3C = 8, W_KG = 12, W_MB = 16, W_SB = 20, W_SX = 24, W_FM = 32, W_FS = 36, W_F3ALL = 40, W_F1ALL = 44,
                   W_F2OK = 48, W_RDY = 64, W_TAG = 80, W_FKC = 96;
constexpr unsigned KTAB = 272;
constexpr unsigned CSP = 8192;
constexpr unsigned CVA = 10240, CV_CH = 27008, CV_USTR = 4640, CV_KOFF = 18560, CV_KSTR = 4224;
constexpr unsigned KG_SCR = CVA, KG_WSTR = 4352, LN_ROWS = KG_SCR + 8 * KG_WSTR, LN_PITCH = 1344, W2P = LN_ROWS + 10752;
constexpr unsigned G1_RING = 64512, G1_STAGE = 28672, G1_BOFF = 16384;
constexpr unsigned CVS = G1_RING, SXCH = CVS + 27136, MXCH = SXCH + 16384;
constexpr unsigned CSF = G1_RING + 3 * G1_STAGE, YST = CSF + 3072;
constexpr unsigned FM = 256, FS = FM + 81920;
static_assert(FS + 61440 <= CSF && CVA + 2 * CV_CH <= G1_RING && W2P + 8192 <= G1_RING && YST + 10240 <= 163840 && MXCH + 16384 <= CSF, "lds map");

#define F1STAGE 6
#define SPRIO2 1
#define F1GAP 16
#define F2GAP 12
#define SPRIO 1
#define STAMP
#define ST(i) do { if ((i) == 0) { stv0 = __builtin_amdgcn_s_memrealtime(); stv1 = stv0; } if ((i) == 1 && (w < 4)) { asm volatile("s_memrealtime %0\n\ts_waitcnt lgkmcnt(0)" : "=s"(stv1) :: "memory"); } } while (0)
#define STEND() do { while (__builtin_amdgcn_s_memrealtime() - stv1 < 4000) __builtin_amdgcn_s_sleep(2); } while (0)

DI float bf2f(unsigned v) { return __builtin_bit_cast(float, v << 16); }
DI float bfhi(unsigned v) { return __builtin_bit_cast(float, v & 0xffff0000u); }
DI unsigned pk2(float lo, float hi) { bf16x2_t r; r.x = (__bf16)lo; r.y = (__bf16)hi; return __builtin_bit_cast(unsigned, r); }
DI float rbf(float f) { return bf2f(pk2(f, f) & 0xffffu); }
DI f32x4 mfma(v4u a, v4u b, f32x4 c) {
  return __builtin_amdgcn_mfma_f32_16x16x32_bf16(__builtin_bit_cast(bf16x8, a), __builtin_bit_cast(bf16x8, b), c, 0, 0, 0);
}
DI v2u trread(LAS char* p) { return __builtin_bit_cast(v2u, __builtin_amdgcn_ds_read_tr16_b64_v4i16((LAS s16x4*)p)); }
template <int N> DI void waitv() { asm volatile("s_waitcnt vmcnt(%0)" ::"i"(N) : "memory"); }
DI void bar() { asm volatile("s_waitcnt lgkmcnt(0)\n\ts_barrier" ::: "memory"); }
#define PIN(x) asm volatile("" : "+v"(x))
#define FENCE() asm volatile("" ::: "memory")
#define SCHED() __builtin_amdgcn_sched_barrier(0)
#define RFL(x) __builtin_amdgcn_readfirstlane(x)

DI void ld16(unsigned& d, unsigned vo, const void* sb) { asm volatile("global_load_ushort %0, %1, %2" : "=v"(d) : "v"(vo), "s"(sb) : "memory"); }
template <int O> DI void ld32(unsigned& d, unsigned vo, const void* sb) { asm volatile("global_load_dword %0, %1, %2 offset:%3" : "=v"(d) : "v"(vo), "s"(sb), "i"(O) : "memory"); }
DI void ld64(v2u& d, unsigned vo, const void* sb) { asm volatile("global_load_dwordx2 %0, %1, %2" : "=v"(d) : "v"(vo), "s"(sb) : "memory"); }
DI void ld128(v4u& d, unsigned vo, const void* sb) { asm volatile("global_load_dwordx4 %0, %1, %2" : "=v"(d) : "v"(vo), "s"(sb) : "memory"); }
DI void ld64c(v2u& d, unsigned vo, const void* sb) { asm volatile("global_load_dwordx2 %0, %1, %2 sc1" : "=v"(d) : "v"(vo), "s"(sb) : "memory"); }
template <int O> DI void ld128c(v4u& d, unsigned vo, const void* sb) { asm volatile("global_load_dwordx4 %0, %1, %2 offset:%3 sc1" : "=v"(d) : "v"(vo), "s"(sb), "i"(O) : "memory"); }
DI void st64c(unsigned vo, void* sb, v2u d) { asm volatile("global_store_dwordx2 %0, %1, %2 sc1" ::"v"(vo), "v"(d), "s"(sb) : "memory"); }
template <int O> DI void ld64o(v2u& d, unsigned vo, const void* sb) { asm volatile("global_load_dwordx2 %0, %1, %2 offset:%3" : "=v"(d) : "v"(vo), "s"(sb), "i"(O) : "memory"); }
template <int O> DI void st64co(unsigned vo, void* sb, v2u d) { asm volatile("global_store_dwordx2 %0, %1, %2 offset:%3 sc1" ::"v"(vo), "v"(d), "s"(sb), "i"(O) : "memory"); }
DI void st128c(unsigned vo, void* sb, v4u d) { asm volatile("global_store_dwordx4 %0, %1, %2 sc1\n\ts_nop 1" ::"v"(vo), "v"(d), "s"(sb) : "memory"); }
DI void st128u(unsigned vo, void* sb, v4u d) { asm volatile("global_store_dwordx4 %0, %1, %2 sc1 ; CNT\n\ts_nop 1" ::"v"(vo), "v"(d), "s"(sb) : "memory"); }
DI void dma16(unsigned vo, const void* sb, unsigned lds) {
  asm volatile("s_mov_b32 m0, %2\n\ts_nop 0\n\tglobal_load_lds_dwordx4 %0, %1" ::"v"(vo), "s"(sb), "s"(lds) : "memory");
}
template <int O> DI void dma16o(unsigned vo, const void* sb) {
  asm volatile("global_load_lds_dwordx4 %0, %1 offset:%2" ::"v"(vo), "s"(sb), "i"(O) : "memory");
}
template <bool HIONLY> DI v2u spin64(v2u cur, unsigned vo, char* ws, unsigned tlo, unsigned thi) {
  if (__builtin_expect(__builtin_amdgcn_ballot_w64(HIONLY ? (cur.y != tlo) : (cur.x != tlo || cur.y != thi)) != 0ull, 0)) {
#pragma nounroll
    for (unsigned spins = 0; spins < SPIN_CAP; ++spins) {
      __builtin_amdgcn_s_sleep(6);
      asm volatile("global_load_dwordx2 %0, %1, %2 sc1\n\ts_waitcnt vmcnt(0)" : "=&v"(cur) : "v"(vo), "s"(ws) : "memory");
      if (__builtin_amdgcn_ballot_w64(HIONLY ? (cur.y != tlo) : (cur.x != tlo || cur.y != thi)) == 0ull) break;
    }
  }
  return cur;
}
DI unsigned lw(LAS char* L, unsigned off) { return *(volatile LAS unsigned*)(L + off); }
DI void lds_wait_at(unsigned a, unsigned target) {
  unsigned v, cnt; u64 t;
  asm volatile("s_mov_b32 %1, %5\n"
               "1:\n\tds_read_b32 %0, %3\n\ts_sub_u32 %1, %1, 1\n\ts_waitcnt lgkmcnt(0)\n\tv_cmp_gt_u32 vcc, %4, %0\n\t"
               "s_cselect_b64 %2, 0, vcc\n\ts_cmp_lg_u64 %2, 0\n\ts_cbranch_scc1 1b"
               : "=&v"(v), "=&s"(cnt), "=&s"(t) : "v"(a), "s"(target), "i"(4 * SPIN_CAP) : "vcc", "scc", "memory");
}
DI void lds_wait_ge(LAS char* L, unsigned off, unsigned target) { lds_wait_at((unsigned)(size_t)L + off, target); }
template <int N> DI void lds_wait_min(LAS char* L, unsigned off, unsigned target, int lane) {
  lds_wait_at((unsigned)(size_t)L + off + (lane & (N - 1)) * 4, target);
}
DI void lds_set(LAS char* L, unsigned off, unsigned v) { FENCE(); *(volatile LAS unsigned*)(L + off) = v; FENCE(); }
DI void lds_arrive(LAS char* L, unsigned off, int) {
  asm volatile("s_mov_b64 exec, 1\n\tds_add_u32 %0, %1\n\ts_mov_b64 exec, -1" ::"v"((unsigned)(size_t)L + off), "v"(1u) : "memory");
}
template <unsigned N = 4> DI void arrive_flag(LAS char* L, unsigned off, int, char* ws, unsigned fo, unsigned tlo, unsigned thi) {
  unsigned old;
  asm volatile("s_mov_b64 exec, 1\n\tds_add_rtn_u32 %0, %1, %2\n\ts_waitcnt lgkmcnt(0)\n\tv_cmp_eq_u32 vcc, %6, %0\n\t"
               "s_mov_b64 exec, vcc\n\tglobal_store_dwordx2 %3, %4, %5 sc1\n\ts_mov_b64 exec, -1"
               : "=&v"(old) : "v"((unsigned)(size_t)L + off), "v"(1u), "v"(fo), "v"(v2u{tlo, thi}), "s"(ws), "i"(N - 1) : "vcc", "memory");
}
DI void arrive_flag8(LAS char* L, unsigned off, char* ws, unsigned fo, unsigned tlo, unsigned thi) {
  unsigned old;
  asm volatile("s_mov_b64 exec, 1\n\tds_add_rtn_u32 %0, %1, %2\n\ts_waitcnt lgkmcnt(0)\n\tv_cmp_eq_u32 vcc, 3, %0\n\t"
               "s_mul_i32 exec_lo, vcc_lo, 0xff\n\tglobal_store_dwordx2 %3, %4, %5 sc1\n\ts_mov_b64 exec, -1"
               : "=&v"(old) : "v"((unsigned)(size_t)L + off), "v"(1u), "v"(fo), "v"(v2u{tlo, thi}), "s"(ws) : "vcc", "memory");
}
typedef float f32x16 __attribute__((ext_vector_type(16)));
DI f32x16 mfma32(v4u a, v4u b, f32x16 c) {
  return __builtin_amdgcn_mfma_f32_32x32x16_bf16(__builtin_bit_cast(bf16x8, a), __builtin_bit_cast(bf16x8, b), c, 0, 0, 0);
}
template <int C> DI float dpp_add(float v) {
  return v + __builtin_bit_cast(float, __builtin_amdgcn_update_dpp(0, __builtin_bit_cast(int, v), C, 0xf, 0xf, true));
}
DI float rl(float v, int l) { return __builtin_bit_cast(float, __builtin_amdgcn_readlane(__builtin_bit_cast(int, v), l)); }
DI float half_sum(float v, bool upper) {
  v = dpp_add<0xB1>(v); v = dpp_add<0x4E>(v); v = dpp_add<0x141>(v); v = dpp_add<0x140>(v);
  const float a = rl(v, 0) + rl(v, 16), c = rl(v, 32) + rl(v, 48);
  return upper ? c : a;
}
DI f32x2 unpk(unsigned v) { return f32x2{bf2f(v), bfhi(v)}; }
typedef f32x2 cf;
DI cf cmul1(cf i, cf w) { return cf{__builtin_fmaf(i.x, w.x, -(i.y * w.y)), __builtin_fmaf(i.x, w.y, i.y * w.x)}; }
DI void cmul4(cf& o0, cf& o1, cf& o2, cf& o3, cf i0, cf i1, cf i2, cf i3, cf w0, cf w1, cf w2, cf w3) {
  o0 = cmul1(i0, w0); o1 = cmul1(i1, w1); o2 = cmul1(i2, w2); o3 = cmul1(i3, w3);
}
DI unsigned pkc(cf v) { return pk2(v.x, v.y); }
template <int R> DI void put4(cf a, cf b, cf c, cf d) {
  asm volatile("ds_write_addtid_b32 %0 offset:%4\n\tds_write_addtid_b32 %1 offset:%5\n\tds_write_addtid_b32 %2 offset:%6\n\tds_write_addtid_b32 %3 offset:%7"
               ::"v"(pkc(a)), "v"(pkc(b)), "v"(pkc(c)), "v"(pkc(d)), "i"(R * 272), "i"(R * 272 + 272), "i"(R * 272 + 544), "i"(R * 272 + 816) : "memory");
}
template <int R> DI void put1(cf a) { asm volatile("ds_write_addtid_b32 %0 offset:%1" ::"v"(pkc(a)), "i"(R * 272) : "memory"); }
DI cf cexpk(float e1, float th, float tl, float k, float ks) {
  const float mag = __builtin_amdgcn_exp2f(e1 * k);
  const float fr = __builtin_fmaf(tl, ks, __builtin_amdgcn_fractf(th * ks));
  return cf{__builtin_amdgcn_cosf(fr), __builtin_amdgcn_sinf(fr)} * mag;
}
DI void kgen_slow(cf a, cf W1, cf W256, cf W8, LAS char* sr, f32x4 (&kacc)[2][2]) {
  cf W2, W512, u1, W16, u2, u3, W4, W32, u4, u5, u6, u7, r8, r9, r10, r11, r12, r13, r14, r15, r16, r17, W1024, v3, r18, r19, r20, v1;
  cmul4(W2, W512, u1, W16, W1, W256, a, W8, W1, W256, W1, W8);
  cmul4(u2, u3, W4, W32, a, u1, W2, W16, W2, W2, W2, W16);
  put4<0>(a, u1, u2, u3);
  cmul4(u4, u5, u6, u7, a, u1, u2, u3, W4, W4, W4, W4);
  put4<4>(u4, u5, u6, u7);
  cmul4(r8, r9, r10, r11, a, u2, u4, u6, W256, W256, W256, W256);
  put4<8>(r8, r9, r10, r11);
  cmul4(r12, r13, r14, r15, a, u4, r8, r10, W512, W512, W512, W512);
  put4<12>(r12, r13, r14, r15);
  v4u UA[2][4];
#pragma unroll
  for (int k = 0; k < 4; ++k) UA[0][k] = *(LAS v4u*)(sr + 64 * k);
  cmul4(r16, r17, W1024, v3, r12, r14, W512, W8, W512, W512, W512, W16);
  cmul4(r18, r19, r20, v1, r12, r14, r16, W8, W1024, W1024, W1024, W32);
  put4<0>(r16, r17, r18, r19);
  put1<4>(r20);
#pragma unroll
  for (int k = 0; k < 4; ++k) UA[1][k] = *(LAS v4u*)(sr + 64 * k);
  put4<0>(cf{1.f, 0.f}, W8, W16, v3);
  cf v0 = W32, v2, t0, t1, t2, t3;
  cmul4(v2, t3, t0, t1, W16, v3, v0, v1, W32, W32, W32, W32);
  put4<4>(v0, v1, v2, t3);
  cmul4(t2, v3, v0, v1, v2, t3, t0, t1, W32, W32, W32, W32);
  put4<8>(t0, t1, t2, v3);
  cmul4(v2, t3, t0, t1, t2, v3, v0, v1, W32, W32, W32, W32);
  put4<12>(v0, v1, v2, t3);
  v4u vb[4];
#pragma unroll
  for (int k = 0; k < 4; ++k) vb[k] = *(LAS v4u*)(sr + 64 * k);
  cmul4(t2, v3, v0, v1, v2, t3, t0, t1, W32, W32, W32, W32);
  put4<0>(t0, t1, t2, v3);
  cmul4(v2, t3, t0, t1, t2, v3, v0, v1, W32, W32, W32, W32);
  put4<4>(v0, v1, v2, t3);
#pragma unroll
  for (int k = 0; k < 4; ++k) {
    kacc[0][0] = mfma(UA[0][k], vb[k], kacc[0][0]);
    kacc[1][0] = mfma(UA[1][k], vb[k], kacc[1][0]);
  }
  cmul4(t2, v3, v0, v1, v2, t3, t0, t1, W32, W32, W32, W32);
  put4<8>(t0, t1, t2, v3);
  cmul4(v2, t3, t0, t1, t2, v3, v0, v1, W32, W32, W32, W32);
  put4<12>(v0, v1, v2, t3);
#pragma unroll
  for (int k = 0; k < 4; ++k) vb[k] = *(LAS v4u*)(sr + 64 * k);
  SCHED();
#pragma unroll
  for (int k = 0; k < 4; ++k) {
    kacc[0][1] = mfma(UA[0][k], vb[k], kacc[0][1]);
    kacc[1][1] = mfma(UA[1][k], vb[k], kacc[1][1]);
  }
}
typedef _Float16 f16x8 __attribute__((ext_vector_type(8)));
typedef _Float16 f16x2 __attribute__((ext_vector_type(2)));
DI f32x4 mfmah(v4u a, v4u b, f32x4 c) {
  return __builtin_amdgcn_mfma_f32_16x16x32_f16(__builtin_bit_cast(f16x8, a), __builtin_bit_cast(f16x8, b), c, 0, 0, 0);
}
DI unsigned pkh(cf v) { f16x2 r; r.x = (_Float16)v.x; r.y = (_Float16)v.y; return __builtin_bit_cast(unsigned, r); }
DI cf csq(cf v) { return cf{__builtin_fmaf(v.x, v.x, -(v.y * v.y)), (v.x + v.x) * v.y}; }
#define HMUL(o, i, w) "v_pk_mul_f16 " o ", " i ", " w " op_sel:[1,1] op_sel_hi:[1,0] neg_lo:[0,1]\n\t"
#define HFMA(o, i, w) "v_pk_fma_f16 " o ", " i ", " w ", " o " op_sel_hi:[0,1,1]\n\t"
template <int R0, int R1, int R2, int R3>
DI void h4(unsigned& o0, unsigned& o1, unsigned& o2, unsigned& o3, unsigned i0, unsigned i1, unsigned i2, unsigned i3, unsigned w0, unsigned w1, unsigned w2, unsigned w3) {
  asm volatile(HMUL("%0", "%4", "%8") HMUL("%1", "%5", "%9") HMUL("%2", "%6", "%10") HMUL("%3", "%7", "%11")
               HFMA("%0", "%4", "%8") HFMA("%1", "%5", "%9") HFMA("%2", "%6", "%10") HFMA("%3", "%7", "%11")
               "ds_write_addtid_b32 %0 offset:%12\n\tds_write_addtid_b32 %1 offset:%13\n\tds_write_addtid_b32 %2 offset:%14\n\tds_write_addtid_b32 %3 offset:%15"
               : "=&v"(o0), "=&v"(o1), "=&v"(o2), "=&v"(o3)
               : "v"(i0), "v"(i1), "v"(i2), "v"(i3), "v"(w0), "v"(w1), "v"(w2), "v"(w3), "i"(R0 * 272), "i"(R1 * 272), "i"(R2 * 272), "i"(R3 * 272) : "memory");
}
template <int R> DI void hput(unsigned v) { asm volatile("ds_write_addtid_b32 %0 offset:%1" ::"v"(v), "i"(R * 272) : "memory"); }
template <int C> DI float dpp_max(float v) {
  return __builtin_fmaxf(v, __builtin_bit_cast(float, __builtin_amdgcn_update_dpp(0, __builtin_bit_cast(int, v), C, 0xf, 0xf, true)));
}
template <class F> DI float kgen_task(const unsigned (&pr)[7], bool dead, unsigned sw, LAS char* sr, f32x4 (&kacc)[2][2], F mid) {
  const float dt = rbf(__builtin_amdgcn_exp2f(bf2f(pr[6]) * 1.44269504088896341f));
  const float zr = dt * bf2f(pr[0]), zi = dt * bf2f(pr[1]);
  const float dbr = dt * bf2f(pr[2]), dbi = dt * bf2f(pr[3]), cr = bf2f(pr[4]), ci = bf2f(pr[5]);
  cf a = cf{cr * dbr - ci * dbi, -(cr * dbi + ci * dbr)};
  if (dead) a = cf{0.f, 0.f};
  const float e1 = zr * 1.44269504088896341f, th = zi * 0.15915494309189535f;
  const float tl = __builtin_fmaf(zi, 6.4206383e-9f, __builtin_fmaf(zi, 0.15915494309189535f, -th));
  const cf W1 = cexpk(e1, th, tl, 1.f, -1.f), W256 = cexpk(e1, th, tl, 256.f, -256.f), W8 = cexpk(e1, th, tl, 8.f, 8.f);
  if (__builtin_expect(__builtin_amdgcn_ballot_w64(!(zr <= 0.f)) != 0ull, 0)) {
    mid(1); mid(2);
    asm volatile("s_mov_b32 m0, %0" ::"s"(sw) : "memory");
#pragma unroll
    for (int i = 0; i < 4; ++i) kacc[i >> 1][i & 1] = f32x4{0.f, 0.f, 0.f, 0.f};
    kgen_slow(a, W1, W256, W8, sr, kacc);
    return 1.f;
  }
  float am = __builtin_fmaxf(__builtin_fabsf(a.x), __builtin_fabsf(a.y));
  am = dpp_max<0xB1>(am); am = dpp_max<0x4E>(am); am = dpp_max<0x141>(am); am = dpp_max<0x140>(am);
  const unsigned e0 = __builtin_amdgcn_readlane(__builtin_bit_cast(unsigned, am), 0), e16 = __builtin_amdgcn_readlane(__builtin_bit_cast(unsigned, am), 16);
  const unsigned e32 = __builtin_amdgcn_readlane(__builtin_bit_cast(unsigned, am), 32), e48 = __builtin_amdgcn_readlane(__builtin_bit_cast(unsigned, am), 48);
  unsigned ex = max(max(e0, e16), max(e32, e48)) >> 23;
  ex = min(max(ex, 2u), 252u);
  const float up = __builtin_bit_cast(float, ex << 23), down = __builtin_bit_cast(float, (254u - ex) << 23);
  const cf W2 = csq(W1), W512 = csq(W256), W16 = csq(W8), W4 = csq(W2), W1024 = csq(W512), W32 = csq(W16), W64 = csq(W32), W128 = csq(W64);
  const unsigned u0 = pkh(a * down), h1 = pkh(W1), h2 = pkh(W2), h4_ = pkh(W4), h256 = pkh(W256), h512 = pkh(W512), h1024 = pkh(W1024);
  unsigned u1, u2, u4, r8, u3, u5, u6, r12, u7, r9, r10, r14, r11, r13, r15, r16, r17, r18, r19, r20;
  asm volatile("s_mov_b32 m0, %0\n\ts_nop 0" ::"s"(sw) : "memory");
  hput<0>(u0);
  h4<1, 2, 4, 8>(u1, u2, u4, r8, u0, u0, u0, u0, h1, h2, h4_, h256);
  h4<3, 5, 6, 12>(u3, u5, u6, r12, u2, u4, u4, u0, h1, h1, h2, h512);
  h4<7, 9, 10, 14>(u7, r9, r10, r14, u6, r8, r8, r8, h1, h2, h4_, h512);
  h4<11, 13, 15, 15>(r11, r13, r16, r15, r10, r12, u0, r14, h2, h4_, h1024, h4_);
  v4u UA[2][4];
#pragma unroll
  for (int k = 0; k < 4; ++k) UA[0][k] = *(LAS v4u*)(sr + 64 * k);
  hput<0>(r16);
  h4<1, 2, 3, 4>(r17, r18, r19, r20, r8, r12, r14, r16, h1024, h1024, h1024, h1024);
#pragma unroll
  for (int k = 0; k < 4; ++k) UA[1][k] = *(LAS v4u*)(sr + 64 * k);
  mid(1);
  unsigned v[16], t0, t1, t2, t3;
  v[1] = pkh(W8); v[2] = pkh(W16); v[4] = pkh(W32); v[8] = pkh(W64);
  const unsigned h128 = pkh(W128);
  hput<0>(0x3c00u); hput<1>(v[1]); hput<2>(v[2]); hput<4>(v[4]); hput<8>(v[8]);
  h4<3, 5, 6, 9>(v[3], v[5], v[6], v[9], v[2], v[4], v[4], v[8], v[1], v[1], v[2], v[1]);
  h4<7, 10, 11, 12>(v[7], v[10], v[11], v[12], v[4], v[8], v[8], v[8], v[3], v[2], v[3], v[4]);
  h4<13, 14, 15, 15>(v[13], v[14], t0, v[15], v[8], v[8], v[1], v[8], v[5], v[6], h128, v[7]);
  v4u vb[4];
#pragma unroll
  for (int k = 0; k < 4; ++k) vb[k] = *(LAS v4u*)(sr + 64 * k);
  mid(2);
  asm volatile("s_mov_b32 m0, %0\n\ts_nop 0" ::"s"(sw) : "memory");
  hput<0>(h128); hput<1>(t0);
  h4<2, 3, 4, 5>(t0, t1, t2, t3, v[2], v[3], v[4], v[5], h128, h128, h128, h128);
#pragma unroll
  for (int k = 0; k < 4; ++k) {
    kacc[0][0] = mfmah(UA[0][k], vb[k], k ? kacc[0][0] : f32x4{0.f, 0.f, 0.f, 0.f});
    kacc[1][0] = mfmah(UA[1][k], vb[k], k ? kacc[1][0] : f32x4{0.f, 0.f, 0.f, 0.f});
  }
  h4<6, 7, 8, 9>(t0, t1, t2, t3, v[6], v[7], v[8], v[9], h128, h128, h128, h128);
  h4<10, 11, 12, 13>(t0, t1, t2, t3, v[10], v[11], v[12], v[13], h128, h128, h128, h128);
  h4<14, 15, 14, 15>(t0, t1, t2, t3, v[14], v[15], v[14], v[15], h128, h128, h128, h128);
#pragma unroll
  for (int k = 0; k < 4; ++k) vb[k] = *(LAS v4u*)(sr + 64 * k);
  SCHED();
#pragma unroll
  for (int k = 0; k < 4; ++k) {
    kacc[0][1] = mfmah(UA[0][k], vb[k], k ? kacc[0][1] : f32x4{0.f, 0.f, 0.f, 0.f});
    kacc[1][1] = mfmah(UA[1][k], vb[k], k ? kacc[1][1] : f32x4{0.f, 0.f, 0.f, 0.f});
  }
  return up;
}
DI void kgen_store(LAS float* pt, const f32x4 (&kacc)[2][2], float scale, int lane) {
  const int l15 = lane & 15, g = lane >> 4;
  const int sh = g < 2 ? 3 : 4 - g;
  const int a0 = (l15 << sh) + (g < 2 ? 4 * g : 128 * g);
  const int a1 = g < 3 ? a0 + 2 : a0 + 64;
#pragma unroll
  for (int nh = 0; nh < 2; ++nh) {
    *(LAS f32x2*)(pt + a0 + ((16 * nh) << sh)) = kacc[0][nh].xy;
    *(LAS f32x2*)(pt + a1 + ((16 * nh) << sh)) = kacc[0][nh].zw;
  }
  if (g == 0) {
#pragma unroll
    for (int nh = 0; nh < 2; ++nh)
#pragma unroll
      for (int r = 0; r < 4; ++r) pt[512 + 32 * r + 16 * nh + l15] = kacc[1][nh][r];
  }
  if (lane == 16) { *(LAS f32x4*)(pt + 640) = f32x4{kacc[1][0][0], 0.f, 0.f, 0.f}; pt[644] = scale; }
}

template <int PAR> DI void conv_unit_shared(LAS char* ka, LAS char* ub, f32x4 (&acc)[4]) {
  constexpr int T[4] = {PAR ? 1 : 0, PAR ? 2 : 3, PAR ? 5 : 4, PAR ? 6 : 7};
  v4u A[8], B[8];
#pragma unroll
  for (int i = 0; i <= T[3]; ++i) { A[i] = *(LAS v4u*)(ka - 512 * i); B[i] = *(LAS v4u*)(ub + 512 * i); }
  SCHED();
#pragma unroll
  for (int q = 0; q < 4; ++q)
#pragma unroll
    for (int ip = 0; ip <= T[q]; ++ip) acc[q] = mfma(A[T[q] - ip], B[ip], acc[q]);
}
DI float fadd(float a, float b) { float r; asm("v_add_f32 %0, %1, %2" : "=v"(r) : "v"(a), "v"(b)); return r; }
DI float ffma(float a, float b, float c) { float r; asm("v_fma_f32 %0, %1, %2, %3" : "=v"(r) : "v"(a), "v"(b), "v"(c)); return r; }
DI f32x4 add4(f32x4 a, f32x4 b) { return f32x4{fadd(a.x, b.x), fadd(a.y, b.y), fadd(a.z, b.z), fadd(a.w, b.w)}; }
DI f32x4 fma4(f32x4 a, float b, f32x4 c) { return f32x4{ffma(a.x, b, c.x), ffma(a.y, b, c.y), ffma(a.z, b, c.z), ffma(a.w, b, c.w)}; }
DI int rot12(int v) { return v >= 12 ? v - 12 : v; }
DI void w_loads(v4u (&WF)[5], const char* p, unsigned vo) {
  ld128c<0>(WF[0], vo, p); ld128c<1024>(WF[1], vo, p); ld128c<2048>(WF[2], vo, p); ld128c<3072>(WF[3], vo, p); ld128c<0>(WF[4], vo, p + 4096);
}
DI void a_loads(v4u (&AF)[4], const char* p, const unsigned (&vm)[4]) {
#pragma unroll
  for (int m = 0; m < 4; ++m) ld128c<0>(AF[m], vm[m], p);
}
template <int LIVE> DI void g2_step(f32x4 (&acc)[4][5], v4u (&WF)[5], v4u (&AF)[4]) {
#pragma unroll
  for (int m = 0; m < 4; ++m) PIN(AF[m]);
#pragma unroll
  for (int n = 0; n < 5; ++n) PIN(WF[n]);
#pragma unroll
  for (int m = 0; m < 4; ++m)
#pragma unroll
    for (int n = 0; n < 5; ++n) acc[m][n] = mfma(WF[n], AF[m], m >= LIVE ? f32x4{0.f, 0.f, 0.f, 0.f} : acc[m][n]);
  SCHED();
}
DI void make_kix(unsigned (&kix)[9], int ts) {
#pragma unroll
  for (int i = 0; i < 9; ++i) {
    const int lg = 2047 - 8 * ts - i;
    const int bits = (int)(pk2(__builtin_fmaxf((float)lg, 128.f), 0.f));
    const int ci = lg < bits - 0x4280 ? lg : bits - 0x4280;
    kix[i] = KTAB + 4 * ci;
    PIN(kix[i]);
  }
}
DI void put_k(LAS char* L, const unsigned (&kix)[9], int ts, int c, unsigned reg) {
  float kv[9];
#pragma unroll
  for (int i = 0; i < 9; ++i) kv[i] = *(LAS float*)(L + kix[i] + c * KP_STRIDE);
  LAS char* q = L + reg + CV_KOFF + ts * 16;
  *(LAS v4u*)(q + CV_KSTR) = v4u{pk2(kv[0], kv[1]), pk2(kv[2], kv[3]), pk2(kv[4], kv[5]), pk2(kv[6], kv[7])};
  *(LAS v4u*)q = v4u{pk2(kv[1], kv[2]), pk2(kv[3], kv[4]), pk2(kv[5], kv[6]), pk2(kv[7], kv[8])};
}
DI void put_u(LAS char* q, v4u u, bool first) {
  *(LAS v4u*)(q + 3 * CV_USTR + 384) = u;
  *(LAS v2u*)(q + 1 * CV_USTR + 392) = v2u{u.x, u.y}; *(LAS v2u*)(q + 1 * CV_USTR + 400) = v2u{u.z, u.w};
  *(LAS unsigned*)(q + 2 * CV_USTR + 388) = u.x; *(LAS v2u*)(q + 2 * CV_USTR + 392) = v2u{u.y, u.z}; *(LAS unsigned*)(q + 2 * CV_USTR + 400) = u.w;
  *(LAS unsigned*)(q + 396) = u.x; *(LAS v2u*)(q + 400) = v2u{u.y, u.z}; *(LAS unsigned*)(q + 408) = u.w;
  if (first) {
    *(LAS v2u*)(q + 384) = v2u{0u, 0u}; *(LAS unsigned*)(q + 392) = 0u;
    *(LAS v2u*)(q + CV_USTR + 384) = v2u{0u, 0u}; *(LAS unsigned*)(q + 2 * CV_USTR + 384) = 0u;
  }
}

__global__ __launch_bounds__(512, 2) void fused_s4_mlp(const char* __restrict__ x, const char* __restrict__ pAre, const char* __restrict__ pAim,
    const char* __restrict__ pBre, const char* __restrict__ pBim, const char* __restrict__ pCre, const char* __restrict__ pCim,
    const char* __restrict__ plogdt, const char* __restrict__ pDskip, const char* __restrict__ w1, const char* __restrict__ b1,
    const char* __restrict__ w2, char* __restrict__ out, char* __restrict__ ws) {
  __shared__ __attribute__((aligned(16))) char smem[163840];
  LAS char* L = (LAS char*)smem;
  const unsigned lds0 = (unsigned)(size_t)L;
  const char* pc0; unsigned codelen;
  asm volatile("s_getpc_b64 %0\n98:\n\ts_mov_b32 %1, 97f-98b\n\ts_max_u32 %1, %1, 99f-98b" : "=s"(pc0), "=&s"(codelen) :: "scc");
  const int b = blockIdx.x, lane = threadIdx.x & 63;
  const int w = RFL(threadIdx.x >> 6), wi = w & 3;
  {
    const unsigned co = (unsigned)(4 * (16 * w + (lane & 15)) + ((b >> 3) & 3)) * 128;
    asm volatile("s_mov_b32 m0, %2\n\ts_nop 0\n\tglobal_load_lds_dword %0, %1" ::"v"((co < codelen ? co : codelen) & ~3u), "s"(pc0), "s"(lds0 + YST + w * 256) : "memory");
  }
  const int xcd = b & 7, j = b >> 3, rg = xcd >> 1, chf = xcd & 1;
  const int ct1 = 8 * chf + (j & 7), rb1 = 4 * rg + (j >> 3);
  const int wid = rb1 * 16 + ct1;
  const int a2 = j >> 2, cb2 = 4 * chf + (j & 3);
  const int l15 = lane & 15, g = lane >> 4, qq = (lane >> 2) & 3, p4 = lane & 3, l31 = lane & 31, h = lane >> 5;
  const int par = j & 1, cbase = 80 * xcd + 5 * (j >> 1);
  const int chA = cbase + 3 * par, chS = cbase + 2;
  const int ts = 64 * wi + lane;
  u64 stv0, stv1;
  ST(0);
  const int row2 = 512 * rg + 64 * a2 + 16 * wi;
  const char* w2b = ws + WS_W2T + (size_t)(cb2 * 48 + 12 * wi) * 5120;
  const char* actb = ws + WS_ACT + (size_t)((32 * rg + 4 * a2) * 48 + 12 * wi) * 1024;
  const int r2 = (3 * (j & 3) + (3 * a2) / 2 + (w < 4 ? 0 : 6)) % 12;
#define WL(s) w_loads(WF[s], w2b + rot12(r2 + (s)) * 5120, vo)
#define AL(s) a_loads(AF[s], actb + rot12(r2 + (s)) * 1024, vm)
#define LATE_LANE() int ln_ = lane; PIN(ln_); const int c15 = ln_ & 15, cg = ln_ >> 4
#define KAO (CV_KOFF + (c15 & 1) * CV_KSTR + 2 * (2072 - 8 * (c15 >> 1) + 8 * cg))
#define UBO ((c15 & 3) * CV_USTR + 2 * (64 * (c15 >> 2) + 8 * cg + 224))
#define YOFF ((unsigned)((c15 >> 2) * 128 + cg * 32 + (c15 & 3) * 8))
#define OROW ((unsigned)(2 * (c15 >> 2) + (c15 & 1) + 8 * ((c15 >> 1) & 1)))
#define ZOFF (lane < 24 ? wi * CV_USTR + lane * 16 : CV_KOFF + wi * CV_KSTR + 4096 + (lane - 24) * 16)
#define ZCOND (lane < 24 || (lane < 31 && wi < 2))

  unsigned pr[7];
  {
    const int kc = w < 3 ? chA : (w < 6 ? chA + 1 : chS);
    const int m0 = w < 6 ? (w < 3 ? w : w - 3) * 64 : par * 96 + (w - 6) * 48;
    const unsigned vo = (unsigned)(kc * 192 + m0 + ((w >= 6 && lane >= 48) ? 47 : lane)) * 2;
    ld16(pr[6], (unsigned)kc * 2, plogdt);
    ld16(pr[0], vo, pAre); ld16(pr[1], vo, pAim); ld16(pr[2], vo, pBre); ld16(pr[3], vo, pBim); ld16(pr[4], vo, pCre); ld16(pr[5], vo, pCim);
  }
  unsigned voA[4], voB[3];
#pragma unroll
  for (int i = 0; i < 4; ++i) voA[i] = (8 * i + (lane >> 3)) * 1280 + (((lane & 7) ^ (4 * (i & 1) + (lane >> 4))) * 16) - 1024 * i;
#pragma unroll
  for (int i = 0; i < 3; ++i) { const int c = 64 * i + lane; voB[i] = (c / 12) * 3072 + (c % 12) * 16 - 1024 * i; }
  const char* g1a = x + (size_t)(rb1 * 128 + 32 * wi) * 1280;
  const char* g1b = w1 + ct1 * 192 + wi * 49152;
  const unsigned la = lds0 + G1_RING + wi * 4096, lb = lds0 + G1_RING + G1_BOFF + wi * 3072;
  const int r1 = ((j & 7) + (5 * (j >> 3)) / 2 + 5 * chf + rg) % 10;
  auto slab = [&](int s) { const int v = s + r1; return v >= 10 ? v - 10 : v; };
  auto issue = [&](int s) {
    const char* pa = g1a + slab(s) * 128; const char* pb = g1b + (size_t)slab(s) * 64 * 3072;
    dma16(voA[0], pa, la + s * G1_STAGE); dma16o<1024>(voA[1], pa); dma16o<2048>(voA[2], pa); dma16o<3072>(voA[3], pa);
    dma16(voB[0], pb, lb + s * G1_STAGE); dma16o<1024>(voB[1], pb); dma16o<2048>(voB[2], pb);
  };
  unsigned tlo = 0, thi = 0;
  unsigned kix[9];
  if (w < 4) {
    bar();
    issue(0);
#pragma unroll
    for (int i = 0; i < 9; ++i) kix[i] = 0;
    waitv<7>();
  } else {
    __builtin_amdgcn_s_setprio(SPRIO);
    v2u E; v4u xa, xb; v2u xc;
    const int pu = b % 240, pks = pu / 5, pcg = pu % 5;
    const char* xrow = x + (size_t)(8 * wid + 2 * wi) * 1280;
    ld128(xa, h * 1280 + l31 * 16, xrow); ld128(xb, h * 1280 + 512 + l31 * 16, xrow); ld64(xc, h * 1280 + 1024 + l31 * 8, xrow);
    ld64c(E, 0u, ws);
#pragma unroll
    for (int i = 0; i < 2; ++i)
      dma16((unsigned)((lane >> 1) * 1280 + (lane & 1) * 16), w2 + (size_t)pks * 32 * 1280 + (128 * pcg + 32 * wi + 16 * i) * 2, lds0 + W2P + wi * 2048 + i * 1024);
    if (w == 4) *(LAS unsigned*)(L + lane * 4) = 0u;
    bar();
    make_kix(kix, ts);
    waitv<0>();
    PIN(E); PIN(xa); PIN(xb); PIN(xc);
    ST(6);
  const u64 tag64 = (((u64)RFL(E.y) << 32) | (unsigned)RFL(E.x)) + 1ull;
  tlo = (unsigned)tag64; thi = (unsigned)(tag64 >> 32);
  if (wi == 0) { lds_set(L, W_TAG, tlo); lds_set(L, W_TAG + 4, thi); lds_set(L, W_TAG + 8, 1); }
  {
    f32x2 v[10] = {unpk(xa.x), unpk(xa.y), unpk(xa.z), unpk(xa.w), unpk(xb.x), unpk(xb.y), unpk(xb.z), unpk(xb.w), unpk(xc.x), unpk(xc.y)};
    f32x2 s2 = ((v[0] + v[1]) + (v[2] + v[3])) + ((v[4] + v[5]) + (v[6] + v[7])) + (v[8] + v[9]);
    const float mean = half_sum(s2.x + s2.y, h) * (1.0f / 640.0f);
    f32x2 q2 = f32x2{0.f, 0.f};
#pragma unroll
    for (int i = 0; i < 10; ++i) { const f32x2 d = v[i] - mean; q2 = __builtin_elementwise_fma(d, d, q2); }
    const float var = rbf(half_sum(q2.x + q2.y, h) * (1.0f / 640.0f));
    const float m = rbf(mean), sd = rbf(__builtin_amdgcn_sqrtf(rbf(var + 1e-5f))), rs = __builtin_amdgcn_rcpf(sd);
    unsigned o[10];
#pragma unroll
    for (int i = 0; i < 10; ++i) { const f32x2 t = (v[i] - m) * rs; o[i] = pk2(t.x, t.y); }
    LAS char* rp = L + LN_ROWS + (2 * wi + h) * LN_PITCH;
    *(LAS v4u*)(rp + l31 * 16) = v4u{o[0], o[1], o[2], o[3]};
    *(LAS v4u*)(rp + 512 + l31 * 16) = v4u{o[4], o[5], o[6], o[7]};
    *(LAS v2u*)(rp + 1024 + l31 * 8) = v2u{o[8], o[9]};
    lds_arrive(L, W_SB, lane);
    if (l31 == 0) st64c(WS_STATS + (unsigned)(8 * wid + 2 * wi + h) * 8, ws,
                        v2u{(__builtin_bit_cast(unsigned, m) & 0xffff0000u) | (__builtin_bit_cast(unsigned, sd) >> 16), tlo});
  }
#pragma unroll
  for (int i = 0; i < 2; ++i) {
    LAS char* sp = L + W2P + wi * 2048 + i * 1024 + (16 * (g >> 1) + 4 * (g & 1) + qq) * 32 + 8 * p4;
    const v2u lo = trread(sp), hi = trread(sp + 256);
    const int ntg = 8 * pcg + 2 * wi + i;
    if (b < 240) st128c((unsigned)lane * 16, ws + WS_W2T + (size_t)(((ntg / 5) * 48 + pks) * 5 + ntg % 5) * 1024, v4u{lo.x, lo.y, hi.x, hi.y});
  }
  lds_wait_ge(L, W_SB, 4);
  {
    LAS char* sp = L + LN_ROWS + qq * LN_PITCH + (160 * wi + 16 * g) * 2 + 8 * p4;
#pragma unroll
    for (int i = 0; i < 3; ++i) {
      const v2u lo = trread(sp + 128 * i), hi = trread(sp + 128 * i + 4 * LN_PITCH);
      const unsigned ch = 64 * i + lane;
      if (i < 2 || lane < 32) st128c((ch / 5) * 20480 + (ch % 5) * 16, ws + WS_XNB + (size_t)wi * 32 * 20480 + wid * 80, v4u{lo.x, lo.y, hi.x, hi.y});
    }
  }
  }
  asm volatile("; AUDITCLR");
#pragma unroll
  for (int i = 0; i < 7; ++i) PIN(pr[i]);
  ST(1);
  {
    f32x4 kacc[2][2];
    const float scale = kgen_task(pr, w >= 6 && lane >= 48, lds0 + KG_SCR + w * KG_WSTR, L + KG_SCR + w * KG_WSTR + l15 * 272 + g * 16, kacc, [&](int i) {
      if (i == 2) {
        if (w < 4) issue(1);
        else { waitv<0>(); arrive_flag8(L, W_F1C, ws, WS_F1 + (unsigned)(lane & 7) * 2048 + wid * 8, tlo, thi); }
      }
    });
    kgen_store((LAS float*)(L + KG_SCR + w * KG_WSTR), kacc, scale, lane);
    lds_arrive(L, W_KG, lane);
  }
  ST(2);
  auto mish1 = [&](f32x4 a0, f32x4 a1, int n, int r, float rs, float mrs, unsigned ao, unsigned co) {
    unsigned pk[4];
#pragma unroll
    for (int c = 0; c < 2; ++c) {
      LAS char* cp = L + co + (32 * n + 16 * r + 8 * c) * 4;
      const f32x4 cs = *(LAS f32x4*)cp, bv = *(LAS f32x4*)(cp + 384), av = c ? a1 : a0;
#pragma unroll
      for (int e = 0; e < 2; ++e) {
        const f32x2 h2 = __builtin_elementwise_fma(e ? av.zw : av.xy, f32x2{rs, rs},
                                                   __builtin_elementwise_fma(f32x2{mrs, mrs}, e ? cs.zw : cs.xy, e ? bv.zw : bv.xy));
        const f32x2 s1 = f32x2{__builtin_amdgcn_exp2f(h2.x), __builtin_amdgcn_exp2f(h2.y)} + 1.0f;
        const f32x2 d = __builtin_elementwise_fma(s1, s1, f32x2{1.f, 1.f});
        const f32x2 rr = f32x2{__builtin_amdgcn_rcpf(d.x), __builtin_amdgcn_rcpf(d.y)};
        const f32x2 o = h2 * __builtin_elementwise_fma(rr, f32x2{-1.38629436111989062f, -1.38629436111989062f},
                                                       f32x2{0.69314718055994531f, 0.69314718055994531f});
        pk[2 * c + e] = pk2(o.x, o.y);
      }
    }
    st128c(ao, ws + WS_ACT + (size_t)((rb1 * 8 + 2 * wi) * 48 + 3 * ct1 + n) * 1024 + r * 512, v4u{pk[0], pk[1], pk[2], pk[3]});
  };
#define MISH_SETUP() const float rs = __builtin_amdgcn_rcpf(bf2f(gran.x)) * 1.44269504088896341f, mrs = -bfhi(gran.x) * rs; \
  const unsigned ao = (unsigned)((l31 >> 4) * 49152 + h * 256 + (4 * ((l15 & 7) >> 1) + (l15 & 1) + 2 * (l15 >> 3)) * 16), co = CSF + wi * 768 + h * 16

  if (w < 4) {
    issue(2);
    int lane = __builtin_amdgcn_mbcnt_hi(~0u, __builtin_amdgcn_mbcnt_lo(~0u, 0u)); PIN(lane);
    const int l15 = lane & 15, g = lane >> 4, qq = (lane >> 2) & 3, p4 = lane & 3, l31 = lane & 31, h = lane >> 5;
    const unsigned vo = lane * 16;
    f32x16 acc1[3]; f32x4 csa[3];
#pragma unroll
    for (int n = 0; n < 3; ++n) {
      csa[n] = f32x4{0.f, 0.f, 0.f, 0.f};
#pragma unroll
      for (int i = 0; i < 16; ++i) acc1[n][i] = 0.f;
    }
    unsigned xo[4], wo[4];
#pragma unroll
    for (int k = 0; k < 4; ++k) {
      const int kr = (k + wi) & 3;
      xo[k] = G1_RING + (32 * wi + l31) * 128 + (((2 * kr + h) ^ ((l31 >> 1) & 7)) * 16);
      wo[k] = G1_RING + G1_BOFF + (16 * kr + 8 * h + qq) * 192 + 32 * (g & 1) + 8 * p4;
      PIN(xo[k]); PIN(wo[k]);
    }
    const unsigned sel1 = ((l15 ^ g) & 1) ? 0u : 0x3f803f80u;
    const v4u sel = v4u{sel1, sel1, sel1, sel1};
    v4u X[4], W[4][3];
    auto rdw = [&](int s, int k, int n) {
      LAS char* p = L + wo[k] + (s % 3) * G1_STAGE + 64 * n; const v2u lo = trread(p), hi = trread(p + 768); W[k][n] = v4u{lo.x, lo.y, hi.x, hi.y};
    };
    auto rdx = [&](int s, int k) { X[k] = *(LAS v4u*)(L + xo[k] + (s % 3) * G1_STAGE); };
#define MM(k, n) acc1[n] = mfma32(W[k][n], X[k], acc1[n])
#define CS(n) csa[n] = mfma(W[0][n], sel, csa[n])
    v2u gran; unsigned bb;
    v4u WF[6][5], AF[6][4];
    const unsigned go = WS_STATS + (unsigned)(rb1 * 128 + 32 * wi + l31) * 8;
    waitv<14>();
    lds_set(L, W_RDY + wi * 4, 1); lds_wait_min<4>(L, W_RDY, 1, lane);
    ST(3);
    rdx(0, 0); rdw(0, 0, 0); rdw(0, 0, 1); rdw(0, 0, 2); rdx(0, 1); rdw(0, 1, 0); rdw(0, 1, 1); rdw(0, 1, 2);
#pragma unroll
    for (int s = 0; s < 10; ++s) {
      const bool refill = s < 7;
      const char* pa = g1a + slab(s + 3) * 128; const char* pb = g1b + (size_t)slab(s + 3) * 64 * 3072;
      SCHED();
      MM(0, 0); rdx(s, 2); rdw(s, 2, 0); SCHED();
      MM(0, 1); rdw(s, 2, 1); rdw(s, 2, 2); SCHED();
      MM(0, 2); rdx(s, 3); rdw(s, 3, 0); SCHED();
      CS(0); rdw(s, 3, 1); SCHED();
      CS(1); rdw(s, 3, 2); SCHED();
      CS(2);
      if (s < 8) waitv<7>(); else if (s == 8) waitv<2>();
      if (s == 9 && l15 < 2) {
#pragma unroll
        for (int n = 0; n < 3; ++n) *(LAS f32x4*)(L + CSP + wi * 384 + (32 * n + 16 * l15 + 4 * g) * 4) = csa[n];
      }
      lds_set(L, W_RDY + wi * 4, s + 2);
      SCHED();
      MM(1, 0);
      if (refill) { asm volatile("s_waitcnt lgkmcnt(7)" ::: "memory"); dma16(voA[0], pa, la + (s % 3) * G1_STAGE); }

      SCHED();
      MM(1, 1); if (refill) dma16o<1024>(voA[1], pa);
      unsigned fl = 0;
      if (s < 9) fl = lw(L, W_RDY + p4 * 4);
      SCHED();
      MM(1, 2); if (refill) dma16o<2048>(voA[2], pa); SCHED();
      MM(2, 0); if (refill) dma16o<3072>(voA[3], pa);
      SCHED();
      if (s == 9) { MM(2, 1); SCHED(); MM(2, 2); SCHED(); MM(3, 0); SCHED(); MM(3, 1); MM(3, 2); SCHED(); break; }
      if (__builtin_expect(__builtin_amdgcn_ballot_w64(fl < (unsigned)(s + 2)) != 0ull, 0)) lds_wait_min<4>(L, W_RDY, s + 2, lane);
      FENCE();
      SCHED();
      rdx(s + 1, 0); rdw(s + 1, 0, 0); SCHED();
      MM(2, 1); rdw(s + 1, 0, 1); rdw(s + 1, 0, 2); if (refill) dma16(voB[0], pb, lb + (s % 3) * G1_STAGE); SCHED();
      MM(2, 2); rdx(s + 1, 1); rdw(s + 1, 1, 0); if (refill) dma16o<1024>(voB[1], pb); SCHED();
      MM(3, 0); rdw(s + 1, 1, 1); if (refill) dma16o<2048>(voB[2], pb); SCHED();
      MM(3, 1); rdw(s + 1, 1, 2); SCHED();
      MM(3, 2);
      if (s == 7) { ld64c(gran, go, ws); ld32<0>(bb, (unsigned)(lane < 48 ? lane : 47) * 4, b1 + ct1 * 192); }
      SCHED();
      if (s == 0) ST(4); if (s == 3) ST(5); if (s == 6) ST(7);
    }
#undef MM
#undef CS
    ST(11);
    lds_wait_ge(L, W_TAG + 8, 1);
    const unsigned tlo = RFL(lw(L, W_TAG)), thi = RFL(lw(L, W_TAG + 4));
    waitv<0>(); PIN(gran); PIN(bb);
    gran = spin64<true>(gran, go, ws, tlo, thi);
    MISH_SETUP();
    lds_wait_min<4>(L, W_RDY, 11, lane);
    if (lane < 48) {
      LAS f32x2* p = (LAS f32x2*)(L + CSP + lane * 8);
      *(LAS f32x2*)(L + CSF + wi * 768 + lane * 8) = (p[0] + p[48]) + (p[96] + p[144]);
      *(LAS f32x2*)(L + CSF + wi * 768 + 384 + lane * 8) = unpk(bb) * 1.44269504088896341f;
    }
    FENCE();
    ST(12);
#pragma unroll
    for (int n = 0; n < 3; ++n) { mish1(acc1[n].s0123, acc1[n].s4567, n, 0, rs, mrs, ao, co); mish1(acc1[n].s89ab, acc1[n].scdef, n, 1, rs, mrs, ao, co); }
    lds_set(L, W_RDY + wi * 4, 12);
    ST(13);
    __builtin_amdgcn_s_setprio(2);
    LATE_LANE();
    v2u xq[5], dq[5], gr2, f3;
    const unsigned f2o = WS_F2 + (unsigned)((4 * rg + (a2 >> 1)) * 16 + 4 * wi + (ln_ & 3)) * 128;
    v2u f2;
    {
      f32x4 sacc[4];
#pragma unroll
      for (int i = 0; i < 4; ++i) sacc[i] = f32x4{0.f, 0.f, 0.f, 0.f};
      LAS char* ka = L + CVS + KAO - wi * 128; LAS char* ub = L + CVS + UBO - wi * 128;
      lds_wait_ge(L, W_F1ALL, 4);
      WL(0); WL(1);
      waitv<10>();
      arrive_flag<4>(L, W_F2C, lane, ws, WS_F2 + (unsigned)wid * 128, tlo, thi);
      ST(14);
      WL(2); WL(3); WL(4);
      lds_wait_ge(L, W_SB, 16);
      if (par) conv_unit_shared<1>(ka, ub, sacc); else conv_unit_shared<0>(ka, ub, sacc);
      {
        const char* xb2 = x + (size_t)row2 * 1280 + cb2 * 160; const unsigned xo2 = OROW * 1280 + cg * 8;
        ld64o<0>(xq[0], xo2, xb2); ld64o<32>(xq[1], xo2, xb2); ld64o<64>(xq[2], xo2, xb2); ld64o<96>(xq[3], xo2, xb2); ld64o<128>(xq[4], xo2, xb2);
        const char* db = pDskip + cb2 * 160;
        ld64o<0>(dq[0], cg * 8, db); ld64o<32>(dq[1], cg * 8, db); ld64o<64>(dq[2], cg * 8, db); ld64o<96>(dq[3], cg * 8, db); ld64o<128>(dq[4], cg * 8, db);
        ld64c(gr2, WS_STATS + (row2 + OROW) * 8, ws);
      }
      if (par) conv_unit_shared<1>(ka - 64, ub - 64, sacc); else conv_unit_shared<0>(ka - 64, ub - 64, sacc);
#pragma unroll
      for (int q = 0; q < 4; ++q) *(LAS f32x4*)(L + MXCH + (q * 4 + wi) * 1024 + vo) = sacc[q];
      lds_arrive(L, W_MB, lane);
    }
    ld64c(f2, f2o, ws);
    ST(15);
    unsigned vm[4];
#pragma unroll
    for (int m = 0; m < 4; ++m) vm[m] = vo + ((m + wi) & 3) * 49152;
    v2u ysh;
    {
      lds_wait_ge(L, W_MB, 4);
      LAS char* p = L + MXCH + wi * 4096 + vo;
      const f32x4 s = add4(add4(*(LAS f32x4*)p, *(LAS f32x4*)(p + 1024)), add4(*(LAS f32x4*)(p + 2048), *(LAS f32x4*)(p + 3072)));
      ysh = v2u{pk2(s.x, s.y), pk2(s.z, s.w)};
    }
    waitv<0>(); PIN(f2); PIN(gr2);
    f2 = spin64<false>(f2, f2o, ws, tlo, thi);
    lds_set(L, W_F2OK + wi * 4, 1);
    ST(16);
    st64c(YOFF, ws + WS_Y + (size_t)chS * 4096 + (2 * wi + ((wi & 1) ^ par)) * 512, ysh);
    AL(0); AL(1); AL(2);
    __builtin_amdgcn_s_setprio(0);
    f32x4 acc2[4][5];
    const float rs2 = __builtin_amdgcn_rcpf(bf2f(gr2.x)), m2 = -bfhi(gr2.x) * rs2;
#pragma unroll
    for (int n = 0; n < 5; ++n) {
      PIN(xq[n]); PIN(dq[n]);
      const f32x2 x0 = unpk(xq[n].x), x1 = unpk(xq[n].y);
      const f32x2 d0 = unpk(dq[n].x), d1 = unpk(dq[n].y);
      acc2[0][n] = f32x4{ffma(d0.x, ffma(x0.x, rs2, m2), x0.x), ffma(d0.y, ffma(x0.y, rs2, m2), x0.y), ffma(d1.x, ffma(x1.x, rs2, m2), x1.x), ffma(d1.y, ffma(x1.y, rs2, m2), x1.y)};
      PIN(acc2[0][n]);
    }
    ST(17);
    asm volatile("s_nop 1");
    waitv<8>();
    arrive_flag<8>(L, W_F3C, lane, ws, WS_F3 + (unsigned)(xcd * 32 + j) * 128, tlo, thi);
    g2_step<1>(acc2, WF[0], AF[0]);
    AL(3); AL(4);
    waitv<12>(); g2_step<4>(acc2, WF[1], AF[1]);
    WL(5); AL(5);
    waitv<17>(); g2_step<4>(acc2, WF[2], AF[2]);
    ST(20);
    waitv<13>(); g2_step<4>(acc2, WF[3], AF[3]);
    waitv<9>(); g2_step<4>(acc2, WF[4], AF[4]);
    lds_wait_ge(L, W_F3C, 8);
    const unsigned f3o = WS_F3 + (unsigned)(cb2 * 32 + 8 * wi + (ln_ & 7)) * 128;
    ld64c(f3, f3o, ws);
    waitv<1>(); g2_step<4>(acc2, WF[5], AF[5]);
    ST(22);
    waitv<0>(); PIN(f3);
    f3 = spin64<false>(f3, f3o, ws, tlo, thi);
    lds_arrive(L, W_F3ALL, lane);
    LAS char* fp = L + FM + wi * 20480 + vo;
#pragma unroll
    for (int n = 0; n < 5; ++n) *(LAS f32x4*)(fp + wi * 5120 + n * 1024) = acc2[0][n];
    lds_wait_ge(L, W_F3ALL, 4);
    ST(21);
    v4u yv[3];
    {
      const char* yb = ws + WS_Y + (size_t)cb2 * 80 * 4096 + row2 * 2;
      const unsigned yo = (unsigned)(ln_ >> 1) * 4096 + (ln_ & 1) * 16;
      ld128c<0>(yv[0], yo, yb); ld128c<0>(yv[1], yo, yb + 32 * 4096); ld128c<0>(yv[2], yo & 0xffffu, yb + 64 * 4096);
    }
#pragma unroll
    for (int m = 1; m < 4; ++m)
#pragma unroll
      for (int n = 0; n < 5; ++n) *(LAS f32x4*)(fp + ((m + wi) & 3) * 5120 + n * 1024) = acc2[m][n];
    lds_arrive(L, W_FM, lane);
    waitv<0>(); PIN(yv[0]); PIN(yv[1]); PIN(yv[2]);
    LAS char* ys = L + YST + wi * 2560;
    *(LAS v4u*)(ys + vo) = yv[0]; *(LAS v4u*)(ys + 1024 + vo) = yv[1];
    if (ln_ < 32) *(LAS v4u*)(ys + 2048 + vo) = yv[2];
    lds_set(L, W_F2OK + wi * 4, 2);
    STEND();
    asm volatile("97:");
    __builtin_amdgcn_endpgm();
  }

  const bool odd = wi & 1;
  const int kch = chA + (wi >> 1);
  const unsigned vo = lane * 16;
  v4u WF[6][5], AF[6][4];
  auto reduce = [&](int c, int i, bool three) {
    LAS char* pp = L + KG_SCR + 3 * c * KG_WSTR;
    f32x4 s = fma4(*(LAS f32x4*)(pp + i * 16), *(LAS float*)(pp + 2576), f32x4{0.f, 0.f, 0.f, 0.f});
    s = fma4(*(LAS f32x4*)(pp + KG_WSTR + i * 16), *(LAS float*)(pp + KG_WSTR + 2576), s);
    if (three) s = fma4(*(LAS f32x4*)(pp + 2 * KG_WSTR + i * 16), *(LAS float*)(pp + 2 * KG_WSTR + 2576), s);
    *(LAS f32x4*)(L + KTAB + c * KP_STRIDE + i * 16) = s;
    return s;
  };
  lds_wait_ge(L, W_KG, 8);
  if (ts < 161) { st128c((unsigned)ts * 16, ws + WS_KP + (size_t)wid * KP_STRIDE, __builtin_bit_cast(v4u, reduce(2, ts, false))); reduce(0, ts, true); }
  if (ts >= 95) reduce(1, ts - 95, true);
  if (ts < 3) *(LAS float*)(L + KTAB + ts * KP_STRIDE - 4) = 0.f;
  lds_arrive(L, W_SB, lane);
  waitv<0>();
  arrive_flag(L, W_FKC, lane, ws, WS_FK + (unsigned)wid * 64, tlo, thi);
  ST(9);
  __builtin_amdgcn_s_setprio(0);
  lds_wait_min<4>(L, W_RDY, F1STAGE, lane);
  const unsigned f1o = WS_F1 + (unsigned)xcd * 2048 + ts * 8;
  v2u f1own; ld64c(f1own, f1o, ws);
  waitv<0>(); PIN(f1own);
  f1own = spin64<false>(f1own, f1o, ws, tlo, thi);
  v4u uu[3]; unsigned ulo[3];
#pragma unroll
  for (int r = 0; r < 3; ++r) {
    const int e = 64 * r + lane, tb = 64 * wi + e / 3, c = e % 3;
    ld128c<0>(uu[r], tb * 80 + c * 16, ws + WS_XNB + (size_t)(16 * xcd + (j >> 1)) * 20480 + par * 32);
    ulo[r] = (c == 2 - 2 * par ? CVS : CVA + (c - par) * CV_CH) + tb * 16;
  }
  if (b == 0) arrive_flag(L, W_F1ALL, lane, ws, WS_E, tlo, thi);
  else lds_arrive(L, W_F1ALL, lane);
  ST(4);
  const unsigned fko = WS_FK + (unsigned)(wid ^ 1) * 64;
  v2u fk; ld64c(fk, fko, ws);
  waitv<0>(); PIN(fk); PIN(uu[0]); PIN(uu[1]); PIN(uu[2]);
  fk = spin64<false>(fk, fko, ws, tlo, thi);
  v4u kp; ld128c<0>(kp, (unsigned)(ts < 161 ? ts : 160) * 16, ws + WS_KP + (size_t)(wid ^ 1) * KP_STRIDE);
  lds_wait_ge(L, W_SB, 8);
  waitv<0>(); PIN(kp);
  if (ts < 161) { LAS f32x4* tp = (LAS f32x4*)(L + KTAB + 2 * KP_STRIDE + ts * 16); *tp = add4(*tp, __builtin_bit_cast(f32x4, kp)); }
  lds_arrive(L, W_SB, lane);
  lds_wait_ge(L, W_F1ALL, 4);
  lds_wait_min<4>(L, W_RDY, 9, lane);
  WL(0); WL(1); WL(2);
  ST(7);
  lds_wait_min<4>(L, W_RDY, 11, lane);
  ST(11);
  put_k(L, kix, ts, 0, CVA); put_k(L, kix, ts, 1, CVA + CV_CH);
  WL(3);
  put_u(L + ulo[0], uu[0], ts < 3); put_u(L + ulo[1], uu[1], false); put_u(L + ulo[2], uu[2], false);
  WL(4);
  if (ZCOND) {
    *(LAS v4u*)(L + CVA + ZOFF) = v4u{0u, 0u, 0u, 0u}; *(LAS v4u*)(L + CVA + CV_CH + ZOFF) = v4u{0u, 0u, 0u, 0u}; *(LAS v4u*)(L + CVS + ZOFF) = v4u{0u, 0u, 0u, 0u};
  }
  lds_wait_ge(L, W_SB, 12);
  put_k(L, kix, ts, 2, CVS);
  lds_arrive(L, W_SB, lane);
  ST(5);
  lds_wait_ge(L, W_SB, 16);
  lds_wait_ge(L, W_RDY + wi * 4, 12);
  ST(13);
  __builtin_amdgcn_s_setprio(SPRIO2);
  LATE_LANE();
  f32x4 cacc[8];
#pragma unroll
  for (int i = 0; i < 8; ++i) cacc[i] = f32x4{0.f, 0.f, 0.f, 0.f};
  {
    const unsigned o1 = CVA + (wi >> 1) * CV_CH - (odd ? 4 * 64 : 0);
    LAS char* ka = L + KAO + o1; LAS char* ub = L + UBO + o1;
    v4u A[8], B[8];
#define LA(i, u) (*(LAS v4u*)(ka - 512 * (i) - 64 * (u)))
#define LB(i, u) (*(LAS v4u*)(ub + 512 * (i) - 64 * (u)))
    A[7] = LA(7, 0); B[0] = LB(0, 0); A[0] = LA(0, 0); B[7] = LB(7, 0); A[6] = LA(6, 0); B[6] = LB(6, 0); A[1] = LA(1, 0); B[1] = LB(1, 0);
    A[5] = LA(5, 0); B[5] = LB(5, 0); A[2] = LA(2, 0); B[2] = LB(2, 0); A[4] = LA(4, 0); B[4] = LB(4, 0); A[3] = LA(3, 0); B[3] = LB(3, 0);
    SCHED();
#pragma unroll
    for (int u = 0; u < 4; ++u) {
      v4u An[2], Bn[2];
#pragma unroll
      for (int sd = 7; sd >= 0; --sd) {
#pragma unroll
        for (int o = 0; o <= sd; ++o) {
          if (sd + o < 8) cacc[sd + o] = mfma(A[sd], B[o], cacc[sd + o]);
          if (o != sd && sd + o < 8) cacc[sd + o] = mfma(A[o], B[sd], cacc[sd + o]);
        }
        SCHED();
        if (u < 3) {
          if (sd >= 2) { A[sd] = LA(sd, u + 1); B[sd] = LB(sd, u + 1); }
          if (sd == 6 || sd == 5) { An[6 - sd] = LA(6 - sd, u + 1); Bn[6 - sd] = LB(6 - sd, u + 1); }
        }
        SCHED();
      }
      if (u < 3) { A[0] = An[0]; A[1] = An[1]; B[0] = Bn[0]; B[1] = Bn[1]; }
    }
#undef LA
#undef LB
  }
  ST(10);
  unsigned vm[4];
  {
    LAS char* px = L + SXCH + wi * 4096 + vo;
    if (odd) {
#pragma unroll
      for (int i = 0; i < 4; ++i) { *(LAS f32x4*)(px + i * 1024) = cacc[i]; cacc[i] = cacc[4 + i]; }
    } else {
#pragma unroll
      for (int i = 0; i < 4; ++i) *(LAS f32x4*)(px + i * 1024) = cacc[4 + i];
    }
    lds_arrive(L, W_SX + (wi >> 1) * 4, lane);
#pragma unroll
    for (int m = 0; m < 4; ++m) vm[m] = vo + ((m + wi) & 3) * 49152;
    lds_wait_ge(L, W_SX + (wi >> 1) * 4, 2);
    px = L + SXCH + (wi ^ 1) * 4096 + vo;
    v2u yo[4];
#pragma unroll
    for (int i = 0; i < 4; ++i) { const f32x4 s = add4(cacc[i], *(LAS f32x4*)(px + i * 1024)); yo[i] = v2u{pk2(s.x, s.y), pk2(s.z, s.w)}; }
    char* yb = ws + WS_Y + (size_t)kch * 4096 + (odd ? 2048 : 0);
    st64co<0>(YOFF, yb, yo[0]); st64co<512>(YOFF, yb, yo[1]); st64co<1024>(YOFF, yb, yo[2]); st64co<1536>(YOFF, yb, yo[3]);
  }
  ST(14);
  lds_wait_ge(L, W_F2OK + wi * 4, 1);
  ST(16);
  AL(0); AL(1); AL(2);
  ST(17);
  f32x4 acc2[4][5];
  waitv<12>();
  arrive_flag<8>(L, W_F3C, lane, ws, WS_F3 + (unsigned)(xcd * 32 + j) * 128, tlo, thi);
  __builtin_amdgcn_s_setprio(0);
  waitv<8>();
  g2_step<0>(acc2, WF[0], AF[0]);
  AL(3); AL(4);
  waitv<12>(); g2_step<4>(acc2, WF[1], AF[1]);
  WL(5); AL(5);
  waitv<17>(); g2_step<4>(acc2, WF[2], AF[2]);
  ST(20);
  waitv<13>(); g2_step<4>(acc2, WF[3], AF[3]);
  waitv<9>(); g2_step<4>(acc2, WF[4], AF[4]);
  waitv<0>(); g2_step<4>(acc2, WF[5], AF[5]);
  ST(22);
  lds_wait_ge(L, W_F3C, 8);
#pragma unroll
  for (int m = 1; m < 4; ++m)
#pragma unroll
    for (int n = 0; n < 5; ++n) *(LAS f32x4*)(L + FS + wi * 15360 + (m - 1) * 5120 + n * 1024 + vo) = acc2[m][n];
  lds_arrive(L, W_FS, lane);
  ST(23);
  lds_wait_ge(L, W_FS, 4);
#pragma unroll
  for (int d = 1; d < 4; ++d)
#pragma unroll
    for (int n = 0; n < 5; ++n) acc2[0][n] += *(LAS f32x4*)(L + FS + ((wi - d) & 3) * 15360 + (d - 1) * 5120 + n * 1024 + vo);
  lds_wait_ge(L, W_FM, 4);
  ST(24);
  {
    char* ob = out + (size_t)row2 * 1280 + cb2 * 160; const unsigned oo = OROW * 1280 + cg * 8;
    v2u o[5];
#pragma unroll
    for (int n = 0; n < 5; ++n) {
      LAS char* p = L + FM + wi * 5120 + n * 1024 + vo;
      acc2[0][n] += (*(LAS f32x4*)p + *(LAS f32x4*)(p + 20480)) + (*(LAS f32x4*)(p + 40960) + *(LAS f32x4*)(p + 61440));
    }
    lds_wait_ge(L, W_F2OK + wi * 4, 2);
#pragma unroll
    for (int n = 0; n < 5; ++n) {
      const v2u t = trread(L + YST + wi * 2560 + n * 512 + ln_ * 8);
      const f32x2 s0 = acc2[0][n].xy + unpk(t.x), s1 = acc2[0][n].zw + unpk(t.y);
      o[n] = v2u{pk2(s0.x, s0.y), pk2(s1.x, s1.y)};
    }
    st64co<0>(oo, ob, o[0]); st64co<32>(oo, ob, o[1]); st64co<64>(oo, ob, o[2]); st64co<96>(oo, ob, o[3]); st64co<128>(oo, ob, o[4]);
  }
  STEND();
  asm volatile("99:");
}

extern "C" void kernel_launch(void* const* d_in, const int* in_sizes, int n_in, void* d_out, int out_size, void* d_ws, size_t ws_size,
                              hipStream_t stream) {
  fused_s4_mlp<<<256, 512, 0, stream>>>((const char*)d_in[0], (const char*)d_in[1], (const char*)d_in[2], (const char*)d_in[3],
      (const char*)d_in[4], (const char*)d_in[5], (const char*)d_in[6], (const char*)d_in[7], (const char*)d_in[8], (const char*)d_in[10],
      (const char*)d_in[11], (const char*)d_in[12], (char*)d_out, (char*)d_ws);
}
```

```cpp
#include <hip/hip_runtime.h>
#include <stdint.h>

typedef unsigned short u16;
typedef unsigned long long u64;
typedef unsigned v4u __attribute__((ext_vector_type(4)));
typedef unsigned v2u __attribute__((ext_vector_type(2)));
typedef float f32x4 __attribute__((ext_vector_type(4)));
typedef float f32x2 __attribute__((ext_vector_type(2)));
typedef short bf16x8 __attribute__((ext_vector_type(8)));
typedef short s16x4 __attribute__((ext_vector_type(4)));
typedef __bf16 bf16x2_t __attribute__((ext_vector_type(2)));
#define LAS __attribute__((address_space(3)))
#define GAS __attribute__((address_space(1)))
#define DI __device__ __forceinline__

constexpr unsigned WS_E = 0, WS_TMO = 64, WS_F1 = 131072, WS_STATS = 16384, WS_F2 = 32768, WS_F3 = 65536, WS_FK = 98304, WS_STAMP = 262144;
constexpr unsigned WS_XNB = 1u << 20, WS_W2T = 4u << 20, WS_Y = 7u << 20, WS_ACT = 10u << 20, WS_KP = 17u << 20;
constexpr unsigned KP_STRIDE = 2624;
constexpr unsigned SPIN_CAP = 400000;

constexpr unsigned W_F1C = 0, W_F2C = 4, W_F3C = 8, W_KG = 12, W_MB = 16, W_SB = 20, W_SX = 24, W_FM = 32, W_FS = 36, W_F3ALL = 40, W_F1ALL = 44,
                   W_F2OK = 48, W_RDY = 64, W_TAG = 80, W_FKC = 96;
constexpr unsigned KTAB = 272;
constexpr unsigned CSP = 8192;
constexpr unsigned CVA = 10240, CV_CH = 27008, CV_USTR = 4640, CV_KOFF = 18560, CV_KSTR = 4224;
constexpr unsigned KG_SCR = CVA, KG_WSTR = 4352, LN_ROWS = KG_SCR + 8 * KG_WSTR, LN_PITCH = 1344, W2P = LN_ROWS + 10752;
constexpr unsigned G1_RING = 64512, G1_STAGE = 28672, G1_BOFF = 16384;
constexpr unsigned CVS = G1_RING, SXCH = CVS + 27136, MXCH = SXCH + 16384;
constexpr unsigned CSF = G1_RING + 3 * G1_STAGE, YST = CSF + 3072;
constexpr unsigned FM = 256, FS = FM + 81920;
static_assert(FS + 61440 <= CSF && CVA + 2 * CV_CH <= G1_RING && W2P + 8192 <= G1_RING && YST + 10240 <= 163840 && MXCH + 16384 <= CSF, "lds map");

#define F1STAGE 6
#define SPRIO2 1
#define F1GAP 16
#define F2GAP 12
#define SPRIO 1
#define ST(i) do {} while (0)
#define STEND() do {} while (0)
#define STARG
#define STPASS

DI float bf2f(unsigned v) { return __builtin_bit_cast(float, v << 16); }
DI float bfhi(unsigned v) { return __builtin_bit_cast(float, v & 0xffff0000u); }
DI unsigned pk2(float lo, float hi) { bf16x2_t r; r.x = (__bf16)lo; r.y = (__bf16)hi; return __builtin_bit_cast(unsigned, r); }
DI float rbf(float f) { return bf2f(pk2(f, f) & 0xffffu); }
DI f32x4 mfma(v4u a, v4u b, f32x4 c) {
  return __builtin_amdgcn_mfma_f32_16x16x32_bf16(__builtin_bit_cast(bf16x8, a), __builtin_bit_cast(bf16x8, b), c, 0, 0, 0);
}
DI v2u trread(LAS char* p) { return __builtin_bit_cast(v2u, __builtin_amdgcn_ds_read_tr16_b64_v4i16((LAS s16x4*)p)); }
template <int N> DI void waitv() { asm volatile("s_waitcnt vmcnt(%0)" ::"i"(N) : "memory"); }
DI void bar() { asm volatile("s_waitcnt lgkmcnt(0)\n\ts_barrier" ::: "memory"); }
#define PIN(x) asm volatile("" : "+v"(x))
#define FENCE() asm volatile("" ::: "memory")
#define SCHED() __builtin_amdgcn_sched_barrier(0)
#define RFL(x) __builtin_amdgcn_readfirstlane(x)

DI void ld16(unsigned& d, unsigned vo, const void* sb) { asm volatile("global_load_ushort %0, %1, %2" : "=v"(d) : "v"(vo), "s"(sb) : "memory"); }
template <int O> DI void ld32(unsigned& d, unsigned vo, const void* sb) { asm volatile("global_load_dword %0, %1, %2 offset:%3" : "=v"(d) : "v"(vo), "s"(sb), "i"(O) : "memory"); }
DI void ld64(v2u& d, unsigned vo, const void* sb) { asm volatile("global_load_dwordx2 %0, %1, %2" : "=v"(d) : "v"(vo), "s"(sb) : "memory"); }
DI void ld128(v4u& d, unsigned vo, const void* sb) { asm volatile("global_load_dwordx4 %0, %1, %2" : "=v"(d) : "v"(vo), "s"(sb) : "memory"); }
DI void ld64c(v2u& d, unsigned vo, const void* sb) { asm volatile("global_load_dwordx2 %0, %1, %2 sc1" : "=v"(d) : "v"(vo), "s"(sb) : "memory"); }
template <int O> DI void ld128c(v4u& d, unsigned vo, const void* sb) { asm volatile("global_load_dwordx4 %0, %1, %2 offset:%3 sc1" : "=v"(d) : "v"(vo), "s"(sb), "i"(O) : "memory"); }
DI void st64c(unsigned vo, void* sb, v2u d) { asm volatile("global_store_dwordx2 %0, %1, %2 sc1" ::"v"(vo), "v"(d), "s"(sb) : "memory"); }
template <int O> DI void ld64o(v2u& d, unsigned vo, const void* sb) { asm volatile("global_load_dwordx2 %0, %1, %2 offset:%3" : "=v"(d) : "v"(vo), "s"(sb), "i"(O) : "memory"); }
template <int O> DI void st64o(unsigned vo, void* sb, v2u d) { asm volatile("global_store_dwordx2 %0, %1, %2 offset:%3" ::"v"(vo), "v"(d), "s"(sb), "i"(O) : "memory"); }
template <int O> DI void st64co(unsigned vo, void* sb, v2u d) { asm volatile("global_store_dwordx2 %0, %1, %2 offset:%3 sc1" ::"v"(vo), "v"(d), "s"(sb), "i"(O) : "memory"); }
DI void st128c(unsigned vo, void* sb, v4u d) { asm volatile("global_store_dwordx4 %0, %1, %2 sc1\n\ts_nop 1" ::"v"(vo), "v"(d), "s"(sb) : "memory"); }
DI void st128u(unsigned vo, void* sb, v4u d) { asm volatile("global_store_dwordx4 %0, %1, %2 sc1 ; CNT\n\ts_nop 1" ::"v"(vo), "v"(d), "s"(sb) : "memory"); }
DI void dma16(unsigned vo, const void* sb, unsigned lds) {
  asm volatile("s_mov_b32 m0, %2\n\ts_nop 0\n\tglobal_load_lds_dwordx4 %0, %1" ::"v"(vo), "s"(sb), "s"(lds) : "memory");
}
template <int O> DI void dma16o(unsigned vo, const void* sb) {
  asm volatile("global_load_lds_dwordx4 %0, %1 offset:%2" ::"v"(vo), "s"(sb), "i"(O) : "memory");
}
template <bool HIONLY> DI v2u spin64(v2u cur, unsigned vo, char* ws, unsigned tlo, unsigned thi) {
  if (__builtin_expect(__builtin_amdgcn_ballot_w64(HIONLY ? (cur.y != tlo) : (cur.x != tlo || cur.y != thi)) != 0ull, 0)) {
#pragma nounroll
    for (unsigned spins = 0; spins < SPIN_CAP; ++spins) {
      __builtin_amdgcn_s_sleep(6);
      asm volatile("global_load_dwordx2 %0, %1, %2 sc1\n\ts_waitcnt vmcnt(0)" : "=&v"(cur) : "v"(vo), "s"(ws) : "memory");
      if (__builtin_amdgcn_ballot_w64(HIONLY ? (cur.y != tlo) : (cur.x != tlo || cur.y != thi)) == 0ull) break;
    }
  }
  return cur;
}
DI unsigned lw(LAS char* L, unsigned off) { return *(volatile LAS unsigned*)(L + off); }
DI void lds_wait_at(unsigned a, unsigned target) {
  unsigned v, cnt; u64 t;
  asm volatile("s_mov_b32 %1, %5\n"
               "1:\n\tds_read_b32 %0, %3\n\ts_sub_u32 %1, %1, 1\n\ts_waitcnt lgkmcnt(0)\n\tv_cmp_gt_u32 vcc, %4, %0\n\t"
               "s_cselect_b64 %2, 0, vcc\n\ts_cmp_lg_u64 %2, 0\n\ts_cbranch_scc1 1b"
               : "=&v"(v), "=&s"(cnt), "=&s"(t) : "v"(a), "s"(target), "i"(4 * SPIN_CAP) : "vcc", "scc", "memory");
}
DI void lds_wait_ge(LAS char* L, unsigned off, unsigned target) { lds_wait_at((unsigned)(size_t)L + off, target); }
template <int N> DI void lds_wait_min(LAS char* L, unsigned off, unsigned target, int lane) {
  lds_wait_at((unsigned)(size_t)L + off + (lane & (N - 1)) * 4, target);
}
DI void lds_set(LAS char* L, unsigned off, unsigned v) { FENCE(); *(volatile LAS unsigned*)(L + off) = v; FENCE(); }
DI void lds_arrive(LAS char* L, unsigned off, int) {
  asm volatile("s_mov_b64 exec, 1\n\tds_add_u32 %0, %1\n\ts_mov_b64 exec, -1" ::"v"((unsigned)(size_t)L + off), "v"(1u) : "memory");
}
template <unsigned N = 4> DI void arrive_flag(LAS char* L, unsigned off, int, char* ws, unsigned fo, unsigned tlo, unsigned thi) {
  unsigned old;
  asm volatile("s_mov_b64 exec, 1\n\tds_add_rtn_u32 %0, %1, %2\n\ts_waitcnt lgkmcnt(0)\n\tv_cmp_eq_u32 vcc, %6, %0\n\t"
               "s_mov_b64 exec, vcc\n\tglobal_store_dwordx2 %3, %4, %5 sc1\n\ts_mov_b64 exec, -1"
               : "=&v"(old) : "v"((unsigned)(size_t)L + off), "v"(1u), "v"(fo), "v"(v2u{tlo, thi}), "s"(ws), "i"(N - 1) : "vcc", "memory");
}
DI void arrive_flag8(LAS char* L, unsigned off, char* ws, unsigned fo, unsigned tlo, unsigned thi) {
  unsigned old;
  asm volatile("s_mov_b64 exec, 1\n\tds_add_rtn_u32 %0, %1, %2\n\ts_waitcnt lgkmcnt(0)\n\tv_cmp_eq_u32 vcc, 3, %0\n\t"
               "s_mul_i32 exec_lo, vcc_lo, 0xff\n\tglobal_store_dwordx2 %3, %4, %5 sc1\n\ts_mov_b64 exec, -1"
               : "=&v"(old) : "v"((unsigned)(size_t)L + off), "v"(1u), "v"(fo), "v"(v2u{tlo, thi}), "s"(ws) : "vcc", "memory");
}
typedef float f32x16 __attribute__((ext_vector_type(16)));
DI f32x16 mfma32(v4u a, v4u b, f32x16 c) {
  return __builtin_amdgcn_mfma_f32_32x32x16_bf16(__builtin_bit_cast(bf16x8, a), __builtin_bit_cast(bf16x8, b), c, 0, 0, 0);
}
template <int C> DI float dpp_add(float v) {
  return v + __builtin_bit_cast(float, __builtin_amdgcn_update_dpp(0, __builtin_bit_cast(int, v), C, 0xf, 0xf, true));
}
DI float rl(float v, int l) { return __builtin_bit_cast(float, __builtin_amdgcn_readlane(__builtin_bit_cast(int, v), l)); }
DI float half_sum(float v, bool upper) {
  v = dpp_add<0xB1>(v); v = dpp_add<0x4E>(v); v = dpp_add<0x141>(v); v = dpp_add<0x140>(v);
  const float a = rl(v, 0) + rl(v, 16), c = rl(v, 32) + rl(v, 48);
  return upper ? c : a;
}
DI f32x2 unpk(unsigned v) { return f32x2{bf2f(v), bfhi(v)}; }
typedef f32x2 cf;
DI cf cmul1(cf i, cf w) { return cf{__builtin_fmaf(i.x, w.x, -(i.y * w.y)), __builtin_fmaf(i.x, w.y, i.y * w.x)}; }
DI void cmul4(cf& o0, cf& o1, cf& o2, cf& o3, cf i0, cf i1, cf i2, cf i3, cf w0, cf w1, cf w2, cf w3) {
  o0 = cmul1(i0, w0); o1 = cmul1(i1, w1); o2 = cmul1(i2, w2); o3 = cmul1(i3, w3);
}
DI unsigned pkc(cf v) { return pk2(v.x, v.y); }
template <int R> DI void put4(cf a, cf b, cf c, cf d) {
  asm volatile("ds_write_addtid_b32 %0 offset:%4\n\tds_write_addtid_b32 %1 offset:%5\n\tds_write_addtid_b32 %2 offset:%6\n\tds_write_addtid_b32 %3 offset:%7"
               ::"v"(pkc(a)), "v"(pkc(b)), "v"(pkc(c)), "v"(pkc(d)), "i"(R * 272), "i"(R * 272 + 272), "i"(R * 272 + 544), "i"(R * 272 + 816) : "memory");
}
template <int R> DI void put1(cf a) { asm volatile("ds_write_addtid_b32 %0 offset:%1" ::"v"(pkc(a)), "i"(R * 272) : "memory"); }
DI cf cexpk(float e1, float th, float tl, float k, float ks) {
  const float mag = __builtin_amdgcn_exp2f(e1 * k);
  const float fr = __builtin_fmaf(tl, ks, __builtin_amdgcn_fractf(th * ks));
  return cf{__builtin_amdgcn_cosf(fr), __builtin_amdgcn_sinf(fr)} * mag;
}
DI void kgen_slow(cf a, cf W1, cf W256, cf W8, LAS char* sr, f32x4 (&kacc)[2][2]) {
  cf W2, W512, u1, W16, u2, u3, W4, W32, u4, u5, u6, u7, r8, r9, r10, r11, r12, r13, r14, r15, r16, r17, W1024, v3, r18, r19, r20, v1;
  cmul4(W2, W512, u1, W16, W1, W256, a, W8, W1, W256, W1, W8);
  cmul4(u2, u3, W4, W32, a, u1, W2, W16, W2, W2, W2, W16);
  put4<0>(a, u1, u2, u3);
  cmul4(u4, u5, u6, u7, a, u1, u2, u3, W4, W4, W4, W4);
  put4<4>(u4, u5, u6, u7);
  cmul4(r8, r9, r10, r11, a, u2, u4, u6, W256, W256, W256, W256);
  put4<8>(r8, r9, r10, r11);
  cmul4(r12, r13, r14, r15, a, u4, r8, r10, W512, W512, W512, W512);
  put4<12>(r12, r13, r14, r15);
  v4u UA[2][4];
#pragma unroll
  for (int k = 0; k < 4; ++k) UA[0][k] = *(LAS v4u*)(sr + 64 * k);
  cmul4(r16, r17, W1024, v3, r12, r14, W512, W8, W512, W512, W512, W16);
  cmul4(r18, r19, r20, v1, r12, r14, r16, W8, W1024, W1024, W1024, W32);
  put4<0>(r16, r17, r18, r19);
  put1<4>(r20);
#pragma unroll
  for (int k = 0; k < 4; ++k) UA[1][k] = *(LAS v4u*)(sr + 64 * k);
  put4<0>(cf{1.f, 0.f}, W8, W16, v3);
  cf v0 = W32, v2, t0, t1, t2, t3;
  cmul4(v2, t3, t0, t1, W16, v3, v0, v1, W32, W32, W32, W32);
  put4<4>(v0, v1, v2, t3);
  cmul4(t2, v3, v0, v1, v2, t3, t0, t1, W32, W32, W32, W32);
  put4<8>(t0, t1, t2, v3);
  cmul4(v2, t3, t0, t1, t2, v3, v0, v1, W32, W32, W32, W32);
  put4<12>(v0, v1, v2, t3);
  v4u vb[4];
#pragma unroll
  for (int k = 0; k < 4; ++k) vb[k] = *(LAS v4u*)(sr + 64 * k);
  cmul4(t2, v3, v0, v1, v2, t3, t0, t1, W32, W32, W32, W32);
  put4<0>(t0, t1, t2, v3);
  cmul4(v2, t3, t0, t1, t2, v3, v0, v1, W32, W32, W32, W32);
  put4<4>(v0, v1, v2, t3);
#pragma unroll
  for (int k = 0; k < 4; ++k) {
    kacc[0][0] = mfma(UA[0][k], vb[k], kacc[0][0]);
    kacc[1][0] = mfma(UA[1][k], vb[k], kacc[1][0]);
  }
  cmul4(t2, v3, v0, v1, v2, t3, t0, t1, W32, W32, W32, W32);
  put4<8>(t0, t1, t2, v3);
  cmul4(v2, t3, t0, t1, t2, v3, v0, v1, W32, W32, W32, W32);
  put4<12>(v0, v1, v2, t3);
#pragma unroll
  for (int k = 0; k < 4; ++k) vb[k] = *(LAS v4u*)(sr + 64 * k);
  SCHED();
#pragma unroll
  for (int k = 0; k < 4; ++k) {
    kacc[0][1] = mfma(UA[0][k], vb[k], kacc[0][1]);
    kacc[1][1] = mfma(UA[1][k], vb[k], kacc[1][1]);
  }
}
typedef _Float16 f16x8 __attribute__((ext_vector_type(8)));
typedef _Float16 f16x2 __attribute__((ext_vector_type(2)));
DI f32x4 mfmah(v4u a, v4u b, f32x4 c) {
  return __builtin_amdgcn_mfma_f32_16x16x32_f16(__builtin_bit_cast(f16x8, a), __builtin_bit_cast(f16x8, b), c, 0, 0, 0);
}
DI unsigned pkh(cf v) { f16x2 r; r.x = (_Float16)v.x; r.y = (_Float16)v.y; return __builtin_bit_cast(unsigned, r); }
DI cf csq(cf v) { return cf{__builtin_fmaf(v.x, v.x, -(v.y * v.y)), (v.x + v.x) * v.y}; }
#define HMUL(o, i, w) "v_pk_mul_f16 " o ", " i ", " w " op_sel:[1,1] op_sel_hi:[1,0] neg_lo:[0,1]\n\t"
#define HFMA(o, i, w) "v_pk_fma_f16 " o ", " i ", " w ", " o " op_sel_hi:[0,1,1]\n\t"
template <int R0, int R1, int R2, int R3>
DI void h4(unsigned& o0, unsigned& o1, unsigned& o2, unsigned& o3, unsigned i0, unsigned i1, unsigned i2, unsigned i3, unsigned w0, unsigned w1, unsigned w2, unsigned w3) {
  asm volatile(HMUL("%0", "%4", "%8") HMUL("%1", "%5", "%9") HMUL("%2", "%6", "%10") HMUL("%3", "%7", "%11")
               HFMA("%0", "%4", "%8") HFMA("%1", "%5", "%9") HFMA("%2", "%6", "%10") HFMA("%3", "%7", "%11")
               "ds_write_addtid_b32 %0 offset:%12\n\tds_write_addtid_b32 %1 offset:%13\n\tds_write_addtid_b32 %2 offset:%14\n\tds_write_addtid_b32 %3 offset:%15"
               : "=&v"(o0), "=&v"(o1), "=&v"(o2), "=&v"(o3)
               : "v"(i0), "v"(i1), "v"(i2), "v"(i3), "v"(w0), "v"(w1), "v"(w2), "v"(w3), "i"(R0 * 272), "i"(R1 * 272), "i"(R2 * 272), "i"(R3 * 272) : "memory");
}
template <int R> DI void hput(unsigned v) { asm volatile("ds_write_addtid_b32 %0 offset:%1" ::"v"(v), "i"(R * 272) : "memory"); }
template <int C> DI float dpp_max(float v) {
  return __builtin_fmaxf(v, __builtin_bit_cast(float, __builtin_amdgcn_update_dpp(0, __builtin_bit_cast(int, v), C, 0xf, 0xf, true)));
}
template <class F> DI float kgen_task(const unsigned (&pr)[7], bool dead, unsigned sw, LAS char* sr, f32x4 (&kacc)[2][2], F mid) {
  const float dt = rbf(__builtin_amdgcn_exp2f(bf2f(pr[6]) * 1.44269504088896341f));
  const float zr = dt * bf2f(pr[0]), zi = dt * bf2f(pr[1]);
  const float dbr = dt * bf2f(pr[2]), dbi = dt * bf2f(pr[3]), cr = bf2f(pr[4]), ci = bf2f(pr[5]);
  cf a = cf{cr * dbr - ci * dbi, -(cr * dbi + ci * dbr)};
  if (dead) a = cf{0.f, 0.f};
  const float e1 = zr * 1.44269504088896341f, th = zi * 0.15915494309189535f;
  const float tl = __builtin_fmaf(zi, 6.4206383e-9f, __builtin_fmaf(zi, 0.15915494309189535f, -th));
  const cf W1 = cexpk(e1, th, tl, 1.f, -1.f), W256 = cexpk(e1, th, tl, 256.f, -256.f), W8 = cexpk(e1, th, tl, 8.f, 8.f);
  if (__builtin_expect(__builtin_amdgcn_ballot_w64(!(zr <= 0.f)) != 0ull, 0)) {
    mid(1); mid(2);
    asm volatile("s_mov_b32 m0, %0" ::"s"(sw) : "memory");
#pragma unroll
    for (int i = 0; i < 4; ++i) kacc[i >> 1][i & 1] = f32x4{0.f, 0.f, 0.f, 0.f};
    kgen_slow(a, W1, W256, W8, sr, kacc);
    return 1.f;
  }
  float am = __builtin_fmaxf(__builtin_fabsf(a.x), __builtin_fabsf(a.y));
  am = dpp_max<0xB1>(am); am = dpp_max<0x4E>(am); am = dpp_max<0x141>(am); am = dpp_max<0x140>(am);
  const unsigned e0 = __builtin_amdgcn_readlane(__builtin_bit_cast(unsigned, am), 0), e16 = __builtin_amdgcn_readlane(__builtin_bit_cast(unsigned, am), 16);
  const unsigned e32 = __builtin_amdgcn_readlane(__builtin_bit_cast(unsigned, am), 32), e48 = __builtin_amdgcn_readlane(__builtin_bit_cast(unsigned, am), 48);
  unsigned ex = max(max(e0, e16), max(e32, e48)) >> 23;
  ex = min(max(ex, 2u), 252u);
  const float up = __builtin_bit_cast(float, ex << 23), down = __builtin_bit_cast(float, (254u - ex) << 23);
  const cf W2 = csq(W1), W512 = csq(W256), W16 = csq(W8), W4 = csq(W2), W1024 = csq(W512), W32 = csq(W16), W64 = csq(W32), W128 = csq(W64);
  const unsigned u0 = pkh(a * down), h1 = pkh(W1), h2 = pkh(W2), h4_ = pkh(W4), h256 = pkh(W256), h512 = pkh(W512), h1024 = pkh(W1024);
  unsigned u1, u2, u4, r8, u3, u5, u6, r12, u7, r9, r10, r14, r11, r13, r15, r16, r17, r18, r19, r20;
  asm volatile("s_mov_b32 m0, %0\n\ts_nop 0" ::"s"(sw) : "memory");
  hput<0>(u0);
  h4<1, 2, 4, 8>(u1, u2, u4, r8, u0, u0, u0, u0, h1, h2, h4_, h256);
  h4<3, 5, 6, 12>(u3, u5, u6, r12, u2, u4, u4, u0, h1, h1, h2, h512);
  h4<7, 9, 10, 14>(u7, r9, r10, r14, u6, r8, r8, r8, h1, h2, h4_, h512);
  h4<11, 13, 15, 15>(r11, r13, r16, r15, r10, r12, u0, r14, h2, h4_, h1024, h4_);
  v4u UA[2][4];
#pragma unroll
  for (int k = 0; k < 4; ++k) UA[0][k] = *(LAS v4u*)(sr + 64 * k);
  hput<0>(r16);
  h4<1, 2, 3, 4>(r17, r18, r19, r20, r8, r12, r14, r16, h1024, h1024, h1024, h1024);
#pragma unroll
  for (int k = 0; k < 4; ++k) UA[1][k] = *(LAS v4u*)(sr + 64 * k);
  mid(1);
  unsigned v[16], t0, t1, t2, t3;
  v[1] = pkh(W8); v[2] = pkh(W16); v[4] = pkh(W32); v[8] = pkh(W64);
  const unsigned h128 = pkh(W128);
  hput<0>(0x3c00u); hput<1>(v[1]); hput<2>(v[2]); hput<4>(v[4]); hput<8>(v[8]);
  h4<3, 5, 6, 9>(v[3], v[5], v[6], v[9], v[2], v[4], v[4], v[8], v[1], v[1], v[2], v[1]);
  h4<7, 10, 11, 12>(v[7], v[10], v[11], v[12], v[4], v[8], v[8], v[8], v[3], v[2], v[3], v[4]);
  h4<13, 14, 15, 15>(v[13], v[14], t0, v[15], v[8], v[8], v[1], v[8], v[5], v[6], h128, v[7]);
  v4u vb[4];
#pragma unroll
  for (int k = 0; k < 4; ++k) vb[k] = *(LAS v4u*)(sr + 64 * k);
  mid(2);
  asm volatile("s_mov_b32 m0, %0\n\ts_nop 0" ::"s"(sw) : "memory");
  hput<0>(h128); hput<1>(t0);
  h4<2, 3, 4, 5>(t0, t1, t2, t3, v[2], v[3], v[4], v[5], h128, h128, h128, h128);
#pragma unroll
  for (int k = 0; k < 4; ++k) {
    kacc[0][0] = mfmah(UA[0][k], vb[k], k ? kacc[0][0] : f32x4{0.f, 0.f, 0.f, 0.f});
    kacc[1][0] = mfmah(UA[1][k], vb[k], k ? kacc[1][0] : f32x4{0.f, 0.f, 0.f, 0.f});
  }
  h4<6, 7, 8, 9>(t0, t1, t2, t3, v[6], v[7], v[8], v[9], h128, h128, h128, h128);
  h4<10, 11, 12, 13>(t0, t1, t2, t3, v[10], v[11], v[12], v[13], h128, h128, h128, h128);
  h4<14, 15, 14, 15>(t0, t1, t2, t3, v[14], v[15], v[14], v[15], h128, h128, h128, h128);
#pragma unroll
  for (int k = 0; k < 4; ++k) vb[k] = *(LAS v4u*)(sr + 64 * k);
  SCHED();
#pragma unroll
  for (int k = 0; k < 4; ++k) {
    kacc[0][1] = mfmah(UA[0][k], vb[k], k ? kacc[0][1] : f32x4{0.f, 0.f, 0.f, 0.f});
    kacc[1][1] = mfmah(UA[1][k], vb[k], k ? kacc[1][1] : f32x4{0.f, 0.f, 0.f, 0.f});
  }
  return up;
}
DI void kgen_store(LAS float* pt, const f32x4 (&kacc)[2][2], float scale, int lane) {
  const int l15 = lane & 15, g = lane >> 4;
  const int sh = g < 2 ? 3 : 4 - g;
  const int a0 = (l15 << sh) + (g < 2 ? 4 * g : 128 * g);
  const int a1 = g < 3 ? a0 + 2 : a0 + 64;
#pragma unroll
  for (int nh = 0; nh < 2; ++nh) {
    *(LAS f32x2*)(pt + a0 + ((16 * nh) << sh)) = kacc[0][nh].xy;
    *(LAS f32x2*)(pt + a1 + ((16 * nh) << sh)) = kacc[0][nh].zw;
  }
  if (g == 0) {
#pragma unroll
    for (int nh = 0; nh < 2; ++nh)
#pragma unroll
      for (int r = 0; r < 4; ++r) pt[512 + 32 * r + 16 * nh + l15] = kacc[1][nh][r];
  }
  if (lane == 16) { *(LAS f32x4*)(pt + 640) = f32x4{kacc[1][0][0], 0.f, 0.f, 0.f}; pt[644] = scale; }
}

template <int PAR> DI void conv_unit_shared(LAS char* ka, LAS char* ub, f32x4 (&acc)[4]) {
  constexpr int T[4] = {PAR ? 1 : 0, PAR ? 2 : 3, PAR ? 5 : 4, PAR ? 6 : 7};
  v4u A[8], B[8];
#pragma unroll
  for (int i = 0; i <= T[3]; ++i) { A[i] = *(LAS v4u*)(ka - 512 * i); B[i] = *(LAS v4u*)(ub + 512 * i); }
  SCHED();
#pragma unroll
  for (int q = 0; q < 4; ++q)
#pragma unroll
    for (int ip = 0; ip <= T[q]; ++ip) acc[q] = mfma(A[T[q] - ip], B[ip], acc[q]);
}
DI float fadd(float a, float b) { float r; asm("v_add_f32 %0, %1, %2" : "=v"(r) : "v"(a), "v"(b)); return r; }
DI float ffma(float a, float b, float c) { float r; asm("v_fma_f32 %0, %1, %2, %3" : "=v"(r) : "v"(a), "v"(b), "v"(c)); return r; }
DI f32x4 add4(f32x4 a, f32x4 b) { return f32x4{fadd(a.x, b.x), fadd(a.y, b.y), fadd(a.z, b.z), fadd(a.w, b.w)}; }
DI f32x4 fma4(f32x4 a, float b, f32x4 c) { return f32x4{ffma(a.x, b, c.x), ffma(a.y, b, c.y), ffma(a.z, b, c.z), ffma(a.w, b, c.w)}; }
DI int rot12(int v) { return v >= 12 ? v - 12 : v; }
DI void w_loads(v4u (&WF)[5], const char* p, unsigned vo) {
  ld128c<0>(WF[0], vo, p); ld128c<1024>(WF[1], vo, p); ld128c<2048>(WF[2], vo, p); ld128c<3072>(WF[3], vo, p); ld128c<0>(WF[4], vo, p + 4096);
}
DI void a_loads(v4u (&AF)[4], const char* p, const unsigned (&vm)[4]) {
#pragma unroll
  for (int m = 0; m < 4; ++m) ld128c<0>(AF[m], vm[m], p);
}
template <int LIVE> DI void g2_step(f32x4 (&acc)[4][5], v4u (&WF)[5], v4u (&AF)[4]) {
#pragma unroll
  for (int m = 0; m < 4; ++m) PIN(AF[m]);
#pragma unroll
  for (int n = 0; n < 5; ++n) PIN(WF[n]);
#pragma unroll
  for (int m = 0; m < 4; ++m)
#pragma unroll
    for (int n = 0; n < 5; ++n) acc[m][n] = mfma(WF[n], AF[m], m >= LIVE ? f32x4{0.f, 0.f, 0.f, 0.f} : acc[m][n]);
  SCHED();
}
DI void make_kix(unsigned (&kix)[9], int ts) {
#pragma unroll
  for (int i = 0; i < 9; ++i) {
    const int lg = 2047 - 8 * ts - i;
    const int bits = (int)(pk2(__builtin_fmaxf((float)lg, 128.f), 0.f));
    const int ci = lg < bits - 0x4280 ? lg : bits - 0x4280;
    kix[i] = KTAB + 4 * ci;
    PIN(kix[i]);
  }
}
DI void put_k(LAS char* L, const unsigned (&kix)[9], int ts, int c, unsigned reg) {
  float kv[9];
#pragma unroll
  for (int i = 0; i < 9; ++i) kv[i] = *(LAS float*)(L + kix[i] + c * KP_STRIDE);
  LAS char* q = L + reg + CV_KOFF + ts * 16;
  *(LAS v4u*)(q + CV_KSTR) = v4u{pk2(kv[0], kv[1]), pk2(kv[2], kv[3]), pk2(kv[4], kv[5]), pk2(kv[6], kv[7])};
  *(LAS v4u*)q = v4u{pk2(kv[1], kv[2]), pk2(kv[3], kv[4]), pk2(kv[5], kv[6]), pk2(kv[7], kv[8])};
}
DI void put_u(LAS char* q, v4u u, bool first) {
  *(LAS v4u*)(q + 3 * CV_USTR + 384) = u;
  *(LAS v2u*)(q + 1 * CV_USTR + 392) = v2u{u.x, u.y}; *(LAS v2u*)(q + 1 * CV_USTR + 400) = v2u{u.z, u.w};
  *(LAS unsigned*)(q + 2 * CV_USTR + 388) = u.x; *(LAS v2u*)(q + 2 * CV_USTR + 392) = v2u{u.y, u.z}; *(LAS unsigned*)(q + 2 * CV_USTR + 400) = u.w;
  *(LAS unsigned*)(q + 396) = u.x; *(LAS v2u*)(q + 400) = v2u{u.y, u.z}; *(LAS unsigned*)(q + 408) = u.w;
  if (first) {
    *(LAS v2u*)(q + 384) = v2u{0u, 0u}; *(LAS unsigned*)(q + 392) = 0u;
    *(LAS v2u*)(q + CV_USTR + 384) = v2u{0u, 0u}; *(LAS unsigned*)(q + 2 * CV_USTR + 384) = 0u;
  }
}

__global__ __launch_bounds__(512, 2) void fused_s4_mlp(const char* __restrict__ x, const char* __restrict__ pAre, const char* __restrict__ pAim,
    const char* __restrict__ pBre, const char* __restrict__ pBim, const char* __restrict__ pCre, const char* __restrict__ pCim,
    const char* __restrict__ plogdt, const char* __restrict__ pDskip, const char* __restrict__ w1, const char* __restrict__ b1,
    const char* __restrict__ w2, char* __restrict__ out, char* __restrict__ ws) {
  __shared__ __attribute__((aligned(16))) char smem[163840];
  LAS char* L = (LAS char*)smem;
  const unsigned lds0 = (unsigned)(size_t)L;
  const char* pc0; unsigned codelen;
  asm volatile("s_getpc_b64 %0\n98:\n\ts_mov_b32 %1, 97f-98b\n\ts_max_u32 %1, %1, 99f-98b" : "=s"(pc0), "=&s"(codelen) :: "scc");
  const int b = blockIdx.x, lane = threadIdx.x & 63;
  const int w = RFL(threadIdx.x >> 6), wi = w & 3;
  {
    const unsigned co = (unsigned)(4 * (16 * w + (lane & 15)) + ((b >> 3) & 3)) * 128;
    asm volatile("s_mov_b32 m0, %2\n\ts_nop 0\n\tglobal_load_lds_dword %0, %1" ::"v"((co < codelen ? co : codelen) & ~3u), "s"(pc0), "s"(lds0 + YST + w * 256) : "memory");
  }
  const int xcd = b & 7, j = b >> 3, rg = xcd >> 1, chf = xcd & 1;
  const int ct1 = 8 * chf + (j & 7), rb1 = 4 * rg + (j >> 3);
  const int wid = rb1 * 16 + ct1;
  const int a2 = j >> 2, cb2 = 4 * chf + (j & 3);
  const int l15 = lane & 15, g = lane >> 4, qq = (lane >> 2) & 3, p4 = lane & 3, l31 = lane & 31, h = lane >> 5;
  const int par = j & 1, cbase = 80 * xcd + 5 * (j >> 1);
  const int chA = cbase + 3 * par, chS = cbase + 2;
  const int ts = 64 * wi + lane;
  ST(0);
  const int row2 = 512 * rg + 64 * a2 + 16 * wi;
  const char* w2b = ws + WS_W2T + (size_t)(cb2 * 48 + 12 * wi) * 5120;
  const char* actb = ws + WS_ACT + (size_t)((32 * rg + 4 * a2) * 48 + 12 * wi) * 1024;
  const int r2 = (3 * (j & 3) + (3 * a2) / 2 + (w < 4 ? 0 : 6)) % 12;
#define WL(s) w_loads(WF[s], w2b + rot12(r2 + (s)) * 5120, vo)
#define AL(s) a_loads(AF[s], actb + rot12(r2 + (s)) * 1024, vm)
#define LATE_LANE() int ln_ = lane; PIN(ln_); const int c15 = ln_ & 15, cg = ln_ >> 4
#define KAO (CV_KOFF + (c15 & 1) * CV_KSTR + 2 * (2072 - 8 * (c15 >> 1) + 8 * cg))
#define UBO ((c15 & 3) * CV_USTR + 2 * (64 * (c15 >> 2) + 8 * cg + 224))
#define YOFF ((unsigned)((c15 >> 2) * 128 + cg * 32 + (c15 & 3) * 8))
#define OROW ((unsigned)(2 * (c15 >> 2) + (c15 & 1) + 8 * ((c15 >> 1) & 1)))
#define ZOFF (lane < 24 ? wi * CV_USTR + lane * 16 : CV_KOFF + wi * CV_KSTR + 4096 + (lane - 24) * 16)
#define ZCOND (lane < 24 || (lane < 31 && wi < 2))

  unsigned pr[7];
  {
    const int kc = w < 3 ? chA : (w < 6 ? chA + 1 : chS);
    const int m0 = w < 6 ? (w < 3 ? w : w - 3) * 64 : par * 96 + (w - 6) * 48;
    const unsigned vo = (unsigned)(kc * 192 + m0 + ((w >= 6 && lane >= 48) ? 47 : lane)) * 2;
    ld16(pr[6], (unsigned)kc * 2, plogdt);
    ld16(pr[0], vo, pAre); ld16(pr[1], vo, pAim); ld16(pr[2], vo, pBre); ld16(pr[3], vo, pBim); ld16(pr[4], vo, pCre); ld16(pr[5], vo, pCim);
  }
  unsigned voA[4], voB[3];
#pragma unroll
  for (int i = 0; i < 4; ++i) voA[i] = (8 * i + (lane >> 3)) * 1280 + (((lane & 7) ^ (4 * (i & 1) + (lane >> 4))) * 16) - 1024 * i;
#pragma unroll
  for (int i = 0; i < 3; ++i) { const int c = 64 * i + lane; voB[i] = (c / 12) * 3072 + (c % 12) * 16 - 1024 * i; }
  const char* g1a = x + (size_t)(rb1 * 128 + 32 * wi) * 1280;
  const char* g1b = w1 + ct1 * 192 + wi * 49152;
  const unsigned la = lds0 + G1_RING + wi * 4096, lb = lds0 + G1_RING + G1_BOFF + wi * 3072;
  const int r1 = ((j & 7) + (5 * (j >> 3)) / 2 + 5 * chf + rg) % 10;
  auto slab = [&](int s) { const int v = s + r1; return v >= 10 ? v - 10 : v; };
  auto issue = [&](int s) {
    const char* pa = g1a + slab(s) * 128; const char* pb = g1b + (size_t)slab(s) * 64 * 3072;
    dma16(voA[0], pa, la + s * G1_STAGE); dma16o<1024>(voA[1], pa); dma16o<2048>(voA[2], pa); dma16o<3072>(voA[3], pa);
    dma16(voB[0], pb, lb + s * G1_STAGE); dma16o<1024>(voB[1], pb); dma16o<2048>(voB[2], pb);
  };
  unsigned tlo = 0, thi = 0;
  unsigned kix[9];
  if (w < 4) {
    bar();
    issue(0);
#pragma unroll
    for (int i = 0; i < 9; ++i) kix[i] = 0;
    waitv<7>();
  } else {
    __builtin_amdgcn_s_setprio(SPRIO);
    v2u E; v4u xa, xb; v2u xc;
    const int pu = b % 240, pks = pu / 5, pcg = pu % 5;
    const char* xrow = x + (size_t)(8 * wid + 2 * wi) * 1280;
    ld128(xa, h * 1280 + l31 * 16, xrow); ld128(xb, h * 1280 + 512 + l31 * 16, xrow); ld64(xc, h * 1280 + 1024 + l31 * 8, xrow);
    ld64c(E, 0u, ws);
#pragma unroll
    for (int i = 0; i < 2; ++i)
      dma16((unsigned)((lane >> 1) * 1280 + (lane & 1) * 16), w2 + (size_t)pks * 32 * 1280 + (128 * pcg + 32 * wi + 16 * i) * 2, lds0 + W2P + wi * 2048 + i * 1024);
    if (w == 4) *(LAS unsigned*)(L + lane * 4) = 0u;
    bar();
    make_kix(kix, ts);
    waitv<0>();
    PIN(E); PIN(xa); PIN(xb); PIN(xc);
    ST(6);
  const u64 tag64 = (((u64)RFL(E.y) << 32) | (unsigned)RFL(E.x)) + 1ull;
  tlo = (unsigned)tag64; thi = (unsigned)(tag64 >> 32);
  if (wi == 0) { lds_set(L, W_TAG, tlo); lds_set(L, W_TAG + 4, thi); lds_set(L, W_TAG + 8, 1); }
  {
    f32x2 v[10] = {unpk(xa.x), unpk(xa.y), unpk(xa.z), unpk(xa.w), unpk(xb.x), unpk(xb.y), unpk(xb.z), unpk(xb.w), unpk(xc.x), unpk(xc.y)};
    f32x2 s2 = ((v[0] + v[1]) + (v[2] + v[3])) + ((v[4] + v[5]) + (v[6] + v[7])) + (v[8] + v[9]);
    const float mean = half_sum(s2.x + s2.y, h) * (1.0f / 640.0f);
    f32x2 q2 = f32x2{0.f, 0.f};
#pragma unroll
    for (int i = 0; i < 10; ++i) { const f32x2 d = v[i] - mean; q2 = __builtin_elementwise_fma(d, d, q2); }
    const float var = rbf(half_sum(q2.x + q2.y, h) * (1.0f / 640.0f));
    const float m = rbf(mean), sd = rbf(__builtin_amdgcn_sqrtf(rbf(var + 1e-5f))), rs = __builtin_amdgcn_rcpf(sd);
    unsigned o[10];
#pragma unroll
    for (int i = 0; i < 10; ++i) { const f32x2 t = (v[i] - m) * rs; o[i] = pk2(t.x, t.y); }
    LAS char* rp = L + LN_ROWS + (2 * wi + h) * LN_PITCH;
    *(LAS v4u*)(rp + l31 * 16) = v4u{o[0], o[1], o[2], o[3]};
    *(LAS v4u*)(rp + 512 + l31 * 16) = v4u{o[4], o[5], o[6], o[7]};
    *(LAS v2u*)(rp + 1024 + l31 * 8) = v2u{o[8], o[9]};
    lds_arrive(L, W_SB, lane);
    if (l31 == 0) st64c(WS_STATS + (unsigned)(8 * wid + 2 * wi + h) * 8, ws,
                        v2u{(__builtin_bit_cast(unsigned, m) & 0xffff0000u) | (__builtin_bit_cast(unsigned, sd) >> 16), tlo});
  }
#pragma unroll
  for (int i = 0; i < 2; ++i) {
    LAS char* sp = L + W2P + wi * 2048 + i * 1024 + (16 * (g >> 1) + 4 * (g & 1) + qq) * 32 + 8 * p4;
    const v2u lo = trread(sp), hi = trread(sp + 256);
    const int ntg = 8 * pcg + 2 * wi + i;
    if (b < 240) st128c((unsigned)lane * 16, ws + WS_W2T + (size_t)(((ntg / 5) * 48 + pks) * 5 + ntg % 5) * 1024, v4u{lo.x, lo.y, hi.x, hi.y});
  }
  lds_wait_ge(L, W_SB, 4);
  {
    LAS char* sp = L + LN_ROWS + qq * LN_PITCH + (160 * wi + 16 * g) * 2 + 8 * p4;
#pragma unroll
    for (int i = 0; i < 3; ++i) {
      const v2u lo = trread(sp + 128 * i), hi = trread(sp + 128 * i + 4 * LN_PITCH);
      const unsigned ch = 64 * i + lane;
      if (i < 2 || lane < 32) st128c((ch / 5) * 20480 + (ch % 5) * 16, ws + WS_XNB + (size_t)wi * 32 * 20480 + wid * 80, v4u{lo.x, lo.y, hi.x, hi.y});
    }
  }
  }
  asm volatile("; AUDITCLR");
#pragma unroll
  for (int i = 0; i < 7; ++i) PIN(pr[i]);
  ST(1);
  {
    f32x4 kacc[2][2];
    const float scale = kgen_task(pr, w >= 6 && lane >= 48, lds0 + KG_SCR + w * KG_WSTR, L + KG_SCR + w * KG_WSTR + l15 * 272 + g * 16, kacc, [&](int i) {
      if (i == 2) {
        if (w < 4) issue(1);
        else { waitv<0>(); arrive_flag8(L, W_F1C, ws, WS_F1 + (unsigned)(lane & 7) * 2048 + wid * 8, tlo, thi); }
      }
    });
    kgen_store((LAS float*)(L + KG_SCR + w * KG_WSTR), kacc, scale, lane);
    lds_arrive(L, W_KG, lane);
  }
  ST(2);
  auto mish1 = [&](f32x4 a0, f32x4 a1, int n, int r, float rs, float mrs, unsigned ao, unsigned co) {
    unsigned pk[4];
#pragma unroll
    for (int c = 0; c < 2; ++c) {
      LAS char* cp = L + co + (32 * n + 16 * r + 8 * c) * 4;
      const f32x4 cs = *(LAS f32x4*)cp, bv = *(LAS f32x4*)(cp + 384), av = c ? a1 : a0;
#pragma unroll
      for (int e = 0; e < 2; ++e) {
        const f32x2 h2 = __builtin_elementwise_fma(e ? av.zw : av.xy, f32x2{rs, rs},
                                                   __builtin_elementwise_fma(f32x2{mrs, mrs}, e ? cs.zw : cs.xy, e ? bv.zw : bv.xy));
        const f32x2 s1 = f32x2{__builtin_amdgcn_exp2f(h2.x), __builtin_amdgcn_exp2f(h2.y)} + 1.0f;
        const f32x2 d = __builtin_elementwise_fma(s1, s1, f32x2{1.f, 1.f});
        const f32x2 rr = f32x2{__builtin_amdgcn_rcpf(d.x), __builtin_amdgcn_rcpf(d.y)};
        const f32x2 o = h2 * __builtin_elementwise_fma(rr, f32x2{-1.38629436111989062f, -1.38629436111989062f},
                                                       f32x2{0.69314718055994531f, 0.69314718055994531f});
        pk[2 * c + e] = pk2(o.x, o.y);
      }
    }
    st128c(ao, ws + WS_ACT + (size_t)((rb1 * 8 + 2 * wi) * 48 + 3 * ct1 + n) * 1024 + r * 512, v4u{pk[0], pk[1], pk[2], pk[3]});
  };
#define MISH_SETUP() const float rs = __builtin_amdgcn_rcpf(bf2f(gran.x)) * 1.44269504088896341f, mrs = -bfhi(gran.x) * rs; \
  const unsigned ao = (unsigned)((l31 >> 4) * 49152 + h * 256 + (4 * ((l15 & 7) >> 1) + (l15 & 1) + 2 * (l15 >> 3)) * 16), co = CSF + wi * 768 + h * 16

  if (w < 4) {
    issue(2);
    int lane = __builtin_amdgcn_mbcnt_hi(~0u, __builtin_amdgcn_mbcnt_lo(~0u, 0u)); PIN(lane);
    const int l15 = lane & 15, g = lane >> 4, qq = (lane >> 2) & 3, p4 = lane & 3, l31 = lane & 31, h = lane >> 5;
    const unsigned vo = lane * 16;
    f32x16 acc1[3]; f32x4 csa[3];
#pragma unroll
    for (int n = 0; n < 3; ++n) {
      csa[n] = f32x4{0.f, 0.f, 0.f, 0.f};
#pragma unroll
      for (int i = 0; i < 16; ++i) acc1[n][i] = 0.f;
    }
    unsigned xo[4], wo[4];
#pragma unroll
    for (int k = 0; k < 4; ++k) {
      const int kr = (k + wi) & 3;
      xo[k] = G1_RING + (32 * wi + l31) * 128 + (((2 * kr + h) ^ ((l31 >> 1) & 7)) * 16);
      wo[k] = G1_RING + G1_BOFF + (16 * kr + 8 * h + qq) * 192 + 32 * (g & 1) + 8 * p4;
      PIN(xo[k]); PIN(wo[k]);
    }
    const unsigned sel1 = ((l15 ^ g) & 1) ? 0u : 0x3f803f80u;
    const v4u sel = v4u{sel1, sel1, sel1, sel1};
    v4u X[4], W[4][3];
    auto rdw = [&](int s, int k, int n) {
      LAS char* p = L + wo[k] + (s % 3) * G1_STAGE + 64 * n; const v2u lo = trread(p), hi = trread(p + 768); W[k][n] = v4u{lo.x, lo.y, hi.x, hi.y};
    };
    auto rdx = [&](int s, int k) { X[k] = *(LAS v4u*)(L + xo[k] + (s % 3) * G1_STAGE); };
#define MM(k, n) acc1[n] = mfma32(W[k][n], X[k], acc1[n])
#define CS(n) csa[n] = mfma(W[0][n], sel, csa[n])
    v2u gran; unsigned bb;
    v4u WF[6][5], AF[6][4];
    const unsigned go = WS_STATS + (unsigned)(rb1 * 128 + 32 * wi + l31) * 8;
    waitv<14>();
    lds_set(L, W_RDY + wi * 4, 1); lds_wait_min<4>(L, W_RDY, 1, lane);
    ST(3);
    rdx(0, 0); rdw(0, 0, 0); rdw(0, 0, 1); rdw(0, 0, 2); rdx(0, 1); rdw(0, 1, 0); rdw(0, 1, 1); rdw(0, 1, 2);
#pragma unroll
    for (int s = 0; s < 10; ++s) {
      const bool refill = s < 7;
      const char* pa = g1a + slab(s + 3) * 128; const char* pb = g1b + (size_t)slab(s + 3) * 64 * 3072;
      SCHED();
      MM(0, 0); rdx(s, 2); rdw(s, 2, 0); SCHED();
      MM(0, 1); rdw(s, 2, 1); rdw(s, 2, 2); SCHED();
      MM(0, 2); rdx(s, 3); rdw(s, 3, 0); SCHED();
      CS(0); rdw(s, 3, 1); SCHED();
      CS(1); rdw(s, 3, 2); SCHED();
      CS(2);
      if (s < 8) waitv<7>(); else if (s == 8) waitv<2>();
      if (s == 9 && l15 < 2) {
#pragma unroll
        for (int n = 0; n < 3; ++n) *(LAS f32x4*)(L + CSP + wi * 384 + (32 * n + 16 * l15 + 4 * g) * 4) = csa[n];
      }
      lds_set(L, W_RDY + wi * 4, s + 2);
      SCHED();
      MM(1, 0);
      if (refill) { asm volatile("s_waitcnt lgkmcnt(7)" ::: "memory"); dma16(voA[0], pa, la + (s % 3) * G1_STAGE); }

      SCHED();
      MM(1, 1); if (refill) dma16o<1024>(voA[1], pa);
      unsigned fl = 0;
      if (s < 9) fl = lw(L, W_RDY + p4 * 4);
      SCHED();
      MM(1, 2); if (refill) dma16o<2048>(voA[2], pa); SCHED();
      MM(2, 0); if (refill) dma16o<3072>(voA[3], pa);
      SCHED();
      if (s == 9) { MM(2, 1); SCHED(); MM(2, 2); SCHED(); MM(3, 0); SCHED(); MM(3, 1); MM(3, 2); SCHED(); break; }
      if (__builtin_expect(__builtin_amdgcn_ballot_w64(fl < (unsigned)(s + 2)) != 0ull, 0)) lds_wait_min<4>(L, W_RDY, s + 2, lane);
      FENCE();
      SCHED();
      rdx(s + 1, 0); rdw(s + 1, 0, 0); SCHED();
      MM(2, 1); rdw(s + 1, 0, 1); rdw(s + 1, 0, 2); if (refill) dma16(voB[0], pb, lb + (s % 3) * G1_STAGE); SCHED();
      MM(2, 2); rdx(s + 1, 1); rdw(s + 1, 1, 0); if (refill) dma16o<1024>(voB[1], pb); SCHED();
      MM(3, 0); rdw(s + 1, 1, 1); if (refill) dma16o<2048>(voB[2], pb); SCHED();
      MM(3, 1); rdw(s + 1, 1, 2); SCHED();
      MM(3, 2);
      if (s == 7) { ld64c(gran, go, ws); ld32<0>(bb, (unsigned)(lane < 48 ? lane : 47) * 4, b1 + ct1 * 192); }
      SCHED();
      if (s == 0) ST(4); if (s == 3) ST(5); if (s == 6) ST(7);
    }
#undef MM
#undef CS
    ST(11);
    lds_wait_ge(L, W_TAG + 8, 1);
    const unsigned tlo = RFL(lw(L, W_TAG)), thi = RFL(lw(L, W_TAG + 4));
    waitv<0>(); PIN(gran); PIN(bb);
    gran = spin64<true>(gran, go, ws, tlo, thi);
    MISH_SETUP();
    lds_wait_min<4>(L, W_RDY, 11, lane);
    if (lane < 48) {
      LAS f32x2* p = (LAS f32x2*)(L + CSP + lane * 8);
      *(LAS f32x2*)(L + CSF + wi * 768 + lane * 8) = (p[0] + p[48]) + (p[96] + p[144]);
      *(LAS f32x2*)(L + CSF + wi * 768 + 384 + lane * 8) = unpk(bb) * 1.44269504088896341f;
    }
    FENCE();
    ST(12);
#pragma unroll
    for (int n = 0; n < 3; ++n) { mish1(acc1[n].s0123, acc1[n].s4567, n, 0, rs, mrs, ao, co); mish1(acc1[n].s89ab, acc1[n].scdef, n, 1, rs, mrs, ao, co); }
    lds_set(L, W_RDY + wi * 4, 12);
    ST(13);
    __builtin_amdgcn_s_setprio(2);
    LATE_LANE();
    v2u xq[5], dq[5], gr2, f3;
    const unsigned f2o = WS_F2 + (unsigned)((4 * rg + (a2 >> 1)) * 16 + 4 * wi + (ln_ & 3)) * 128;
    v2u f2;
    {
      f32x4 sacc[4];
#pragma unroll
      for (int i = 0; i < 4; ++i) sacc[i] = f32x4{0.f, 0.f, 0.f, 0.f};
      LAS char* ka = L + CVS + KAO - wi * 128; LAS char* ub = L + CVS + UBO - wi * 128;
      lds_wait_ge(L, W_F1ALL, 4);
      WL(0); WL(1);
      waitv<10>();
      arrive_flag<4>(L, W_F2C, lane, ws, WS_F2 + (unsigned)wid * 128, tlo, thi);
      ST(14);
      WL(2); WL(3); WL(4);
      lds_wait_ge(L, W_SB, 16);
      if (par) conv_unit_shared<1>(ka, ub, sacc); else conv_unit_shared<0>(ka, ub, sacc);
      {
        const char* xb2 = x + (size_t)row2 * 1280 + cb2 * 160; const unsigned xo2 = OROW * 1280 + cg * 8;
        ld64o<0>(xq[0], xo2, xb2); ld64o<32>(xq[1], xo2, xb2); ld64o<64>(xq[2], xo2, xb2); ld64o<96>(xq[3], xo2, xb2); ld64o<128>(xq[4], xo2, xb2);
        const char* db = pDskip + cb2 * 160;
        ld64o<0>(dq[0], cg * 8, db); ld64o<32>(dq[1], cg * 8, db); ld64o<64>(dq[2], cg * 8, db); ld64o<96>(dq[3], cg * 8, db); ld64o<128>(dq[4], cg * 8, db);
        ld64c(gr2, WS_STATS + (row2 + OROW) * 8, ws);
      }
      if (par) conv_unit_shared<1>(ka - 64, ub - 64, sacc); else conv_unit_shared<0>(ka - 64, ub - 64, sacc);
#pragma unroll
      for (int q = 0; q < 4; ++q) *(LAS f32x4*)(L + MXCH + (q * 4 + wi) * 1024 + vo) = sacc[q];
      lds_arrive(L, W_MB, lane);
    }
    ld64c(f2, f2o, ws);
    ST(15);
    unsigned vm[4];
#pragma unroll
    for (int m = 0; m < 4; ++m) vm[m] = vo + ((m + wi) & 3) * 49152;
    v2u ysh;
    {
      lds_wait_ge(L, W_MB, 4);
      LAS char* p = L + MXCH + wi * 4096 + vo;
      const f32x4 s = add4(add4(*(LAS f32x4*)p, *(LAS f32x4*)(p + 1024)), add4(*(LAS f32x4*)(p + 2048), *(LAS f32x4*)(p + 3072)));
      ysh = v2u{pk2(s.x, s.y), pk2(s.z, s.w)};
    }
    waitv<0>(); PIN(f2); PIN(gr2);
    f2 = spin64<false>(f2, f2o, ws, tlo, thi);
    lds_set(L, W_F2OK + wi * 4, 1);
    ST(16);
    st64c(YOFF, ws + WS_Y + (size_t)chS * 4096 + (2 * wi + ((wi & 1) ^ par)) * 512, ysh);
    AL(0); AL(1); AL(2);
    __builtin_amdgcn_s_setprio(0);
    f32x4 acc2[4][5];
    const float rs2 = __builtin_amdgcn_rcpf(bf2f(gr2.x)), m2 = -bfhi(gr2.x) * rs2;
#pragma unroll
    for (int n = 0; n < 5; ++n) {
      PIN(xq[n]); PIN(dq[n]);
      const f32x2 x0 = unpk(xq[n].x), x1 = unpk(xq[n].y);
      const f32x2 d0 = unpk(dq[n].x), d1 = unpk(dq[n].y);
      acc2[0][n] = f32x4{ffma(d0.x, ffma(x0.x, rs2, m2), x0.x), ffma(d0.y, ffma(x0.y, rs2, m2), x0.y), ffma(d1.x, ffma(x1.x, rs2, m2), x1.x), ffma(d1.y, ffma(x1.y, rs2, m2), x1.y)};
      PIN(acc2[0][n]);
    }
    ST(17);
    asm volatile("s_nop 1");
    waitv<8>();
    arrive_flag<8>(L, W_F3C, lane, ws, WS_F3 + (unsigned)(xcd * 32 + j) * 128, tlo, thi);
    g2_step<1>(acc2, WF[0], AF[0]);
    AL(3); AL(4);
    waitv<12>(); g2_step<4>(acc2, WF[1], AF[1]);
    WL(5); AL(5);
    waitv<17>(); g2_step<4>(acc2, WF[2], AF[2]);
    ST(20);
    waitv<13>(); g2_step<4>(acc2, WF[3], AF[3]);
    waitv<9>(); g2_step<4>(acc2, WF[4], AF[4]);
    lds_wait_ge(L, W_F3C, 8);
    const unsigned f3o = WS_F3 + (unsigned)(cb2 * 32 + 8 * wi + (ln_ & 7)) * 128;
    ld64c(f3, f3o, ws);
    waitv<1>(); g2_step<4>(acc2, WF[5], AF[5]);
    ST(22);
    waitv<0>(); PIN(f3);
    f3 = spin64<false>(f3, f3o, ws, tlo, thi);
    lds_arrive(L, W_F3ALL, lane);
    LAS char* fp = L + FM + wi * 20480 + vo;
#pragma unroll
    for (int n = 0; n < 5; ++n) *(LAS f32x4*)(fp + wi * 5120 + n * 1024) = acc2[0][n];
    lds_wait_ge(L, W_F3ALL, 4);
    ST(21);
    v4u yv[3];
    {
      const char* yb = ws + WS_Y + (size_t)cb2 * 80 * 4096 + row2 * 2;
      const unsigned yo = (unsigned)(ln_ >> 1) * 4096 + (ln_ & 1) * 16;
      ld128c<0>(yv[0], yo, yb); ld128c<0>(yv[1], yo, yb + 32 * 4096); ld128c<0>(yv[2], yo & 0xffffu, yb + 64 * 4096);
    }
#pragma unroll
    for (int m = 1; m < 4; ++m)
#pragma unroll
      for (int n = 0; n < 5; ++n) *(LAS f32x4*)(fp + ((m + wi) & 3) * 5120 + n * 1024) = acc2[m][n];
    lds_arrive(L, W_FM, lane);
    waitv<0>(); PIN(yv[0]); PIN(yv[1]); PIN(yv[2]);
    LAS char* ys = L + YST + wi * 2560;
    *(LAS v4u*)(ys + vo) = yv[0]; *(LAS v4u*)(ys + 1024 + vo) = yv[1];
    if (ln_ < 32) *(LAS v4u*)(ys + 2048 + vo) = yv[2];
    lds_set(L, W_F2OK + wi * 4, 2);
    STEND();
    asm volatile("97:");
    __builtin_amdgcn_endpgm();
  }

  const bool odd = wi & 1;
  const int kch = chA + (wi >> 1);
  const unsigned vo = lane * 16;
  v4u WF[6][5], AF[6][4];
  auto reduce = [&](int c, int i, bool three) {
    LAS char* pp = L + KG_SCR + 3 * c * KG_WSTR;
    f32x4 s = fma4(*(LAS f32x4*)(pp + i * 16), *(LAS float*)(pp + 2576), f32x4{0.f, 0.f, 0.f, 0.f});
    s = fma4(*(LAS f32x4*)(pp + KG_WSTR + i * 16), *(LAS float*)(pp + KG_WSTR + 2576), s);
    if (three) s = fma4(*(LAS f32x4*)(pp + 2 * KG_WSTR + i * 16), *(LAS float*)(pp + 2 * KG_WSTR + 2576), s);
    *(LAS f32x4*)(L + KTAB + c * KP_STRIDE + i * 16) = s;
    return s;
  };
  lds_wait_ge(L, W_KG, 8);
  if (ts < 161) { st128c((unsigned)ts * 16, ws + WS_KP + (size_t)wid * KP_STRIDE, __builtin_bit_cast(v4u, reduce(2, ts, false))); reduce(0, ts, true); }
  if (ts >= 95) reduce(1, ts - 95, true);
  if (ts < 3) *(LAS float*)(L + KTAB + ts * KP_STRIDE - 4) = 0.f;
  lds_arrive(L, W_SB, lane);
  waitv<0>();
  arrive_flag(L, W_FKC, lane, ws, WS_FK + (unsigned)wid * 64, tlo, thi);
  ST(9);
  __builtin_amdgcn_s_setprio(0);
  lds_wait_min<4>(L, W_RDY, F1STAGE, lane);
  const unsigned f1o = WS_F1 + (unsigned)xcd * 2048 + ts * 8;
  v2u f1own; ld64c(f1own, f1o, ws);
  waitv<0>(); PIN(f1own);
  f1own = spin64<false>(f1own, f1o, ws, tlo, thi);
  v4u uu[3]; unsigned ulo[3];
#pragma unroll
  for (int r = 0; r < 3; ++r) {
    const int e = 64 * r + lane, tb = 64 * wi + e / 3, c = e % 3;
    ld128c<0>(uu[r], tb * 80 + c * 16, ws + WS_XNB + (size_t)(16 * xcd + (j >> 1)) * 20480 + par * 32);
    ulo[r] = (c == 2 - 2 * par ? CVS : CVA + (c - par) * CV_CH) + tb * 16;
  }
  if (b == 0) arrive_flag(L, W_F1ALL, lane, ws, WS_E, tlo, thi);
  else lds_arrive(L, W_F1ALL, lane);
  ST(4);
  const unsigned fko = WS_FK + (unsigned)(wid ^ 1) * 64;
  v2u fk; ld64c(fk, fko, ws);
  waitv<0>(); PIN(fk); PIN(uu[0]); PIN(uu[1]); PIN(uu[2]);
  fk = spin64<false>(fk, fko, ws, tlo, thi);
  v4u kp; ld128c<0>(kp, (unsigned)(ts < 161 ? ts : 160) * 16, ws + WS_KP + (size_t)(wid ^ 1) * KP_STRIDE);
  lds_wait_ge(L, W_SB, 8);
  waitv<0>(); PIN(kp);
  if (ts < 161) { LAS f32x4* tp = (LAS f32x4*)(L + KTAB + 2 * KP_STRIDE + ts * 16); *tp = add4(*tp, __builtin_bit_cast(f32x4, kp)); }
  lds_arrive(L, W_SB, lane);
  lds_wait_ge(L, W_F1ALL, 4);
  lds_wait_min<4>(L, W_RDY, 9, lane);
  WL(0); WL(1); WL(2);
  ST(7);
  lds_wait_min<4>(L, W_RDY, 11, lane);
  ST(11);
  put_k(L, kix, ts, 0, CVA); put_k(L, kix, ts, 1, CVA + CV_CH);
  WL(3);
  put_u(L + ulo[0], uu[0], ts < 3); put_u(L + ulo[1], uu[1], false); put_u(L + ulo[2], uu[2], false);
  WL(4);
  if (ZCOND) {
    *(LAS v4u*)(L + CVA + ZOFF) = v4u{0u, 0u, 0u, 0u}; *(LAS v4u*)(L + CVA + CV_CH + ZOFF) = v4u{0u, 0u, 0u, 0u}; *(LAS v4u*)(L + CVS + ZOFF) = v4u{0u, 0u, 0u, 0u};
  }
  lds_wait_ge(L, W_SB, 12);
  put_k(L, kix, ts, 2, CVS);
  lds_arrive(L, W_SB, lane);
  ST(5);
  lds_wait_ge(L, W_SB, 16);
  lds_wait_ge(L, W_RDY + wi * 4, 12);
  ST(13);
  __builtin_amdgcn_s_setprio(SPRIO2);
  LATE_LANE();
  f32x4 cacc[8];
#pragma unroll
  for (int i = 0; i < 8; ++i) cacc[i] = f32x4{0.f, 0.f, 0.f, 0.f};
  {
    const unsigned o1 = CVA + (wi >> 1) * CV_CH - (odd ? 4 * 64 : 0);
    LAS char* ka = L + KAO + o1; LAS char* ub = L + UBO + o1;
    v4u A[8], B[8];
#define LA(i, u) (*(LAS v4u*)(ka - 512 * (i) - 64 * (u)))
#define LB(i, u) (*(LAS v4u*)(ub + 512 * (i) - 64 * (u)))
    A[7] = LA(7, 0); B[0] = LB(0, 0); A[0] = LA(0, 0); B[7] = LB(7, 0); A[6] = LA(6, 0); B[6] = LB(6, 0); A[1] = LA(1, 0); B[1] = LB(1, 0);
    A[5] = LA(5, 0); B[5] = LB(5, 0); A[2] = LA(2, 0); B[2] = LB(2, 0); A[4] = LA(4, 0); B[4] = LB(4, 0); A[3] = LA(3, 0); B[3] = LB(3, 0);
    SCHED();
#pragma unroll
    for (int u = 0; u < 4; ++u) {
      v4u An[2], Bn[2];
#pragma unroll
      for (int sd = 7; sd >= 0; --sd) {
#pragma unroll
        for (int o = 0; o <= sd; ++o) {
          if (sd + o < 8) cacc[sd + o] = mfma(A[sd], B[o], cacc[sd + o]);
          if (o != sd && sd + o < 8) cacc[sd + o] = mfma(A[o], B[sd], cacc[sd + o]);
        }
        SCHED();
        if (u < 3) {
          if (sd >= 2) { A[sd] = LA(sd, u + 1); B[sd] = LB(sd, u + 1); }
          if (sd == 6 || sd == 5) { An[6 - sd] = LA(6 - sd, u + 1); Bn[6 - sd] = LB(6 - sd, u + 1); }
        }
        SCHED();
      }
      if (u < 3) { A[0] = An[0]; A[1] = An[1]; B[0] = Bn[0]; B[1] = Bn[1]; }
    }
#undef LA
#undef LB
  }
  ST(10);
  unsigned vm[4];
  {
    LAS char* px = L + SXCH + wi * 4096 + vo;
    if (odd) {
#pragma unroll
      for (int i = 0; i < 4; ++i) { *(LAS f32x4*)(px + i * 1024) = cacc[i]; cacc[i] = cacc[4 + i]; }
    } else {
#pragma unroll
      for (int i = 0; i < 4; ++i) *(LAS f32x4*)(px + i * 1024) = cacc[4 + i];
    }
    lds_arrive(L, W_SX + (wi >> 1) * 4, lane);
#pragma unroll
    for (int m = 0; m < 4; ++m) vm[m] = vo + ((m + wi) & 3) * 49152;
    lds_wait_ge(L, W_SX + (wi >> 1) * 4, 2);
    px = L + SXCH + (wi ^ 1) * 4096 + vo;
    v2u yo[4];
#pragma unroll
    for (int i = 0; i < 4; ++i) { const f32x4 s = add4(cacc[i], *(LAS f32x4*)(px + i * 1024)); yo[i] = v2u{pk2(s.x, s.y), pk2(s.z, s.w)}; }
    char* yb = ws + WS_Y + (size_t)kch * 4096 + (odd ? 2048 : 0);
    st64co<0>(YOFF, yb, yo[0]); st64co<512>(YOFF, yb, yo[1]); st64co<1024>(YOFF, yb, yo[2]); st64co<1536>(YOFF, yb, yo[3]);
  }
  ST(14);
  lds_wait_ge(L, W_F2OK + wi * 4, 1);
  ST(16);
  AL(0); AL(1); AL(2);
  ST(17);
  f32x4 acc2[4][5];
  waitv<12>();
  arrive_flag<8>(L, W_F3C, lane, ws, WS_F3 + (unsigned)(xcd * 32 + j) * 128, tlo, thi);
  __builtin_amdgcn_s_setprio(0);
  waitv<8>();
  g2_step<0>(acc2, WF[0], AF[0]);
  AL(3); AL(4);
  waitv<12>(); g2_step<4>(acc2, WF[1], AF[1]);
  WL(5); AL(5);
  waitv<17>(); g2_step<4>(acc2, WF[2], AF[2]);
  ST(20);
  waitv<13>(); g2_step<4>(acc2, WF[3], AF[3]);
  waitv<9>(); g2_step<4>(acc2, WF[4], AF[4]);
  waitv<0>(); g2_step<4>(acc2, WF[5], AF[5]);
  ST(22);
  lds_wait_ge(L, W_F3C, 8);
#pragma unroll
  for (int m = 1; m < 4; ++m)
#pragma unroll
    for (int n = 0; n < 5; ++n) *(LAS f32x4*)(L + FS + wi * 15360 + (m - 1) * 5120 + n * 1024 + vo) = acc2[m][n];
  lds_arrive(L, W_FS, lane);
  ST(23);
  lds_wait_ge(L, W_FS, 4);
#pragma unroll
  for (int d = 1; d < 4; ++d)
#pragma unroll
    for (int n = 0; n < 5; ++n) acc2[0][n] += *(LAS f32x4*)(L + FS + ((wi - d) & 3) * 15360 + (d - 1) * 5120 + n * 1024 + vo);
  lds_wait_ge(L, W_FM, 4);
  ST(24);
  {
    char* ob = out + (size_t)row2 * 1280 + cb2 * 160; const unsigned oo = OROW * 1280 + cg * 8;
    v2u o[5];
#pragma unroll
    for (int n = 0; n < 5; ++n) {
      LAS char* p = L + FM + wi * 5120 + n * 1024 + vo;
      acc2[0][n] += (*(LAS f32x4*)p + *(LAS f32x4*)(p + 20480)) + (*(LAS f32x4*)(p + 40960) + *(LAS f32x4*)(p + 61440));
    }
    lds_wait_ge(L, W_F2OK + wi * 4, 2);
#pragma unroll
    for (int n = 0; n < 5; ++n) {
      const v2u t = trread(L + YST + wi * 2560 + n * 512 + ln_ * 8);
      const f32x2 s0 = acc2[0][n].xy + unpk(t.x), s1 = acc2[0][n].zw + unpk(t.y);
      o[n] = v2u{pk2(s0.x, s0.y), pk2(s1.x, s1.y)};
    }
    st64o<0>(oo, ob, o[0]); st64o<32>(oo, ob, o[1]); st64o<64>(oo, ob, o[2]); st64o<96>(oo, ob, o[3]); st64o<128>(oo, ob, o[4]);
  }
  STEND();
  asm volatile("99:");
}

extern "C" void kernel_launch(void* const* d_in, const int* in_sizes, int n_in, void* d_out, int out_size, void* d_ws, size_t ws_size,
                              hipStream_t stream) {
  fused_s4_mlp<<<256, 512, 0, stream>>>((const char*)d_in[0], (const char*)d_in[1], (const char*)d_in[2], (const char*)d_in[3],
      (const char*)d_in[4], (const char*)d_in[5], (const char*)d_in[6], (const char*)d_in[7], (const char*)d_in[8], (const char*)d_in[10],
      (const char*)d_in[11], (const char*)d_in[12], (char*)d_out, (char*)d_ws);
}
```

```cpp
#include <hip/hip_runtime.h>
#include <stdint.h>

typedef unsigned short u16;
typedef unsigned long long u64;
typedef unsigned v4u __attribute__((ext_vector_type(4)));
typedef unsigned v2u __attribute__((ext_vector_type(2)));
typedef float f32x4 __attribute__((ext_vector_type(4)));
typedef float f32x2 __attribute__((ext_vector_type(2)));
typedef short bf16x8 __attribute__((ext_vector_type(8)));
typedef short s16x4 __attribute__((ext_vector_type(4)));
typedef __bf16 bf16x2_t __attribute__((ext_vector_type(2)));
#define LAS __attribute__((address_space(3)))
#define GAS __attribute__((address_space(1)))
#define DI __device__ __forceinline__

constexpr unsigned WS_E = 0, WS_TMO = 64, WS_F1 = 131072, WS_STATS = 16384, WS_F2 = 32768, WS_F3 = 65536, WS_FK = 98304, WS_STAMP = 262144;
constexpr unsigned WS_XNB = 1u << 20, WS_W2T = 4u << 20, WS_Y = 7u << 20, WS_ACT = 10u << 20, WS_KP = 17u << 20;
constexpr unsigned KP_STRIDE = 2624;
constexpr unsigned SPIN_CAP = 400000;

constexpr unsigned W_F1C = 0, W_F2C = 4, W_F3C = 8, W_KG = 12, W_MB = 16, W_SB = 20, W_SX = 24, W_FM = 32, W_FS = 36, W_F3ALL = 40, W_F1ALL = 44,
                   W_F2OK = 48, W_RDY = 64, W_TAG = 80, W_FKC = 96;
constexpr unsigned KTAB = 272;
constexpr unsigned CSP = 8192;
constexpr unsigned CVA = 10240, CV_CH = 27008, CV_USTR = 4640, CV_KOFF = 18560, CV_KSTR = 4224;
constexpr unsigned KG_SCR = CVA, KG_WSTR = 4352, LN_ROWS = KG_SCR + 8 * KG_WSTR, LN_PITCH = 1344, W2P = LN_ROWS + 10752;
constexpr unsigned G1_RING = 64512, G1_STAGE = 28672, G1_BOFF = 16384;
constexpr unsigned CVS = G1_RING, SXCH = CVS + 27136, MXCH = SXCH + 16384;
constexpr unsigned CSF = G1_RING + 3 * G1_STAGE, YST = CSF + 3072;
constexpr unsigned FM = 256, FS = FM + 81920;
static_assert(FS + 61440 <= CSF && CVA + 2 * CV_CH <= G1_RING && W2P + 8192 <= G1_RING && YST + 10240 <= 163840 && MXCH + 16384 <= CSF, "lds map");

#define ROT2 0
#define F1STAGE 6
#define SPRIO2 1
#define F1GAP 16
#define F2GAP 8
#define SPRIO 1
#define ST(i) do {} while (0)
#define STEND() do {} while (0)
#define STARG
#define STPASS

DI float bf2f(unsigned v) { return __builtin_bit_cast(float, v << 16); }
DI float bfhi(unsigned v) { return __builtin_bit_cast(float, v & 0xffff0000u); }
DI unsigned pk2(float lo, float hi) { bf16x2_t r; r.x = (__bf16)lo; r.y = (__bf16)hi; return __builtin_bit_cast(unsigned, r); }
DI float rbf(float f) { return bf2f(pk2(f, f) & 0xffffu); }
DI f32x4 mfma(v4u a, v4u b, f32x4 c) {
  return __builtin_amdgcn_mfma_f32_16x16x32_bf16(__builtin_bit_cast(bf16x8, a), __builtin_bit_cast(bf16x8, b), c, 0, 0, 0);
}
DI v2u trread(LAS char* p) { return __builtin_bit_cast(v2u, __builtin_amdgcn_ds_read_tr16_b64_v4i16((LAS s16x4*)p)); }
template <int N> DI void waitv() { asm volatile("s_waitcnt vmcnt(%0)" ::"i"(N) : "memory"); }
DI void bar() { asm volatile("s_waitcnt lgkmcnt(0)\n\ts_barrier" ::: "memory"); }
#define PIN(x) asm volatile("" : "+v"(x))
#define FENCE() asm volatile("" ::: "memory")
#define SCHED() __builtin_amdgcn_sched_barrier(0)
#define RFL(x) __builtin_amdgcn_readfirstlane(x)

DI void ld16(unsigned& d, unsigned vo, const void* sb) { asm volatile("global_load_ushort %0, %1, %2" : "=v"(d) : "v"(vo), "s"(sb) : "memory"); }
template <int O> DI void ld32(unsigned& d, unsigned vo, const void* sb) { asm volatile("global_load_dword %0, %1, %2 offset:%3" : "=v"(d) : "v"(vo), "s"(sb), "i"(O) : "memory"); }
DI void ld64(v2u& d, unsigned vo, const void* sb) { asm volatile("global_load_dwordx2 %0, %1, %2" : "=v"(d) : "v"(vo), "s"(sb) : "memory"); }
DI void ld128(v4u& d, unsigned vo, const void* sb) { asm volatile("global_load_dwordx4 %0, %1, %2" : "=v"(d) : "v"(vo), "s"(sb) : "memory"); }
DI void ld64c(v2u& d, unsigned vo, const void* sb) { asm volatile("global_load_dwordx2 %0, %1, %2 sc1" : "=v"(d) : "v"(vo), "s"(sb) : "memory"); }
template <int O> DI void ld128c(v4u& d, unsigned vo, const void* sb) { asm volatile("global_load_dwordx4 %0, %1, %2 offset:%3 sc1" : "=v"(d) : "v"(vo), "s"(sb), "i"(O) : "memory"); }
DI void st64c(unsigned vo, void* sb, v2u d) { asm volatile("global_store_dwordx2 %0, %1, %2 sc1" ::"v"(vo), "v"(d), "s"(sb) : "memory"); }
template <int O> DI void ld64o(v2u& d, unsigned vo, const void* sb) { asm volatile("global_load_dwordx2 %0, %1, %2 offset:%3" : "=v"(d) : "v"(vo), "s"(sb), "i"(O) : "memory"); }
template <int O> DI void st64o(unsigned vo, void* sb, v2u d) { asm volatile("global_store_dwordx2 %0, %1, %2 offset:%3" ::"v"(vo), "v"(d), "s"(sb), "i"(O) : "memory"); }
template <int O> DI void st64co(unsigned vo, void* sb, v2u d) { asm volatile("global_store_dwordx2 %0, %1, %2 offset:%3 sc1" ::"v"(vo), "v"(d), "s"(sb), "i"(O) : "memory"); }
DI void st128c(unsigned vo, void* sb, v4u d) { asm volatile("global_store_dwordx4 %0, %1, %2 sc1\n\ts_nop 1" ::"v"(vo), "v"(d), "s"(sb) : "memory"); }
DI void st64u(unsigned vo, void* sb, v2u d) { asm volatile("global_store_dwordx2 %0, %1, %2 sc1 ; CNT" ::"v"(vo), "v"(d), "s"(sb) : "memory"); }
DI void st128u(unsigned vo, void* sb, v4u d) { asm volatile("global_store_dwordx4 %0, %1, %2 sc1 ; CNT\n\ts_nop 1" ::"v"(vo), "v"(d), "s"(sb) : "memory"); }
DI void dma16(unsigned vo, const void* sb, unsigned lds) {
  asm volatile("s_mov_b32 m0, %2\n\ts_nop 0\n\tglobal_load_lds_dwordx4 %0, %1" ::"v"(vo), "s"(sb), "s"(lds) : "memory");
}
template <int O> DI void dma16o(unsigned vo, const void* sb) {
  asm volatile("global_load_lds_dwordx4 %0, %1 offset:%2" ::"v"(vo), "s"(sb), "i"(O) : "memory");
}
template <bool HIONLY> DI v2u spin64(v2u cur, unsigned vo, char* ws, unsigned tlo, unsigned thi) {
  if (__builtin_expect(__builtin_amdgcn_ballot_w64(HIONLY ? (cur.y != tlo) : (cur.x != tlo || cur.y != thi)) != 0ull, 0)) {
#pragma nounroll
    for (unsigned spins = 0; spins < SPIN_CAP; ++spins) {
      __builtin_amdgcn_s_sleep(6);
      asm volatile("global_load_dwordx2 %0, %1, %2 sc1\n\ts_waitcnt vmcnt(0)" : "=&v"(cur) : "v"(vo), "s"(ws) : "memory");
      if (__builtin_amdgcn_ballot_w64(HIONLY ? (cur.y != tlo) : (cur.x != tlo || cur.y != thi)) == 0ull) break;
    }
  }
  return cur;
}
DI unsigned lw(LAS char* L, unsigned off) { return *(volatile LAS unsigned*)(L + off); }
DI void lds_wait_at(unsigned a, unsigned target) {
  unsigned v, cnt; u64 t;
  asm volatile("s_mov_b32 %1, %5\n"
               "1:\n\tds_read_b32 %0, %3\n\ts_sub_u32 %1, %1, 1\n\ts_waitcnt lgkmcnt(0)\n\tv_cmp_gt_u32 vcc, %4, %0\n\t"
               "s_cselect_b64 %2, 0, vcc\n\ts_cmp_lg_u64 %2, 0\n\ts_cbranch_scc1 1b"
               : "=&v"(v), "=&s"(cnt), "=&s"(t) : "v"(a), "s"(target), "i"(4 * SPIN_CAP) : "vcc", "scc", "memory");
}
DI void lds_wait_ge(LAS char* L, unsigned off, unsigned target) { lds_wait_at((unsigned)(size_t)L + off, target); }
template <int N> DI void lds_wait_min(LAS char* L, unsigned off, unsigned target, int lane) {
  lds_wait_at((unsigned)(size_t)L + off + (lane & (N - 1)) * 4, target);
}
DI void lds_set(LAS char* L, unsigned off, unsigned v) { FENCE(); *(volatile LAS unsigned*)(L + off) = v; FENCE(); }
DI void lds_arrive(LAS char* L, unsigned off, int) {
  asm volatile("s_mov_b64 exec, 1\n\tds_add_u32 %0, %1\n\ts_mov_b64 exec, -1" ::"v"((unsigned)(size_t)L + off), "v"(1u) : "memory");
}
template <unsigned N = 4> DI void arrive_flag(LAS char* L, unsigned off, int, char* ws, unsigned fo, unsigned tlo, unsigned thi) {
  unsigned old;
  asm volatile("s_mov_b64 exec, 1\n\tds_add_rtn_u32 %0, %1, %2\n\ts_waitcnt lgkmcnt(0)\n\tv_cmp_eq_u32 vcc, %6, %0\n\t"
               "s_mov_b64 exec, vcc\n\tglobal_store_dwordx2 %3, %4, %5 sc1\n\ts_mov_b64 exec, -1"
               : "=&v"(old) : "v"((unsigned)(size_t)L + off), "v"(1u), "v"(fo), "v"(v2u{tlo, thi}), "s"(ws), "i"(N - 1) : "vcc", "memory");
}
DI void arrive_flag8(LAS char* L, unsigned off, char* ws, unsigned fo, unsigned tlo, unsigned thi) {
  unsigned old;
  asm volatile("s_mov_b64 exec, 1\n\tds_add_rtn_u32 %0, %1, %2\n\ts_waitcnt lgkmcnt(0)\n\tv_cmp_eq_u32 vcc, 3, %0\n\t"
               "s_mul_i32 exec_lo, vcc_lo, 0xff\n\tglobal_store_dwordx2 %3, %4, %5 sc1\n\ts_mov_b64 exec, -1"
               : "=&v"(old) : "v"((unsigned)(size_t)L + off), "v"(1u), "v"(fo), "v"(v2u{tlo, thi}), "s"(ws) : "vcc", "memory");
}
typedef float f32x16 __attribute__((ext_vector_type(16)));
DI f32x16 mfma32(v4u a, v4u b, f32x16 c) {
  return __builtin_amdgcn_mfma_f32_32x32x16_bf16(__builtin_bit_cast(bf16x8, a), __builtin_bit_cast(bf16x8, b), c, 0, 0, 0);
}
template <int C> DI float dpp_add(float v) {
  return v + __builtin_bit_cast(float, __builtin_amdgcn_update_dpp(0, __builtin_bit_cast(int, v), C, 0xf, 0xf, true));
}
DI float rl(float v, int l) { return __builtin_bit_cast(float, __builtin_amdgcn_readlane(__builtin_bit_cast(int, v), l)); }
DI float half_sum(float v, bool upper) {
  v = dpp_add<0xB1>(v); v = dpp_add<0x4E>(v); v = dpp_add<0x141>(v); v = dpp_add<0x140>(v);
  const float a = rl(v, 0) + rl(v, 16), c = rl(v, 32) + rl(v, 48);
  return upper ? c : a;
}
DI f32x2 unpk(unsigned v) { return f32x2{bf2f(v), bfhi(v)}; }
typedef f32x2 cf;
DI cf cmul1(cf i, cf w) { return cf{__builtin_fmaf(i.x, w.x, -(i.y * w.y)), __builtin_fmaf(i.x, w.y, i.y * w.x)}; }
DI void cmul4(cf& o0, cf& o1, cf& o2, cf& o3, cf i0, cf i1, cf i2, cf i3, cf w0, cf w1, cf w2, cf w3) {
  o0 = cmul1(i0, w0); o1 = cmul1(i1, w1); o2 = cmul1(i2, w2); o3 = cmul1(i3, w3);
}
DI unsigned pkc(cf v) { return pk2(v.x, v.y); }
template <int R> DI void put4(cf a, cf b, cf c, cf d) {
  asm volatile("ds_write_addtid_b32 %0 offset:%4\n\tds_write_addtid_b32 %1 offset:%5\n\tds_write_addtid_b32 %2 offset:%6\n\tds_write_addtid_b32 %3 offset:%7"
               ::"v"(pkc(a)), "v"(pkc(b)), "v"(pkc(c)), "v"(pkc(d)), "i"(R * 272), "i"(R * 272 + 272), "i"(R * 272 + 544), "i"(R * 272 + 816) : "memory");
}
template <int R> DI void put1(cf a) { asm volatile("ds_write_addtid_b32 %0 offset:%1" ::"v"(pkc(a)), "i"(R * 272) : "memory"); }
DI cf cexpk(float e1, float th, float tl, float k, float ks) {
  const float mag = __builtin_amdgcn_exp2f(e1 * k);
  const float fr = __builtin_fmaf(tl, ks, __builtin_amdgcn_fractf(th * ks));
  return cf{__builtin_amdgcn_cosf(fr), __builtin_amdgcn_sinf(fr)} * mag;
}
DI void kgen_slow(cf a, cf W1, cf W256, cf W8, LAS char* sr, f32x4 (&kacc)[2][2]) {
  cf W2, W512, u1, W16, u2, u3, W4, W32, u4, u5, u6, u7, r8, r9, r10, r11, r12, r13, r14, r15, r16, r17, W1024, v3, r18, r19, r20, v1;
  cmul4(W2, W512, u1, W16, W1, W256, a, W8, W1, W256, W1, W8);
  cmul4(u2, u3, W4, W32, a, u1, W2, W16, W2, W2, W2, W16);
  put4<0>(a, u1, u2, u3);
  cmul4(u4, u5, u6, u7, a, u1, u2, u3, W4, W4, W4, W4);
  put4<4>(u4, u5, u6, u7);
  cmul4(r8, r9, r10, r11, a, u2, u4, u6, W256, W256, W256, W256);
  put4<8>(r8, r9, r10, r11);
  cmul4(r12, r13, r14, r15, a, u4, r8, r10, W512, W512, W512, W512);
  put4<12>(r12, r13, r14, r15);
  v4u UA[2][4];
#pragma unroll
  for (int k = 0; k < 4; ++k) UA[0][k] = *(LAS v4u*)(sr + 64 * k);
  cmul4(r16, r17, W1024, v3, r12, r14, W512, W8, W512, W512, W512, W16);
  cmul4(r18, r19, r20, v1, r12, r14, r16, W8, W1024, W1024, W1024, W32);
  put4<0>(r16, r17, r18, r19);
  put1<4>(r20);
#pragma unroll
  for (int k = 0; k < 4; ++k) UA[1][k] = *(LAS v4u*)(sr + 64 * k);
  put4<0>(cf{1.f, 0.f}, W8, W16, v3);
  cf v0 = W32, v2, t0, t1, t2, t3;
  cmul4(v2, t3, t0, t1, W16, v3, v0, v1, W32, W32, W32, W32);
  put4<4>(v0, v1, v2, t3);
  cmul4(t2, v3, v0, v1, v2, t3, t0, t1, W32, W32, W32, W32);
  put4<8>(t0, t1, t2, v3);
  cmul4(v2, t3, t0, t1, t2, v3, v0, v1, W32, W32, W32, W32);
  put4<12>(v0, v1, v2, t3);
  v4u vb[4];
#pragma unroll
  for (int k = 0; k < 4; ++k) vb[k] = *(LAS v4u*)(sr + 64 * k);
  cmul4(t2, v3, v0, v1, v2, t3, t0, t1, W32, W32, W32, W32);
  put4<0>(t0, t1, t2, v3);
  cmul4(v2, t3, t0, t1, t2, v3, v0, v1, W32, W32, W32, W32);
  put4<4>(v0, v1, v2, t3);
#pragma unroll
  for (int k = 0; k < 4; ++k) {
    kacc[0][0] = mfma(UA[0][k], vb[k], kacc[0][0]);
    kacc[1][0] = mfma(UA[1][k], vb[k], kacc[1][0]);
  }
  cmul4(t2, v3, v0, v1, v2, t3, t0, t1, W32, W32, W32, W32);
  put4<8>(t0, t1, t2, v3);
  cmul4(v2, t3, t0, t1, t2, v3, v0, v1, W32, W32, W32, W32);
  put4<12>(v0, v1, v2, t3);
#pragma unroll
  for (int k = 0; k < 4; ++k) vb[k] = *(LAS v4u*)(sr + 64 * k);
  SCHED();
#pragma unroll
  for (int k = 0; k < 4; ++k) {
    kacc[0][1] = mfma(UA[0][k], vb[k], kacc[0][1]);
    kacc[1][1] = mfma(UA[1][k], vb[k], kacc[1][1]);
  }
}
typedef _Float16 f16x8 __attribute__((ext_vector_type(8)));
typedef _Float16 f16x2 __attribute__((ext_vector_type(2)));
DI f32x4 mfmah(v4u a, v4u b, f32x4 c) {
  return __builtin_amdgcn_mfma_f32_16x16x32_f16(__builtin_bit_cast(f16x8, a), __builtin_bit_cast(f16x8, b), c, 0, 0, 0);
}
DI unsigned pkh(cf v) { f16x2 r; r.x = (_Float16)v.x; r.y = (_Float16)v.y; return __builtin_bit_cast(unsigned, r); }
DI cf csq(cf v) { return cf{__builtin_fmaf(v.x, v.x, -(v.y * v.y)), (v.x + v.x) * v.y}; }
#define HMUL(o, i, w) "v_pk_mul_f16 " o ", " i ", " w " op_sel:[1,1] op_sel_hi:[1,0] neg_lo:[0,1]\n\t"
#define HFMA(o, i, w) "v_pk_fma_f16 " o ", " i ", " w ", " o " op_sel_hi:[0,1,1]\n\t"
template <int R0, int R1, int R2, int R3>
DI void h4(unsigned& o0, unsigned& o1, unsigned& o2, unsigned& o3, unsigned i0, unsigned i1, unsigned i2, unsigned i3, unsigned w0, unsigned w1, unsigned w2, unsigned w3) {
  asm volatile(HMUL("%0", "%4", "%8") HMUL("%1", "%5", "%9") HMUL("%2", "%6", "%10") HMUL("%3", "%7", "%11")
               HFMA("%0", "%4", "%8") HFMA("%1", "%5", "%9") HFMA("%2", "%6", "%10") HFMA("%3", "%7", "%11")
               "ds_write_addtid_b32 %0 offset:%12\n\tds_write_addtid_b32 %1 offset:%13\n\tds_write_addtid_b32 %2 offset:%14\n\tds_write_addtid_b32 %3 offset:%15"
               : "=&v"(o0), "=&v"(o1), "=&v"(o2), "=&v"(o3)
               : "v"(i0), "v"(i1), "v"(i2), "v"(i3), "v"(w0), "v"(w1), "v"(w2), "v"(w3), "i"(R0 * 272), "i"(R1 * 272), "i"(R2 * 272), "i"(R3 * 272) : "memory");
}
template <int R> DI void hput(unsigned v) { asm volatile("ds_write_addtid_b32 %0 offset:%1" ::"v"(v), "i"(R * 272) : "memory"); }
template <int C> DI float dpp_max(float v) {
  return __builtin_fmaxf(v, __builtin_bit_cast(float, __builtin_amdgcn_update_dpp(0, __builtin_bit_cast(int, v), C, 0xf, 0xf, true)));
}
template <class F> DI float kgen_task(const unsigned (&pr)[7], bool dead, unsigned sw, LAS char* sr, f32x4 (&kacc)[2][2], F mid) {
  const float dt = rbf(__builtin_amdgcn_exp2f(bf2f(pr[6]) * 1.44269504088896341f));
  const float zr = dt * bf2f(pr[0]), zi = dt * bf2f(pr[1]);
  const float dbr = dt * bf2f(pr[2]), dbi = dt * bf2f(pr[3]), cr = bf2f(pr[4]), ci = bf2f(pr[5]);
  cf a = cf{cr * dbr - ci * dbi, -(cr * dbi + ci * dbr)};
  if (dead) a = cf{0.f, 0.f};
  const float e1 = zr * 1.44269504088896341f, th = zi * 0.15915494309189535f;
  const float tl = __builtin_fmaf(zi, 6.4206383e-9f, __builtin_fmaf(zi, 0.15915494309189535f, -th));
  const cf W1 = cexpk(e1, th, tl, 1.f, -1.f), W256 = cexpk(e1, th, tl, 256.f, -256.f), W8 = cexpk(e1, th, tl, 8.f, 8.f);
  if (__builtin_expect(__builtin_amdgcn_ballot_w64(!(zr <= 0.f)) != 0ull, 0)) {
    mid(1); mid(2);
    asm volatile("s_mov_b32 m0, %0" ::"s"(sw) : "memory");
#pragma unroll
    for (int i = 0; i < 4; ++i) kacc[i >> 1][i & 1] = f32x4{0.f, 0.f, 0.f, 0.f};
    kgen_slow(a, W1, W256, W8, sr, kacc);
    return 1.f;
  }
  float am = __builtin_fmaxf(__builtin_fabsf(a.x), __builtin_fabsf(a.y));
  am = dpp_max<0xB1>(am); am = dpp_max<0x4E>(am); am = dpp_max<0x141>(am); am = dpp_max<0x140>(am);
  const unsigned e0 = __builtin_amdgcn_readlane(__builtin_bit_cast(unsigned, am), 0), e16 = __builtin_amdgcn_readlane(__builtin_bit_cast(unsigned, am), 16);
  const unsigned e32 = __builtin_amdgcn_readlane(__builtin_bit_cast(unsigned, am), 32), e48 = __builtin_amdgcn_readlane(__builtin_bit_cast(unsigned, am), 48);
  unsigned ex = max(max(e0, e16), max(e32, e48)) >> 23;
  ex = min(max(ex, 2u), 252u);
  const float up = __builtin_bit_cast(float, ex << 23), down = __builtin_bit_cast(float, (254u - ex) << 23);
  const cf W2 = csq(W1), W512 = csq(W256), W16 = csq(W8), W4 = csq(W2), W1024 = csq(W512), W32 = csq(W16), W64 = csq(W32), W128 = csq(W64);
  const unsigned u0 = pkh(a * down), h1 = pkh(W1), h2 = pkh(W2), h4_ = pkh(W4), h256 = pkh(W256), h512 = pkh(W512), h1024 = pkh(W1024);
  unsigned u1, u2, u4, r8, u3, u5, u6, r12, u7, r9, r10, r14, r11, r13, r15, r16, r17, r18, r19, r20;
  asm volatile("s_mov_b32 m0, %0\n\ts_nop 0" ::"s"(sw) : "memory");
  hput<0>(u0);
  h4<1, 2, 4, 8>(u1, u2, u4, r8, u0, u0, u0, u0, h1, h2, h4_, h256);
  h4<3, 5, 6, 12>(u3, u5, u6, r12, u2, u4, u4, u0, h1, h1, h2, h512);
  h4<7, 9, 10, 14>(u7, r9, r10, r14, u6, r8, r8, r8, h1, h2, h4_, h512);
  h4<11, 13, 15, 15>(r11, r13, r16, r15, r10, r12, u0, r14, h2, h4_, h1024, h4_);
  v4u UA[2][4];
#pragma unroll
  for (int k = 0; k < 4; ++k) UA[0][k] = *(LAS v4u*)(sr + 64 * k);
  hput<0>(r16);
  h4<1, 2, 3, 4>(r17, r18, r19, r20, r8, r12, r14, r16, h1024, h1024, h1024, h1024);
#pragma unroll
  for (int k = 0; k < 4; ++k) UA[1][k] = *(LAS v4u*)(sr + 64 * k);
  mid(1);
  unsigned v[16], t0, t1, t2, t3;
  v[1] = pkh(W8); v[2] = pkh(W16); v[4] = pkh(W32); v[8] = pkh(W64);
  const unsigned h128 = pkh(W128);
  hput<0>(0x3c00u); hput<1>(v[1]); hput<2>(v[2]); hput<4>(v[4]); hput<8>(v[8]);
  h4<3, 5, 6, 9>(v[3], v[5], v[6], v[9], v[2], v[4], v[4], v[8], v[1], v[1], v[2], v[1]);
  h4<7, 10, 11, 12>(v[7], v[10], v[11], v[12], v[4], v[8], v[8], v[8], v[3], v[2], v[3], v[4]);
  h4<13, 14, 15, 15>(v[13], v[14], t0, v[15], v[8], v[8], v[1], v[8], v[5], v[6], h128, v[7]);
  v4u vb[4];
#pragma unroll
  for (int k = 0; k < 4; ++k) vb[k] = *(LAS v4u*)(sr + 64 * k);
  mid(2);
  asm volatile("s_mov_b32 m0, %0\n\ts_nop 0" ::"s"(sw) : "memory");
  hput<0>(h128); hput<1>(t0);
  h4<2, 3, 4, 5>(t0, t1, t2, t3, v[2], v[3], v[4], v[5], h128, h128, h128, h128);
#pragma unroll
  for (int k = 0; k < 4; ++k) {
    kacc[0][0] = mfmah(UA[0][k], vb[k], k ? kacc[0][0] : f32x4{0.f, 0.f, 0.f, 0.f});
    kacc[1][0] = mfmah(UA[1][k], vb[k], k ? kacc[1][0] : f32x4{0.f, 0.f, 0.f, 0.f});
  }
  h4<6, 7, 8, 9>(t0, t1, t2, t3, v[6], v[7], v[8], v[9], h128, h128, h128, h128);
  h4<10, 11, 12, 13>(t0, t1, t2, t3, v[10], v[11], v[12], v[13], h128, h128, h128, h128);
  h4<14, 15, 14, 15>(t0, t1, t2, t3, v[14], v[15], v[14], v[15], h128, h128, h128, h128);
#pragma unroll
  for (int k = 0; k < 4; ++k) vb[k] = *(LAS v4u*)(sr + 64 * k);
  SCHED();
#pragma unroll
  for (int k = 0; k < 4; ++k) {
    kacc[0][1] = mfmah(UA[0][k], vb[k], k ? kacc[0][1] : f32x4{0.f, 0.f, 0.f, 0.f});
    kacc[1][1] = mfmah(UA[1][k], vb[k], k ? kacc[1][1] : f32x4{0.f, 0.f, 0.f, 0.f});
  }
  return up;
}
DI void kgen_store(LAS float* pt, const f32x4 (&kacc)[2][2], float scale, int lane) {
  const int l15 = lane & 15, g = lane >> 4;
  const int sh = g < 2 ? 3 : 4 - g;
  const int a0 = (l15 << sh) + (g < 2 ? 4 * g : 128 * g);
  const int a1 = g < 3 ? a0 + 2 : a0 + 64;
#pragma unroll
  for (int nh = 0; nh < 2; ++nh) {
    *(LAS f32x2*)(pt + a0 + ((16 * nh) << sh)) = kacc[0][nh].xy;
    *(LAS f32x2*)(pt + a1 + ((16 * nh) << sh)) = kacc[0][nh].zw;
  }
  if (g == 0) {
#pragma unroll
    for (int nh = 0; nh < 2; ++nh)
#pragma unroll
      for (int r = 0; r < 4; ++r) pt[512 + 32 * r + 16 * nh + l15] = kacc[1][nh][r];
  }
  if (lane == 16) { *(LAS f32x4*)(pt + 640) = f32x4{kacc[1][0][0], 0.f, 0.f, 0.f}; pt[644] = scale; }
}

template <int PAR> DI void conv_unit_shared(LAS char* ka, LAS char* ub, f32x4 (&acc)[4]) {
  constexpr int T[4] = {PAR ? 1 : 0, PAR ? 2 : 3, PAR ? 5 : 4, PAR ? 6 : 7};
  v4u A[8], B[8];
#pragma unroll
  for (int i = 0; i <= T[3]; ++i) { A[i] = *(LAS v4u*)(ka + 512 * (7 - i)); B[i] = *(LAS v4u*)(ub + 512 * i); }
  SCHED();
#pragma unroll
  for (int q = 0; q < 4; ++q)
#pragma unroll
    for (int ip = 0; ip <= T[q]; ++ip) acc[q] = mfma(A[T[q] - ip], B[ip], acc[q]);
  SCHED();
}
DI float fadd(float a, float b) { float r; asm("v_add_f32 %0, %1, %2" : "=v"(r) : "v"(a), "v"(b)); return r; }
DI float ffma(float a, float b, float c) { float r; asm("v_fma_f32 %0, %1, %2, %3" : "=v"(r) : "v"(a), "v"(b), "v"(c)); return r; }
DI f32x4 add4(f32x4 a, f32x4 b) { return f32x4{fadd(a.x, b.x), fadd(a.y, b.y), fadd(a.z, b.z), fadd(a.w, b.w)}; }
DI f32x4 fma4(f32x4 a, float b, f32x4 c) { return f32x4{ffma(a.x, b, c.x), ffma(a.y, b, c.y), ffma(a.z, b, c.z), ffma(a.w, b, c.w)}; }
DI unsigned nbr(unsigned v) { return (unsigned)__builtin_amdgcn_update_dpp(0, (int)v, 0xB1, 0xf, 0xf, true); }
DI v4u pair16(v2u a, v2u b, bool oddl) {
  const unsigned rx = nbr(oddl ? a.x : b.x), ry = nbr(oddl ? a.y : b.y);
  return oddl ? v4u{rx, ry, b.x, b.y} : v4u{a.x, a.y, rx, ry};
}
DI int rot12(int v) { return v >= 12 ? v - 12 : v; }
DI void w_loads(v4u (&WF)[5], const char* p, unsigned vo) {
  ld128c<0>(WF[0], vo, p); ld128c<1024>(WF[1], vo, p); ld128c<2048>(WF[2], vo, p); ld128c<3072>(WF[3], vo, p); ld128c<0>(WF[4], vo, p + 4096);
}
DI void a_loads(v4u (&AF)[4], const char* p, const unsigned (&vm)[4]) {
#pragma unroll
  for (int m = 0; m < 4; ++m) ld128c<0>(AF[m], vm[m], p);
}
template <int LIVE, class F> DI void g2_step(f32x4 (&acc)[4][5], v4u (&WF)[5], v4u (&AF)[4], F ld) {
#pragma unroll
  for (int m = 0; m < 4; ++m) PIN(AF[m]);
#pragma unroll
  for (int n = 0; n < 5; ++n) PIN(WF[n]);
#pragma unroll
  for (int e = 0; e < 20; ++e) {
    const int m = e / 5, n = e % 5;
    acc[m][n] = mfma(WF[n], AF[m], m >= LIVE ? f32x4{0.f, 0.f, 0.f, 0.f} : acc[m][n]);
    if (e & 1) { SCHED(); ld(e >> 1); SCHED(); }
  }
}
DI void make_kix(unsigned (&kix)[9], int ts) {
#pragma unroll
  for (int i = 0; i < 9; ++i) {
    const int lg = 2047 - 8 * ts - i;
    const int bits = (int)(pk2(__builtin_fmaxf((float)lg, 128.f), 0.f));
    const int ci = lg < bits - 0x4280 ? lg : bits - 0x4280;
    kix[i] = KTAB + 4 * ci;
    PIN(kix[i]);
  }
}
DI void put_k(LAS char* L, const unsigned (&kix)[9], int ts, int c, unsigned reg) {
  float kv[9];
#pragma unroll
  for (int i = 0; i < 9; ++i) kv[i] = *(LAS float*)(L + kix[i] + c * KP_STRIDE);
  LAS char* q = L + reg + CV_KOFF + ts * 16;
  *(LAS v4u*)(q + CV_KSTR) = v4u{pk2(kv[0], kv[1]), pk2(kv[2], kv[3]), pk2(kv[4], kv[5]), pk2(kv[6], kv[7])};
  *(LAS v4u*)q = v4u{pk2(kv[1], kv[2]), pk2(kv[3], kv[4]), pk2(kv[5], kv[6]), pk2(kv[7], kv[8])};
}
DI void put_u(LAS char* q, v4u u, bool first) {
  *(LAS v4u*)(q + 3 * CV_USTR + 384) = u;
  *(LAS v2u*)(q + 1 * CV_USTR + 392) = v2u{u.x, u.y}; *(LAS v2u*)(q + 1 * CV_USTR + 400) = v2u{u.z, u.w};
  *(LAS unsigned*)(q + 2 * CV_USTR + 388) = u.x; *(LAS v2u*)(q + 2 * CV_USTR + 392) = v2u{u.y, u.z}; *(LAS unsigned*)(q + 2 * CV_USTR + 400) = u.w;
  *(LAS unsigned*)(q + 396) = u.x; *(LAS v2u*)(q + 400) = v2u{u.y, u.z}; *(LAS unsigned*)(q + 408) = u.w;
  if (first) {
    *(LAS v2u*)(q + 384) = v2u{0u, 0u}; *(LAS unsigned*)(q + 392) = 0u;
    *(LAS v2u*)(q + CV_USTR + 384) = v2u{0u, 0u}; *(LAS unsigned*)(q + 2 * CV_USTR + 384) = 0u;
  }
}

__global__ __launch_bounds__(512, 2) void fused_s4_mlp(const char* __restrict__ x, const char* __restrict__ pAre, const char* __restrict__ pAim,
    const char* __restrict__ pBre, const char* __restrict__ pBim, const char* __restrict__ pCre, const char* __restrict__ pCim,
    const char* __restrict__ plogdt, const char* __restrict__ pDskip, const char* __restrict__ w1, const char* __restrict__ b1,
    const char* __restrict__ w2, char* __restrict__ out, char* __restrict__ ws) {
  __shared__ __attribute__((aligned(16))) char smem[163840];
  LAS char* L = (LAS char*)smem;
  const unsigned lds0 = (unsigned)(size_t)L;
  const char* pc0; unsigned codelen;
  asm volatile("s_getpc_b64 %0\n98:\n\ts_mov_b32 %1, 97f-98b\n\ts_max_u32 %1, %1, 99f-98b" : "=s"(pc0), "=&s"(codelen) :: "scc");
  const int b = blockIdx.x, lane = threadIdx.x & 63;
  const int w = RFL(threadIdx.x >> 6), wi = w & 3;
  {
    const unsigned co = (unsigned)(4 * (16 * w + (lane & 15)) + ((b >> 3) & 3)) * 128;
    asm volatile("s_mov_b32 m0, %2\n\ts_nop 0\n\tglobal_load_lds_dword %0, %1" ::"v"((co < codelen ? co : codelen) & ~3u), "s"(pc0), "s"(lds0 + YST + w * 256) : "memory");
  }
  const int xcd = b & 7, j = b >> 3, rg = xcd >> 1, chf = xcd & 1;
  const int ct1 = 8 * chf + (j & 7), rb1 = 4 * rg + (j >> 3);
  const int wid = rb1 * 16 + ct1;
  const int a2 = j >> 2, cb2 = 4 * chf + (j & 3);
  const int l15 = lane & 15, g = lane >> 4, qq = (lane >> 2) & 3, p4 = lane & 3, l31 = lane & 31, h = lane >> 5;
  const int par = j & 1, cbase = 80 * xcd + 5 * (j >> 1);
  const int chA = cbase + 3 * par, chS = cbase + 2;
  const int ts = 64 * wi + lane;
  ST(0);
  const int row2 = 512 * rg + 64 * a2 + 16 * wi;
  const char* w2b = ws + WS_W2T + (size_t)(cb2 * 48 + 12 * wi) * 5120;
  const char* actb = ws + WS_ACT + (size_t)((32 * rg + 4 * a2) * 48 + 12 * wi) * 1024;
  const int r2 = (ROT2 * (3 * (j & 3) + (3 * a2) / 2) + (w < 4 ? 0 : 6)) % 12;
#define WL(s) w_loads(WF[s], w2b + rot12(r2 + (s)) * 5120, vo)
#define AL(s) a_loads(AF[s], actb + rot12(r2 + (s)) * 1024, vm)
#define AL1(s, m) ld128c<0>(AF[s][m], vm[m], actb + rot12(r2 + (s)) * 1024)
#define WL1(s, n) ld128c<0>(WF[s][n], vo, w2b + rot12(r2 + (s)) * 5120 + (n) * 1024)
#define NOLD [&](int) {}
#define LATE_LANE() int ln_ = lane; PIN(ln_); const int c15 = ln_ & 15, cg = ln_ >> 4
#define KAO (CV_KOFF + (c15 & 1) * CV_KSTR + 2 * (2072 - 8 * (c15 >> 1) + 8 * cg))
#define UBO ((c15 & 3) * CV_USTR + 2 * (64 * (c15 >> 2) + 8 * cg + 224))
#define YOFF ((unsigned)((c15 >> 2) * 128 + cg * 32 + (c15 & 3) * 8))
#define OROW ((unsigned)(2 * (c15 >> 2) + (c15 & 1) + 8 * ((c15 >> 1) & 1)))
#define ZOFF (lane < 24 ? wi * CV_USTR + lane * 16 : CV_KOFF + wi * CV_KSTR + 4096 + (lane - 24) * 16)
#define ZCOND (lane < 24 || (lane < 31 && wi < 2))

  unsigned pr[7];
  {
    const int kc = w < 3 ? chA : (w < 6 ? chA + 1 : chS);
    const int m0 = w < 6 ? (w < 3 ? w : w - 3) * 64 : par * 96 + (w - 6) * 48;
    const unsigned vo = (unsigned)(kc * 192 + m0 + ((w >= 6 && lane >= 48) ? 47 : lane)) * 2;
    ld16(pr[6], (unsigned)kc * 2, plogdt);
    ld16(pr[0], vo, pAre); ld16(pr[1], vo, pAim); ld16(pr[2], vo, pBre); ld16(pr[3], vo, pBim); ld16(pr[4], vo, pCre); ld16(pr[5], vo, pCim);
  }
  unsigned voA[4], voB[3];
#pragma unroll
  for (int i = 0; i < 4; ++i) voA[i] = (8 * i + (lane >> 3)) * 1280 + (((lane & 7) ^ (4 * (i & 1) + (lane >> 4))) * 16) - 1024 * i;
#pragma unroll
  for (int i = 0; i < 3; ++i) { const int c = 64 * i + lane; voB[i] = (c / 12) * 3072 + (c % 12) * 16 - 1024 * i; }
  const char* g1a = x + (size_t)(rb1 * 128 + 32 * wi) * 1280;
  const char* g1b = w1 + ct1 * 192 + wi * 49152;
  const unsigned la = lds0 + G1_RING + wi * 4096, lb = lds0 + G1_RING + G1_BOFF + wi * 3072;
  const int r1 = ((j & 7) + (5 * (j >> 3)) / 2 + 5 * chf + rg) % 10;
  auto slab = [&](int s) { const int v = s + r1; return v >= 10 ? v - 10 : v; };
  auto issue = [&](int s) {
    const char* pa = g1a + slab(s) * 128; const char* pb = g1b + (size_t)slab(s) * 64 * 3072;
    dma16(voA[0], pa, la + s * G1_STAGE); dma16o<1024>(voA[1], pa); dma16o<2048>(voA[2], pa); dma16o<3072>(voA[3], pa);
    dma16(voB[0], pb, lb + s * G1_STAGE); dma16o<1024>(voB[1], pb); dma16o<2048>(voB[2], pb);
  };
  unsigned tlo = 0, thi = 0;
  unsigned kix[9];
  if (w < 4) {
    bar();
    issue(0);
#pragma unroll
    for (int i = 0; i < 9; ++i) kix[i] = 0;
    waitv<7>();
  } else {
    __builtin_amdgcn_s_setprio(SPRIO);
    v2u E; v4u xa, xb; v2u xc;
    const int pu = b % 240, pks = pu / 5, pcg = pu % 5;
    const char* xrow = x + (size_t)(8 * wid + 2 * wi) * 1280;
    ld128(xa, h * 1280 + l31 * 16, xrow); ld128(xb, h * 1280 + 512 + l31 * 16, xrow); ld64(xc, h * 1280 + 1024 + l31 * 8, xrow);
    ld64c(E, 0u, ws);
#pragma unroll
    for (int i = 0; i < 2; ++i)
      dma16((unsigned)((lane >> 1) * 1280 + (lane & 1) * 16), w2 + (size_t)pks * 32 * 1280 + (128 * pcg + 32 * wi + 16 * i) * 2, lds0 + W2P + wi * 2048 + i * 1024);
    if (w == 4) *(LAS unsigned*)(L + lane * 4) = 0u;
    bar();
    make_kix(kix, ts);
    waitv<0>();
    PIN(E); PIN(xa); PIN(xb); PIN(xc);
    ST(6);
  const u64 tag64 = (((u64)RFL(E.y) << 32) | (unsigned)RFL(E.x)) + 1ull;
  tlo = (unsigned)tag64; thi = (unsigned)(tag64 >> 32);
  if (wi == 0) { lds_set(L, W_TAG, tlo); lds_set(L, W_TAG + 4, thi); lds_set(L, W_TAG + 8, 1); }
  {
    f32x2 v[10] = {unpk(xa.x), unpk(xa.y), unpk(xa.z), unpk(xa.w), unpk(xb.x), unpk(xb.y), unpk(xb.z), unpk(xb.w), unpk(xc.x), unpk(xc.y)};
    f32x2 s2 = ((v[0] + v[1]) + (v[2] + v[3])) + ((v[4] + v[5]) + (v[6] + v[7])) + (v[8] + v[9]);
    const float mean = half_sum(s2.x + s2.y, h) * (1.0f / 640.0f);
    f32x2 q2 = f32x2{0.f, 0.f};
#pragma unroll
    for (int i = 0; i < 10; ++i) { const f32x2 d = v[i] - mean; q2 = __builtin_elementwise_fma(d, d, q2); }
    const float var = rbf(half_sum(q2.x + q2.y, h) * (1.0f / 640.0f));
    const float m = rbf(mean), sd = rbf(__builtin_amdgcn_sqrtf(rbf(var + 1e-5f))), rs = __builtin_amdgcn_rcpf(sd);
    unsigned o[10];
#pragma unroll
    for (int i = 0; i < 10; ++i) { const f32x2 t = (v[i] - m) * rs; o[i] = pk2(t.x, t.y); }
    LAS char* rp = L + LN_ROWS + (2 * wi + h) * LN_PITCH;
    *(LAS v4u*)(rp + l31 * 16) = v4u{o[0], o[1], o[2], o[3]};
    *(LAS v4u*)(rp + 512 + l31 * 16) = v4u{o[4], o[5], o[6], o[7]};
    *(LAS v2u*)(rp + 1024 + l31 * 8) = v2u{o[8], o[9]};
    lds_arrive(L, W_SB, lane);
    if (l31 == 0) st64c(WS_STATS + (unsigned)(8 * wid + 2 * wi + h) * 8, ws,
                        v2u{(__builtin_bit_cast(unsigned, m) & 0xffff0000u) | (__builtin_bit_cast(unsigned, sd) >> 16), tlo});
  }
#pragma unroll
  for (int i = 0; i < 2; ++i) {
    LAS char* sp = L + W2P + wi * 2048 + i * 1024 + (16 * (g >> 1) + 4 * (g & 1) + qq) * 32 + 8 * p4;
    const v2u lo = trread(sp), hi = trread(sp + 256);
    const int ntg = 8 * pcg + 2 * wi + i;
    if (b < 240) st128c((unsigned)lane * 16, ws + WS_W2T + (size_t)(((ntg / 5) * 48 + pks) * 5 + ntg % 5) * 1024, v4u{lo.x, lo.y, hi.x, hi.y});
  }
  lds_wait_ge(L, W_SB, 4);
  {
    LAS char* sp = L + LN_ROWS + qq * LN_PITCH + (160 * wi + 16 * g) * 2 + 8 * p4;
#pragma unroll
    for (int i = 0; i < 3; ++i) {
      const v2u lo = trread(sp + 128 * i), hi = trread(sp + 128 * i + 4 * LN_PITCH);
      const unsigned ch = 64 * i + lane;
      if (i < 2 || lane < 32) st128c((ch / 5) * 20480 + (ch % 5) * 16, ws + WS_XNB + (size_t)wi * 32 * 20480 + wid * 80, v4u{lo.x, lo.y, hi.x, hi.y});
    }
  }
  }
  asm volatile("; AUDITCLR");
#pragma unroll
  for (int i = 0; i < 7; ++i) PIN(pr[i]);
  ST(1);
  {
    f32x4 kacc[2][2];
    const float scale = kgen_task(pr, w >= 6 && lane >= 48, lds0 + KG_SCR + w * KG_WSTR, L + KG_SCR + w * KG_WSTR + l15 * 272 + g * 16, kacc, [&](int i) {
      if (i == 2) {
        if (w < 4) issue(1);
        else { waitv<0>(); arrive_flag8(L, W_F1C, ws, WS_F1 + (unsigned)(lane & 7) * 2048 + wid * 8, tlo, thi); }
      }
    });
    kgen_store((LAS float*)(L + KG_SCR + w * KG_WSTR), kacc, scale, lane);
    lds_arrive(L, W_KG, lane);
  }
  ST(2);
  auto mish1 = [&](f32x4 a0, f32x4 a1, int n, int r, float rs, float mrs, unsigned ao, unsigned co) {
    unsigned pk[4];
#pragma unroll
    for (int c = 0; c < 2; ++c) {
      LAS char* cp = L + co + (32 * n + 16 * r + 8 * c) * 4;
      const f32x4 cs = *(LAS f32x4*)cp, bv = *(LAS f32x4*)(cp + 384), av = c ? a1 : a0;
#pragma unroll
      for (int e = 0; e < 2; ++e) {
        const f32x2 h2 = __builtin_elementwise_fma(e ? av.zw : av.xy, f32x2{rs, rs},
                                                   __builtin_elementwise_fma(f32x2{mrs, mrs}, e ? cs.zw : cs.xy, e ? bv.zw : bv.xy));
        const f32x2 s1 = f32x2{__builtin_amdgcn_exp2f(h2.x), __builtin_amdgcn_exp2f(h2.y)} + 1.0f;
        const f32x2 d = __builtin_elementwise_fma(s1, s1, f32x2{1.f, 1.f});
        const f32x2 rr = f32x2{__builtin_amdgcn_rcpf(d.x), __builtin_amdgcn_rcpf(d.y)};
        const f32x2 o = h2 * __builtin_elementwise_fma(rr, f32x2{-1.38629436111989062f, -1.38629436111989062f},
                                                       f32x2{0.69314718055994531f, 0.69314718055994531f});
        pk[2 * c + e] = pk2(o.x, o.y);
      }
    }
    st128c(ao, ws + WS_ACT + (size_t)((rb1 * 8 + 2 * wi) * 48 + 3 * ct1 + n) * 1024 + r * 512, v4u{pk[0], pk[1], pk[2], pk[3]});
  };
#define MISH_SETUP() const float rs = __builtin_amdgcn_rcpf(bf2f(gran.x)) * 1.44269504088896341f, mrs = -bfhi(gran.x) * rs; \
  const unsigned ao = (unsigned)((l31 >> 4) * 49152 + h * 256 + (4 * ((l15 & 7) >> 1) + (l15 & 1) + 2 * (l15 >> 3)) * 16), co = CSF + wi * 768 + h * 16

  if (w < 4) {
    issue(2);
    int lane = __builtin_amdgcn_mbcnt_hi(~0u, __builtin_amdgcn_mbcnt_lo(~0u, 0u)); PIN(lane);
    const int l15 = lane & 15, g = lane >> 4, qq = (lane >> 2) & 3, p4 = lane & 3, l31 = lane & 31, h = lane >> 5;
    const unsigned vo = lane * 16;
    f32x16 acc1[3]; f32x4 csa[3];
#pragma unroll
    for (int n = 0; n < 3; ++n) {
      csa[n] = f32x4{0.f, 0.f, 0.f, 0.f};
#pragma unroll
      for (int i = 0; i < 16; ++i) acc1[n][i] = 0.f;
    }
    unsigned xo[4], wo[4];
#pragma unroll
    for (int k = 0; k < 4; ++k) {
      const int kr = (k + wi) & 3;
      xo[k] = G1_RING + (32 * wi + l31) * 128 + (((2 * kr + h) ^ ((l31 >> 1) & 7)) * 16);
      wo[k] = G1_RING + G1_BOFF + (16 * kr + 8 * h + qq) * 192 + 32 * (g & 1) + 8 * p4;
      PIN(xo[k]); PIN(wo[k]);
    }
    const unsigned sel1 = ((l15 ^ g) & 1) ? 0u : 0x3f803f80u;
    const v4u sel = v4u{sel1, sel1, sel1, sel1};
    v4u X[4], W[4][3];
    auto rdw = [&](int s, int k, int n) {
      LAS char* p = L + wo[k] + (s % 3) * G1_STAGE + 64 * n; const v2u lo = trread(p), hi = trread(p + 768); W[k][n] = v4u{lo.x, lo.y, hi.x, hi.y};
    };
    auto rdx = [&](int s, int k) { X[k] = *(LAS v4u*)(L + xo[k] + (s % 3) * G1_STAGE); };
#define MM(k, n) acc1[n] = mfma32(W[k][n], X[k], acc1[n])
#define CS(n) csa[n] = mfma(W[0][n], sel, csa[n])
    v2u gran; unsigned bb;
    v4u WF[6][5], AF[6][4];
    const unsigned go = WS_STATS + (unsigned)(rb1 * 128 + 32 * wi + l31) * 8;
    auto wl1 = [&](int s, int e0) { const int e = 7 * (s - 7) + e0; if (s >= 7 && e0 < 7 && e < 20) ld128c<0>(WF[e / 5][e % 5], vo, w2b + rot12(r2 + e / 5) * 5120 + (e % 5) * 1024); };
    waitv<14>();
    lds_set(L, W_RDY + wi * 4, 1); lds_wait_min<4>(L, W_RDY, 1, lane);
    ST(3);
    rdx(0, 0); rdw(0, 0, 0); rdw(0, 0, 1); rdw(0, 0, 2); rdx(0, 1); rdw(0, 1, 0); rdw(0, 1, 1); rdw(0, 1, 2);
#pragma unroll
    for (int s = 0; s < 10; ++s) {
      const bool refill = s < 7;
      const char* pa = g1a + slab(s + 3) * 128; const char* pb = g1b + (size_t)slab(s + 3) * 64 * 3072;
      SCHED();
      MM(0, 0); rdx(s, 2); rdw(s, 2, 0); SCHED();
      MM(0, 1); rdw(s, 2, 1); rdw(s, 2, 2); SCHED();
      MM(0, 2); rdx(s, 3); rdw(s, 3, 0); SCHED();
      CS(0); rdw(s, 3, 1); SCHED();
      CS(1); rdw(s, 3, 2); SCHED();
      CS(2);
      if (s < 7) waitv<7>(); else if (s == 7) waitv<9>(); else if (s == 8) waitv<9>();
      if (s == 9 && l15 < 2) {
#pragma unroll
        for (int n = 0; n < 3; ++n) *(LAS f32x4*)(L + CSP + wi * 384 + (32 * n + 16 * l15 + 4 * g) * 4) = csa[n];
      }
      lds_set(L, W_RDY + wi * 4, s + 2);
      SCHED();
      MM(1, 0);
      if (refill) { asm volatile("s_waitcnt lgkmcnt(7)" ::: "memory"); dma16(voA[0], pa, la + (s % 3) * G1_STAGE); }
      wl1(s, 0);

      SCHED();
      MM(1, 1); if (refill) dma16o<1024>(voA[1], pa); wl1(s, 1);
      unsigned fl = 0;
      if (s < 9) fl = lw(L, W_RDY + p4 * 4);
      SCHED();
      MM(1, 2); if (refill) dma16o<2048>(voA[2], pa); wl1(s, 2); SCHED();
      MM(2, 0); if (refill) dma16o<3072>(voA[3], pa); wl1(s, 3);
      SCHED();
      if (s == 9) { MM(2, 1); wl1(s, 4); SCHED(); MM(2, 2); wl1(s, 5); SCHED(); MM(3, 0); wl1(s, 6); SCHED(); MM(3, 1); MM(3, 2); SCHED(); break; }
      if (__builtin_expect(__builtin_amdgcn_ballot_w64(fl < (unsigned)(s + 2)) != 0ull, 0)) lds_wait_min<4>(L, W_RDY, s + 2, lane);
      FENCE();
      SCHED();
      rdx(s + 1, 0); rdw(s + 1, 0, 0); SCHED();
      MM(2, 1); rdw(s + 1, 0, 1); rdw(s + 1, 0, 2); if (refill) dma16(voB[0], pb, lb + (s % 3) * G1_STAGE); wl1(s, 4); SCHED();
      MM(2, 2); rdx(s + 1, 1); rdw(s + 1, 1, 0); if (refill) dma16o<1024>(voB[1], pb); wl1(s, 5); SCHED();
      MM(3, 0); rdw(s + 1, 1, 1); if (refill) dma16o<2048>(voB[2], pb); wl1(s, 6); SCHED();
      MM(3, 1); rdw(s + 1, 1, 2); wl1(s, 7); SCHED();
      MM(3, 2); wl1(s, 8);
      if (s == 6) { lds_wait_ge(L, W_F1ALL, 4); ld64c(gran, go, ws); ld32<0>(bb, (unsigned)(lane < 48 ? lane : 47) * 4, b1 + ct1 * 192); }
      SCHED();
      if (s == 0) ST(4); if (s == 3) ST(5); if (s == 6) ST(7);
    }
#undef MM
#undef CS
    ST(11);
    const unsigned tlo = RFL(lw(L, W_TAG)), thi = RFL(lw(L, W_TAG + 4));
    waitv<20>(); PIN(gran); PIN(bb);
    MISH_SETUP();
    lds_wait_min<4>(L, W_RDY, 11, lane);
    if (lane < 48) {
      LAS f32x2* p = (LAS f32x2*)(L + CSP + lane * 8);
      *(LAS f32x2*)(L + CSF + wi * 768 + lane * 8) = (p[0] + p[48]) + (p[96] + p[144]);
      *(LAS f32x2*)(L + CSF + wi * 768 + 384 + lane * 8) = unpk(bb) * 1.44269504088896341f;
    }
    FENCE();
    ST(12);
#pragma unroll
    for (int n = 0; n < 3; ++n) { mish1(acc1[n].s0123, acc1[n].s4567, n, 0, rs, mrs, ao, co); mish1(acc1[n].s89ab, acc1[n].scdef, n, 1, rs, mrs, ao, co); }
    lds_set(L, W_RDY + wi * 4, 12);
    ST(13);
    __builtin_amdgcn_s_setprio(2);
    LATE_LANE();
    v2u xq[5], dq[5], gr2, f3;
    const unsigned f2o = WS_F2 + (unsigned)((4 * rg + (a2 >> 1)) * 16 + 4 * wi + (ln_ & 3)) * 128;
    unsigned vm[4];
#pragma unroll
    for (int m = 0; m < 4; ++m) vm[m] = vo + ((m + wi) & 3) * 49152;
    v2u f2; v4u ysh;
    {
      f32x4 sacc[4];
#pragma unroll
      for (int i = 0; i < 4; ++i) sacc[i] = f32x4{0.f, 0.f, 0.f, 0.f};
      unsigned kb = CVS + KAO - wi * 128 - 3584 - 64, ubb = CVS + UBO - wi * 128 - 64; PIN(kb); PIN(ubb);
      LAS char* ka = L + kb; LAS char* ub = L + ubb;
      const bool late = RFL(lw(L, W_SB)) < 16;
      if (late) { waitv<0>(); arrive_flag<4>(L, W_F2C, lane, ws, WS_F2 + (unsigned)wid * 128, tlo, thi); }
      lds_wait_ge(L, W_SB, 16);
      if (par) conv_unit_shared<1>(ka + 64, ub + 64, sacc); else conv_unit_shared<0>(ka + 64, ub + 64, sacc);
      if (!late) { waitv<0>(); arrive_flag<4>(L, W_F2C, lane, ws, WS_F2 + (unsigned)wid * 128, tlo, thi); }
      ST(14);
      WL(4); WL(5);
      if (par) conv_unit_shared<1>(ka, ub, sacc); else conv_unit_shared<0>(ka, ub, sacc);
#pragma unroll
      for (int q = 0; q < 4; ++q) *(LAS f32x4*)(L + MXCH + (q * 4 + wi) * 1024 + vo) = sacc[q];
      lds_arrive(L, W_MB, lane);
    {
      const char* xb2 = x + (size_t)row2 * 1280 + cb2 * 160; const unsigned xo2 = OROW * 1280 + cg * 8;
      ld64o<0>(xq[0], xo2, xb2); ld64o<32>(xq[1], xo2, xb2); ld64o<64>(xq[2], xo2, xb2); ld64o<96>(xq[3], xo2, xb2); ld64o<128>(xq[4], xo2, xb2);
      const char* db = pDskip + cb2 * 160;
      ld64o<0>(dq[0], cg * 8, db); ld64o<32>(dq[1], cg * 8, db); ld64o<64>(dq[2], cg * 8, db); ld64o<96>(dq[3], cg * 8, db); ld64o<128>(dq[4], cg * 8, db);
      ld64c(gr2, WS_STATS + (row2 + OROW) * 8, ws);
    }
      lds_wait_ge(L, W_MB, 4);
      LAS char* p = L + MXCH + wi * 4096 + vo;
      const f32x4 s = add4(add4(*(LAS f32x4*)p, *(LAS f32x4*)(p + 1024)), add4(*(LAS f32x4*)(p + 2048), *(LAS f32x4*)(p + 3072)));
      ld64c(f2, f2o, ws);
      const unsigned y0 = pk2(s.x, s.y), y1 = pk2(s.z, s.w);
      ysh = v4u{y0, y1, nbr(y0), nbr(y1)};
    }
    ST(15);
    waitv<0>(); PIN(f2);
    f2 = spin64<false>(f2, f2o, ws, tlo, thi);
    lds_set(L, W_F2OK + wi * 4, 1);
    ST(16);
    AL(0); AL(1);
    if (!(ln_ & 1)) st128c(YOFF, ws + WS_Y + (size_t)chS * 4096 + (2 * wi + ((wi & 1) ^ par)) * 512, ysh);
    f32x4 acc2[4][5];
    PIN(gr2);
    const float rs2 = __builtin_amdgcn_rcpf(bf2f(gr2.x)), m2 = -bfhi(gr2.x) * rs2;
#pragma unroll
    for (int n = 0; n < 5; ++n) {
      PIN(xq[n]); PIN(dq[n]);
      const f32x2 x0 = unpk(xq[n].x), x1 = unpk(xq[n].y), d0 = unpk(dq[n].x), d1 = unpk(dq[n].y);
      acc2[0][n] = f32x4{ffma(d0.x, ffma(x0.x, rs2, m2), x0.x), ffma(d0.y, ffma(x0.y, rs2, m2), x0.y),
                         ffma(d1.x, ffma(x1.x, rs2, m2), x1.x), ffma(d1.y, ffma(x1.y, rs2, m2), x1.y)};
      PIN(acc2[0][n]);
    }
    __builtin_amdgcn_s_setprio(0);
    ST(17);
    waitv<4>(); g2_step<1>(acc2, WF[0], AF[0], [&](int i) { if (i >= 3 && (i & 1)) AL1(2, (i - 3) >> 1); });
    waitv<4>();
    arrive_flag<8>(L, W_F3C, lane, ws, WS_F3 + (unsigned)(xcd * 32 + j) * 128, tlo, thi);
    g2_step<4>(acc2, WF[1], AF[1], [&](int i) { if (i >= 2) AL1(3 + ((i - 2) >> 2), (i - 2) & 3); });
    waitv<8>(); g2_step<4>(acc2, WF[2], AF[2], [&](int i) { if (i < 4) AL1(5, i); });
    ST(20);
    waitv<8>(); g2_step<4>(acc2, WF[3], AF[3], NOLD);
    waitv<4>(); g2_step<4>(acc2, WF[4], AF[4], NOLD);
    lds_wait_ge(L, W_F3C, 8);
    const unsigned f3o = WS_F3 + (unsigned)(cb2 * 32 + 8 * wi + (ln_ & 7)) * 128;
    ld64c(f3, f3o, ws);
    waitv<1>(); g2_step<4>(acc2, WF[5], AF[5], NOLD);
    ST(22);
    waitv<0>(); PIN(f3);
    f3 = spin64<false>(f3, f3o, ws, tlo, thi);
    lds_arrive(L, W_F3ALL, lane);
#pragma unroll
    for (int n = 0; n < 5; ++n) *(LAS f32x4*)(L + FM + wi * 15360 + n * 1024 + vo) = acc2[1][n];
    lds_wait_ge(L, W_F3ALL, 4);
    ST(21);
    v4u yv[3];
    {
      const char* yb = ws + WS_Y + (size_t)cb2 * 80 * 4096 + row2 * 2;
      const unsigned yo = (unsigned)(ln_ >> 1) * 4096 + (ln_ & 1) * 16;
      ld128c<0>(yv[0], yo, yb); ld128c<0>(yv[1], yo, yb + 32 * 4096); ld128c<0>(yv[2], yo & 0xffffu, yb + 64 * 4096);
    }
#pragma unroll
    for (int m = 2; m < 4; ++m)
#pragma unroll
      for (int n = 0; n < 5; ++n) *(LAS f32x4*)(L + FM + wi * 15360 + (m - 1) * 5120 + n * 1024 + vo) = acc2[m][n];
    lds_arrive(L, W_FM, lane);
    lds_wait_ge(L, W_FM, 4);
#pragma unroll
    for (int d = 1; d < 4; ++d)
#pragma unroll
      for (int n = 0; n < 5; ++n) acc2[0][n] = add4(acc2[0][n], *(LAS f32x4*)(L + FM + ((wi - d) & 3) * 15360 + (d - 1) * 5120 + n * 1024 + vo));
    waitv<0>(); PIN(yv[0]); PIN(yv[1]); PIN(yv[2]);
    LAS char* ys = L + YST + wi * 2560;
    *(LAS v4u*)(ys + vo) = yv[0]; *(LAS v4u*)(ys + 1024 + vo) = yv[1];
    if (ln_ < 32) *(LAS v4u*)(ys + 2048 + vo) = yv[2];
    FENCE();
#pragma unroll
    for (int n = 0; n < 5; ++n) {
      const v2u t = trread(ys + n * 512 + ln_ * 8);
      const f32x2 t0 = unpk(t.x), t1 = unpk(t.y);
      *(LAS f32x4*)(L + FM + 61440 + wi * 5120 + n * 1024 + vo) = add4(acc2[0][n], f32x4{t0.x, t0.y, t1.x, t1.y});
    }
    lds_set(L, W_F2OK + wi * 4, 2);
    STEND();
    asm volatile("97:");
    __builtin_amdgcn_endpgm();
  }

  const bool odd = wi & 1;
  const int kch = chA + (wi >> 1);
  const unsigned vo = lane * 16;
  v4u WF[6][5], AF[6][4];
  auto reduce = [&](int c, int i, bool three) {
    LAS char* pp = L + KG_SCR + 3 * c * KG_WSTR;
    f32x4 s = fma4(*(LAS f32x4*)(pp + i * 16), *(LAS float*)(pp + 2576), f32x4{0.f, 0.f, 0.f, 0.f});
    s = fma4(*(LAS f32x4*)(pp + KG_WSTR + i * 16), *(LAS float*)(pp + KG_WSTR + 2576), s);
    if (three) s = fma4(*(LAS f32x4*)(pp + 2 * KG_WSTR + i * 16), *(LAS float*)(pp + 2 * KG_WSTR + 2576), s);
    *(LAS f32x4*)(L + KTAB + c * KP_STRIDE + i * 16) = s;
    return s;
  };
  lds_wait_ge(L, W_KG, 8);
  if (ts < 161) { st128c((unsigned)ts * 16, ws + WS_KP + (size_t)wid * KP_STRIDE, __builtin_bit_cast(v4u, reduce(2, ts, false))); reduce(0, ts, true); }
  if (ts >= 95) reduce(1, ts - 95, true);
  if (ts < 3) *(LAS float*)(L + KTAB + ts * KP_STRIDE - 4) = 0.f;
  lds_arrive(L, W_SB, lane);
  waitv<0>();
  arrive_flag(L, W_FKC, lane, ws, WS_FK + (unsigned)wid * 64, tlo, thi);
  ST(9);
  __builtin_amdgcn_s_setprio(0);
  lds_wait_min<4>(L, W_RDY, F1STAGE, lane);
  const unsigned f1o = WS_F1 + (unsigned)xcd * 2048 + ts * 8;
  v2u f1own; ld64c(f1own, f1o, ws);
  waitv<0>(); PIN(f1own);
  f1own = spin64<false>(f1own, f1o, ws, tlo, thi);
  v4u uu[3]; unsigned ulo[3];
#pragma unroll
  for (int r = 0; r < 3; ++r) {
    const int e = 64 * r + lane, tb = 64 * wi + e / 3, c = e % 3;
    ld128c<0>(uu[r], tb * 80 + c * 16, ws + WS_XNB + (size_t)(16 * xcd + (j >> 1)) * 20480 + par * 32);
    ulo[r] = (c == 2 - 2 * par ? CVS : CVA + (c - par) * CV_CH) + tb * 16;
  }
  if (b == 0) arrive_flag(L, W_F1ALL, lane, ws, WS_E, tlo, thi);
  else lds_arrive(L, W_F1ALL, lane);
  ST(4);
  const unsigned fko = WS_FK + (unsigned)(wid ^ 1) * 64;
  v2u fk; ld64c(fk, fko, ws);
  waitv<0>(); PIN(fk); PIN(uu[0]); PIN(uu[1]); PIN(uu[2]);
  fk = spin64<false>(fk, fko, ws, tlo, thi);
  v4u kp; ld128c<0>(kp, (unsigned)(ts < 161 ? ts : 160) * 16, ws + WS_KP + (size_t)(wid ^ 1) * KP_STRIDE);
  lds_wait_ge(L, W_SB, 8);
  waitv<0>(); PIN(kp);
  if (ts < 161) { LAS f32x4* tp = (LAS f32x4*)(L + KTAB + 2 * KP_STRIDE + ts * 16); *tp = add4(*tp, __builtin_bit_cast(f32x4, kp)); }
  lds_arrive(L, W_SB, lane);
  lds_wait_ge(L, W_F1ALL, 4);
  WL(0);
  lds_wait_min<4>(L, W_RDY, 9, lane);
  put_k(L, kix, ts, 0, CVA);
  WL(1);
  lds_wait_min<4>(L, W_RDY, 10, lane);
  put_k(L, kix, ts, 1, CVA + CV_CH);
  if (ZCOND) { *(LAS v4u*)(L + CVA + ZOFF) = v4u{0u, 0u, 0u, 0u}; *(LAS v4u*)(L + CVA + CV_CH + ZOFF) = v4u{0u, 0u, 0u, 0u}; }
  ST(7);
  lds_wait_min<4>(L, W_RDY, 11, lane);
  ST(11);
  put_u(L + ulo[0], uu[0], ts < 3); put_u(L + ulo[1], uu[1], false); put_u(L + ulo[2], uu[2], false);
  if (ZCOND) *(LAS v4u*)(L + CVS + ZOFF) = v4u{0u, 0u, 0u, 0u};
  lds_wait_ge(L, W_SB, 12);
  put_k(L, kix, ts, 2, CVS);
  lds_arrive(L, W_SB, lane);
  WL(2); WL(3); WL(4); WL(5);
  ST(5);
  lds_wait_ge(L, W_SB, 16);
  lds_wait_ge(L, W_RDY + wi * 4, 12);
  ST(13);
  __builtin_amdgcn_s_setprio(SPRIO2);
  LATE_LANE();
  f32x4 cacc[8];
#pragma unroll
  for (int i = 0; i < 8; ++i) cacc[i] = f32x4{0.f, 0.f, 0.f, 0.f};
  {
    const unsigned o1 = CVA + (wi >> 1) * CV_CH - (odd ? 4 * 64 : 0);
    unsigned kb = KAO + o1 - 3584 - 192, ubb = UBO + o1 - 192; PIN(kb); PIN(ubb);
    LAS char* ka = L + kb; LAS char* ub = L + ubb;
    v4u A[8], B[8];
#define LA(i, u) (*(LAS v4u*)(ka + 512 * (7 - (i)) + 64 * (3 - (u))))
#define LB(i, u) (*(LAS v4u*)(ub + 512 * (i) + 64 * (3 - (u))))
    A[7] = LA(7, 0); B[0] = LB(0, 0); A[0] = LA(0, 0); B[7] = LB(7, 0); A[6] = LA(6, 0); B[6] = LB(6, 0); A[1] = LA(1, 0); B[1] = LB(1, 0);
    A[5] = LA(5, 0); B[5] = LB(5, 0); A[2] = LA(2, 0); B[2] = LB(2, 0); A[4] = LA(4, 0); B[4] = LB(4, 0); A[3] = LA(3, 0); B[3] = LB(3, 0);
    SCHED();
#pragma unroll
    for (int u = 0; u < 4; ++u) {
      v4u An[2], Bn[2];
#pragma unroll
      for (int sd = 7; sd >= 0; --sd) {
#pragma unroll
        for (int o = 0; o <= sd; ++o) {
          if (sd + o < 8) cacc[sd + o] = mfma(A[sd], B[o], cacc[sd + o]);
          if (o != sd && sd + o < 8) cacc[sd + o] = mfma(A[o], B[sd], cacc[sd + o]);
        }
        SCHED();
        if (u < 3) {
          if (sd >= 2) { A[sd] = LA(sd, u + 1); B[sd] = LB(sd, u + 1); }
          if (sd == 6 || sd == 5) { An[6 - sd] = LA(6 - sd, u + 1); Bn[6 - sd] = LB(6 - sd, u + 1); }
        }
        SCHED();
      }
      if (u < 3) { A[0] = An[0]; A[1] = An[1]; B[0] = Bn[0]; B[1] = Bn[1]; }
    }
#undef LA
#undef LB
  }
  ST(10);
  unsigned vm[4];
  {
    LAS char* px = L + SXCH + wi * 4096 + vo;
    if (odd) {
#pragma unroll
      for (int i = 0; i < 4; ++i) { *(LAS f32x4*)(px + i * 1024) = cacc[i]; cacc[i] = cacc[4 + i]; }
    } else {
#pragma unroll
      for (int i = 0; i < 4; ++i) *(LAS f32x4*)(px + i * 1024) = cacc[4 + i];
    }
    lds_arrive(L, W_SX + (wi >> 1) * 4, lane);
#pragma unroll
    for (int m = 0; m < 4; ++m) vm[m] = vo + ((m + wi) & 3) * 49152;
    lds_wait_ge(L, W_SX + (wi >> 1) * 4, 2);
    px = L + SXCH + (wi ^ 1) * 4096 + vo;
    v2u yo[4];
#pragma unroll
    for (int i = 0; i < 4; ++i) { const f32x4 s = add4(cacc[i], *(LAS f32x4*)(px + i * 1024)); yo[i] = v2u{pk2(s.x, s.y), pk2(s.z, s.w)}; }
    char* yb = ws + WS_Y + (size_t)kch * 4096 + (odd ? 2048 : 0);
    const bool oddl = ln_ & 1;
    const unsigned yo16 = YOFF + (oddl ? 504 : 0);
    st128c(yo16, yb, pair16(yo[0], yo[1], oddl)); st128c(yo16, yb + 1024, pair16(yo[2], yo[3], oddl));
  }
  ST(14);
  lds_wait_ge(L, W_F2OK + wi * 4, 1);
  ST(16);
  AL(0); AL(1);
  ST(17);
  f32x4 acc2[4][5];
  waitv<8>();
  arrive_flag<8>(L, W_F3C, lane, ws, WS_F3 + (unsigned)(xcd * 32 + j) * 128, tlo, thi);
  __builtin_amdgcn_s_setprio(0);
  waitv<4>(); g2_step<0>(acc2, WF[0], AF[0], [&](int i) { if (i >= 3 && (i & 1)) AL1(2, (i - 3) >> 1); });
  waitv<4>(); g2_step<4>(acc2, WF[1], AF[1], [&](int i) { if (i >= 2) AL1(3 + ((i - 2) >> 2), (i - 2) & 3); });
  waitv<8>(); g2_step<4>(acc2, WF[2], AF[2], [&](int i) { if (i < 4) AL1(5, i); });
  ST(20);
  waitv<8>(); g2_step<4>(acc2, WF[3], AF[3], NOLD);
  lds_wait_ge(L, W_F3C, 8);
  waitv<0>();
#pragma unroll
  for (int m = 0; m < 4; ++m) { PIN(AF[4][m]); PIN(AF[5][m]); }
#pragma unroll
  for (int n = 0; n < 5; ++n) { PIN(WF[4][n]); PIN(WF[5][n]); }
#pragma unroll
  for (int mm = 1; mm < 5; ++mm) {
    const int m = mm & 3;
#pragma unroll
    for (int n = 0; n < 5; ++n) {
      acc2[m][n] = mfma(WF[4][n], AF[4][m], acc2[m][n]);
      acc2[m][n] = mfma(WF[5][n], AF[5][m], acc2[m][n]);
      if (mm > 1) *(LAS f32x4*)(L + FS + wi * 15360 + (mm - 2) * 5120 + n * 1024 + vo) = acc2[mm - 1][n];
      SCHED();
    }
  }
  lds_arrive(L, W_FS, lane);
  ST(22);
  lds_wait_ge(L, W_FS, 4);
#pragma unroll
  for (int d = 1; d < 4; ++d)
#pragma unroll
    for (int n = 0; n < 5; ++n) acc2[0][n] += *(LAS f32x4*)(L + FS + ((wi - d) & 3) * 15360 + (d - 1) * 5120 + n * 1024 + vo);
  ST(23);
  lds_wait_ge(L, W_F2OK + wi * 4, 2);
  ST(24);
  {
    char* ob = out + (size_t)row2 * 1280 + cb2 * 160; const unsigned oo = OROW * 1280 + cg * 8;
    v2u o[5];
#pragma unroll
    for (int n = 0; n < 5; ++n) {
      const f32x4 s = acc2[0][n] + *(LAS f32x4*)(L + FM + 61440 + wi * 5120 + n * 1024 + vo);
      o[n] = v2u{pk2(s.x, s.y), pk2(s.z, s.w)};
    }
    st64o<0>(oo, ob, o[0]); st64o<32>(oo, ob, o[1]); st64o<64>(oo, ob, o[2]); st64o<96>(oo, ob, o[3]); st64o<128>(oo, ob, o[4]);
  }
  STEND();
  asm volatile("99:");
}

extern "C" void kernel_launch(void* const* d_in, const int* in_sizes, int n_in, void* d_out, int out_size, void* d_ws, size_t ws_size,
                              hipStream_t stream) {
  fused_s4_mlp<<<256, 512, 0, stream>>>((const char*)d_in[0], (const char*)d_in[1], (const char*)d_in[2], (const char*)d_in[3],
      (const char*)d_in[4], (const char*)d_in[5], (const char*)d_in[6], (const char*)d_in[7], (const char*)d_in[8], (const char*)d_in[10],
      (const char*)d_in[11], (const char*)d_in[12], (char*)d_out, (char*)d_ws);
}
```

```cpp
#include <hip/hip_runtime.h>
#include <stdint.h>

typedef unsigned short u16;
typedef unsigned long long u64;
typedef unsigned v4u __attribute__((ext_vector_type(4)));
typedef unsigned v2u __attribute__((ext_vector_type(2)));
typedef float f32x4 __attribute__((ext_vector_type(4)));
typedef float f32x2 __attribute__((ext_vector_type(2)));
typedef short bf16x8 __attribute__((ext_vector_type(8)));
typedef short s16x4 __attribute__((ext_vector_type(4)));
typedef __bf16 bf16x2_t __attribute__((ext_vector_type(2)));
#define LAS __attribute__((address_space(3)))
#define GAS __attribute__((address_space(1)))
#define DI __device__ __forceinline__

constexpr unsigned WS_E = 0, WS_TMO = 64, WS_F1 = 131072, WS_STATS = 16384, WS_F2 = 32768, WS_F3 = 65536, WS_FK = 98304, WS_STAMP = 262144;
constexpr unsigned WS_XNB = 1u << 20, WS_W2T = 4u << 20, WS_Y = 7u << 20, WS_ACT = 10u << 20, WS_KP = 17u << 20;
constexpr unsigned KP_STRIDE = 2624;
constexpr unsigned SPIN_CAP = 400000;

constexpr unsigned W_F1C = 0, W_F2C = 4, W_F3C = 8, W_KG = 12, W_MB = 16, W_SB = 20, W_SX = 24, W_FM = 32, W_FS = 36, W_F3ALL = 40, W_F1ALL = 44,
                   W_F2OK = 48, W_RDY = 64, W_TAG = 80, W_FKC = 96;
constexpr unsigned KTAB = 272;
constexpr unsigned CSP = 8192;
constexpr unsigned CVA = 10240, CV_CH = 27008, CV_USTR = 4640, CV_KOFF = 18560, CV_KSTR = 4224;
constexpr unsigned KG_SCR = CVA, KG_WSTR = 4352, LN_ROWS = KG_SCR + 8 * KG_WSTR, LN_PITCH = 1344, W2P = LN_ROWS + 10752;
constexpr unsigned G1_RING = 64512, G1_STAGE = 28672, G1_BOFF = 16384;
constexpr unsigned CVS = G1_RING, SXCH = CVS + 27136, MXCH = SXCH + 16384;
constexpr unsigned CSF = G1_RING + 3 * G1_STAGE, YST = CSF + 3072;
constexpr unsigned FM = 256, FS = FM + 81920;
static_assert(FS + 61440 <= CSF && CVA + 2 * CV_CH <= G1_RING && W2P + 8192 <= G1_RING && YST + 10240 <= 163840 && MXCH + 16384 <= CSF, "lds map");

#define ROT2 0
#define F1STAGE 6
#define SPRIO2 1
#define F1GAP 16
#define F2GAP 8
#define SPRIO 1
#define ST(i) do {} while (0)
#define STEND() do {} while (0)
#define STARG
#define STPASS

DI float bf2f(unsigned v) { return __builtin_bit_cast(float, v << 16); }
DI float bfhi(unsigned v) { return __builtin_bit_cast(float, v & 0xffff0000u); }
DI unsigned pk2(float lo, float hi) { bf16x2_t r; r.x = (__bf16)lo; r.y = (__bf16)hi; return __builtin_bit_cast(unsigned, r); }
DI float rbf(float f) { return bf2f(pk2(f, f) & 0xffffu); }
DI f32x4 mfma(v4u a, v4u b, f32x4 c) {
  return __builtin_amdgcn_mfma_f32_16x16x32_bf16(__builtin_bit_cast(bf16x8, a), __builtin_bit_cast(bf16x8, b), c, 0, 0, 0);
}
DI v2u trread(LAS char* p) { return __builtin_bit_cast(v2u, __builtin_amdgcn_ds_read_tr16_b64_v4i16((LAS s16x4*)p)); }
template <int N> DI void waitv() { asm volatile("s_waitcnt vmcnt(%0)" ::"i"(N) : "memory"); }
DI void bar() { asm volatile("s_waitcnt lgkmcnt(0)\n\ts_barrier" ::: "memory"); }
#define PIN(x) asm volatile("" : "+v"(x))
#define FENCE() asm volatile("" ::: "memory")
#define SCHED() __builtin_amdgcn_sched_barrier(0)
#define RFL(x) __builtin_amdgcn_readfirstlane(x)

DI void ld16(unsigned& d, unsigned vo, const void* sb) { asm volatile("global_load_ushort %0, %1, %2" : "=v"(d) : "v"(vo), "s"(sb) : "memory"); }
template <int O> DI void ld32(unsigned& d, unsigned vo, const void* sb) { asm volatile("global_load_dword %0, %1, %2 offset:%3" : "=v"(d) : "v"(vo), "s"(sb), "i"(O) : "memory"); }
DI void ld64(v2u& d, unsigned vo, const void* sb) { asm volatile("global_load_dwordx2 %0, %1, %2" : "=v"(d) : "v"(vo), "s"(sb) : "memory"); }
DI void ld128(v4u& d, unsigned vo, const void* sb) { asm volatile("global_load_dwordx4 %0, %1, %2" : "=v"(d) : "v"(vo), "s"(sb) : "memory"); }
DI void ld64c(v2u& d, unsigned vo, const void* sb) { asm volatile("global_load_dwordx2 %0, %1, %2 sc1" : "=v"(d) : "v"(vo), "s"(sb) : "memory"); }
template <int O> DI void ld128c(v4u& d, unsigned vo, const void* sb) { asm volatile("global_load_dwordx4 %0, %1, %2 offset:%3 sc1" : "=v"(d) : "v"(vo), "s"(sb), "i"(O) : "memory"); }
DI void st64c(unsigned vo, void* sb, v2u d) { asm volatile("global_store_dwordx2 %0, %1, %2 sc1" ::"v"(vo), "v"(d), "s"(sb) : "memory"); }
template <int O> DI void ld64o(v2u& d, unsigned vo, const void* sb) { asm volatile("global_load_dwordx2 %0, %1, %2 offset:%3" : "=v"(d) : "v"(vo), "s"(sb), "i"(O) : "memory"); }
template <int O> DI void st64o(unsigned vo, void* sb, v2u d) { asm volatile("global_store_dwordx2 %0, %1, %2 offset:%3" ::"v"(vo), "v"(d), "s"(sb), "i"(O) : "memory"); }
template <int O> DI void st64co(unsigned vo, void* sb, v2u d) { asm volatile("global_store_dwordx2 %0, %1, %2 offset:%3 sc1" ::"v"(vo), "v"(d), "s"(sb), "i"(O) : "memory"); }
DI void st128c(unsigned vo, void* sb, v4u d) { asm volatile("global_store_dwordx4 %0, %1, %2 sc1\n\ts_nop 1" ::"v"(vo), "v"(d), "s"(sb) : "memory"); }
DI void st64u(unsigned vo, void* sb, v2u d) { asm volatile("global_store_dwordx2 %0, %1, %2 sc1 ; CNT" ::"v"(vo), "v"(d), "s"(sb) : "memory"); }
DI void st128u(unsigned vo, void* sb, v4u d) { asm volatile("global_store_dwordx4 %0, %1, %2 sc1 ; CNT\n\ts_nop 1" ::"v"(vo), "v"(d), "s"(sb) : "memory"); }
DI void dma16(unsigned vo, const void* sb, unsigned lds) {
  asm volatile("s_mov_b32 m0, %2\n\ts_nop 0\n\tglobal_load_lds_dwordx4 %0, %1" ::"v"(vo), "s"(sb), "s"(lds) : "memory");
}
template <int O> DI void dma16o(unsigned vo, const void* sb) {
  asm volatile("global_load_lds_dwordx4 %0, %1 offset:%2" ::"v"(vo), "s"(sb), "i"(O) : "memory");
}
template <bool HIONLY> DI v2u spin64(v2u cur, unsigned vo, char* ws, unsigned tlo, unsigned thi) {
  if (__builtin_expect(__builtin_amdgcn_ballot_w64(HIONLY ? (cur.y != tlo) : (cur.x != tlo || cur.y != thi)) != 0ull, 0)) {
#pragma nounroll
    for (unsigned spins = 0; spins < SPIN_CAP; ++spins) {
      __builtin_amdgcn_s_sleep(6);
      asm volatile("global_load_dwordx2 %0, %1, %2 sc1\n\ts_waitcnt vmcnt(0)" : "=&v"(cur) : "v"(vo), "s"(ws) : "memory");
      if (__builtin_amdgcn_ballot_w64(HIONLY ? (cur.y != tlo) : (cur.x != tlo || cur.y != thi)) == 0ull) break;
    }
  }
  return cur;
}
DI unsigned lw(LAS char* L, unsigned off) { return *(volatile LAS unsigned*)(L + off); }
DI void lds_wait_at(unsigned a, unsigned target) {
  unsigned v, cnt; u64 t;
  asm volatile("s_mov_b32 %1, %5\n"
               "1:\n\tds_read_b32 %0, %3\n\ts_sub_u32 %1, %1, 1\n\ts_waitcnt lgkmcnt(0)\n\tv_cmp_gt_u32 vcc, %4, %0\n\t"
               "s_cselect_b64 %2, 0, vcc\n\ts_cmp_lg_u64 %2, 0\n\ts_cbranch_scc1 1b"
               : "=&v"(v), "=&s"(cnt), "=&s"(t) : "v"(a), "s"(target), "i"(4 * SPIN_CAP) : "vcc", "scc", "memory");
}
DI void lds_wait_ge(LAS char* L, unsigned off, unsigned target) { lds_wait_at((unsigned)(size_t)L + off, target); }
template <int N> DI void lds_wait_min(LAS char* L, unsigned off, unsigned target, int lane) {
  lds_wait_at((unsigned)(size_t)L + off + (lane & (N - 1)) * 4, target);
}
DI void lds_set(LAS char* L, unsigned off, unsigned v) { FENCE(); *(volatile LAS unsigned*)(L + off) = v; FENCE(); }
DI void lds_arrive(LAS char* L, unsigned off, int) {
  asm volatile("s_mov_b64 exec, 1\n\tds_add_u32 %0, %1\n\ts_mov_b64 exec, -1" ::"v"((unsigned)(size_t)L + off), "v"(1u) : "memory");
}
template <unsigned N = 4> DI void arrive_flag(LAS char* L, unsigned off, int, char* ws, unsigned fo, unsigned tlo, unsigned thi) {
  unsigned old;
  asm volatile("s_mov_b64 exec, 1\n\tds_add_rtn_u32 %0, %1, %2\n\ts_waitcnt lgkmcnt(0)\n\tv_cmp_eq_u32 vcc, %6, %0\n\t"
               "s_mov_b64 exec, vcc\n\tglobal_store_dwordx2 %3, %4, %5 sc1\n\ts_mov_b64 exec, -1"
               : "=&v"(old) : "v"((unsigned)(size_t)L + off), "v"(1u), "v"(fo), "v"(v2u{tlo, thi}), "s"(ws), "i"(N - 1) : "vcc", "memory");
}
DI void arrive_flag8(LAS char* L, unsigned off, char* ws, unsigned fo, unsigned tlo, unsigned thi) {
  unsigned old;
  asm volatile("s_mov_b64 exec, 1\n\tds_add_rtn_u32 %0, %1, %2\n\ts_waitcnt lgkmcnt(0)\n\tv_cmp_eq_u32 vcc, 3, %0\n\t"
               "s_mul_i32 exec_lo, vcc_lo, 0xff\n\tglobal_store_dwordx2 %3, %4, %5 sc1\n\ts_mov_b64 exec, -1"
               : "=&v"(old) : "v"((unsigned)(size_t)L + off), "v"(1u), "v"(fo), "v"(v2u{tlo, thi}), "s"(ws) : "vcc", "memory");
}
typedef float f32x16 __attribute__((ext_vector_type(16)));
DI f32x16 mfma32(v4u a, v4u b, f32x16 c) {
  return __builtin_amdgcn_mfma_f32_32x32x16_bf16(__builtin_bit_cast(bf16x8, a), __builtin_bit_cast(bf16x8, b), c, 0, 0, 0);
}
template <int C> DI float dpp_add(float v) {
  return v + __builtin_bit_cast(float, __builtin_amdgcn_update_dpp(0, __builtin_bit_cast(int, v), C, 0xf, 0xf, true));
}
DI float rl(float v, int l) { return __builtin_bit_cast(float, __builtin_amdgcn_readlane(__builtin_bit_cast(int, v), l)); }
DI float half_sum(float v, bool upper) {
  v = dpp_add<0xB1>(v); v = dpp_add<0x4E>(v); v = dpp_add<0x141>(v); v = dpp_add<0x140>(v);
  const float a = rl(v, 0) + rl(v, 16), c = rl(v, 32) + rl(v, 48);
  return upper ? c : a;
}
DI f32x2 unpk(unsigned v) { return f32x2{bf2f(v), bfhi(v)}; }
typedef f32x2 cf;
DI cf cmul1(cf i, cf w) { return cf{__builtin_fmaf(i.x, w.x, -(i.y * w.y)), __builtin_fmaf(i.x, w.y, i.y * w.x)}; }
DI void cmul4(cf& o0, cf& o1, cf& o2, cf& o3, cf i0, cf i1, cf i2, cf i3, cf w0, cf w1, cf w2, cf w3) {
  o0 = cmul1(i0, w0); o1 = cmul1(i1, w1); o2 = cmul1(i2, w2); o3 = cmul1(i3, w3);
}
DI unsigned pkc(cf v) { return pk2(v.x, v.y); }
template <int R> DI void put4(cf a, cf b, cf c, cf d) {
  asm volatile("ds_write_addtid_b32 %0 offset:%4\n\tds_write_addtid_b32 %1 offset:%5\n\tds_write_addtid_b32 %2 offset:%6\n\tds_write_addtid_b32 %3 offset:%7"
               ::"v"(pkc(a)), "v"(pkc(b)), "v"(pkc(c)), "v"(pkc(d)), "i"(R * 272), "i"(R * 272 + 272), "i"(R * 272 + 544), "i"(R * 272 + 816) : "memory");
}
template <int R> DI void put1(cf a) { asm volatile("ds_write_addtid_b32 %0 offset:%1" ::"v"(pkc(a)), "i"(R * 272) : "memory"); }
DI cf cexpk(float e1, float th, float tl, float k, float ks) {
  const float mag = __builtin_amdgcn_exp2f(e1 * k);
  const float fr = __builtin_fmaf(tl, ks, __builtin_amdgcn_fractf(th * ks));
  return cf{__builtin_amdgcn_cosf(fr), __builtin_amdgcn_sinf(fr)} * mag;
}
DI void kgen_slow(cf a, cf W1, cf W256, cf W8, LAS char* sr, f32x4 (&kacc)[2][2]) {
  cf W2, W512, u1, W16, u2, u3, W4, W32, u4, u5, u6, u7, r8, r9, r10, r11, r12, r13, r14, r15, r16, r17, W1024, v3, r18, r19, r20, v1;
  cmul4(W2, W512, u1, W16, W1, W256, a, W8, W1, W256, W1, W8);
  cmul4(u2, u3, W4, W32, a, u1, W2, W16, W2, W2, W2, W16);
  put4<0>(a, u1, u2, u3);
  cmul4(u4, u5, u6, u7, a, u1, u2, u3, W4, W4, W4, W4);
  put4<4>(u4, u5, u6, u7);
  cmul4(r8, r9, r10, r11, a, u2, u4, u6, W256, W256, W256, W256);
  put4<8>(r8, r9, r10, r11);
  cmul4(r12, r13, r14, r15, a, u4, r8, r10, W512, W512, W512, W512);
  put4<12>(r12, r13, r14, r15);
  v4u UA[2][4];
#pragma unroll
  for (int k = 0; k < 4; ++k) UA[0][k] = *(LAS v4u*)(sr + 64 * k);
  cmul4(r16, r17, W1024, v3, r12, r14, W512, W8, W512, W512, W512, W16);
  cmul4(r18, r19, r20, v1, r12, r14, r16, W8, W1024, W1024, W1024, W32);
  put4<0>(r16, r17, r18, r19);
  put1<4>(r20);
#pragma unroll
  for (int k = 0; k < 4; ++k) UA[1][k] = *(LAS v4u*)(sr + 64 * k);
  put4<0>(cf{1.f, 0.f}, W8, W16, v3);
  cf v0 = W32, v2, t0, t1, t2, t3;
  cmul4(v2, t3, t0, t1, W16, v3, v0, v1, W32, W32, W32, W32);
  put4<4>(v0, v1, v2, t3);
  cmul4(t2, v3, v0, v1, v2, t3, t0, t1, W32, W32, W32, W32);
  put4<8>(t0, t1, t2, v3);
  cmul4(v2, t3, t0, t1, t2, v3, v0, v1, W32, W32, W32, W32);
  put4<12>(v0, v1, v2, t3);
  v4u vb[4];
#pragma unroll
  for (int k = 0; k < 4; ++k) vb[k] = *(LAS v4u*)(sr + 64 * k);
  cmul4(t2, v3, v0, v1, v2, t3, t0, t1, W32, W32, W32, W32);
  put4<0>(t0, t1, t2, v3);
  cmul4(v2, t3, t0, t1, t2, v3, v0, v1, W32, W32, W32, W32);
  put4<4>(v0, v1, v2, t3);
#pragma unroll
  for (int k = 0; k < 4; ++k) {
    kacc[0][0] = mfma(UA[0][k], vb[k], kacc[0][0]);
    kacc[1][0] = mfma(UA[1][k], vb[k], kacc[1][0]);
  }
  cmul4(t2, v3, v0, v1, v2, t3, t0, t1, W32, W32, W32, W32);
  put4<8>(t0, t1, t2, v3);
  cmul4(v2, t3, t0, t1, t2, v3, v0, v1, W32, W32, W32, W32);
  put4<12>(v0, v1, v2, t3);
#pragma unroll
  for (int k = 0; k < 4; ++k) vb[k] = *(LAS v4u*)(sr + 64 * k);
  SCHED();
#pragma unroll
  for (int k = 0; k < 4; ++k) {
    kacc[0][1] = mfma(UA[0][k], vb[k], kacc[0][1]);
    kacc[1][1] = mfma(UA[1][k], vb[k], kacc[1][1]);
  }
}
typedef _Float16 f16x8 __attribute__((ext_vector_type(8)));
typedef _Float16 f16x2 __attribute__((ext_vector_type(2)));
DI f32x4 mfmah(v4u a, v4u b, f32x4 c) {
  return __builtin_amdgcn_mfma_f32_16x16x32_f16(__builtin_bit_cast(f16x8, a), __builtin_bit_cast(f16x8, b), c, 0, 0, 0);
}
DI unsigned pkh(cf v) { f16x2 r; r.x = (_Float16)v.x; r.y = (_Float16)v.y; return __builtin_bit_cast(unsigned, r); }
DI cf csq(cf v) { return cf{__builtin_fmaf(v.x, v.x, -(v.y * v.y)), (v.x + v.x) * v.y}; }
#define HMUL(o, i, w) "v_pk_mul_f16 " o ", " i ", " w " op_sel:[1,1] op_sel_hi:[1,0] neg_lo:[0,1]\n\t"
#define HFMA(o, i, w) "v_pk_fma_f16 " o ", " i ", " w ", " o " op_sel_hi:[0,1,1]\n\t"
template <int R0, int R1, int R2, int R3>
DI void h4(unsigned& o0, unsigned& o1, unsigned& o2, unsigned& o3, unsigned i0, unsigned i1, unsigned i2, unsigned i3, unsigned w0, unsigned w1, unsigned w2, unsigned w3) {
  asm volatile(HMUL("%0", "%4", "%8") HMUL("%1", "%5", "%9") HMUL("%2", "%6", "%10") HMUL("%3", "%7", "%11")
               HFMA("%0", "%4", "%8") HFMA("%1", "%5", "%9") HFMA("%2", "%6", "%10") HFMA("%3", "%7", "%11")
               "ds_write_addtid_b32 %0 offset:%12\n\tds_write_addtid_b32 %1 offset:%13\n\tds_write_addtid_b32 %2 offset:%14\n\tds_write_addtid_b32 %3 offset:%15"
               : "=&v"(o0), "=&v"(o1), "=&v"(o2), "=&v"(o3)
               : "v"(i0), "v"(i1), "v"(i2), "v"(i3), "v"(w0), "v"(w1), "v"(w2), "v"(w3), "i"(R0 * 272), "i"(R1 * 272), "i"(R2 * 272), "i"(R3 * 272) : "memory");
}
template <int R> DI void hput(unsigned v) { asm volatile("ds_write_addtid_b32 %0 offset:%1" ::"v"(v), "i"(R * 272) : "memory"); }
template <int C> DI float dpp_max(float v) {
  return __builtin_fmaxf(v, __builtin_bit_cast(float, __builtin_amdgcn_update_dpp(0, __builtin_bit_cast(int, v), C, 0xf, 0xf, true)));
}
template <class F> DI float kgen_task(const unsigned (&pr)[7], bool dead, unsigned sw, LAS char* sr, f32x4 (&kacc)[2][2], F mid) {
  const float dt = rbf(__builtin_amdgcn_exp2f(bf2f(pr[6]) * 1.44269504088896341f));
  const float zr = dt * bf2f(pr[0]), zi = dt * bf2f(pr[1]);
  const float dbr = dt * bf2f(pr[2]), dbi = dt * bf2f(pr[3]), cr = bf2f(pr[4]), ci = bf2f(pr[5]);
  cf a = cf{cr * dbr - ci * dbi, -(cr * dbi + ci * dbr)};
  if (dead) a = cf{0.f, 0.f};
  const float e1 = zr * 1.44269504088896341f, th = zi * 0.15915494309189535f;
  const float tl = __builtin_fmaf(zi, 6.4206383e-9f, __builtin_fmaf(zi, 0.15915494309189535f, -th));
  const cf W1 = cexpk(e1, th, tl, 1.f, -1.f), W256 = cexpk(e1, th, tl, 256.f, -256.f), W8 = cexpk(e1, th, tl, 8.f, 8.f);
  if (__builtin_expect(__builtin_amdgcn_ballot_w64(!(zr <= 0.f)) != 0ull, 0)) {
    mid(1); mid(2);
    asm volatile("s_mov_b32 m0, %0" ::"s"(sw) : "memory");
#pragma unroll
    for (int i = 0; i < 4; ++i) kacc[i >> 1][i & 1] = f32x4{0.f, 0.f, 0.f, 0.f};
    kgen_slow(a, W1, W256, W8, sr, kacc);
    return 1.f;
  }
  float am = __builtin_fmaxf(__builtin_fabsf(a.x), __builtin_fabsf(a.y));
  am = dpp_max<0xB1>(am); am = dpp_max<0x4E>(am); am = dpp_max<0x141>(am); am = dpp_max<0x140>(am);
  const unsigned e0 = __builtin_amdgcn_readlane(__builtin_bit_cast(unsigned, am), 0), e16 = __builtin_amdgcn_readlane(__builtin_bit_cast(unsigned, am), 16);
  const unsigned e32 = __builtin_amdgcn_readlane(__builtin_bit_cast(unsigned, am), 32), e48 = __builtin_amdgcn_readlane(__builtin_bit_cast(unsigned, am), 48);
  unsigned ex = max(max(e0, e16), max(e32, e48)) >> 23;
  ex = min(max(ex, 2u), 252u);
  const float up = __builtin_bit_cast(float, ex << 23), down = __builtin_bit_cast(float, (254u - ex) << 23);
  const cf W2 = csq(W1), W512 = csq(W256), W16 = csq(W8), W4 = csq(W2), W1024 = csq(W512), W32 = csq(W16), W64 = csq(W32), W128 = csq(W64);
  const unsigned u0 = pkh(a * down), h1 = pkh(W1), h2 = pkh(W2), h4_ = pkh(W4), h256 = pkh(W256), h512 = pkh(W512), h1024 = pkh(W1024);
  unsigned u1, u2, u4, r8, u3, u5, u6, r12, u7, r9, r10, r14, r11, r13, r15, r16, r17, r18, r19, r20;
  asm volatile("s_mov_b32 m0, %0\n\ts_nop 0" ::"s"(sw) : "memory");
  hput<0>(u0);
  h4<1, 2, 4, 8>(u1, u2, u4, r8, u0, u0, u0, u0, h1, h2, h4_, h256);
  h4<3, 5, 6, 12>(u3, u5, u6, r12, u2, u4, u4, u0, h1, h1, h2, h512);
  h4<7, 9, 10, 14>(u7, r9, r10, r14, u6, r8, r8, r8, h1, h2, h4_, h512);
  h4<11, 13, 15, 15>(r11, r13, r16, r15, r10, r12, u0, r14, h2, h4_, h1024, h4_);
  v4u UA[2][4];
#pragma unroll
  for (int k = 0; k < 4; ++k) UA[0][k] = *(LAS v4u*)(sr + 64 * k);
  hput<0>(r16);
  h4<1, 2, 3, 4>(r17, r18, r19, r20, r8, r12, r14, r16, h1024, h1024, h1024, h1024);
#pragma unroll
  for (int k = 0; k < 4; ++k) UA[1][k] = *(LAS v4u*)(sr + 64 * k);
  mid(1);
  unsigned v[16], t0, t1, t2, t3;
  v[1] = pkh(W8); v[2] = pkh(W16); v[4] = pkh(W32); v[8] = pkh(W64);
  const unsigned h128 = pkh(W128);
  hput<0>(0x3c00u); hput<1>(v[1]); hput<2>(v[2]); hput<4>(v[4]); hput<8>(v[8]);
  h4<3, 5, 6, 9>(v[3], v[5], v[6], v[9], v[2], v[4], v[4], v[8], v[1], v[1], v[2], v[1]);
  h4<7, 10, 11, 12>(v[7], v[10], v[11], v[12], v[4], v[8], v[8], v[8], v[3], v[2], v[3], v[4]);
  h4<13, 14, 15, 15>(v[13], v[14], t0, v[15], v[8], v[8], v[1], v[8], v[5], v[6], h128, v[7]);
  v4u vb[4];
#pragma unroll
  for (int k = 0; k < 4; ++k) vb[k] = *(LAS v4u*)(sr + 64 * k);
  mid(2);
  asm volatile("s_mov_b32 m0, %0\n\ts_nop 0" ::"s"(sw) : "memory");
  hput<0>(h128); hput<1>(t0);
  h4<2, 3, 4, 5>(t0, t1, t2, t3, v[2], v[3], v[4], v[5], h128, h128, h128, h128);
#pragma unroll
  for (int k = 0; k < 4; ++k) {
    kacc[0][0] = mfmah(UA[0][k], vb[k], k ? kacc[0][0] : f32x4{0.f, 0.f, 0.f, 0.f});
    kacc[1][0] = mfmah(UA[1][k], vb[k], k ? kacc[1][0] : f32x4{0.f, 0.f, 0.f, 0.f});
  }
  h4<6, 7, 8, 9>(t0, t1, t2, t3, v[6], v[7], v[8], v[9], h128, h128, h128, h128);
  h4<10, 11, 12, 13>(t0, t1, t2, t3, v[10], v[11], v[12], v[13], h128, h128, h128, h128);
  h4<14, 15, 14, 15>(t0, t1, t2, t3, v[14], v[15], v[14], v[15], h128, h128, h128, h128);
#pragma unroll
  for (int k = 0; k < 4; ++k) vb[k] = *(LAS v4u*)(sr + 64 * k);
  SCHED();
#pragma unroll
  for (int k = 0; k < 4; ++k) {
    kacc[0][1] = mfmah(UA[0][k], vb[k], k ? kacc[0][1] : f32x4{0.f, 0.f, 0.f, 0.f});
    kacc[1][1] = mfmah(UA[1][k], vb[k], k ? kacc[1][1] : f32x4{0.f, 0.f, 0.f, 0.f});
  }
  return up;
}
DI void kgen_store(LAS float* pt, const f32x4 (&kacc)[2][2], float scale, int lane) {
  const int l15 = lane & 15, g = lane >> 4;
  const int sh = g < 2 ? 3 : 4 - g;
  const int a0 = (l15 << sh) + (g < 2 ? 4 * g : 128 * g);
  const int a1 = g < 3 ? a0 + 2 : a0 + 64;
#pragma unroll
  for (int nh = 0; nh < 2; ++nh) {
    *(LAS f32x2*)(pt + a0 + ((16 * nh) << sh)) = kacc[0][nh].xy;
    *(LAS f32x2*)(pt + a1 + ((16 * nh) << sh)) = kacc[0][nh].zw;
  }
  if (g == 0) {
#pragma unroll
    for (int nh = 0; nh < 2; ++nh)
#pragma unroll
      for (int r = 0; r < 4; ++r) pt[512 + 32 * r + 16 * nh + l15] = kacc[1][nh][r];
  }
  if (lane == 16) { *(LAS f32x4*)(pt + 640) = f32x4{kacc[1][0][0], 0.f, 0.f, 0.f}; pt[644] = scale; }
}

template <int PAR> DI void conv_unit_shared(LAS char* ka, LAS char* ub, f32x4 (&acc)[4]) {
  constexpr int T[4] = {PAR ? 1 : 0, PAR ? 2 : 3, PAR ? 5 : 4, PAR ? 6 : 7};
  v4u A[8], B[8];
#pragma unroll
  for (int i = 0; i <= T[3]; ++i) { A[i] = *(LAS v4u*)(ka + 512 * (7 - i)); B[i] = *(LAS v4u*)(ub + 512 * i); }
  SCHED();
#pragma unroll
  for (int q = 0; q < 4; ++q)
#pragma unroll
    for (int ip = 0; ip <= T[q]; ++ip) acc[q] = mfma(A[T[q] - ip], B[ip], acc[q]);
  SCHED();
}
DI float fadd(float a, float b) { float r; asm("v_add_f32 %0, %1, %2" : "=v"(r) : "v"(a), "v"(b)); return r; }
DI float ffma(float a, float b, float c) { float r; asm("v_fma_f32 %0, %1, %2, %3" : "=v"(r) : "v"(a), "v"(b), "v"(c)); return r; }
DI f32x4 add4(f32x4 a, f32x4 b) { return f32x4{fadd(a.x, b.x), fadd(a.y, b.y), fadd(a.z, b.z), fadd(a.w, b.w)}; }
DI f32x4 fma4(f32x4 a, float b, f32x4 c) { return f32x4{ffma(a.x, b, c.x), ffma(a.y, b, c.y), ffma(a.z, b, c.z), ffma(a.w, b, c.w)}; }
DI int rot12(int v) { return v >= 12 ? v - 12 : v; }
DI void w_loads(v4u (&WF)[5], const char* p, unsigned vo) {
  ld128c<0>(WF[0], vo, p); ld128c<1024>(WF[1], vo, p); ld128c<2048>(WF[2], vo, p); ld128c<3072>(WF[3], vo, p); ld128c<0>(WF[4], vo, p + 4096);
}
DI void a_loads(v4u (&AF)[4], const char* p, const unsigned (&vm)[4]) {
#pragma unroll
  for (int m = 0; m < 4; ++m) ld128c<0>(AF[m], vm[m], p);
}
template <int LIVE, class F> DI void g2_step(f32x4 (&acc)[4][5], v4u (&WF)[5], v4u (&AF)[4], F ld) {
#pragma unroll
  for (int m = 0; m < 4; ++m) PIN(AF[m]);
#pragma unroll
  for (int n = 0; n < 5; ++n) PIN(WF[n]);
#pragma unroll
  for (int e = 0; e < 20; ++e) {
    const int m = e / 5, n = e % 5;
    acc[m][n] = mfma(WF[n], AF[m], m >= LIVE ? f32x4{0.f, 0.f, 0.f, 0.f} : acc[m][n]);
    if (e & 1) { SCHED(); ld(e >> 1); SCHED(); }
  }
}
DI void make_kix(unsigned (&kix)[9], int ts) {
#pragma unroll
  for (int i = 0; i < 9; ++i) {
    const int lg = 2047 - 8 * ts - i;
    const int bits = (int)(pk2(__builtin_fmaxf((float)lg, 128.f), 0.f));
    const int ci = lg < bits - 0x4280 ? lg : bits - 0x4280;
    kix[i] = KTAB + 4 * ci;
    PIN(kix[i]);
  }
}
DI void put_k(LAS char* L, const unsigned (&kix)[9], int ts, int c, unsigned reg) {
  float kv[9];
#pragma unroll
  for (int i = 0; i < 9; ++i) kv[i] = *(LAS float*)(L + kix[i] + c * KP_STRIDE);
  LAS char* q = L + reg + CV_KOFF + ts * 16;
  *(LAS v4u*)(q + CV_KSTR) = v4u{pk2(kv[0], kv[1]), pk2(kv[2], kv[3]), pk2(kv[4], kv[5]), pk2(kv[6], kv[7])};
  *(LAS v4u*)q = v4u{pk2(kv[1], kv[2]), pk2(kv[3], kv[4]), pk2(kv[5], kv[6]), pk2(kv[7], kv[8])};
}
DI void put_u(LAS char* q, v4u u, bool first) {
  *(LAS v4u*)(q + 3 * CV_USTR + 384) = u;
  *(LAS v2u*)(q + 1 * CV_USTR + 392) = v2u{u.x, u.y}; *(LAS v2u*)(q + 1 * CV_USTR + 400) = v2u{u.z, u.w};
  *(LAS unsigned*)(q + 2 * CV_USTR + 388) = u.x; *(LAS v2u*)(q + 2 * CV_USTR + 392) = v2u{u.y, u.z}; *(LAS unsigned*)(q + 2 * CV_USTR + 400) = u.w;
  *(LAS unsigned*)(q + 396) = u.x; *(LAS v2u*)(q + 400) = v2u{u.y, u.z}; *(LAS unsigned*)(q + 408) = u.w;
  if (first) {
    *(LAS v2u*)(q + 384) = v2u{0u, 0u}; *(LAS unsigned*)(q + 392) = 0u;
    *(LAS v2u*)(q + CV_USTR + 384) = v2u{0u, 0u}; *(LAS unsigned*)(q + 2 * CV_USTR + 384) = 0u;
  }
}

__global__ __launch_bounds__(512, 2) void fused_s4_mlp(const char* __restrict__ x, const char* __restrict__ pAre, const char* __restrict__ pAim,
    const char* __restrict__ pBre, const char* __restrict__ pBim, const char* __restrict__ pCre, const char* __restrict__ pCim,
    const char* __restrict__ plogdt, const char* __restrict__ pDskip, const char* __restrict__ w1, const char* __restrict__ b1,
    const char* __restrict__ w2, char* __restrict__ out, char* __restrict__ ws) {
  __shared__ __attribute__((aligned(16))) char smem[163840];
  LAS char* L = (LAS char*)smem;
  const unsigned lds0 = (unsigned)(size_t)L;
  const char* pc0; unsigned codelen;
  asm volatile("s_getpc_b64 %0\n98:\n\ts_mov_b32 %1, 97f-98b\n\ts_max_u32 %1, %1, 99f-98b" : "=s"(pc0), "=&s"(codelen) :: "scc");
  const int b = blockIdx.x, lane = threadIdx.x & 63;
  const int w = RFL(threadIdx.x >> 6), wi = w & 3;
  {
    const unsigned co = (unsigned)(4 * (16 * w + (lane & 15)) + ((b >> 3) & 3)) * 128;
    asm volatile("s_mov_b32 m0, %2\n\ts_nop 0\n\tglobal_load_lds_dword %0, %1" ::"v"((co < codelen ? co : codelen) & ~3u), "s"(pc0), "s"(lds0 + YST + w * 256) : "memory");
  }
  const int xcd = b & 7, j = b >> 3, rg = xcd >> 1, chf = xcd & 1;
  const int ct1 = 8 * chf + (j & 7), rb1 = 4 * rg + (j >> 3);
  const int wid = rb1 * 16 + ct1;
  const int a2 = 4 * chf + (j >> 3), cb2 = j & 7;
  const int l15 = lane & 15, g = lane >> 4, qq = (lane >> 2) & 3, p4 = lane & 3, l31 = lane & 31, h = lane >> 5;
  const int par = j & 1, cbase = 80 * xcd + 5 * (j >> 1);
  const int chA = cbase + 3 * par, chS = cbase + 2;
  const int ts = 64 * wi + lane;
  ST(0);
  const int row2 = 512 * rg + 64 * a2 + 16 * wi;
  const char* w2b = ws + WS_W2T + (size_t)(cb2 * 48 + 12 * wi) * 5120;
  const char* actb = ws + WS_ACT + (size_t)((32 * rg + 4 * a2) * 48 + 12 * wi) * 1024;
  const int r2 = ((a2 ^ (w >> 2)) & 1) * 6;
#define WL(s) w_loads(WF[s], w2b + rot12(r2 + (s)) * 5120, vo)
#define AL(s) a_loads(AF[s], actb + rot12(r2 + (s)) * 1024, vm)
#define AL1(s, m) ld128c<0>(AF[s][m], vm[m], actb + rot12(r2 + (s)) * 1024)
#define WL1(s, n) ld128c<0>(WF[s][n], vo, w2b + rot12(r2 + (s)) * 5120 + (n) * 1024)
#define NOLD [&](int) {}
#define LATE_LANE() int ln_ = lane; PIN(ln_); const int c15 = ln_ & 15, cg = ln_ >> 4
#define KAO (CV_KOFF + (c15 & 1) * CV_KSTR + 2 * (2072 - 8 * (c15 >> 1) + 8 * cg))
#define UBO ((c15 & 3) * CV_USTR + 2 * (64 * (c15 >> 2) + 8 * cg + 224))
#define YOFF ((unsigned)((c15 >> 2) * 128 + cg * 32 + (c15 & 3) * 8))
#define OROW ((unsigned)(2 * (c15 >> 2) + (c15 & 1) + 8 * ((c15 >> 1) & 1)))
#define ZOFF (lane < 24 ? wi * CV_USTR + lane * 16 : CV_KOFF + wi * CV_KSTR + 4096 + (lane - 24) * 16)
#define ZCOND (lane < 24 || (lane < 31 && wi < 2))

  unsigned pr[7];
  {
    const int kc = w < 3 ? chA : (w < 6 ? chA + 1 : chS);
    const int m0 = w < 6 ? (w < 3 ? w : w - 3) * 64 : par * 96 + (w - 6) * 48;
    const unsigned vo = (unsigned)(kc * 192 + m0 + ((w >= 6 && lane >= 48) ? 47 : lane)) * 2;
    ld16(pr[6], (unsigned)kc * 2, plogdt);
    ld16(pr[0], vo, pAre); ld16(pr[1], vo, pAim); ld16(pr[2], vo, pBre); ld16(pr[3], vo, pBim); ld16(pr[4], vo, pCre); ld16(pr[5], vo, pCim);
  }
  unsigned voA[4], voB[3];
#pragma unroll
  for (int i = 0; i < 4; ++i) voA[i] = (8 * i + (lane >> 3)) * 1280 + (((lane & 7) ^ (4 * (i & 1) + (lane >> 4))) * 16) - 1024 * i;
#pragma unroll
  for (int i = 0; i < 3; ++i) { const int c = 64 * i + lane; voB[i] = (c / 12) * 3072 + (c % 12) * 16 - 1024 * i; }
  const char* g1a = x + (size_t)(rb1 * 128 + 32 * wi) * 1280;
  const char* g1b = w1 + ct1 * 192 + wi * 49152;
  const unsigned la = lds0 + G1_RING + wi * 4096, lb = lds0 + G1_RING + G1_BOFF + wi * 3072;
  const int r1 = ((j & 7) + (5 * (j >> 3)) / 2 + 5 * chf + rg) % 10;
  auto slab = [&](int s) { const int v = s + r1; return v >= 10 ? v - 10 : v; };
  auto issue = [&](int s) {
    const char* pa = g1a + slab(s) * 128; const char* pb = g1b + (size_t)slab(s) * 64 * 3072;
    dma16(voA[0], pa, la + s * G1_STAGE); dma16o<1024>(voA[1], pa); dma16o<2048>(voA[2], pa); dma16o<3072>(voA[3], pa);
    dma16(voB[0], pb, lb + s * G1_STAGE); dma16o<1024>(voB[1], pb); dma16o<2048>(voB[2], pb);
  };
  unsigned tlo = 0, thi = 0;
  unsigned kix[9];
  if (w < 4) {
    bar();
#pragma unroll
    for (int i = 0; i < 9; ++i) kix[i] = 0;
    waitv<0>();
  } else {
    __builtin_amdgcn_s_setprio(SPRIO);
    v2u E; v4u xa, xb; v2u xc;
    const int pu = b % 240, pks = pu / 5, pcg = pu % 5;
    const char* xrow = x + (size_t)(8 * wid + 2 * wi) * 1280;
    ld128(xa, h * 1280 + l31 * 16, xrow); ld128(xb, h * 1280 + 512 + l31 * 16, xrow); ld64(xc, h * 1280 + 1024 + l31 * 8, xrow);
    ld64c(E, 0u, ws);
#pragma unroll
    for (int i = 0; i < 2; ++i)
      dma16((unsigned)((lane >> 1) * 1280 + (lane & 1) * 16), w2 + (size_t)pks * 32 * 1280 + (128 * pcg + 32 * wi + 16 * i) * 2, lds0 + W2P + wi * 2048 + i * 1024);
    if (w == 4) *(LAS unsigned*)(L + lane * 4) = 0u;
    bar();
    issue(0);
    make_kix(kix, ts);
    issue(1);
    waitv<14>();
    PIN(E); PIN(xa); PIN(xb); PIN(xc);
    ST(6);
  const u64 tag64 = (((u64)RFL(E.y) << 32) | (unsigned)RFL(E.x)) + 1ull;
  tlo = (unsigned)tag64; thi = (unsigned)(tag64 >> 32);
  if (wi == 0) { lds_set(L, W_TAG, tlo); lds_set(L, W_TAG + 4, thi); lds_set(L, W_TAG + 8, 1); }
  {
    f32x2 v[10] = {unpk(xa.x), unpk(xa.y), unpk(xa.z), unpk(xa.w), unpk(xb.x), unpk(xb.y), unpk(xb.z), unpk(xb.w), unpk(xc.x), unpk(xc.y)};
    f32x2 s2 = ((v[0] + v[1]) + (v[2] + v[3])) + ((v[4] + v[5]) + (v[6] + v[7])) + (v[8] + v[9]);
    const float mean = half_sum(s2.x + s2.y, h) * (1.0f / 640.0f);
    f32x2 q2 = f32x2{0.f, 0.f};
#pragma unroll
    for (int i = 0; i < 10; ++i) { const f32x2 d = v[i] - mean; q2 = __builtin_elementwise_fma(d, d, q2); }
    const float var = rbf(half_sum(q2.x + q2.y, h) * (1.0f / 640.0f));
    const float m = rbf(mean), sd = rbf(__builtin_amdgcn_sqrtf(rbf(var + 1e-5f))), rs = __builtin_amdgcn_rcpf(sd);
    unsigned o[10];
#pragma unroll
    for (int i = 0; i < 10; ++i) { const f32x2 t = (v[i] - m) * rs; o[i] = pk2(t.x, t.y); }
    LAS char* rp = L + LN_ROWS + (2 * wi + h) * LN_PITCH;
    *(LAS v4u*)(rp + l31 * 16) = v4u{o[0], o[1], o[2], o[3]};
    *(LAS v4u*)(rp + 512 + l31 * 16) = v4u{o[4], o[5], o[6], o[7]};
    *(LAS v2u*)(rp + 1024 + l31 * 8) = v2u{o[8], o[9]};
    waitv<0>();
    lds_arrive(L, W_SB, lane);
    if (l31 == 0) st64c(WS_STATS + (unsigned)(8 * wid + 2 * wi + h) * 8, ws,
                        v2u{(__builtin_bit_cast(unsigned, m) & 0xffff0000u) | (__builtin_bit_cast(unsigned, sd) >> 16), tlo});
  }
#pragma unroll
  for (int i = 0; i < 2; ++i) {
    LAS char* sp = L + W2P + wi * 2048 + i * 1024 + (16 * (g >> 1) + 4 * (g & 1) + qq) * 32 + 8 * p4;
    const v2u lo = trread(sp), hi = trread(sp + 256);
    const int ntg = 8 * pcg + 2 * wi + i;
    if (b < 240) st128c((unsigned)lane * 16, ws + WS_W2T + (size_t)(((ntg / 5) * 48 + pks) * 5 + ntg % 5) * 1024, v4u{lo.x, lo.y, hi.x, hi.y});
  }
  lds_wait_ge(L, W_SB, 4);
  {
    LAS char* sp = L + LN_ROWS + qq * LN_PITCH + (160 * wi + 16 * g) * 2 + 8 * p4;
#pragma unroll
    for (int i = 0; i < 3; ++i) {
      const v2u lo = trread(sp + 128 * i), hi = trread(sp + 128 * i + 4 * LN_PITCH);
      const unsigned ch = 64 * i + lane;
      if (i < 2 || lane < 32) st128c((ch / 5) * 20480 + (ch % 5) * 16, ws + WS_XNB + (size_t)wi * 32 * 20480 + wid * 80, v4u{lo.x, lo.y, hi.x, hi.y});
    }
  }
  }
  asm volatile("; AUDITCLR");
#pragma unroll
  for (int i = 0; i < 7; ++i) PIN(pr[i]);
  ST(1);
  {
    f32x4 kacc[2][2];
    const float scale = kgen_task(pr, w >= 6 && lane >= 48, lds0 + KG_SCR + w * KG_WSTR, L + KG_SCR + w * KG_WSTR + l15 * 272 + g * 16, kacc, [&](int i) {
      if (i == 2) {
        if (w >= 4) { waitv<0>(); arrive_flag8(L, W_F1C, ws, WS_F1 + (unsigned)(lane & 7) * 2048 + wid * 8, tlo, thi); }
      }
    });
    kgen_store((LAS float*)(L + KG_SCR + w * KG_WSTR), kacc, scale, lane);
    lds_arrive(L, W_KG, lane);
  }
  ST(2);
  auto mish1 = [&](f32x4 a0, f32x4 a1, int n, int r, float rs, float mrs, unsigned ao, unsigned co) {
    unsigned pk[4];
#pragma unroll
    for (int c = 0; c < 2; ++c) {
      LAS char* cp = L + co + (32 * n + 16 * r + 8 * c) * 4;
      const f32x4 cs = *(LAS f32x4*)cp, bv = *(LAS f32x4*)(cp + 384), av = c ? a1 : a0;
#pragma unroll
      for (int e = 0; e < 2; ++e) {
        const f32x2 h2 = __builtin_elementwise_fma(e ? av.zw : av.xy, f32x2{rs, rs},
                                                   __builtin_elementwise_fma(f32x2{mrs, mrs}, e ? cs.zw : cs.xy, e ? bv.zw : bv.xy));
        const f32x2 s1 = f32x2{__builtin_amdgcn_exp2f(h2.x), __builtin_amdgcn_exp2f(h2.y)} + 1.0f;
        const f32x2 d = __builtin_elementwise_fma(s1, s1, f32x2{1.f, 1.f});
        const f32x2 rr = f32x2{__builtin_amdgcn_rcpf(d.x), __builtin_amdgcn_rcpf(d.y)};
        const f32x2 o = h2 * __builtin_elementwise_fma(rr, f32x2{-1.38629436111989062f, -1.38629436111989062f},
                                                       f32x2{0.69314718055994531f, 0.69314718055994531f});
        pk[2 * c + e] = pk2(o.x, o.y);
      }
    }
    st128c(ao, ws + WS_ACT + (size_t)((rb1 * 8 + 2 * wi) * 48 + 3 * ct1 + n) * 1024 + r * 512, v4u{pk[0], pk[1], pk[2], pk[3]});
  };
#define MISH_SETUP() const float rs = __builtin_amdgcn_rcpf(bf2f(gran.x)) * 1.44269504088896341f, mrs = -bfhi(gran.x) * rs; \
  const unsigned ao = (unsigned)((l31 >> 4) * 49152 + h * 256 + l15 * 16), co = CSF + wi * 768 + h * 16

  if (w < 4) {
    issue(2);
    int lane = __builtin_amdgcn_mbcnt_hi(~0u, __builtin_amdgcn_mbcnt_lo(~0u, 0u)); PIN(lane);
    const int l15 = lane & 15, g = lane >> 4, qq = (lane >> 2) & 3, p4 = lane & 3, l31 = lane & 31, h = lane >> 5;
    const unsigned vo = lane * 16;
    f32x16 acc1[3]; f32x4 csa[3];
#pragma unroll
    for (int n = 0; n < 3; ++n) {
      csa[n] = f32x4{0.f, 0.f, 0.f, 0.f};
#pragma unroll
      for (int i = 0; i < 16; ++i) acc1[n][i] = 0.f;
    }
    const int xrow = (l31 & 17) + 2 * (l15 >> 2) + 8 * ((l15 >> 1) & 1);
    unsigned xo[4], wo[4];
#pragma unroll
    for (int k = 0; k < 4; ++k) {
      const int kr = (k + wi) & 3;
      xo[k] = G1_RING + (32 * wi + xrow) * 128 + (((2 * kr + h) ^ ((xrow >> 1) & 7)) * 16);
      wo[k] = G1_RING + G1_BOFF + (16 * kr + 8 * h + qq) * 192 + 32 * (g & 1) + 8 * p4;
      PIN(xo[k]); PIN(wo[k]);
    }
    const unsigned sel1 = ((l15 ^ g) & 1) ? 0u : 0x3f803f80u;
    const v4u sel = v4u{sel1, sel1, sel1, sel1};
    v4u X[4], W[4][3];
    auto rdw = [&](int s, int k, int n) {
      LAS char* p = L + wo[k] + (s % 3) * G1_STAGE + 64 * n; const v2u lo = trread(p), hi = trread(p + 768); W[k][n] = v4u{lo.x, lo.y, hi.x, hi.y};
    };
    auto rdx = [&](int s, int k) { X[k] = *(LAS v4u*)(L + xo[k] + (s % 3) * G1_STAGE); };
#define MM(k, n) acc1[n] = mfma32(W[k][n], X[k], acc1[n])
#define CS(n) csa[n] = mfma(W[0][n], sel, csa[n])
    v2u gran; unsigned bb;
    v4u WF[6][5], AF[6][4];
    const unsigned go = WS_STATS + (unsigned)(rb1 * 128 + 32 * wi + xrow) * 8;
    auto wl1 = [&](int s, int e0) { const int e = 7 * (s - 7) + e0; if (s >= 7 && e0 < 7 && e < 20) ld128c<0>(WF[e / 5][e % 5], vo, w2b + rot12(r2 + e / 5) * 5120 + (e % 5) * 1024); };
    lds_wait_ge(L, W_SB, 4);
    ST(3);
    rdx(0, 0); rdw(0, 0, 0); rdw(0, 0, 1); rdw(0, 0, 2); rdx(0, 1); rdw(0, 1, 0); rdw(0, 1, 1); rdw(0, 1, 2);
#pragma unroll
    for (int s = 0; s < 10; ++s) {
      const bool refill = s < 7;
      const char* pa = g1a + slab(s + 3) * 128; const char* pb = g1b + (size_t)slab(s + 3) * 64 * 3072;
      SCHED();
      MM(0, 0); rdx(s, 2); rdw(s, 2, 0); SCHED();
      MM(0, 1); rdw(s, 2, 1); rdw(s, 2, 2); SCHED();
      MM(0, 2); rdx(s, 3); rdw(s, 3, 0); SCHED();
      CS(0); rdw(s, 3, 1); SCHED();
      CS(1); rdw(s, 3, 2); SCHED();
      CS(2);
      if (s == 0) {} else if (s < 7) waitv<7>(); else if (s == 7) waitv<9>(); else if (s == 8) waitv<9>();
      if (s == 9 && l15 < 2) {
#pragma unroll
        for (int n = 0; n < 3; ++n) *(LAS f32x4*)(L + CSP + wi * 384 + (32 * n + 16 * l15 + 4 * g) * 4) = csa[n];
      }
      lds_set(L, W_RDY + wi * 4, s + 2);
      SCHED();
      MM(1, 0);
      if (refill) { asm volatile("s_waitcnt lgkmcnt(7)" ::: "memory"); dma16(voA[0], pa, la + (s % 3) * G1_STAGE); }
      wl1(s, 0);

      SCHED();
      MM(1, 1); if (refill) dma16o<1024>(voA[1], pa); wl1(s, 1);
      unsigned fl = 0;
      if (s < 9) fl = lw(L, W_RDY + p4 * 4);
      SCHED();
      MM(1, 2); if (refill) dma16o<2048>(voA[2], pa); wl1(s, 2); SCHED();
      MM(2, 0); if (refill) dma16o<3072>(voA[3], pa); wl1(s, 3);
      SCHED();
      if (s == 9) { MM(2, 1); wl1(s, 4); SCHED(); MM(2, 2); wl1(s, 5); SCHED(); MM(3, 0); wl1(s, 6); SCHED(); MM(3, 1); MM(3, 2); SCHED(); break; }
      if (__builtin_expect(__builtin_amdgcn_ballot_w64(fl < (unsigned)(s + 2)) != 0ull, 0)) lds_wait_min<4>(L, W_RDY, s + 2, lane);
      FENCE();
      SCHED();
      rdx(s + 1, 0); rdw(s + 1, 0, 0); SCHED();
      MM(2, 1); rdw(s + 1, 0, 1); rdw(s + 1, 0, 2); if (refill) dma16(voB[0], pb, lb + (s % 3) * G1_STAGE); wl1(s, 4); SCHED();
      MM(2, 2); rdx(s + 1, 1); rdw(s + 1, 1, 0); if (refill) dma16o<1024>(voB[1], pb); wl1(s, 5); SCHED();
      MM(3, 0); rdw(s + 1, 1, 1); if (refill) dma16o<2048>(voB[2], pb); wl1(s, 6); SCHED();
      MM(3, 1); rdw(s + 1, 1, 2); wl1(s, 7); SCHED();
      MM(3, 2); wl1(s, 8);
      if (s == 6) { lds_wait_ge(L, W_F1ALL, 4); ld64c(gran, go, ws); ld32<0>(bb, (unsigned)(lane < 48 ? lane : 47) * 4, b1 + ct1 * 192); }
      SCHED();
      if (s == 0) ST(4); if (s == 3) ST(5); if (s == 6) ST(7);
    }
#undef MM
#undef CS
    ST(11);
    const unsigned tlo = RFL(lw(L, W_TAG)), thi = RFL(lw(L, W_TAG + 4));
    waitv<20>(); PIN(gran); PIN(bb);
    MISH_SETUP();
    lds_wait_min<4>(L, W_RDY, 11, lane);
    if (lane < 48) {
      LAS f32x2* p = (LAS f32x2*)(L + CSP + lane * 8);
      *(LAS f32x2*)(L + CSF + wi * 768 + lane * 8) = (p[0] + p[48]) + (p[96] + p[144]);
      *(LAS f32x2*)(L + CSF + wi * 768 + 384 + lane * 8) = unpk(bb) * 1.44269504088896341f;
    }
    FENCE();
    ST(12);
#pragma unroll
    for (int n = 0; n < 3; ++n) { mish1(acc1[n].s0123, acc1[n].s4567, n, 0, rs, mrs, ao, co); mish1(acc1[n].s89ab, acc1[n].scdef, n, 1, rs, mrs, ao, co); }
    lds_set(L, W_RDY + wi * 4, 12);
    ST(13);
    __builtin_amdgcn_s_setprio(2);
    LATE_LANE();
    v2u xq[5], dq[5], gr2, f3;
    const unsigned f2o = WS_F2 + (unsigned)((4 * rg + (a2 >> 1)) * 16 + 4 * wi + (ln_ & 3)) * 128;
    unsigned vm[4];
#pragma unroll
    for (int m = 0; m < 4; ++m) vm[m] = vo + ((m + wi) & 3) * 49152;
    v2u f2; v4u ysh;
    {
      f32x4 sacc[4];
#pragma unroll
      for (int i = 0; i < 4; ++i) sacc[i] = f32x4{0.f, 0.f, 0.f, 0.f};
      unsigned kb = CVS + KAO - wi * 128 - 3584 - 64, ubb = CVS + UBO - wi * 128 - 64; PIN(kb); PIN(ubb);
      LAS char* ka = L + kb; LAS char* ub = L + ubb;
      const bool late = RFL(lw(L, W_SB)) < 16;
      if (late) { waitv<0>(); arrive_flag<4>(L, W_F2C, lane, ws, WS_F2 + (unsigned)wid * 128, tlo, thi); }
      lds_wait_ge(L, W_SB, 16);
      if (par) conv_unit_shared<1>(ka + 64, ub + 64, sacc); else conv_unit_shared<0>(ka + 64, ub + 64, sacc);
      if (!late) { waitv<0>(); arrive_flag<4>(L, W_F2C, lane, ws, WS_F2 + (unsigned)wid * 128, tlo, thi); }
      ST(14);
      WL(4); WL(5);
      if (par) conv_unit_shared<1>(ka, ub, sacc); else conv_unit_shared<0>(ka, ub, sacc);
#pragma unroll
      for (int q = 0; q < 4; ++q) *(LAS f32x4*)(L + MXCH + (q * 4 + wi) * 1024 + vo) = sacc[q];
      lds_arrive(L, W_MB, lane);
    {
      const char* xb2 = x + (size_t)row2 * 1280 + cb2 * 160; const unsigned xo2 = OROW * 1280 + cg * 8;
      ld64o<0>(xq[0], xo2, xb2); ld64o<32>(xq[1], xo2, xb2); ld64o<64>(xq[2], xo2, xb2); ld64o<96>(xq[3], xo2, xb2); ld64o<128>(xq[4], xo2, xb2);
      const char* db = pDskip + cb2 * 160;
      ld64o<0>(dq[0], cg * 8, db); ld64o<32>(dq[1], cg * 8, db); ld64o<64>(dq[2], cg * 8, db); ld64o<96>(dq[3], cg * 8, db); ld64o<128>(dq[4], cg * 8, db);
      ld64c(gr2, WS_STATS + (row2 + OROW) * 8, ws);
    }
      lds_wait_ge(L, W_MB, 4);
      LAS char* p = L + MXCH + wi * 4096 + vo;
      const f32x4 s = add4(add4(*(LAS f32x4*)p, *(LAS f32x4*)(p + 1024)), add4(*(LAS f32x4*)(p + 2048), *(LAS f32x4*)(p + 3072)));
      ld64c(f2, f2o, ws);
      const unsigned y0 = pk2(s.x, s.y), y1 = pk2(s.z, s.w);
      *(LAS v2u*)(L + MXCH + wi * 4096 + YOFF) = v2u{y0, y1};
      FENCE();
      ysh = *(LAS v4u*)p;
    }
    ST(15);
    waitv<0>(); PIN(f2);
    f2 = spin64<false>(f2, f2o, ws, tlo, thi);
    lds_set(L, W_F2OK + wi * 4, 1);
    ST(16);
    AL(0); AL(1);
    if (ln_ < 32) st128c(vo, ws + WS_Y + (size_t)chS * 4096 + (2 * wi + ((wi & 1) ^ par)) * 512, ysh);
    f32x4 acc2[4][5];
    PIN(gr2);
    const float rs2 = __builtin_amdgcn_rcpf(bf2f(gr2.x)), m2 = -bfhi(gr2.x) * rs2;
#pragma unroll
    for (int n = 0; n < 5; ++n) {
      PIN(xq[n]); PIN(dq[n]);
      const f32x2 x0 = unpk(xq[n].x), x1 = unpk(xq[n].y), d0 = unpk(dq[n].x), d1 = unpk(dq[n].y);
      acc2[0][n] = f32x4{ffma(d0.x, ffma(x0.x, rs2, m2), x0.x), ffma(d0.y, ffma(x0.y, rs2, m2), x0.y),
                         ffma(d1.x, ffma(x1.x, rs2, m2), x1.x), ffma(d1.y, ffma(x1.y, rs2, m2), x1.y)};
      PIN(acc2[0][n]);
    }
    __builtin_amdgcn_s_setprio(0);
    ST(17);
    waitv<4>(); g2_step<1>(acc2, WF[0], AF[0], [&](int i) { if (i >= 3 && (i & 1)) AL1(2, (i - 3) >> 1); });
    waitv<4>();
    arrive_flag<8>(L, W_F3C, lane, ws, WS_F3 + (unsigned)(xcd * 32 + j) * 128, tlo, thi);
    g2_step<4>(acc2, WF[1], AF[1], [&](int i) { if (i >= 2) AL1(3 + ((i - 2) >> 2), (i - 2) & 3); });
    waitv<8>(); g2_step<4>(acc2, WF[2], AF[2], [&](int i) { if (i < 4) AL1(5, i); });
    ST(20);
    waitv<8>(); g2_step<4>(acc2, WF[3], AF[3], NOLD);
    waitv<4>(); g2_step<4>(acc2, WF[4], AF[4], NOLD);
    lds_wait_ge(L, W_F3C, 8);
    const unsigned f3o = WS_F3 + (unsigned)(cb2 * 32 + 8 * wi + (ln_ & 7)) * 128;
    ld64c(f3, f3o, ws);
    waitv<1>(); g2_step<4>(acc2, WF[5], AF[5], NOLD);
    ST(22);
    waitv<0>(); PIN(f3);
    f3 = spin64<false>(f3, f3o, ws, tlo, thi);
    lds_arrive(L, W_F3ALL, lane);
#pragma unroll
    for (int n = 0; n < 5; ++n) *(LAS f32x4*)(L + FM + wi * 15360 + n * 1024 + vo) = acc2[1][n];
    lds_wait_ge(L, W_F3ALL, 4);
    ST(21);
    v4u yv[3];
    {
      const char* yb = ws + WS_Y + (size_t)cb2 * 80 * 4096 + row2 * 2;
      const unsigned yo = (unsigned)(ln_ >> 1) * 4096 + (ln_ & 1) * 16;
      ld128c<0>(yv[0], yo, yb); ld128c<0>(yv[1], yo, yb + 32 * 4096); ld128c<0>(yv[2], yo & 0xffffu, yb + 64 * 4096);
    }
#pragma unroll
    for (int m = 2; m < 4; ++m)
#pragma unroll
      for (int n = 0; n < 5; ++n) *(LAS f32x4*)(L + FM + wi * 15360 + (m - 1) * 5120 + n * 1024 + vo) = acc2[m][n];
    lds_arrive(L, W_FM, lane);
    lds_wait_ge(L, W_FM, 4);
#pragma unroll
    for (int d = 1; d < 4; ++d)
#pragma unroll
      for (int n = 0; n < 5; ++n) acc2[0][n] = add4(acc2[0][n], *(LAS f32x4*)(L + FM + ((wi - d) & 3) * 15360 + (d - 1) * 5120 + n * 1024 + vo));
    waitv<0>(); PIN(yv[0]); PIN(yv[1]); PIN(yv[2]);
    LAS char* ys = L + YST + wi * 2560;
    *(LAS v4u*)(ys + vo) = yv[0]; *(LAS v4u*)(ys + 1024 + vo) = yv[1];
    if (ln_ < 32) *(LAS v4u*)(ys + 2048 + vo) = yv[2];
    FENCE();
#pragma unroll
    for (int n = 0; n < 5; ++n) {
      const v2u t = trread(ys + n * 512 + ln_ * 8);
      const f32x2 t0 = unpk(t.x), t1 = unpk(t.y);
      *(LAS f32x4*)(L + FM + 61440 + wi * 5120 + n * 1024 + vo) = add4(acc2[0][n], f32x4{t0.x, t0.y, t1.x, t1.y});
    }
    lds_set(L, W_F2OK + wi * 4, 2);
    STEND();
    asm volatile("97:");
    __builtin_amdgcn_endpgm();
  }

  const bool odd = wi & 1;
  const int kch = chA + (wi >> 1);
  const unsigned vo = lane * 16;
  v4u WF[6][5], AF[6][4];
  auto reduce = [&](int c, int i, bool three) {
    LAS char* pp = L + KG_SCR + 3 * c * KG_WSTR;
    f32x4 s = fma4(*(LAS f32x4*)(pp + i * 16), *(LAS float*)(pp + 2576), f32x4{0.f, 0.f, 0.f, 0.f});
    s = fma4(*(LAS f32x4*)(pp + KG_WSTR + i * 16), *(LAS float*)(pp + KG_WSTR + 2576), s);
    if (three) s = fma4(*(LAS f32x4*)(pp + 2 * KG_WSTR + i * 16), *(LAS float*)(pp + 2 * KG_WSTR + 2576), s);
    *(LAS f32x4*)(L + KTAB + c * KP_STRIDE + i * 16) = s;
    return s;
  };
  lds_wait_ge(L, W_KG, 8);
  if (ts < 161) { st128c((unsigned)ts * 16, ws + WS_KP + (size_t)wid * KP_STRIDE, __builtin_bit_cast(v4u, reduce(2, ts, false))); reduce(0, ts, true); }
  if (ts >= 95) reduce(1, ts - 95, true);
  if (ts < 3) *(LAS float*)(L + KTAB + ts * KP_STRIDE - 4) = 0.f;
  lds_arrive(L, W_SB, lane);
  waitv<0>();
  arrive_flag(L, W_FKC, lane, ws, WS_FK + (unsigned)wid * 64, tlo, thi);
  ST(9);
  __builtin_amdgcn_s_setprio(0);
  lds_wait_min<4>(L, W_RDY, F1STAGE, lane);
  const unsigned f1o = WS_F1 + (unsigned)xcd * 2048 + ts * 8;
  v2u f1own; ld64c(f1own, f1o, ws);
  waitv<0>(); PIN(f1own);
  f1own = spin64<false>(f1own, f1o, ws, tlo, thi);
  v4u uu[3]; unsigned ulo[3];
#pragma unroll
  for (int r = 0; r < 3; ++r) {
    const int e = 64 * r + lane, tb = 64 * wi + e / 3, c = e % 3;
    ld128c<0>(uu[r], tb * 80 + c * 16, ws + WS_XNB + (size_t)(16 * xcd + (j >> 1)) * 20480 + par * 32);
    ulo[r] = (c == 2 - 2 * par ? CVS : CVA + (c - par) * CV_CH) + tb * 16;
  }
  if (b == 0) arrive_flag(L, W_F1ALL, lane, ws, WS_E, tlo, thi);
  else lds_arrive(L, W_F1ALL, lane);
  ST(4);
  const unsigned fko = WS_FK + (unsigned)(wid ^ 1) * 64;
  v2u fk; ld64c(fk, fko, ws);
  waitv<0>(); PIN(fk); PIN(uu[0]); PIN(uu[1]); PIN(uu[2]);
  fk = spin64<false>(fk, fko, ws, tlo, thi);
  v4u kp; ld128c<0>(kp, (unsigned)(ts < 161 ? ts : 160) * 16, ws + WS_KP + (size_t)(wid ^ 1) * KP_STRIDE);
  lds_wait_ge(L, W_SB, 8);
  waitv<0>(); PIN(kp);
  if (ts < 161) { LAS f32x4* tp = (LAS f32x4*)(L + KTAB + 2 * KP_STRIDE + ts * 16); *tp = add4(*tp, __builtin_bit_cast(f32x4, kp)); }
  lds_arrive(L, W_SB, lane);
  lds_wait_ge(L, W_F1ALL, 4);
  WL(0);
  lds_wait_min<4>(L, W_RDY, 9, lane);
  put_k(L, kix, ts, 0, CVA);
  WL(1);
  lds_wait_min<4>(L, W_RDY, 10, lane);
  put_k(L, kix, ts, 1, CVA + CV_CH);
  if (ZCOND) { *(LAS v4u*)(L + CVA + ZOFF) = v4u{0u, 0u, 0u, 0u}; *(LAS v4u*)(L + CVA + CV_CH + ZOFF) = v4u{0u, 0u, 0u, 0u}; }
  ST(7);
  lds_wait_min<4>(L, W_RDY, 11, lane);
  ST(11);
  put_u(L + ulo[0], uu[0], ts < 3); put_u(L + ulo[1], uu[1], false); put_u(L + ulo[2], uu[2], false);
  if (ZCOND) *(LAS v4u*)(L + CVS + ZOFF) = v4u{0u, 0u, 0u, 0u};
  lds_wait_ge(L, W_SB, 12);
  put_k(L, kix, ts, 2, CVS);
  lds_arrive(L, W_SB, lane);
  WL(2); WL(3); WL(4); WL(5);
  ST(5);
  lds_wait_ge(L, W_SB, 16);
  lds_wait_ge(L, W_RDY + wi * 4, 12);
  ST(13);
  __builtin_amdgcn_s_setprio(SPRIO2);
  LATE_LANE();
  f32x4 cacc[8];
#pragma unroll
  for (int i = 0; i < 8; ++i) cacc[i] = f32x4{0.f, 0.f, 0.f, 0.f};
  {
    const unsigned o1 = CVA + (wi >> 1) * CV_CH - (odd ? 4 * 64 : 0);
    unsigned kb = KAO + o1 - 3584 - 192, ubb = UBO + o1 - 192; PIN(kb); PIN(ubb);
    LAS char* ka = L + kb; LAS char* ub = L + ubb;
    v4u A[8], B[8];
#define LA(i, u) (*(LAS v4u*)(ka + 512 * (7 - (i)) + 64 * (3 - (u))))
#define LB(i, u) (*(LAS v4u*)(ub + 512 * (i) + 64 * (3 - (u))))
    A[7] = LA(7, 0); B[0] = LB(0, 0); A[0] = LA(0, 0); B[7] = LB(7, 0); A[6] = LA(6, 0); B[6] = LB(6, 0); A[1] = LA(1, 0); B[1] = LB(1, 0);
    A[5] = LA(5, 0); B[5] = LB(5, 0); A[2] = LA(2, 0); B[2] = LB(2, 0); A[4] = LA(4, 0); B[4] = LB(4, 0); A[3] = LA(3, 0); B[3] = LB(3, 0);
    SCHED();
#pragma unroll
    for (int u = 0; u < 4; ++u) {
      v4u An[2], Bn[2];
#pragma unroll
      for (int sd = 7; sd >= 0; --sd) {
#pragma unroll
        for (int o = 0; o <= sd; ++o) {
          if (sd + o < 8) cacc[sd + o] = mfma(A[sd], B[o], cacc[sd + o]);
          if (o != sd && sd + o < 8) cacc[sd + o] = mfma(A[o], B[sd], cacc[sd + o]);
        }
        SCHED();
        if (u < 3) {
          if (sd >= 2) { A[sd] = LA(sd, u + 1); B[sd] = LB(sd, u + 1); }
          if (sd == 6 || sd == 5) { An[6 - sd] = LA(6 - sd, u + 1); Bn[6 - sd] = LB(6 - sd, u + 1); }
        }
        SCHED();
      }
      if (u < 3) { A[0] = An[0]; A[1] = An[1]; B[0] = Bn[0]; B[1] = Bn[1]; }
    }
#undef LA
#undef LB
  }
  ST(10);
  unsigned vm[4];
  {
    LAS char* px = L + SXCH + wi * 4096 + vo;
    if (odd) {
#pragma unroll
      for (int i = 0; i < 4; ++i) { *(LAS f32x4*)(px + i * 1024) = cacc[i]; cacc[i] = cacc[4 + i]; }
    } else {
#pragma unroll
      for (int i = 0; i < 4; ++i) *(LAS f32x4*)(px + i * 1024) = cacc[4 + i];
    }
    lds_arrive(L, W_SX + (wi >> 1) * 4, lane);
#pragma unroll
    for (int m = 0; m < 4; ++m) vm[m] = vo + ((m + wi) & 3) * 49152;
    lds_wait_ge(L, W_SX + (wi >> 1) * 4, 2);
    px = L + SXCH + (wi ^ 1) * 4096 + vo;
    v2u yo[4];
#pragma unroll
    for (int i = 0; i < 4; ++i) { const f32x4 s = add4(cacc[i], *(LAS f32x4*)(px + i * 1024)); yo[i] = v2u{pk2(s.x, s.y), pk2(s.z, s.w)}; }
    char* yb = ws + WS_Y + (size_t)kch * 4096 + (odd ? 2048 : 0);
    LAS char* yl = L + SXCH + (wi ^ 1) * 4096;
#pragma unroll
    for (int i = 0; i < 4; ++i) *(LAS v2u*)(yl + i * 512 + YOFF) = yo[i];
    FENCE();
    st128c(vo, yb, *(LAS v4u*)(yl + vo)); st128c(vo, yb + 1024, *(LAS v4u*)(yl + 1024 + vo));
  }
  ST(14);
  lds_wait_ge(L, W_F2OK + wi * 4, 1);
  ST(16);
  AL(0); AL(1);
  ST(17);
  f32x4 acc2[4][5];
  waitv<8>();
  arrive_flag<8>(L, W_F3C, lane, ws, WS_F3 + (unsigned)(xcd * 32 + j) * 128, tlo, thi);
  __builtin_amdgcn_s_setprio(0);
  waitv<4>(); g2_step<0>(acc2, WF[0], AF[0], [&](int i) { if (i >= 3 && (i & 1)) AL1(2, (i - 3) >> 1); });
  waitv<4>(); g2_step<4>(acc2, WF[1], AF[1], [&](int i) { if (i >= 2) AL1(3 + ((i - 2) >> 2), (i - 2) & 3); });
  waitv<8>(); g2_step<4>(acc2, WF[2], AF[2], [&](int i) { if (i < 4) AL1(5, i); });
  ST(20);
  waitv<8>(); g2_step<4>(acc2, WF[3], AF[3], NOLD);
  lds_wait_ge(L, W_F3C, 8);
  waitv<0>();
#pragma unroll
  for (int m = 0; m < 4; ++m) { PIN(AF[4][m]); PIN(AF[5][m]); }
#pragma unroll
  for (int n = 0; n < 5; ++n) { PIN(WF[4][n]); PIN(WF[5][n]); }
#pragma unroll
  for (int mm = 1; mm < 5; ++mm) {
    const int m = mm & 3;
#pragma unroll
    for (int n = 0; n < 5; ++n) {
      acc2[m][n] = mfma(WF[4][n], AF[4][m], acc2[m][n]);
      acc2[m][n] = mfma(WF[5][n], AF[5][m], acc2[m][n]);
      if (mm > 1) *(LAS f32x4*)(L + FS + wi * 15360 + (mm - 2) * 5120 + n * 1024 + vo) = acc2[mm - 1][n];
      SCHED();
    }
  }
  lds_arrive(L, W_FS, lane);
  ST(22);
  lds_wait_ge(L, W_FS, 4);
#pragma unroll
  for (int d = 1; d < 4; ++d)
#pragma unroll
    for (int n = 0; n < 5; ++n) acc2[0][n] += *(LAS f32x4*)(L + FS + ((wi - d) & 3) * 15360 + (d - 1) * 5120 + n * 1024 + vo);
  ST(23);
  lds_wait_ge(L, W_F2OK + wi * 4, 2);
  ST(24);
  {
    char* ob = out + (size_t)row2 * 1280 + cb2 * 160; const unsigned oo = OROW * 1280 + cg * 8;
    v2u o[5];
#pragma unroll
    for (int n = 0; n < 5; ++n) {
      const f32x4 s = acc2[0][n] + *(LAS f32x4*)(L + FM + 61440 + wi * 5120 + n * 1024 + vo);
      o[n] = v2u{pk2(s.x, s.y), pk2(s.z, s.w)};
    }
    st64o<0>(oo, ob, o[0]); st64o<32>(oo, ob, o[1]); st64o<64>(oo, ob, o[2]); st64o<96>(oo, ob, o[3]); st64o<128>(oo, ob, o[4]);
  }
  STEND();
  asm volatile("99:");
}

extern "C" void kernel_launch(void* const* d_in, const int* in_sizes, int n_in, void* d_out, int out_size, void* d_ws, size_t ws_size,
                              hipStream_t stream) {
  fused_s4_mlp<<<256, 512, 0, stream>>>((const char*)d_in[0], (const char*)d_in[1], (const char*)d_in[2], (const char*)d_in[3],
      (const char*)d_in[4], (const char*)d_in[5], (const char*)d_in[6], (const char*)d_in[7], (const char*)d_in[8], (const char*)d_in[10],
      (const char*)d_in[11], (const char*)d_in[12], (char*)d_out, (char*)d_ws);
}
```

```cpp
#include <hip/hip_runtime.h>
#include <stdint.h>

typedef unsigned short u16;
typedef unsigned long long u64;
typedef unsigned v4u __attribute__((ext_vector_type(4)));
typedef unsigned v2u __attribute__((ext_vector_type(2)));
typedef float f32x4 __attribute__((ext_vector_type(4)));
typedef float f32x2 __attribute__((ext_vector_type(2)));
typedef short bf16x8 __attribute__((ext_vector_type(8)));
typedef short s16x4 __attribute__((ext_vector_type(4)));
typedef __bf16 bf16x2_t __attribute__((ext_vector_type(2)));
#define LAS __attribute__((address_space(3)))
#define GAS __attribute__((address_space(1)))
#define DI __device__ __forceinline__

constexpr unsigned WS_E = 0, WS_TMO = 64, WS_F1 = 131072, WS_STATS = 16384, WS_F2 = 32768, WS_F3 = 65536, WS_FK = 98304, WS_STAMP = 262144;
constexpr unsigned WS_XNB = 1u << 20, WS_W2T = 4u << 20, WS_Y = 7u << 20, WS_ACT = 10u << 20, WS_KP = 17u << 20;
constexpr unsigned KP_STRIDE = 2624;
constexpr unsigned SPIN_CAP = 400000;

constexpr unsigned W_F1C = 0, W_F2C = 4, W_F3C = 8, W_KG = 12, W_MB = 16, W_SB = 20, W_SX = 24, W_FM = 32, W_FS = 36, W_F3ALL = 40, W_F1ALL = 44,
                   W_F2OK = 48, W_RDY = 64, W_TAG = 80, W_FKC = 96, W_ST = 100;
constexpr unsigned KTAB = 272;
constexpr unsigned CSP = 8192;
constexpr unsigned CVA = 10240, CV_CH = 27008, CV_USTR = 4640, CV_KOFF = 18560, CV_KSTR = 4224;
constexpr unsigned KG_SCR = CVA, KG_WSTR = 4352, LN_ROWS = KG_SCR + 8 * KG_WSTR, LN_PITCH = 1344, W2P = LN_ROWS + 10752;
constexpr unsigned KPART = KG_SCR + 4 * KG_WSTR;
constexpr unsigned G1_RING = 64512, G1_STAGE = 28672, G1_BOFF = 16384;
constexpr unsigned CVS = G1_RING, SXCH = CVS + 27136, MXCH = SXCH + 16384;
constexpr unsigned CSF = G1_RING + 3 * G1_STAGE, YST = CSF + 3072;
constexpr unsigned FM = 256, FS = FM + 81920;
static_assert(FS + 61440 <= CSF && CVA + 2 * CV_CH <= G1_RING && W2P + 8192 <= G1_RING && YST + 10240 <= 163840 && MXCH + 16384 <= CSF, "lds map");

#define ROT2 0
#define F1STAGE 5
#define SPRIO2 1
#define F1GAP 16
#define F2GAP 8
#define SPRIO 1
#define ST(i) do {} while (0)
#define STEND() do {} while (0)
#define STARG
#define STPASS

DI float bf2f(unsigned v) { return __builtin_bit_cast(float, v << 16); }
DI float bfhi(unsigned v) { return __builtin_bit_cast(float, v & 0xffff0000u); }
DI unsigned pk2(float lo, float hi) { bf16x2_t r; r.x = (__bf16)lo; r.y = (__bf16)hi; return __builtin_bit_cast(unsigned, r); }
DI float rbf(float f) { return bf2f(pk2(f, f) & 0xffffu); }
DI f32x4 mfma(v4u a, v4u b, f32x4 c) {
  return __builtin_amdgcn_mfma_f32_16x16x32_bf16(__builtin_bit_cast(bf16x8, a), __builtin_bit_cast(bf16x8, b), c, 0, 0, 0);
}
DI v2u trread(LAS char* p) { return __builtin_bit_cast(v2u, __builtin_amdgcn_ds_read_tr16_b64_v4i16((LAS s16x4*)p)); }
template <int N> DI void waitv() { asm volatile("s_waitcnt vmcnt(%0)" ::"i"(N) : "memory"); }
DI void bar() { asm volatile("s_waitcnt lgkmcnt(0)\n\ts_barrier" ::: "memory"); }
#define PIN(x) asm volatile("" : "+v"(x))
#define FENCE() asm volatile("" ::: "memory")
#define SCHED() __builtin_amdgcn_sched_barrier(0)
#define RFL(x) __builtin_amdgcn_readfirstlane(x)

DI void ld16(unsigned& d, unsigned vo, const void* sb) { asm volatile("global_load_ushort %0, %1, %2" : "=v"(d) : "v"(vo), "s"(sb) : "memory"); }
template <int O> DI void ld32(unsigned& d, unsigned vo, const void* sb) { asm volatile("global_load_dword %0, %1, %2 offset:%3" : "=v"(d) : "v"(vo), "s"(sb), "i"(O) : "memory"); }
DI void ld64(v2u& d, unsigned vo, const void* sb) { asm volatile("global_load_dwordx2 %0, %1, %2" : "=v"(d) : "v"(vo), "s"(sb) : "memory"); }
DI void ld128(v4u& d, unsigned vo, const void* sb) { asm volatile("global_load_dwordx4 %0, %1, %2" : "=v"(d) : "v"(vo), "s"(sb) : "memory"); }
DI void ld64c(v2u& d, unsigned vo, const void* sb) { asm volatile("global_load_dwordx2 %0, %1, %2 sc1" : "=v"(d) : "v"(vo), "s"(sb) : "memory"); }
template <int O> DI void ld128c(v4u& d, unsigned vo, const void* sb) { asm volatile("global_load_dwordx4 %0, %1, %2 offset:%3 sc1" : "=v"(d) : "v"(vo), "s"(sb), "i"(O) : "memory"); }
DI void st64c(unsigned vo, void* sb, v2u d) { asm volatile("global_store_dwordx2 %0, %1, %2 sc1" ::"v"(vo), "v"(d), "s"(sb) : "memory"); }
template <int O> DI void ld64o(v2u& d, unsigned vo, const void* sb) { asm volatile("global_load_dwordx2 %0, %1, %2 offset:%3" : "=v"(d) : "v"(vo), "s"(sb), "i"(O) : "memory"); }
template <int O> DI void st64o(unsigned vo, void* sb, v2u d) { asm volatile("global_store_dwordx2 %0, %1, %2 offset:%3" ::"v"(vo), "v"(d), "s"(sb), "i"(O) : "memory"); }
template <int O> DI void st64co(unsigned vo, void* sb, v2u d) { asm volatile("global_store_dwordx2 %0, %1, %2 offset:%3 sc1" ::"v"(vo), "v"(d), "s"(sb), "i"(O) : "memory"); }
DI void st128c(unsigned vo, void* sb, v4u d) { asm volatile("global_store_dwordx4 %0, %1, %2 sc1\n\ts_nop 1" ::"v"(vo), "v"(d), "s"(sb) : "memory"); }
DI void st64u(unsigned vo, void* sb, v2u d) { asm volatile("global_store_dwordx2 %0, %1, %2 sc1 ; CNT" ::"v"(vo), "v"(d), "s"(sb) : "memory"); }
DI void st128u(unsigned vo, void* sb, v4u d) { asm volatile("global_store_dwordx4 %0, %1, %2 sc1 ; CNT\n\ts_nop 1" ::"v"(vo), "v"(d), "s"(sb) : "memory"); }
DI void dma16(unsigned vo, const void* sb, unsigned lds) {
  asm volatile("s_mov_b32 m0, %2\n\ts_nop 0\n\tglobal_load_lds_dwordx4 %0, %1" ::"v"(vo), "s"(sb), "s"(lds) : "memory");
}
template <int O> DI void dma16o(unsigned vo, const void* sb) {
  asm volatile("global_load_lds_dwordx4 %0, %1 offset:%2" ::"v"(vo), "s"(sb), "i"(O) : "memory");
}
template <bool HIONLY> DI v2u spin64(v2u cur, unsigned vo, char* ws, unsigned tlo, unsigned thi) {
  if (__builtin_expect(__builtin_amdgcn_ballot_w64(HIONLY ? (cur.y != tlo) : (cur.x != tlo || cur.y != thi)) != 0ull, 0)) {
#pragma nounroll
    for (unsigned spins = 0; spins < SPIN_CAP; ++spins) {
      __builtin_amdgcn_s_sleep(6);
      asm volatile("global_load_dwordx2 %0, %1, %2 sc1\n\ts_waitcnt vmcnt(0)" : "=&v"(cur) : "v"(vo), "s"(ws) : "memory");
      if (__builtin_amdgcn_ballot_w64(HIONLY ? (cur.y != tlo) : (cur.x != tlo || cur.y != thi)) == 0ull) break;
    }
  }
  return cur;
}
DI unsigned lw(LAS char* L, unsigned off) { return *(volatile LAS unsigned*)(L + off); }
DI void lds_wait_at(unsigned a, unsigned target) {
  unsigned v, cnt; u64 t;
  asm volatile("s_mov_b32 %1, %5\n"
               "1:\n\tds_read_b32 %0, %3\n\ts_sub_u32 %1, %1, 1\n\ts_waitcnt lgkmcnt(0)\n\tv_cmp_gt_u32 vcc, %4, %0\n\t"
               "s_cselect_b64 %2, 0, vcc\n\ts_cmp_lg_u64 %2, 0\n\ts_cbranch_scc1 1b"
               : "=&v"(v), "=&s"(cnt), "=&s"(t) : "v"(a), "s"(target), "i"(4 * SPIN_CAP) : "vcc", "scc", "memory");
}
DI void lds_wait_ge(LAS char* L, unsigned off, unsigned target) { lds_wait_at((unsigned)(size_t)L + off, target); }
template <int N> DI void lds_wait_min(LAS char* L, unsigned off, unsigned target, int lane) {
  lds_wait_at((unsigned)(size_t)L + off + (lane & (N - 1)) * 4, target);
}
DI void lds_set(LAS char* L, unsigned off, unsigned v) { FENCE(); *(volatile LAS unsigned*)(L + off) = v; FENCE(); }
DI void lds_arrive(LAS char* L, unsigned off, int) {
  asm volatile("s_mov_b64 exec, 1\n\tds_add_u32 %0, %1\n\ts_mov_b64 exec, -1" ::"v"((unsigned)(size_t)L + off), "v"(1u) : "memory");
}
template <unsigned N = 4> DI void arrive_flag(LAS char* L, unsigned off, int, char* ws, unsigned fo, unsigned tlo, unsigned thi) {
  unsigned old;
  asm volatile("s_mov_b64 exec, 1\n\tds_add_rtn_u32 %0, %1, %2\n\ts_waitcnt lgkmcnt(0)\n\tv_cmp_eq_u32 vcc, %6, %0\n\t"
               "s_mov_b64 exec, vcc\n\tglobal_store_dwordx2 %3, %4, %5 sc1\n\ts_mov_b64 exec, -1"
               : "=&v"(old) : "v"((unsigned)(size_t)L + off), "v"(1u), "v"(fo), "v"(v2u{tlo, thi}), "s"(ws), "i"(N - 1) : "vcc", "memory");
}
DI void arrive_flag8(LAS char* L, unsigned off, char* ws, unsigned fo, unsigned tlo, unsigned thi) {
  unsigned old;
  asm volatile("s_mov_b64 exec, 1\n\tds_add_rtn_u32 %0, %1, %2\n\ts_waitcnt lgkmcnt(0)\n\tv_cmp_eq_u32 vcc, 3, %0\n\t"
               "s_mul_i32 exec_lo, vcc_lo, 0xff\n\tglobal_store_dwordx2 %3, %4, %5 sc1\n\ts_mov_b64 exec, -1"
               : "=&v"(old) : "v"((unsigned)(size_t)L + off), "v"(1u), "v"(fo), "v"(v2u{tlo, thi}), "s"(ws) : "vcc", "memory");
}
typedef float f32x16 __attribute__((ext_vector_type(16)));
DI f32x16 mfma32(v4u a, v4u b, f32x16 c) {
  return __builtin_amdgcn_mfma_f32_32x32x16_bf16(__builtin_bit_cast(bf16x8, a), __builtin_bit_cast(bf16x8, b), c, 0, 0, 0);
}
template <int C> DI float dpp_add(float v) {
  return v + __builtin_bit_cast(float, __builtin_amdgcn_update_dpp(0, __builtin_bit_cast(int, v), C, 0xf, 0xf, true));
}
DI float rl(float v, int l) { return __builtin_bit_cast(float, __builtin_amdgcn_readlane(__builtin_bit_cast(int, v), l)); }
DI float half_sum(float v, bool upper) {
  v = dpp_add<0xB1>(v); v = dpp_add<0x4E>(v); v = dpp_add<0x141>(v); v = dpp_add<0x140>(v);
  const float a = rl(v, 0) + rl(v, 16), c = rl(v, 32) + rl(v, 48);
  return upper ? c : a;
}
DI f32x2 unpk(unsigned v) { return f32x2{bf2f(v), bfhi(v)}; }
typedef f32x2 cf;
DI cf cmul1(cf i, cf w) { return cf{__builtin_fmaf(i.x, w.x, -(i.y * w.y)), __builtin_fmaf(i.x, w.y, i.y * w.x)}; }
DI void cmul4(cf& o0, cf& o1, cf& o2, cf& o3, cf i0, cf i1, cf i2, cf i3, cf w0, cf w1, cf w2, cf w3) {
  o0 = cmul1(i0, w0); o1 = cmul1(i1, w1); o2 = cmul1(i2, w2); o3 = cmul1(i3, w3);
}
DI unsigned pkc(cf v) { return pk2(v.x, v.y); }
template <int R> DI void put4(cf a, cf b, cf c, cf d) {
  asm volatile("ds_write_addtid_b32 %0 offset:%4\n\tds_write_addtid_b32 %1 offset:%5\n\tds_write_addtid_b32 %2 offset:%6\n\tds_write_addtid_b32 %3 offset:%7"
               ::"v"(pkc(a)), "v"(pkc(b)), "v"(pkc(c)), "v"(pkc(d)), "i"(R * 272), "i"(R * 272 + 272), "i"(R * 272 + 544), "i"(R * 272 + 816) : "memory");
}
template <int R> DI void put1(cf a) { asm volatile("ds_write_addtid_b32 %0 offset:%1" ::"v"(pkc(a)), "i"(R * 272) : "memory"); }
DI cf cexpk(float e1, float th, float tl, float k, float ks) {
  const float mag = __builtin_amdgcn_exp2f(e1 * k);
  const float fr = __builtin_fmaf(tl, ks, __builtin_amdgcn_fractf(th * ks));
  return cf{__builtin_amdgcn_cosf(fr), __builtin_amdgcn_sinf(fr)} * mag;
}
DI void kgen_slow(cf a, cf W1, cf W256, cf W8, LAS char* sr, f32x4 (&kacc)[2][2]) {
  cf W2, W512, u1, W16, u2, u3, W4, W32, u4, u5, u6, u7, r8, r9, r10, r11, r12, r13, r14, r15, r16, r17, W1024, v3, r18, r19, r20, v1;
  cmul4(W2, W512, u1, W16, W1, W256, a, W8, W1, W256, W1, W8);
  cmul4(u2, u3, W4, W32, a, u1, W2, W16, W2, W2, W2, W16);
  put4<0>(a, u1, u2, u3);
  cmul4(u4, u5, u6, u7, a, u1, u2, u3, W4, W4, W4, W4);
  put4<4>(u4, u5, u6, u7);
  cmul4(r8, r9, r10, r11, a, u2, u4, u6, W256, W256, W256, W256);
  put4<8>(r8, r9, r10, r11);
  cmul4(r12, r13, r14, r15, a, u4, r8, r10, W512, W512, W512, W512);
  put4<12>(r12, r13, r14, r15);
  v4u UA[2][4];
#pragma unroll
  for (int k = 0; k < 4; ++k) UA[0][k] = *(LAS v4u*)(sr + 64 * k);
  cmul4(r16, r17, W1024, v3, r12, r14, W512, W8, W512, W512, W512, W16);
  cmul4(r18, r19, r20, v1, r12, r14, r16, W8, W1024, W1024, W1024, W32);
  put4<0>(r16, r17, r18, r19);
  put1<4>(r20);
#pragma unroll
  for (int k = 0; k < 4; ++k) UA[1][k] = *(LAS v4u*)(sr + 64 * k);
  put4<0>(cf{1.f, 0.f}, W8, W16, v3);
  cf v0 = W32, v2, t0, t1, t2, t3;
  cmul4(v2, t3, t0, t1, W16, v3, v0, v1, W32, W32, W32, W32);
  put4<4>(v0, v1, v2, t3);
  cmul4(t2, v3, v0, v1, v2, t3, t0, t1, W32, W32, W32, W32);
  put4<8>(t0, t1, t2, v3);
  cmul4(v2, t3, t0, t1, t2, v3, v0, v1, W32, W32, W32, W32);
  put4<12>(v0, v1, v2, t3);
  v4u vb[4];
#pragma unroll
  for (int k = 0; k < 4; ++k) vb[k] = *(LAS v4u*)(sr + 64 * k);
  cmul4(t2, v3, v0, v1, v2, t3, t0, t1, W32, W32, W32, W32);
  put4<0>(t0, t1, t2, v3);
  cmul4(v2, t3, t0, t1, t2, v3, v0, v1, W32, W32, W32, W32);
  put4<4>(v0, v1, v2, t3);
#pragma unroll
  for (int k = 0; k < 4; ++k) {
    kacc[0][0] = mfma(UA[0][k], vb[k], kacc[0][0]);
    kacc[1][0] = mfma(UA[1][k], vb[k], kacc[1][0]);
  }
  cmul4(t2, v3, v0, v1, v2, t3, t0, t1, W32, W32, W32, W32);
  put4<8>(t0, t1, t2, v3);
  cmul4(v2, t3, t0, t1, t2, v3, v0, v1, W32, W32, W32, W32);
  put4<12>(v0, v1, v2, t3);
#pragma unroll
  for (int k = 0; k < 4; ++k) vb[k] = *(LAS v4u*)(sr + 64 * k);
  SCHED();
#pragma unroll
  for (int k = 0; k < 4; ++k) {
    kacc[0][1] = mfma(UA[0][k], vb[k], kacc[0][1]);
    kacc[1][1] = mfma(UA[1][k], vb[k], kacc[1][1]);
  }
}
typedef _Float16 f16x8 __attribute__((ext_vector_type(8)));
typedef _Float16 f16x2 __attribute__((ext_vector_type(2)));
DI f32x4 mfmah(v4u a, v4u b, f32x4 c) {
  return __builtin_amdgcn_mfma_f32_16x16x32_f16(__builtin_bit_cast(f16x8, a), __builtin_bit_cast(f16x8, b), c, 0, 0, 0);
}
DI unsigned pkh(cf v) { f16x2 r; r.x = (_Float16)v.x; r.y = (_Float16)v.y; return __builtin_bit_cast(unsigned, r); }
DI cf csq(cf v) { return cf{__builtin_fmaf(v.x, v.x, -(v.y * v.y)), (v.x + v.x) * v.y}; }
#define HMUL(o, i, w) "v_pk_mul_f16 " o ", " i ", " w " op_sel:[1,1] op_sel_hi:[1,0] neg_lo:[0,1]\n\t"
#define HFMA(o, i, w) "v_pk_fma_f16 " o ", " i ", " w ", " o " op_sel_hi:[0,1,1]\n\t"
template <int R0, int R1, int R2, int R3>
DI void h4(unsigned& o0, unsigned& o1, unsigned& o2, unsigned& o3, unsigned i0, unsigned i1, unsigned i2, unsigned i3, unsigned w0, unsigned w1, unsigned w2, unsigned w3) {
  asm volatile(HMUL("%0", "%4", "%8") HMUL("%1", "%5", "%9") HMUL("%2", "%6", "%10") HMUL("%3", "%7", "%11")
               HFMA("%0", "%4", "%8") HFMA("%1", "%5", "%9") HFMA("%2", "%6", "%10") HFMA("%3", "%7", "%11")
               "ds_write_addtid_b32 %0 offset:%12\n\tds_write_addtid_b32 %1 offset:%13\n\tds_write_addtid_b32 %2 offset:%14\n\tds_write_addtid_b32 %3 offset:%15"
               : "=&v"(o0), "=&v"(o1), "=&v"(o2), "=&v"(o3)
               : "v"(i0), "v"(i1), "v"(i2), "v"(i3), "v"(w0), "v"(w1), "v"(w2), "v"(w3), "i"(R0 * 272), "i"(R1 * 272), "i"(R2 * 272), "i"(R3 * 272) : "memory");
}
template <int R> DI void hput(unsigned v) { asm volatile("ds_write_addtid_b32 %0 offset:%1" ::"v"(v), "i"(R * 272) : "memory"); }
template <int C> DI float dpp_max(float v) {
  return __builtin_fmaxf(v, __builtin_bit_cast(float, __builtin_amdgcn_update_dpp(0, __builtin_bit_cast(int, v), C, 0xf, 0xf, true)));
}
template <class F> DI float kgen_task(const unsigned (&pr)[7], bool dead, unsigned sw, LAS char* sr, f32x4 (&kacc)[2][2], F mid) {
  const float dt = rbf(__builtin_amdgcn_exp2f(bf2f(pr[6]) * 1.44269504088896341f));
  const float zr = dt * bf2f(pr[0]), zi = dt * bf2f(pr[1]);
  const float dbr = dt * bf2f(pr[2]), dbi = dt * bf2f(pr[3]), cr = bf2f(pr[4]), ci = bf2f(pr[5]);
  cf a = cf{cr * dbr - ci * dbi, -(cr * dbi + ci * dbr)};
  if (dead) a = cf{0.f, 0.f};
  const float e1 = zr * 1.44269504088896341f, th = zi * 0.15915494309189535f;
  const float tl = __builtin_fmaf(zi, 6.4206383e-9f, __builtin_fmaf(zi, 0.15915494309189535f, -th));
  const cf W1 = cexpk(e1, th, tl, 1.f, -1.f), W256 = cexpk(e1, th, tl, 256.f, -256.f), W8 = cexpk(e1, th, tl, 8.f, 8.f);
  if (__builtin_expect(__builtin_amdgcn_ballot_w64(!(zr <= 0.f)) != 0ull, 0)) {
    mid(1); mid(2);
    asm volatile("s_mov_b32 m0, %0" ::"s"(sw) : "memory");
#pragma unroll
    for (int i = 0; i < 4; ++i) kacc[i >> 1][i & 1] = f32x4{0.f, 0.f, 0.f, 0.f};
    kgen_slow(a, W1, W256, W8, sr, kacc);
    return 1.f;
  }
  float am = __builtin_fmaxf(__builtin_fabsf(a.x), __builtin_fabsf(a.y));
  am = dpp_max<0xB1>(am); am = dpp_max<0x4E>(am); am = dpp_max<0x141>(am); am = dpp_max<0x140>(am);
  const unsigned e0 = __builtin_amdgcn_readlane(__builtin_bit_cast(unsigned, am), 0), e16 = __builtin_amdgcn_readlane(__builtin_bit_cast(unsigned, am), 16);
  const unsigned e32 = __builtin_amdgcn_readlane(__builtin_bit_cast(unsigned, am), 32), e48 = __builtin_amdgcn_readlane(__builtin_bit_cast(unsigned, am), 48);
  unsigned ex = max(max(e0, e16), max(e32, e48)) >> 23;
  ex = min(max(ex, 2u), 252u);
  const float up = __builtin_bit_cast(float, ex << 23), down = __builtin_bit_cast(float, (254u - ex) << 23);
  const cf W2 = csq(W1), W512 = csq(W256), W16 = csq(W8), W4 = csq(W2), W1024 = csq(W512), W32 = csq(W16), W64 = csq(W32), W128 = csq(W64);
  const unsigned u0 = pkh(a * down), h1 = pkh(W1), h2 = pkh(W2), h4_ = pkh(W4), h256 = pkh(W256), h512 = pkh(W512), h1024 = pkh(W1024);
  unsigned u1, u2, u4, r8, u3, u5, u6, r12, u7, r9, r10, r14, r11, r13, r15, r16, r17, r18, r19, r20;
  asm volatile("s_mov_b32 m0, %0\n\ts_nop 0" ::"s"(sw) : "memory");
  hput<0>(u0);
  h4<1, 2, 4, 8>(u1, u2, u4, r8, u0, u0, u0, u0, h1, h2, h4_, h256);
  h4<3, 5, 6, 12>(u3, u5, u6, r12, u2, u4, u4, u0, h1, h1, h2, h512);
  h4<7, 9, 10, 14>(u7, r9, r10, r14, u6, r8, r8, r8, h1, h2, h4_, h512);
  h4<11, 13, 15, 15>(r11, r13, r16, r15, r10, r12, u0, r14, h2, h4_, h1024, h4_);
  v4u UA[2][4];
#pragma unroll
  for (int k = 0; k < 4; ++k) UA[0][k] = *(LAS v4u*)(sr + 64 * k);
  hput<0>(r16);
  h4<1, 2, 3, 4>(r17, r18, r19, r20, r8, r12, r14, r16, h1024, h1024, h1024, h1024);
#pragma unroll
  for (int k = 0; k < 4; ++k) UA[1][k] = *(LAS v4u*)(sr + 64 * k);
  mid(1);
  unsigned v[16], t0, t1, t2, t3;
  v[1] = pkh(W8); v[2] = pkh(W16); v[4] = pkh(W32); v[8] = pkh(W64);
  const unsigned h128 = pkh(W128);
  hput<0>(0x3c00u); hput<1>(v[1]); hput<2>(v[2]); hput<4>(v[4]); hput<8>(v[8]);
  h4<3, 5, 6, 9>(v[3], v[5], v[6], v[9], v[2], v[4], v[4], v[8], v[1], v[1], v[2], v[1]);
  h4<7, 10, 11, 12>(v[7], v[10], v[11], v[12], v[4], v[8], v[8], v[8], v[3], v[2], v[3], v[4]);
  h4<13, 14, 15, 15>(v[13], v[14], t0, v[15], v[8], v[8], v[1], v[8], v[5], v[6], h128, v[7]);
  v4u vb[4];
#pragma unroll
  for (int k = 0; k < 4; ++k) vb[k] = *(LAS v4u*)(sr + 64 * k);
  mid(2);
  asm volatile("s_mov_b32 m0, %0\n\ts_nop 0" ::"s"(sw) : "memory");
  hput<0>(h128); hput<1>(t0);
  h4<2, 3, 4, 5>(t0, t1, t2, t3, v[2], v[3], v[4], v[5], h128, h128, h128, h128);
#pragma unroll
  for (int k = 0; k < 4; ++k) {
    kacc[0][0] = mfmah(UA[0][k], vb[k], k ? kacc[0][0] : f32x4{0.f, 0.f, 0.f, 0.f});
    kacc[1][0] = mfmah(UA[1][k], vb[k], k ? kacc[1][0] : f32x4{0.f, 0.f, 0.f, 0.f});
  }
  h4<6, 7, 8, 9>(t0, t1, t2, t3, v[6], v[7], v[8], v[9], h128, h128, h128, h128);
  h4<10, 11, 12, 13>(t0, t1, t2, t3, v[10], v[11], v[12], v[13], h128, h128, h128, h128);
  h4<14, 15, 14, 15>(t0, t1, t2, t3, v[14], v[15], v[14], v[15], h128, h128, h128, h128);
#pragma unroll
  for (int k = 0; k < 4; ++k) vb[k] = *(LAS v4u*)(sr + 64 * k);
  SCHED();
#pragma unroll
  for (int k = 0; k < 4; ++k) {
    kacc[0][1] = mfmah(UA[0][k], vb[k], k ? kacc[0][1] : f32x4{0.f, 0.f, 0.f, 0.f});
    kacc[1][1] = mfmah(UA[1][k], vb[k], k ? kacc[1][1] : f32x4{0.f, 0.f, 0.f, 0.f});
  }
  return up;
}
DI void kgen_store(LAS float* pt, const f32x4 (&kacc)[2][2], int lane) {
  const int l15 = lane & 15, g = lane >> 4;
  const int sh = g < 2 ? 3 : 4 - g;
  const int a0 = (l15 << sh) + (g < 2 ? 4 * g : 128 * g);
  const int a1 = g < 3 ? a0 + 2 : a0 + 64;
#pragma unroll
  for (int nh = 0; nh < 2; ++nh) {
    *(LAS f32x2*)(pt + a0 + ((16 * nh) << sh)) = kacc[0][nh].xy;
    *(LAS f32x2*)(pt + a1 + ((16 * nh) << sh)) = kacc[0][nh].zw;
  }
  if (g == 0) {
#pragma unroll
    for (int nh = 0; nh < 2; ++nh)
#pragma unroll
      for (int r = 0; r < 4; ++r) pt[512 + 32 * r + 16 * nh + l15] = kacc[1][nh][r];
  }
  if (lane == 16) { *(LAS f32x4*)(pt + 640) = f32x4{kacc[1][0][0], 0.f, 0.f, 0.f}; }
}

template <int PAR> DI void conv_unit_shared(LAS char* ka, LAS char* ub, f32x4 (&acc)[4]) {
  constexpr int T[4] = {PAR ? 1 : 0, PAR ? 2 : 3, PAR ? 5 : 4, PAR ? 6 : 7};
  v4u A[8], B[8];
#pragma unroll
  for (int i = 0; i <= T[3]; ++i) { A[i] = *(LAS v4u*)(ka + 512 * (7 - i)); B[i] = *(LAS v4u*)(ub + 512 * i); }
  SCHED();
#pragma unroll
  for (int q = 0; q < 4; ++q)
#pragma unroll
    for (int ip = 0; ip <= T[q]; ++ip) acc[q] = mfma(A[T[q] - ip], B[ip], acc[q]);
  SCHED();
}
DI float fadd(float a, float b) { float r; asm("v_add_f32 %0, %1, %2" : "=v"(r) : "v"(a), "v"(b)); return r; }
DI float ffma(float a, float b, float c) { float r; asm("v_fma_f32 %0, %1, %2, %3" : "=v"(r) : "v"(a), "v"(b), "v"(c)); return r; }
DI f32x4 add4(f32x4 a, f32x4 b) { return f32x4{fadd(a.x, b.x), fadd(a.y, b.y), fadd(a.z, b.z), fadd(a.w, b.w)}; }
DI f32x4 fma4(f32x4 a, float b, f32x4 c) { return f32x4{ffma(a.x, b, c.x), ffma(a.y, b, c.y), ffma(a.z, b, c.z), ffma(a.w, b, c.w)}; }
DI int rot12(int v) { return v >= 12 ? v - 12 : v; }
DI void w_loads(v4u (&WF)[5], const char* p, unsigned vo) {
  ld128c<0>(WF[0], vo, p); ld128c<1024>(WF[1], vo, p); ld128c<2048>(WF[2], vo, p); ld128c<3072>(WF[3], vo, p); ld128c<0>(WF[4], vo, p + 4096);
}
DI void a_loads(v4u (&AF)[4], const char* p, const unsigned (&vm)[4]) {
#pragma unroll
  for (int m = 0; m < 4; ++m) ld128c<0>(AF[m], vm[m], p);
}
template <int LIVE, class F> DI void g2_step(f32x4 (&acc)[4][5], v4u (&WF)[5], v4u (&AF)[4], F ld) {
#pragma unroll
  for (int m = 0; m < 4; ++m) PIN(AF[m]);
#pragma unroll
  for (int n = 0; n < 5; ++n) PIN(WF[n]);
#pragma unroll
  for (int e = 0; e < 20; ++e) {
    const int m = e / 5, n = e % 5;
    acc[m][n] = mfma(WF[n], AF[m], m >= LIVE ? f32x4{0.f, 0.f, 0.f, 0.f} : acc[m][n]);
    if (e & 1) { SCHED(); ld(e >> 1); SCHED(); }
  }
}
DI void make_kix(unsigned (&kix)[9], int ts) {
#pragma unroll
  for (int i = 0; i < 9; ++i) {
    const int lg = 2047 - 8 * ts - i;
    const int bits = (int)(pk2(__builtin_fmaxf((float)lg, 128.f), 0.f));
    const int ci = lg < bits - 0x4280 ? lg : bits - 0x4280;
    kix[i] = KTAB + 4 * ci;
    PIN(kix[i]);
  }
}
DI void put_k(LAS char* L, const unsigned (&kix)[9], int ts, int c, unsigned reg) {
  float kv[9];
#pragma unroll
  for (int i = 0; i < 9; ++i) kv[i] = *(LAS float*)(L + kix[i] + c * KP_STRIDE);
  LAS char* q = L + reg + CV_KOFF + ts * 16;
  *(LAS v4u*)(q + CV_KSTR) = v4u{pk2(kv[0], kv[1]), pk2(kv[2], kv[3]), pk2(kv[4], kv[5]), pk2(kv[6], kv[7])};
  *(LAS v4u*)q = v4u{pk2(kv[1], kv[2]), pk2(kv[3], kv[4]), pk2(kv[5], kv[6]), pk2(kv[7], kv[8])};
}
DI void put_u(LAS char* q, v4u u, bool first) {
  *(LAS v4u*)(q + 3 * CV_USTR + 384) = u;
  *(LAS v2u*)(q + 1 * CV_USTR + 392) = v2u{u.x, u.y}; *(LAS v2u*)(q + 1 * CV_USTR + 400) = v2u{u.z, u.w};
  *(LAS unsigned*)(q + 2 * CV_USTR + 388) = u.x; *(LAS v2u*)(q + 2 * CV_USTR + 392) = v2u{u.y, u.z}; *(LAS unsigned*)(q + 2 * CV_USTR + 400) = u.w;
  *(LAS unsigned*)(q + 396) = u.x; *(LAS v2u*)(q + 400) = v2u{u.y, u.z}; *(LAS unsigned*)(q + 408) = u.w;
  if (first) {
    *(LAS v2u*)(q + 384) = v2u{0u, 0u}; *(LAS unsigned*)(q + 392) = 0u;
    *(LAS v2u*)(q + CV_USTR + 384) = v2u{0u, 0u}; *(LAS unsigned*)(q + 2 * CV_USTR + 384) = 0u;
  }
}

__global__ __launch_bounds__(512, 2) void fused_s4_mlp(const char* __restrict__ x, const char* __restrict__ pAre, const char* __restrict__ pAim,
    const char* __restrict__ pBre, const char* __restrict__ pBim, const char* __restrict__ pCre, const char* __restrict__ pCim,
    const char* __restrict__ plogdt, const char* __restrict__ pDskip, const char* __restrict__ w1, const char* __restrict__ b1,
    const char* __restrict__ w2, char* __restrict__ out, char* __restrict__ ws) {
  __shared__ __attribute__((aligned(16))) char smem[163840];
  LAS char* L = (LAS char*)smem;
  const unsigned lds0 = (unsigned)(size_t)L;
  const char* pc0; unsigned codelen;
  asm volatile("s_getpc_b64 %0\n98:\n\ts_mov_b32 %1, 97f-98b\n\ts_max_u32 %1, %1, 99f-98b" : "=s"(pc0), "=&s"(codelen) :: "scc");
  const int b = blockIdx.x, lane = threadIdx.x & 63;
  const int w = RFL(threadIdx.x >> 6), wi = w & 3;
  {
    const unsigned co = (unsigned)(4 * (16 * w + (lane & 15)) + ((b >> 3) & 3)) * 128;
    asm volatile("s_mov_b32 m0, %2\n\ts_nop 0\n\tglobal_load_lds_dword %0, %1" ::"v"((co < codelen ? co : codelen) & ~3u), "s"(pc0), "s"(lds0 + YST + w * 256) : "memory");
  }
  const int xcd = b & 7, j = b >> 3, rg = xcd >> 1, chf = xcd & 1;
  const int ct1 = 8 * chf + (j & 7), rb1 = 4 * rg + (j >> 3);
  const int wid = rb1 * 16 + ct1;
  const int a2 = 4 * chf + (j >> 3), cb2 = j & 7;
  const int l15 = lane & 15, g = lane >> 4, qq = (lane >> 2) & 3, p4 = lane & 3, l31 = lane & 31, h = lane >> 5;
  const int par = j & 1, cbase = 80 * xcd + 5 * (j >> 1);
  const int chA = cbase + 3 * par, chS = cbase + 2;
  const int ts = 64 * wi + lane;
  ST(0);
  const int row2 = 512 * rg + 64 * a2 + 16 * wi;
  const char* w2b = ws + WS_W2T + (size_t)(cb2 * 48 + 12 * wi) * 5120;
  const char* actb = ws + WS_ACT + (size_t)((32 * rg + 4 * a2) * 48 + 12 * wi) * 1024;
  const int r2 = ((a2 ^ (w >> 2)) & 1) * 6;
#define WL(s) w_loads(WF[s], w2b + rot12(r2 + (s)) * 5120, vo)
#define AL(s) a_loads(AF[s], actb + rot12(r2 + (s)) * 1024, vm)
#define AL1(s, m) ld128c<0>(AF[s][m], vm[m], actb + rot12(r2 + (s)) * 1024)
#define WL1(s, n) ld128c<0>(WF[s][n], vo, w2b + rot12(r2 + (s)) * 5120 + (n) * 1024)
#define NOLD [&](int) {}
#define LATE_LANE() int ln_ = lane; PIN(ln_); const int c15 = ln_ & 15, cg = ln_ >> 4
#define KAO (CV_KOFF + (c15 & 1) * CV_KSTR + 2 * (2072 - 8 * (c15 >> 1) + 8 * cg))
#define UBO ((c15 & 3) * CV_USTR + 2 * (64 * (c15 >> 2) + 8 * cg + 224))
#define YOFF ((unsigned)((c15 >> 2) * 128 + cg * 32 + (c15 & 3) * 8))
#define OROW ((unsigned)(2 * (c15 >> 2) + (c15 & 1) + 8 * ((c15 >> 1) & 1)))
#define ZOFF (lane < 24 ? wi * CV_USTR + lane * 16 : CV_KOFF + wi * CV_KSTR + 4096 + (lane - 24) * 16)
#define ZCOND (lane < 24 || (lane < 31 && wi < 2))

  unsigned voA[4], voB[3];
#pragma unroll
  for (int i = 0; i < 4; ++i) voA[i] = (8 * i + (lane >> 3)) * 1280 + (((lane & 7) ^ (4 * (i & 1) + (lane >> 4))) * 16) - 1024 * i;
#pragma unroll
  for (int i = 0; i < 3; ++i) { const int c = 64 * i + lane; voB[i] = (c / 12) * 3072 + (c % 12) * 16 - 1024 * i; }
  const char* g1a = x + (size_t)(rb1 * 128 + 32 * wi) * 1280;
  const char* g1b = w1 + ct1 * 192 + wi * 49152;
  const unsigned la = lds0 + G1_RING + wi * 4096, lb = lds0 + G1_RING + G1_BOFF + wi * 3072;
  const int r1 = ((j & 7) + (5 * (j >> 3)) / 2 + 5 * chf + rg) % 10;
  auto slab = [&](int s) { const int v = s + r1; return v >= 10 ? v - 10 : v; };
  auto issue = [&](int s) {
    const char* pa = g1a + slab(s) * 128; const char* pb = g1b + (size_t)slab(s) * 64 * 3072;
    dma16(voA[0], pa, la + s * G1_STAGE); dma16o<1024>(voA[1], pa); dma16o<2048>(voA[2], pa); dma16o<3072>(voA[3], pa);
    dma16(voB[0], pb, lb + s * G1_STAGE); dma16o<1024>(voB[1], pb); dma16o<2048>(voB[2], pb);
  };
  unsigned tlo = 0, thi = 0;
  unsigned kix[9];
  if (w < 4) {
    unsigned pr[2][7];
    const bool odd = wi & 1;
    {
      const int kch = chA + (wi >> 1), c0 = odd ? chS : kch;
      const unsigned vo0 = (unsigned)(odd ? chS * 192 + par * 96 + (wi >> 1) * 48 + (lane < 48 ? lane : 47) : kch * 192 + lane) * 2;
      const unsigned vo1 = (unsigned)(kch * 192 + (odd ? 128 : 64) + lane) * 2;
      ld16(pr[0][6], (unsigned)c0 * 2, plogdt);
      ld16(pr[0][0], vo0, pAre); ld16(pr[0][1], vo0, pAim); ld16(pr[0][2], vo0, pBre); ld16(pr[0][3], vo0, pBim); ld16(pr[0][4], vo0, pCre); ld16(pr[0][5], vo0, pCim);
      ld16(pr[1][6], (unsigned)kch * 2, plogdt);
      ld16(pr[1][0], vo1, pAre); ld16(pr[1][1], vo1, pAim); ld16(pr[1][2], vo1, pBre); ld16(pr[1][3], vo1, pBim); ld16(pr[1][4], vo1, pCre); ld16(pr[1][5], vo1, pCim);
    }
    bar();
#pragma unroll
    for (int i = 0; i < 9; ++i) kix[i] = 0;
    waitv<7>();
#pragma unroll
    for (int i = 0; i < 7; ++i) PIN(pr[0][i]);
    ST(1);
    f32x4 ksum[2][2];
#pragma unroll
    for (int i = 0; i < 4; ++i) ksum[i >> 1][i & 1] = f32x4{0.f, 0.f, 0.f, 0.f};
#pragma nounroll
    for (int t = 0; t < 2; ++t) {
      f32x4 kacc[2][2];
      const float scale = kgen_task(pr[0], t == 0 && odd && lane >= 48, lds0 + KG_SCR + wi * KG_WSTR, L + KG_SCR + wi * KG_WSTR + l15 * 272 + g * 16, kacc, [&](int) {});
#pragma unroll
      for (int i = 0; i < 4; ++i) ksum[i >> 1][i & 1] += kacc[i >> 1][i & 1] * scale;
      if (t == 1 || odd) {
        kgen_store((LAS float*)(L + KPART + (t == 0 ? 4 + (wi >> 1) : wi) * KP_STRIDE), ksum, lane);
#pragma unroll
        for (int i = 0; i < 4; ++i) ksum[i >> 1][i & 1] = f32x4{0.f, 0.f, 0.f, 0.f};
      }
      waitv<0>();
#pragma unroll
      for (int i = 0; i < 7; ++i) { PIN(pr[1][i]); pr[0][i] = pr[1][i]; }
    }
    lds_arrive(L, W_KG, lane);
  } else {
    if (w == 4) *(LAS unsigned*)(L + lane * 4) = 0u;
    bar();
    v2u E; v4u xa, xb; v2u xc;
    const int pu = b % 240, pks = pu / 5, pcg = pu % 5;
    const char* xrow = x + (size_t)(8 * wid + 2 * wi) * 1280;
    ld128(xa, h * 1280 + l31 * 16, xrow); ld128(xb, h * 1280 + 512 + l31 * 16, xrow); ld64(xc, h * 1280 + 1024 + l31 * 8, xrow);
    ld64c(E, 0u, ws);
#pragma unroll
    for (int i = 0; i < 2; ++i)
      dma16((unsigned)((lane >> 1) * 1280 + (lane & 1) * 16), w2 + (size_t)pks * 32 * 1280 + (128 * pcg + 32 * wi + 16 * i) * 2, lds0 + W2P + wi * 2048 + i * 1024);
    issue(0);
    make_kix(kix, ts);
    issue(1);
    waitv<14>();
    PIN(E); PIN(xa); PIN(xb); PIN(xc);
    ST(6);
  const u64 tag64 = (((u64)RFL(E.y) << 32) | (unsigned)RFL(E.x)) + 1ull;
  tlo = (unsigned)tag64; thi = (unsigned)(tag64 >> 32);
  if (wi == 0) { lds_set(L, W_TAG, tlo); lds_set(L, W_TAG + 4, thi); lds_set(L, W_TAG + 8, 1); }
  {
    f32x2 v[10] = {unpk(xa.x), unpk(xa.y), unpk(xa.z), unpk(xa.w), unpk(xb.x), unpk(xb.y), unpk(xb.z), unpk(xb.w), unpk(xc.x), unpk(xc.y)};
    f32x2 s2 = ((v[0] + v[1]) + (v[2] + v[3])) + ((v[4] + v[5]) + (v[6] + v[7])) + (v[8] + v[9]);
    const float mean = half_sum(s2.x + s2.y, h) * (1.0f / 640.0f);
    f32x2 q2 = f32x2{0.f, 0.f};
#pragma unroll
    for (int i = 0; i < 10; ++i) { const f32x2 d = v[i] - mean; q2 = __builtin_elementwise_fma(d, d, q2); }
    const float var = rbf(half_sum(q2.x + q2.y, h) * (1.0f / 640.0f));
    const float m = rbf(mean), sd = rbf(__builtin_amdgcn_sqrtf(rbf(var + 1e-5f))), rs = __builtin_amdgcn_rcpf(sd);
    unsigned o[10];
#pragma unroll
    for (int i = 0; i < 10; ++i) { const f32x2 t = (v[i] - m) * rs; o[i] = pk2(t.x, t.y); }
    LAS char* rp = L + LN_ROWS + (2 * wi + h) * LN_PITCH;
    *(LAS v4u*)(rp + l31 * 16) = v4u{o[0], o[1], o[2], o[3]};
    *(LAS v4u*)(rp + 512 + l31 * 16) = v4u{o[4], o[5], o[6], o[7]};
    *(LAS v2u*)(rp + 1024 + l31 * 8) = v2u{o[8], o[9]};
    waitv<0>();
    lds_arrive(L, W_SB, lane);
    issue(2);
    if (l31 == 0) st64c(WS_STATS + (unsigned)(8 * wid + 2 * wi + h) * 8, ws,
                        v2u{(__builtin_bit_cast(unsigned, m) & 0xffff0000u) | (__builtin_bit_cast(unsigned, sd) >> 16), tlo});
  }
#pragma unroll
  for (int i = 0; i < 2; ++i) {
    LAS char* sp = L + W2P + wi * 2048 + i * 1024 + (16 * (g >> 1) + 4 * (g & 1) + qq) * 32 + 8 * p4;
    const v2u lo = trread(sp), hi = trread(sp + 256);
    const int ntg = 8 * pcg + 2 * wi + i;
    st128c((unsigned)lane * 16, ws + WS_W2T + (size_t)(((ntg / 5) * 48 + pks) * 5 + ntg % 5) * 1024, v4u{lo.x, lo.y, hi.x, hi.y});
  }
  lds_wait_ge(L, W_SB, 4);
  {
    LAS char* sp = L + LN_ROWS + qq * LN_PITCH + (160 * wi + 16 * g) * 2 + 8 * p4;
#pragma unroll
    for (int i = 0; i < 3; ++i) {
      const v2u lo = trread(sp + 128 * i), hi = trread(sp + 128 * i + 4 * LN_PITCH);
      const unsigned ch = 64 * i + lane;
      if (i < 2 || lane < 32) st128c((ch / 5) * 20480 + (ch % 5) * 16, ws + WS_XNB + (size_t)wi * 32 * 20480 + wid * 80, v4u{lo.x, lo.y, hi.x, hi.y});
    }
  }
  }
  ST(2);
  int role = w; asm volatile("" : "+s"(role));
  if (role < 4) {
    int lane = __builtin_amdgcn_mbcnt_hi(~0u, __builtin_amdgcn_mbcnt_lo(~0u, 0u)); PIN(lane);
    const int l15 = lane & 15, g = lane >> 4, qq = (lane >> 2) & 3, p4 = lane & 3, l31 = lane & 31, h = lane >> 5;
    const unsigned vo = lane * 16;
    f32x16 acc1[3]; f32x4 csa[3];
#pragma unroll
    for (int n = 0; n < 3; ++n) {
      csa[n] = f32x4{0.f, 0.f, 0.f, 0.f};
#pragma unroll
      for (int i = 0; i < 16; ++i) acc1[n][i] = 0.f;
    }
    const int xrow = (l31 & 17) + 2 * (l15 >> 2) + 8 * ((l15 >> 1) & 1);
    unsigned xo[4], wo[4];
#pragma unroll
    for (int k = 0; k < 4; ++k) {
      const int kr = (k + wi) & 3;
      xo[k] = G1_RING + (32 * wi + xrow) * 128 + (((2 * kr + h) ^ ((xrow >> 1) & 7)) * 16);
      wo[k] = G1_RING + G1_BOFF + (16 * kr + 8 * h + qq) * 192 + 32 * (g & 1) + 8 * p4;
      PIN(xo[k]); PIN(wo[k]);
    }
    const unsigned sel1 = ((l15 ^ g) & 1) ? 0u : 0x3f803f80u;
    const v4u sel = v4u{sel1, sel1, sel1, sel1};
    v4u X[4], W[4][3];
    auto rdw = [&](int s, int k, int n) {
      LAS char* p = L + wo[k] + (s % 3) * G1_STAGE + 64 * n; const v2u lo = trread(p), hi = trread(p + 768); W[k][n] = v4u{lo.x, lo.y, hi.x, hi.y};
    };
    auto rdx = [&](int s, int k) { X[k] = *(LAS v4u*)(L + xo[k] + (s % 3) * G1_STAGE); };
#define MM(k, n) acc1[n] = mfma32(W[k][n], X[k], acc1[n])
#define CS(n) csa[n] = mfma(W[0][n], sel, csa[n])
    v2u gran; unsigned bb[3];
    v4u WF[6][5], AF[6][4];
    const unsigned go = WS_STATS + (unsigned)(rb1 * 128 + 32 * wi + xrow) * 8;
    auto wl1 = [&](int s, int e0) { const int e = 7 * (s - 7) + e0; if (s >= 7 && e0 < 7 && e < 20) ld128c<0>(WF[e / 5][e % 5], vo, w2b + rot12(r2 + e / 5) * 5120 + (e % 5) * 1024); };
    lds_wait_ge(L, W_SB, 4);
    ST(3);
    rdx(0, 0); rdw(0, 0, 0); rdw(0, 0, 1); rdw(0, 0, 2); rdx(0, 1); rdw(0, 1, 0); rdw(0, 1, 1); rdw(0, 1, 2);
#pragma unroll
    for (int s = 0; s < 10; ++s) {
      const bool refill = s < 7;
      const char* pa = g1a + slab(s + 3) * 128; const char* pb = g1b + (size_t)slab(s + 3) * 64 * 3072;
      SCHED();
      MM(0, 0); rdx(s, 2); rdw(s, 2, 0); SCHED();
      MM(0, 1); rdw(s, 2, 1); rdw(s, 2, 2); SCHED();
      MM(0, 2); rdx(s, 3); rdw(s, 3, 0); SCHED();
      CS(0); rdw(s, 3, 1); SCHED();
      CS(1); rdw(s, 3, 2); SCHED();
      CS(2);
      if (s == 0) {} else if (s == 1) lds_wait_ge(L, W_ST, 4); else if (s < 7) waitv<7>(); else if (s == 7) waitv<11>(); else if (s == 8) waitv<11>();
      if (s == 9 && l15 < 2) {
#pragma unroll
        for (int n = 0; n < 3; ++n) *(LAS f32x4*)(L + CSP + wi * 384 + (32 * n + 16 * l15 + 4 * g) * 4) = csa[n];
      }
      lds_set(L, W_RDY + wi * 4, s + 2);
      SCHED();
      MM(1, 0);
      if (refill) { asm volatile("s_waitcnt lgkmcnt(7)" ::: "memory"); dma16(voA[0], pa, la + (s % 3) * G1_STAGE); }
      wl1(s, 0);

      SCHED();
      MM(1, 1); if (refill) dma16o<1024>(voA[1], pa); wl1(s, 1);
      unsigned fl = 0;
      if (s < 9) fl = lw(L, W_RDY + p4 * 4);
      SCHED();
      MM(1, 2); if (refill) dma16o<2048>(voA[2], pa); wl1(s, 2); SCHED();
      MM(2, 0); if (refill) dma16o<3072>(voA[3], pa); wl1(s, 3);
      SCHED();
      if (s == 9) {
        MM(2, 1); wl1(s, 4); SCHED();
        lds_wait_min<4>(L, W_RDY, 11, lane);
        float cs[3];
#pragma unroll
        for (int n = 0; n < 3; ++n) { LAS float* p = (LAS float*)(L + CSP) + 32 * n + l31; cs[n] = fadd(fadd(p[0], p[96]), fadd(p[192], p[288])); }
        MM(2, 2); wl1(s, 5); SCHED();
        waitv<20>(); PIN(gran); PIN(bb[0]); PIN(bb[1]); PIN(bb[2]);
        MM(3, 0); MM(3, 1); MM(3, 2);
        const unsigned keep = h ? 0u : ~0u, nm = (gran.x >> 16) ^ 0x8000u;
        const v4u xe = v4u{(nm | (nm << 16)) & keep, (nm | (gran.x << 16)) & keep, 0u, 0u};
#pragma unroll
        for (int n = 0; n < 3; ++n) {
          const float c0 = bfhi(__builtin_bit_cast(unsigned, cs[n])), r0 = cs[n] - c0, c1 = bfhi(__builtin_bit_cast(unsigned, r0)), r1 = r0 - c1;
          const v4u we = v4u{(__builtin_bit_cast(unsigned, c0) >> 16) | __builtin_bit_cast(unsigned, c1), (__builtin_bit_cast(unsigned, r1) >> 16) | (bb[n] << 16), 0u, 0u};
          acc1[n] = mfma32(we, xe, acc1[n]);
        }
        SCHED();
        break;
      }
      if (__builtin_expect(__builtin_amdgcn_ballot_w64(fl < (unsigned)(s + 2)) != 0ull, 0)) lds_wait_min<4>(L, W_RDY, s + 2, lane);
      FENCE();
      SCHED();
      rdx(s + 1, 0); rdw(s + 1, 0, 0); SCHED();
      MM(2, 1); rdw(s + 1, 0, 1); rdw(s + 1, 0, 2); if (refill) dma16(voB[0], pb, lb + (s % 3) * G1_STAGE); wl1(s, 4); SCHED();
      MM(2, 2); rdx(s + 1, 1); rdw(s + 1, 1, 0); if (refill) dma16o<1024>(voB[1], pb); wl1(s, 5); SCHED();
      MM(3, 0); rdw(s + 1, 1, 1); if (refill) dma16o<2048>(voB[2], pb); wl1(s, 6); SCHED();
      MM(3, 1); rdw(s + 1, 1, 2); wl1(s, 7); SCHED();
      MM(3, 2); wl1(s, 8);
      if (s == 6) { lds_wait_ge(L, W_F1ALL, 4); ld64c(gran, go, ws); ld16(bb[0], l31 * 2, b1 + ct1 * 192); ld16(bb[1], l31 * 2, b1 + ct1 * 192 + 64); ld16(bb[2], l31 * 2, b1 + ct1 * 192 + 128); }
      SCHED();
      if (s == 0) ST(4); if (s == 3) ST(5); if (s == 6) ST(7);
    }
#undef MM
#undef CS
    ST(11);
    const unsigned tlo = RFL(lw(L, W_TAG)), thi = RFL(lw(L, W_TAG + 4));
    {
      const float rs = __builtin_amdgcn_rcpf(bf2f(gran.x)) * 1.44269504088896341f;
      char* ab = ws + WS_ACT + (size_t)((rb1 * 8 + 2 * wi) * 48 + 3 * ct1) * 1024;
      const unsigned ao = (unsigned)((l31 >> 4) * 49152 + h * 256 + l15 * 16);
#pragma unroll
      for (int q = 0; q < 6; ++q) {
        unsigned pk[4];
#pragma unroll
        for (int e = 0; e < 4; ++e) {
          const f32x2 h2 = f32x2{acc1[q >> 1][8 * (q & 1) + 2 * e], acc1[q >> 1][8 * (q & 1) + 2 * e + 1]} * rs;
          const f32x2 s1 = f32x2{__builtin_amdgcn_exp2f(h2.x), __builtin_amdgcn_exp2f(h2.y)} + 1.0f;
          const f32x2 d = __builtin_elementwise_fma(s1, s1, f32x2{1.f, 1.f});
          const f32x2 rr = f32x2{__builtin_amdgcn_rcpf(d.x), __builtin_amdgcn_rcpf(d.y)};
          const f32x2 o = h2 * __builtin_elementwise_fma(rr, f32x2{-1.38629436111989062f, -1.38629436111989062f},
                                                         f32x2{0.69314718055994531f, 0.69314718055994531f});
          pk[e] = pk2(o.x, o.y);
        }
        st128c(ao + q * 512, ab, v4u{pk[0], pk[1], pk[2], pk[3]});
      }
    }
    lds_set(L, W_RDY + wi * 4, 12);
    ST(13);
    __builtin_amdgcn_s_setprio(2);
    LATE_LANE();
    v2u xq[5], dq[5], gr2, f3;
    const unsigned f2o = WS_F2 + (unsigned)((4 * rg + (a2 >> 1)) * 16 + 4 * wi + (ln_ & 3)) * 128;
    unsigned vm[4];
#pragma unroll
    for (int m = 0; m < 4; ++m) vm[m] = vo + ((m + wi) & 3) * 49152;
    v2u f2; v4u ysh;
    {
      f32x4 sacc[4];
#pragma unroll
      for (int i = 0; i < 4; ++i) sacc[i] = f32x4{0.f, 0.f, 0.f, 0.f};
      unsigned kb = CVS + KAO - wi * 128 - 3584 - 64, ubb = CVS + UBO - wi * 128 - 64; PIN(kb); PIN(ubb);
      LAS char* ka = L + kb; LAS char* ub = L + ubb;
      const bool late = RFL(lw(L, W_SB)) < 16;
      if (late) { waitv<0>(); arrive_flag<4>(L, W_F2C, lane, ws, WS_F2 + (unsigned)wid * 128, tlo, thi); }
      lds_wait_ge(L, W_SB, 16);
      if (par) conv_unit_shared<1>(ka + 64, ub + 64, sacc); else conv_unit_shared<0>(ka + 64, ub + 64, sacc);
      if (!late) { waitv<0>(); arrive_flag<4>(L, W_F2C, lane, ws, WS_F2 + (unsigned)wid * 128, tlo, thi); }
      ST(14);
      WL(4); WL(5);
      if (par) conv_unit_shared<1>(ka, ub, sacc); else conv_unit_shared<0>(ka, ub, sacc);
#pragma unroll
      for (int q = 0; q < 4; ++q) *(LAS f32x4*)(L + MXCH + (q * 4 + wi) * 1024 + vo) = sacc[q];
      lds_arrive(L, W_MB, lane);
    {
      const char* xb2 = x + (size_t)row2 * 1280 + cb2 * 160; const unsigned xo2 = OROW * 1280 + cg * 8;
      ld64o<0>(xq[0], xo2, xb2); ld64o<32>(xq[1], xo2, xb2); ld64o<64>(xq[2], xo2, xb2); ld64o<96>(xq[3], xo2, xb2); ld64o<128>(xq[4], xo2, xb2);
      const char* db = pDskip + cb2 * 160;
      ld64o<0>(dq[0], cg * 8, db); ld64o<32>(dq[1], cg * 8, db); ld64o<64>(dq[2], cg * 8, db); ld64o<96>(dq[3], cg * 8, db); ld64o<128>(dq[4], cg * 8, db);
      ld64c(gr2, WS_STATS + (row2 + OROW) * 8, ws);
    }
      lds_wait_ge(L, W_MB, 4);
      LAS char* p = L + MXCH + wi * 4096 + vo;
      const f32x4 s = add4(add4(*(LAS f32x4*)p, *(LAS f32x4*)(p + 1024)), add4(*(LAS f32x4*)(p + 2048), *(LAS f32x4*)(p + 3072)));
      ld64c(f2, f2o, ws);
      const unsigned y0 = pk2(s.x, s.y), y1 = pk2(s.z, s.w);
      *(LAS v2u*)(L + MXCH + wi * 4096 + YOFF) = v2u{y0, y1};
      FENCE();
      ysh = *(LAS v4u*)p;
    }
    ST(15);
    waitv<0>(); PIN(f2);
    f2 = spin64<false>(f2, f2o, ws, tlo, thi);
    lds_set(L, W_F2OK + wi * 4, 1);
    ST(16);
    AL(0); AL(1);
    if (ln_ < 32) st128c(vo, ws + WS_Y + (size_t)chS * 4096 + (2 * wi + ((wi & 1) ^ par)) * 512, ysh);
    f32x4 acc2[4][5];
    PIN(gr2);
    const float rs2 = __builtin_amdgcn_rcpf(bf2f(gr2.x)), m2 = -bfhi(gr2.x) * rs2;
#pragma unroll
    for (int n = 0; n < 5; ++n) {
      PIN(xq[n]); PIN(dq[n]);
      const f32x2 x0 = unpk(xq[n].x), x1 = unpk(xq[n].y), d0 = unpk(dq[n].x), d1 = unpk(dq[n].y);
      acc2[0][n] = f32x4{ffma(d0.x, ffma(x0.x, rs2, m2), x0.x), ffma(d0.y, ffma(x0.y, rs2, m2), x0.y),
                         ffma(d1.x, ffma(x1.x, rs2, m2), x1.x), ffma(d1.y, ffma(x1.y, rs2, m2), x1.y)};
      PIN(acc2[0][n]);
    }
    __builtin_amdgcn_s_setprio(0);
    ST(17);
    waitv<4>(); g2_step<1>(acc2, WF[0], AF[0], [&](int i) { if (i >= 3 && (i & 1)) AL1(2, (i - 3) >> 1); });
    waitv<4>();
    arrive_flag<8>(L, W_F3C, lane, ws, WS_F3 + (unsigned)(xcd * 32 + j) * 128, tlo, thi);
    g2_step<4>(acc2, WF[1], AF[1], [&](int i) { if (i >= 2) AL1(3 + ((i - 2) >> 2), (i - 2) & 3); });
    waitv<8>(); g2_step<4>(acc2, WF[2], AF[2], [&](int i) { if (i < 4) AL1(5, i); });
    ST(20);
    waitv<8>(); g2_step<4>(acc2, WF[3], AF[3], NOLD);
    waitv<4>(); g2_step<4>(acc2, WF[4], AF[4], NOLD);
    lds_wait_ge(L, W_F3C, 8);
    const unsigned f3o = WS_F3 + (unsigned)(cb2 * 32 + 8 * wi + (ln_ & 7)) * 128;
    ld64c(f3, f3o, ws);
    waitv<1>(); g2_step<4>(acc2, WF[5], AF[5], NOLD);
    ST(22);
    waitv<0>(); PIN(f3);
    f3 = spin64<false>(f3, f3o, ws, tlo, thi);
    lds_arrive(L, W_F3ALL, lane);
#pragma unroll
    for (int n = 0; n < 5; ++n) *(LAS f32x4*)(L + FM + wi * 15360 + n * 1024 + vo) = acc2[1][n];
    lds_wait_ge(L, W_F3ALL, 4);
    ST(21);
    v4u yv[3];
    {
      const char* yb = ws + WS_Y + (size_t)cb2 * 80 * 4096 + row2 * 2;
      const unsigned yo = (unsigned)(ln_ >> 1) * 4096 + (ln_ & 1) * 16;
      ld128c<0>(yv[0], yo, yb); ld128c<0>(yv[1], yo, yb + 32 * 4096); ld128c<0>(yv[2], yo & 0xffffu, yb + 64 * 4096);
    }
#pragma unroll
    for (int m = 2; m < 4; ++m)
#pragma unroll
      for (int n = 0; n < 5; ++n) *(LAS f32x4*)(L + FM + wi * 15360 + (m - 1) * 5120 + n * 1024 + vo) = acc2[m][n];
    lds_arrive(L, W_FM, lane);
    lds_wait_ge(L, W_FM, 4);
#pragma unroll
    for (int d = 1; d < 4; ++d)
#pragma unroll
      for (int n = 0; n < 5; ++n) acc2[0][n] = add4(acc2[0][n], *(LAS f32x4*)(L + FM + ((wi - d) & 3) * 15360 + (d - 1) * 5120 + n * 1024 + vo));
    waitv<0>(); PIN(yv[0]); PIN(yv[1]); PIN(yv[2]);
    LAS char* ys = L + YST + wi * 2560;
    *(LAS v4u*)(ys + vo) = yv[0]; *(LAS v4u*)(ys + 1024 + vo) = yv[1];
    if (ln_ < 32) *(LAS v4u*)(ys + 2048 + vo) = yv[2];
    FENCE();
#pragma unroll
    for (int n = 0; n < 5; ++n) {
      const v2u t = trread(ys + n * 512 + ln_ * 8);
      const f32x2 t0 = unpk(t.x), t1 = unpk(t.y);
      *(LAS f32x4*)(L + FM + 61440 + wi * 5120 + n * 1024 + vo) = add4(acc2[0][n], f32x4{t0.x, t0.y, t1.x, t1.y});
    }
    lds_set(L, W_F2OK + wi * 4, 2);
    STEND();
    asm volatile("97:");
    __builtin_amdgcn_endpgm();
  }

  const bool odd = wi & 1;
  const int kch = chA + (wi >> 1);
  const unsigned vo = lane * 16;
  v4u WF[6][5], AF[6][4];
  waitv<6>();
  lds_arrive(L, W_ST, lane);
  waitv<0>();
  arrive_flag8(L, W_F1C, ws, WS_F1 + (unsigned)(lane & 7) * 2048 + wid * 8, tlo, thi);
  ST(8);
  auto reduce = [&](int c, int i) {
    LAS char* pp = L + KPART + 2 * c * KP_STRIDE + i * 16;
    const f32x4 s = add4(*(LAS f32x4*)pp, *(LAS f32x4*)(pp + KP_STRIDE));
    *(LAS f32x4*)(L + KTAB + c * KP_STRIDE + i * 16) = s;
    return s;
  };
  lds_wait_ge(L, W_KG, 4);
  if (ts < 161) { st128c((unsigned)ts * 16, ws + WS_KP + (size_t)wid * KP_STRIDE, __builtin_bit_cast(v4u, reduce(2, ts))); reduce(0, ts); }
  if (ts >= 95) reduce(1, ts - 95);
  if (ts < 3) *(LAS float*)(L + KTAB + ts * KP_STRIDE - 4) = 0.f;
  lds_arrive(L, W_SB, lane);
  waitv<0>();
  arrive_flag(L, W_FKC, lane, ws, WS_FK + (unsigned)wid * 64, tlo, thi);
  ST(9);
  __builtin_amdgcn_s_setprio(0);
  lds_wait_min<4>(L, W_RDY, F1STAGE, lane);
  const unsigned f1o = WS_F1 + (unsigned)xcd * 2048 + ts * 8;
  v2u f1own; ld64c(f1own, f1o, ws);
  waitv<0>(); PIN(f1own);
  f1own = spin64<false>(f1own, f1o, ws, tlo, thi);
  v4u uu[3]; unsigned ulo[3];
#pragma unroll
  for (int r = 0; r < 3; ++r) {
    const int e = 64 * r + lane, tb = 64 * wi + e / 3, c = e % 3;
    ld128c<0>(uu[r], tb * 80 + c * 16, ws + WS_XNB + (size_t)(16 * xcd + (j >> 1)) * 20480 + par * 32);
    ulo[r] = (c == 2 - 2 * par ? CVS : CVA + (c - par) * CV_CH) + tb * 16;
  }
  if (b == 0) arrive_flag(L, W_F1ALL, lane, ws, WS_E, tlo, thi);
  else lds_arrive(L, W_F1ALL, lane);
  ST(4);
  const unsigned fko = WS_FK + (unsigned)(wid ^ 1) * 64;
  v2u fk; ld64c(fk, fko, ws);
  waitv<0>(); PIN(fk); PIN(uu[0]); PIN(uu[1]); PIN(uu[2]);
  fk = spin64<false>(fk, fko, ws, tlo, thi);
  v4u kp; ld128c<0>(kp, (unsigned)(ts < 161 ? ts : 160) * 16, ws + WS_KP + (size_t)(wid ^ 1) * KP_STRIDE);
  lds_wait_ge(L, W_SB, 8);
  waitv<0>(); PIN(kp);
  if (ts < 161) { LAS f32x4* tp = (LAS f32x4*)(L + KTAB + 2 * KP_STRIDE + ts * 16); *tp = add4(*tp, __builtin_bit_cast(f32x4, kp)); }
  lds_arrive(L, W_SB, lane);
  lds_wait_ge(L, W_F1ALL, 4);
  WL(0);
  lds_wait_min<4>(L, W_RDY, 9, lane);
  put_k(L, kix, ts, 0, CVA);
  WL(1);
  lds_wait_min<4>(L, W_RDY, 10, lane);
  put_k(L, kix, ts, 1, CVA + CV_CH);
  if (ZCOND) { *(LAS v4u*)(L + CVA + ZOFF) = v4u{0u, 0u, 0u, 0u}; *(LAS v4u*)(L + CVA + CV_CH + ZOFF) = v4u{0u, 0u, 0u, 0u}; }
  ST(7);
  lds_wait_min<4>(L, W_RDY, 11, lane);
  ST(11);
  put_u(L + ulo[0], uu[0], ts < 3); put_u(L + ulo[1], uu[1], false); put_u(L + ulo[2], uu[2], false);
  if (ZCOND) *(LAS v4u*)(L + CVS + ZOFF) = v4u{0u, 0u, 0u, 0u};
  lds_wait_ge(L, W_SB, 12);
  put_k(L, kix, ts, 2, CVS);
  lds_arrive(L, W_SB, lane);
  WL(2); WL(3); WL(4); WL(5);
  ST(5);
  lds_wait_ge(L, W_SB, 16);
  lds_wait_ge(L, W_RDY + wi * 4, 12);
  ST(13);
  __builtin_amdgcn_s_setprio(SPRIO2);
  LATE_LANE();
  f32x4 cacc[8];
#pragma unroll
  for (int i = 0; i < 8; ++i) cacc[i] = f32x4{0.f, 0.f, 0.f, 0.f};
  {
    const unsigned o1 = CVA + (wi >> 1) * CV_CH - (odd ? 4 * 64 : 0);
    unsigned kb = KAO + o1 - 3584 - 192, ubb = UBO + o1 - 192; PIN(kb); PIN(ubb);
    LAS char* ka = L + kb; LAS char* ub = L + ubb;
    v4u A[8], B[8];
#define LA(i, u) (*(LAS v4u*)(ka + 512 * (7 - (i)) + 64 * (3 - (u))))
#define LB(i, u) (*(LAS v4u*)(ub + 512 * (i) + 64 * (3 - (u))))
    A[7] = LA(7, 0); B[0] = LB(0, 0); A[0] = LA(0, 0); B[7] = LB(7, 0); A[6] = LA(6, 0); B[6] = LB(6, 0); A[1] = LA(1, 0); B[1] = LB(1, 0);
    A[5] = LA(5, 0); B[5] = LB(5, 0); A[2] = LA(2, 0); B[2] = LB(2, 0); A[4] = LA(4, 0); B[4] = LB(4, 0); A[3] = LA(3, 0); B[3] = LB(3, 0);
    SCHED();
#pragma unroll
    for (int u = 0; u < 4; ++u) {
      v4u An[2], Bn[2];
#pragma unroll
      for (int sd = 7; sd >= 0; --sd) {
#pragma unroll
        for (int o = 0; o <= sd; ++o) {
          if (sd + o < 8) cacc[sd + o] = mfma(A[sd], B[o], cacc[sd + o]);
          if (o != sd && sd + o < 8) cacc[sd + o] = mfma(A[o], B[sd], cacc[sd + o]);
        }
        SCHED();
        if (u < 3) {
          if (sd >= 2) { A[sd] = LA(sd, u + 1); B[sd] = LB(sd, u + 1); }
          if (sd == 6 || sd == 5) { An[6 - sd] = LA(6 - sd, u + 1); Bn[6 - sd] = LB(6 - sd, u + 1); }
        }
        SCHED();
      }
      if (u < 3) { A[0] = An[0]; A[1] = An[1]; B[0] = Bn[0]; B[1] = Bn[1]; }
    }
#undef LA
#undef LB
  }
  ST(10);
  unsigned vm[4];
  {
    LAS char* px = L + SXCH + wi * 4096 + vo;
    if (odd) {
#pragma unroll
      for (int i = 0; i < 4; ++i) { *(LAS f32x4*)(px + i * 1024) = cacc[i]; cacc[i] = cacc[4 + i]; }
    } else {
#pragma unroll
      for (int i = 0; i < 4; ++i) *(LAS f32x4*)(px + i * 1024) = cacc[4 + i];
    }
    lds_arrive(L, W_SX + (wi >> 1) * 4, lane);
#pragma unroll
    for (int m = 0; m < 4; ++m) vm[m] = vo + ((m + wi) & 3) * 49152;
    lds_wait_ge(L, W_SX + (wi >> 1) * 4, 2);
    px = L + SXCH + (wi ^ 1) * 4096 + vo;
    v2u yo[4];
#pragma unroll
    for (int i = 0; i < 4; ++i) { const f32x4 s = add4(cacc[i], *(LAS f32x4*)(px + i * 1024)); yo[i] = v2u{pk2(s.x, s.y), pk2(s.z, s.w)}; }
    char* yb = ws + WS_Y + (size_t)kch * 4096 + (odd ? 2048 : 0);
    LAS char* yl = L + SXCH + (wi ^ 1) * 4096;
#pragma unroll
    for (int i = 0; i < 4; ++i) *(LAS v2u*)(yl + i * 512 + YOFF) = yo[i];
    FENCE();
    st128c(vo, yb, *(LAS v4u*)(yl + vo)); st128c(vo, yb + 1024, *(LAS v4u*)(yl + 1024 + vo));
  }
  ST(14);
  lds_wait_ge(L, W_F2OK + wi * 4, 1);
  ST(16);
  AL(0); AL(1);
  ST(17);
  f32x4 acc2[4][5];
  waitv<8>();
  arrive_flag<8>(L, W_F3C, lane, ws, WS_F3 + (unsigned)(xcd * 32 + j) * 128, tlo, thi);
  __builtin_amdgcn_s_setprio(0);
  waitv<4>(); g2_step<0>(acc2, WF[0], AF[0], [&](int i) { if (i >= 3 && (i & 1)) AL1(2, (i - 3) >> 1); });
  waitv<4>(); g2_step<4>(acc2, WF[1], AF[1], [&](int i) { if (i >= 2) AL1(3 + ((i - 2) >> 2), (i - 2) & 3); });
  waitv<8>(); g2_step<4>(acc2, WF[2], AF[2], [&](int i) { if (i < 4) AL1(5, i); });
  ST(20);
  waitv<8>(); g2_step<4>(acc2, WF[3], AF[3], NOLD);
  lds_wait_ge(L, W_F3C, 8);
  waitv<0>();
#pragma unroll
  for (int m = 0; m < 4; ++m) { PIN(AF[4][m]); PIN(AF[5][m]); }
#pragma unroll
  for (int n = 0; n < 5; ++n) { PIN(WF[4][n]); PIN(WF[5][n]); }
#pragma unroll
  for (int mm = 1; mm < 5; ++mm) {
    const int m = mm & 3;
#pragma unroll
    for (int n = 0; n < 5; ++n) {
      acc2[m][n] = mfma(WF[4][n], AF[4][m], acc2[m][n]);
      acc2[m][n] = mfma(WF[5][n], AF[5][m], acc2[m][n]);
      if (mm > 1) *(LAS f32x4*)(L + FS + wi * 15360 + (mm - 2) * 5120 + n * 1024 + vo) = acc2[mm - 1][n];
      SCHED();
    }
  }
  lds_arrive(L, W_FS, lane);
  ST(22);
  lds_wait_ge(L, W_FS, 4);
#pragma unroll
  for (int d = 1; d < 4; ++d)
#pragma unroll
    for (int n = 0; n < 5; ++n) acc2[0][n] += *(LAS f32x4*)(L + FS + ((wi - d) & 3) * 15360 + (d - 1) * 5120 + n * 1024 + vo);
  ST(23);
  lds_wait_ge(L, W_F2OK + wi * 4, 2);
  ST(24);
  {
    char* ob = out + (size_t)row2 * 1280 + cb2 * 160; const unsigned oo = OROW * 1280 + cg * 8;
    v2u o[5];
#pragma unroll
    for (int n = 0; n < 5; ++n) {
      const f32x4 s = acc2[0][n] + *(LAS f32x4*)(L + FM + 61440 + wi * 5120 + n * 1024 + vo);
      o[n] = v2u{pk2(s.x, s.y), pk2(s.z, s.w)};
    }
    st64o<0>(oo, ob, o[0]); st64o<32>(oo, ob, o[1]); st64o<64>(oo, ob, o[2]); st64o<96>(oo, ob, o[3]); st64o<128>(oo, ob, o[4]);
  }
  STEND();
  asm volatile("99:");
}

extern "C" void kernel_launch(void* const* d_in, const int* in_sizes, int n_in, void* d_out, int out_size, void* d_ws, size_t ws_size,
                              hipStream_t stream) {
  fused_s4_mlp<<<256, 512, 0, stream>>>((const char*)d_in[0], (const char*)d_in[1], (const char*)d_in[2], (const char*)d_in[3],
      (const char*)d_in[4], (const char*)d_in[5], (const char*)d_in[6], (const char*)d_in[7], (const char*)d_in[8], (const char*)d_in[10],
      (const char*)d_in[11], (const char*)d_in[12], (char*)d_out, (char*)d_ws);
}
```

```cpp
#include <hip/hip_runtime.h>
#include <stdint.h>

typedef unsigned short u16;
typedef unsigned long long u64;
typedef unsigned v4u __attribute__((ext_vector_type(4)));
typedef unsigned v2u __attribute__((ext_vector_type(2)));
typedef float f32x4 __attribute__((ext_vector_type(4)));
typedef float f32x2 __attribute__((ext_vector_type(2)));
typedef short bf16x8 __attribute__((ext_vector_type(8)));
typedef short s16x4 __attribute__((ext_vector_type(4)));
typedef __bf16 bf16x2_t __attribute__((ext_vector_type(2)));
#define LAS __attribute__((address_space(3)))
#define GAS __attribute__((address_space(1)))
#define DI __device__ __forceinline__

constexpr unsigned WS_E = 0, WS_TMO = 64, WS_F1 = 131072, WS_STATS = 16384, WS_F2 = 32768, WS_F3 = 65536, WS_FK = 98304, WS_STAMP = 262144;
constexpr unsigned WS_XNB = 1u << 20, WS_W2T = 4u << 20, WS_Y = 7u << 20, WS_ACT = 10u << 20, WS_KP = 17u << 20;
constexpr unsigned KP_STRIDE = 2624;
constexpr unsigned SPIN_CAP = 400000;

constexpr unsigned W_F1C = 0, W_F2C = 4, W_F3C = 8, W_KG = 12, W_MB = 16, W_SB = 20, W_SX = 24, W_FM = 32, W_FS = 36, W_F3ALL = 40, W_F1ALL = 44,
                   W_F2OK = 48, W_RDY = 64, W_TAG = 80, W_FKC = 96, W_ST = 100;
constexpr unsigned KTAB = 272;
constexpr unsigned CSP = 8192;
constexpr unsigned CVA = 10240, CV_CH = 27008, CV_USTR = 4640, CV_KOFF = 18560, CV_KSTR = 4224;
constexpr unsigned KG_SCR = CVA, KG_WSTR = 4352, LN_ROWS = KG_SCR + 8 * KG_WSTR, LN_PITCH = 1344, W2P = LN_ROWS + 10752;
constexpr unsigned KPART = KG_SCR + 4 * KG_WSTR;
constexpr unsigned G1_RING = 64512, G1_STAGE = 28672, G1_BOFF = 16384;
constexpr unsigned CVS = G1_RING, SXCH = CVS + 27136, MXCH = SXCH + 16384;
constexpr unsigned CSF = G1_RING + 3 * G1_STAGE, YST = CSF + 3072;
constexpr unsigned FM = 256, FS = FM + 81920;
static_assert(FS + 61440 <= CSF && CVA + 2 * CV_CH <= G1_RING && W2P + 8192 <= G1_RING && YST + 10240 <= 163840 && MXCH + 16384 <= CSF, "lds map");

#define ROT2 0
#define F1STAGE 5
#define SPRIO2 1
#define F1GAP 16
#define F2GAP 8
#define SPRIO 1
#define STAMP
#define ST(i) do { if ((i) == 0) { stv0 = __builtin_amdgcn_s_memrealtime(); stv1 = stv0; } if ((i) == 2 && (w < 4)) { asm volatile("s_memrealtime %0\n\ts_waitcnt lgkmcnt(0)" : "=s"(stv1) :: "memory"); } } while (0)
#define STEND() do { while (__builtin_amdgcn_s_memrealtime() - stv1 < 4000) __builtin_amdgcn_s_sleep(2); } while (0)

DI float bf2f(unsigned v) { return __builtin_bit_cast(float, v << 16); }
DI float bfhi(unsigned v) { return __builtin_bit_cast(float, v & 0xffff0000u); }
DI unsigned pk2(float lo, float hi) { bf16x2_t r; r.x = (__bf16)lo; r.y = (__bf16)hi; return __builtin_bit_cast(unsigned, r); }
DI float rbf(float f) { return bf2f(pk2(f, f) & 0xffffu); }
DI f32x4 mfma(v4u a, v4u b, f32x4 c) {
  return __builtin_amdgcn_mfma_f32_16x16x32_bf16(__builtin_bit_cast(bf16x8, a), __builtin_bit_cast(bf16x8, b), c, 0, 0, 0);
}
DI v2u trread(LAS char* p) { return __builtin_bit_cast(v2u, __builtin_amdgcn_ds_read_tr16_b64_v4i16((LAS s16x4*)p)); }
template <int N> DI void waitv() { asm volatile("s_waitcnt vmcnt(%0)" ::"i"(N) : "memory"); }
DI void bar() { asm volatile("s_waitcnt lgkmcnt(0)\n\ts_barrier" ::: "memory"); }
#define PIN(x) asm volatile("" : "+v"(x))
#define FENCE() asm volatile("" ::: "memory")
#define SCHED() __builtin_amdgcn_sched_barrier(0)
#define RFL(x) __builtin_amdgcn_readfirstlane(x)

DI void ld16(unsigned& d, unsigned vo, const void* sb) { asm volatile("global_load_ushort %0, %1, %2" : "=v"(d) : "v"(vo), "s"(sb) : "memory"); }
template <int O> DI void ld32(unsigned& d, unsigned vo, const void* sb) { asm volatile("global_load_dword %0, %1, %2 offset:%3" : "=v"(d) : "v"(vo), "s"(sb), "i"(O) : "memory"); }
DI void ld64(v2u& d, unsigned vo, const void* sb) { asm volatile("global_load_dwordx2 %0, %1, %2" : "=v"(d) : "v"(vo), "s"(sb) : "memory"); }
DI void ld128(v4u& d, unsigned vo, const void* sb) { asm volatile("global_load_dwordx4 %0, %1, %2" : "=v"(d) : "v"(vo), "s"(sb) : "memory"); }
DI void ld64c(v2u& d, unsigned vo, const void* sb) { asm volatile("global_load_dwordx2 %0, %1, %2 sc1" : "=v"(d) : "v"(vo), "s"(sb) : "memory"); }
template <int O> DI void ld128c(v4u& d, unsigned vo, const void* sb) { asm volatile("global_load_dwordx4 %0, %1, %2 offset:%3 sc1" : "=v"(d) : "v"(vo), "s"(sb), "i"(O) : "memory"); }
DI void st64c(unsigned vo, void* sb, v2u d) { asm volatile("global_store_dwordx2 %0, %1, %2 sc1" ::"v"(vo), "v"(d), "s"(sb) : "memory"); }
template <int O> DI void ld64o(v2u& d, unsigned vo, const void* sb) { asm volatile("global_load_dwordx2 %0, %1, %2 offset:%3" : "=v"(d) : "v"(vo), "s"(sb), "i"(O) : "memory"); }
template <int O> DI void st64o(unsigned vo, void* sb, v2u d) { asm volatile("global_store_dwordx2 %0, %1, %2 offset:%3" ::"v"(vo), "v"(d), "s"(sb), "i"(O) : "memory"); }
template <int O> DI void st64co(unsigned vo, void* sb, v2u d) { asm volatile("global_store_dwordx2 %0, %1, %2 offset:%3 sc1" ::"v"(vo), "v"(d), "s"(sb), "i"(O) : "memory"); }
DI void st128c(unsigned vo, void* sb, v4u d) { asm volatile("global_store_dwordx4 %0, %1, %2 sc1\n\ts_nop 1" ::"v"(vo), "v"(d), "s"(sb) : "memory"); }
DI void st64u(unsigned vo, void* sb, v2u d) { asm volatile("global_store_dwordx2 %0, %1, %2 sc1 ; CNT" ::"v"(vo), "v"(d), "s"(sb) : "memory"); }
DI void st128u(unsigned vo, void* sb, v4u d) { asm volatile("global_store_dwordx4 %0, %1, %2 sc1 ; CNT\n\ts_nop 1" ::"v"(vo), "v"(d), "s"(sb) : "memory"); }
DI void dma16(unsigned vo, const void* sb, unsigned lds) {
  asm volatile("s_mov_b32 m0, %2\n\ts_nop 0\n\tglobal_load_lds_dwordx4 %0, %1" ::"v"(vo), "s"(sb), "s"(lds) : "memory");
}
template <int O> DI void dma16o(unsigned vo, const void* sb) {
  asm volatile("global_load_lds_dwordx4 %0, %1 offset:%2" ::"v"(vo), "s"(sb), "i"(O) : "memory");
}
template <bool HIONLY> DI v2u spin64(v2u cur, unsigned vo, char* ws, unsigned tlo, unsigned thi) {
  if (__builtin_expect(__builtin_amdgcn_ballot_w64(HIONLY ? (cur.y != tlo) : (cur.x != tlo || cur.y != thi)) != 0ull, 0)) {
#pragma nounroll
    for (unsigned spins = 0; spins < SPIN_CAP; ++spins) {
      __builtin_amdgcn_s_sleep(6);
      asm volatile("global_load_dwordx2 %0, %1, %2 sc1\n\ts_waitcnt vmcnt(0)" : "=&v"(cur) : "v"(vo), "s"(ws) : "memory");
      if (__builtin_amdgcn_ballot_w64(HIONLY ? (cur.y != tlo) : (cur.x != tlo || cur.y != thi)) == 0ull) break;
    }
  }
  return cur;
}
DI unsigned lw(LAS char* L, unsigned off) { return *(volatile LAS unsigned*)(L + off); }
DI void lds_wait_at(unsigned a, unsigned target) {
  unsigned v, cnt; u64 t;
  asm volatile("s_mov_b32 %1, %5\n"
               "1:\n\tds_read_b32 %0, %3\n\ts_sub_u32 %1, %1, 1\n\ts_waitcnt lgkmcnt(0)\n\tv_cmp_gt_u32 vcc, %4, %0\n\t"
               "s_cselect_b64 %2, 0, vcc\n\ts_cmp_lg_u64 %2, 0\n\ts_cbranch_scc1 1b"
               : "=&v"(v), "=&s"(cnt), "=&s"(t) : "v"(a), "s"(target), "i"(4 * SPIN_CAP) : "vcc", "scc", "memory");
}
DI void lds_wait_ge(LAS char* L, unsigned off, unsigned target) { lds_wait_at((unsigned)(size_t)L + off, target); }
template <int N> DI void lds_wait_min(LAS char* L, unsigned off, unsigned target, int lane) {
  lds_wait_at((unsigned)(size_t)L + off + (lane & (N - 1)) * 4, target);
}
DI void lds_set(LAS char* L, unsigned off, unsigned v) { FENCE(); *(volatile LAS unsigned*)(L + off) = v; FENCE(); }
DI void lds_arrive(LAS char* L, unsigned off, int) {
  asm volatile("s_mov_b64 exec, 1\n\tds_add_u32 %0, %1\n\ts_mov_b64 exec, -1" ::"v"((unsigned)(size_t)L + off), "v"(1u) : "memory");
}
template <unsigned N = 4> DI void arrive_flag(LAS char* L, unsigned off, int, char* ws, unsigned fo, unsigned tlo, unsigned thi) {
  unsigned old;
  asm volatile("s_mov_b64 exec, 1\n\tds_add_rtn_u32 %0, %1, %2\n\ts_waitcnt lgkmcnt(0)\n\tv_cmp_eq_u32 vcc, %6, %0\n\t"
               "s_mov_b64 exec, vcc\n\tglobal_store_dwordx2 %3, %4, %5 sc1\n\ts_mov_b64 exec, -1"
               : "=&v"(old) : "v"((unsigned)(size_t)L + off), "v"(1u), "v"(fo), "v"(v2u{tlo, thi}), "s"(ws), "i"(N - 1) : "vcc", "memory");
}
DI void arrive_flag8(LAS char* L, unsigned off, char* ws, unsigned fo, unsigned tlo, unsigned thi) {
  unsigned old;
  asm volatile("s_mov_b64 exec, 1\n\tds_add_rtn_u32 %0, %1, %2\n\ts_waitcnt lgkmcnt(0)\n\tv_cmp_eq_u32 vcc, 3, %0\n\t"
               "s_mul_i32 exec_lo, vcc_lo, 0xff\n\tglobal_store_dwordx2 %3, %4, %5 sc1\n\ts_mov_b64 exec, -1"
               : "=&v"(old) : "v"((unsigned)(size_t)L + off), "v"(1u), "v"(fo), "v"(v2u{tlo, thi}), "s"(ws) : "vcc", "memory");
}
typedef float f32x16 __attribute__((ext_vector_type(16)));
DI f32x16 mfma32(v4u a, v4u b, f32x16 c) {
  return __builtin_amdgcn_mfma_f32_32x32x16_bf16(__builtin_bit_cast(bf16x8, a), __builtin_bit_cast(bf16x8, b), c, 0, 0, 0);
}
template <int C> DI float dpp_add(float v) {
  return v + __builtin_bit_cast(float, __builtin_amdgcn_update_dpp(0, __builtin_bit_cast(int, v), C, 0xf, 0xf, true));
}
DI float rl(float v, int l) { return __builtin_bit_cast(float, __builtin_amdgcn_readlane(__builtin_bit_cast(int, v), l)); }
DI float half_sum(float v, bool upper) {
  v = dpp_add<0xB1>(v); v = dpp_add<0x4E>(v); v = dpp_add<0x141>(v); v = dpp_add<0x140>(v);
  const float a = rl(v, 0) + rl(v, 16), c = rl(v, 32) + rl(v, 48);
  return upper ? c : a;
}
DI f32x2 unpk(unsigned v) { return f32x2{bf2f(v), bfhi(v)}; }
typedef f32x2 cf;
DI cf cmul1(cf i, cf w) { return cf{__builtin_fmaf(i.x, w.x, -(i.y * w.y)), __builtin_fmaf(i.x, w.y, i.y * w.x)}; }
DI void cmul4(cf& o0, cf& o1, cf& o2, cf& o3, cf i0, cf i1, cf i2, cf i3, cf w0, cf w1, cf w2, cf w3) {
  o0 = cmul1(i0, w0); o1 = cmul1(i1, w1); o2 = cmul1(i2, w2); o3 = cmul1(i3, w3);
}
DI unsigned pkc(cf v) { return pk2(v.x, v.y); }
template <int R> DI void put4(cf a, cf b, cf c, cf d) {
  asm volatile("ds_write_addtid_b32 %0 offset:%4\n\tds_write_addtid_b32 %1 offset:%5\n\tds_write_addtid_b32 %2 offset:%6\n\tds_write_addtid_b32 %3 offset:%7"
               ::"v"(pkc(a)), "v"(pkc(b)), "v"(pkc(c)), "v"(pkc(d)), "i"(R * 272), "i"(R * 272 + 272), "i"(R * 272 + 544), "i"(R * 272 + 816) : "memory");
}
template <int R> DI void put1(cf a) { asm volatile("ds_write_addtid_b32 %0 offset:%1" ::"v"(pkc(a)), "i"(R * 272) : "memory"); }
DI cf cexpk(float e1, float th, float tl, float k, float ks) {
  const float mag = __builtin_amdgcn_exp2f(e1 * k);
  const float fr = __builtin_fmaf(tl, ks, __builtin_amdgcn_fractf(th * ks));
  return cf{__builtin_amdgcn_cosf(fr), __builtin_amdgcn_sinf(fr)} * mag;
}
DI void kgen_slow(cf a, cf W1, cf W256, cf W8, LAS char* sr, f32x4 (&kacc)[2][2]) {
  cf W2, W512, u1, W16, u2, u3, W4, W32, u4, u5, u6, u7, r8, r9, r10, r11, r12, r13, r14, r15, r16, r17, W1024, v3, r18, r19, r20, v1;
  cmul4(W2, W512, u1, W16, W1, W256, a, W8, W1, W256, W1, W8);
  cmul4(u2, u3, W4, W32, a, u1, W2, W16, W2, W2, W2, W16);
  put4<0>(a, u1, u2, u3);
  cmul4(u4, u5, u6, u7, a, u1, u2, u3, W4, W4, W4, W4);
  put4<4>(u4, u5, u6, u7);
  cmul4(r8, r9, r10, r11, a, u2, u4, u6, W256, W256, W256, W256);
  put4<8>(r8, r9, r10, r11);
  cmul4(r12, r13, r14, r15, a, u4, r8, r10, W512, W512, W512, W512);
  put4<12>(r12, r13, r14, r15);
  v4u UA[2][4];
#pragma unroll
  for (int k = 0; k < 4; ++k) UA[0][k] = *(LAS v4u*)(sr + 64 * k);
  cmul4(r16, r17, W1024, v3, r12, r14, W512, W8, W512, W512, W512, W16);
  cmul4(r18, r19, r20, v1, r12, r14, r16, W8, W1024, W1024, W1024, W32);
  put4<0>(r16, r17, r18, r19);
  put1<4>(r20);
#pragma unroll
  for (int k = 0; k < 4; ++k) UA[1][k] = *(LAS v4u*)(sr + 64 * k);
  put4<0>(cf{1.f, 0.f}, W8, W16, v3);
  cf v0 = W32, v2, t0, t1, t2, t3;
  cmul4(v2, t3, t0, t1, W16, v3, v0, v1, W32, W32, W32, W32);
  put4<4>(v0, v1, v2, t3);
  cmul4(t2, v3, v0, v1, v2, t3, t0, t1, W32, W32, W32, W32);
  put4<8>(t0, t1, t2, v3);
  cmul4(v2, t3, t0, t1, t2, v3, v0, v1, W32, W32, W32, W32);
  put4<12>(v0, v1, v2, t3);
  v4u vb[4];
#pragma unroll
  for (int k = 0; k < 4; ++k) vb[k] = *(LAS v4u*)(sr + 64 * k);
  cmul4(t2, v3, v0, v1, v2, t3, t0, t1, W32, W32, W32, W32);
  put4<0>(t0, t1, t2, v3);
  cmul4(v2, t3, t0, t1, t2, v3, v0, v1, W32, W32, W32, W32);
  put4<4>(v0, v1, v2, t3);
#pragma unroll
  for (int k = 0; k < 4; ++k) {
    kacc[0][0] = mfma(UA[0][k], vb[k], kacc[0][0]);
    kacc[1][0] = mfma(UA[1][k], vb[k], kacc[1][0]);
  }
  cmul4(t2, v3, v0, v1, v2, t3, t0, t1, W32, W32, W32, W32);
  put4<8>(t0, t1, t2, v3);
  cmul4(v2, t3, t0, t1, t2, v3, v0, v1, W32, W32, W32, W32);
  put4<12>(v0, v1, v2, t3);
#pragma unroll
  for (int k = 0; k < 4; ++k) vb[k] = *(LAS v4u*)(sr + 64 * k);
  SCHED();
#pragma unroll
  for (int k = 0; k < 4; ++k) {
    kacc[0][1] = mfma(UA[0][k], vb[k], kacc[0][1]);
    kacc[1][1] = mfma(UA[1][k], vb[k], kacc[1][1]);
  }
}
typedef _Float16 f16x8 __attribute__((ext_vector_type(8)));
typedef _Float16 f16x2 __attribute__((ext_vector_type(2)));
DI f32x4 mfmah(v4u a, v4u b, f32x4 c) {
  return __builtin_amdgcn_mfma_f32_16x16x32_f16(__builtin_bit_cast(f16x8, a), __builtin_bit_cast(f16x8, b), c, 0, 0, 0);
}
DI unsigned pkh(cf v) { f16x2 r; r.x = (_Float16)v.x; r.y = (_Float16)v.y; return __builtin_bit_cast(unsigned, r); }
DI cf csq(cf v) { return cf{__builtin_fmaf(v.x, v.x, -(v.y * v.y)), (v.x + v.x) * v.y}; }
#define HMUL(o, i, w) "v_pk_mul_f16 " o ", " i ", " w " op_sel:[1,1] op_sel_hi:[1,0] neg_lo:[0,1]\n\t"
#define HFMA(o, i, w) "v_pk_fma_f16 " o ", " i ", " w ", " o " op_sel_hi:[0,1,1]\n\t"
template <int R0, int R1, int R2, int R3>
DI void h4(unsigned& o0, unsigned& o1, unsigned& o2, unsigned& o3, unsigned i0, unsigned i1, unsigned i2, unsigned i3, unsigned w0, unsigned w1, unsigned w2, unsigned w3) {
  asm volatile(HMUL("%0", "%4", "%8") HMUL("%1", "%5", "%9") HMUL("%2", "%6", "%10") HMUL("%3", "%7", "%11")
               HFMA("%0", "%4", "%8") HFMA("%1", "%5", "%9") HFMA("%2", "%6", "%10") HFMA("%3", "%7", "%11")
               "ds_write_addtid_b32 %0 offset:%12\n\tds_write_addtid_b32 %1 offset:%13\n\tds_write_addtid_b32 %2 offset:%14\n\tds_write_addtid_b32 %3 offset:%15"
               : "=&v"(o0), "=&v"(o1), "=&v"(o2), "=&v"(o3)
               : "v"(i0), "v"(i1), "v"(i2), "v"(i3), "v"(w0), "v"(w1), "v"(w2), "v"(w3), "i"(R0 * 272), "i"(R1 * 272), "i"(R2 * 272), "i"(R3 * 272) : "memory");
}
template <int R> DI void hput(unsigned v) { asm volatile("ds_write_addtid_b32 %0 offset:%1" ::"v"(v), "i"(R * 272) : "memory"); }
template <int C> DI float dpp_max(float v) {
  return __builtin_fmaxf(v, __builtin_bit_cast(float, __builtin_amdgcn_update_dpp(0, __builtin_bit_cast(int, v), C, 0xf, 0xf, true)));
}
template <class F> DI float kgen_task(const unsigned (&pr)[7], bool dead, unsigned sw, LAS char* sr, f32x4 (&kacc)[2][2], F mid) {
  const float dt = rbf(__builtin_amdgcn_exp2f(bf2f(pr[6]) * 1.44269504088896341f));
  const float zr = dt * bf2f(pr[0]), zi = dt * bf2f(pr[1]);
  const float dbr = dt * bf2f(pr[2]), dbi = dt * bf2f(pr[3]), cr = bf2f(pr[4]), ci = bf2f(pr[5]);
  cf a = cf{cr * dbr - ci * dbi, -(cr * dbi + ci * dbr)};
  if (dead) a = cf{0.f, 0.f};
  const float e1 = zr * 1.44269504088896341f, th = zi * 0.15915494309189535f;
  const float tl = __builtin_fmaf(zi, 6.4206383e-9f, __builtin_fmaf(zi, 0.15915494309189535f, -th));
  const cf W1 = cexpk(e1, th, tl, 1.f, -1.f), W256 = cexpk(e1, th, tl, 256.f, -256.f), W8 = cexpk(e1, th, tl, 8.f, 8.f);
  if (__builtin_expect(__builtin_amdgcn_ballot_w64(!(zr <= 0.f)) != 0ull, 0)) {
    mid(1); mid(2);
    asm volatile("s_mov_b32 m0, %0" ::"s"(sw) : "memory");
#pragma unroll
    for (int i = 0; i < 4; ++i) kacc[i >> 1][i & 1] = f32x4{0.f, 0.f, 0.f, 0.f};
    kgen_slow(a, W1, W256, W8, sr, kacc);
    return 1.f;
  }
  float am = __builtin_fmaxf(__builtin_fabsf(a.x), __builtin_fabsf(a.y));
  am = dpp_max<0xB1>(am); am = dpp_max<0x4E>(am); am = dpp_max<0x141>(am); am = dpp_max<0x140>(am);
  const unsigned e0 = __builtin_amdgcn_readlane(__builtin_bit_cast(unsigned, am), 0), e16 = __builtin_amdgcn_readlane(__builtin_bit_cast(unsigned, am), 16);
  const unsigned e32 = __builtin_amdgcn_readlane(__builtin_bit_cast(unsigned, am), 32), e48 = __builtin_amdgcn_readlane(__builtin_bit_cast(unsigned, am), 48);
  unsigned ex = max(max(e0, e16), max(e32, e48)) >> 23;
  ex = min(max(ex, 2u), 252u);
  const float up = __builtin_bit_cast(float, ex << 23), down = __builtin_bit_cast(float, (254u - ex) << 23);
  const cf W2 = csq(W1), W512 = csq(W256), W16 = csq(W8), W4 = csq(W2), W1024 = csq(W512), W32 = csq(W16), W64 = csq(W32), W128 = csq(W64);
  const unsigned u0 = pkh(a * down), h1 = pkh(W1), h2 = pkh(W2), h4_ = pkh(W4), h256 = pkh(W256), h512 = pkh(W512), h1024 = pkh(W1024);
  unsigned u1, u2, u4, r8, u3, u5, u6, r12, u7, r9, r10, r14, r11, r13, r15, r16, r17, r18, r19, r20;
  asm volatile("s_mov_b32 m0, %0\n\ts_nop 0" ::"s"(sw) : "memory");
  hput<0>(u0);
  h4<1, 2, 4, 8>(u1, u2, u4, r8, u0, u0, u0, u0, h1, h2, h4_, h256);
  h4<3, 5, 6, 12>(u3, u5, u6, r12, u2, u4, u4, u0, h1, h1, h2, h512);
  h4<7, 9, 10, 14>(u7, r9, r10, r14, u6, r8, r8, r8, h1, h2, h4_, h512);
  h4<11, 13, 15, 15>(r11, r13, r16, r15, r10, r12, u0, r14, h2, h4_, h1024, h4_);
  v4u UA[2][4];
#pragma unroll
  for (int k = 0; k < 4; ++k) UA[0][k] = *(LAS v4u*)(sr + 64 * k);
  hput<0>(r16);
  h4<1, 2, 3, 4>(r17, r18, r19, r20, r8, r12, r14, r16, h1024, h1024, h1024, h1024);
#pragma unroll
  for (int k = 0; k < 4; ++k) UA[1][k] = *(LAS v4u*)(sr + 64 * k);
  mid(1);
  unsigned v[16], t0, t1, t2, t3;
  v[1] = pkh(W8); v[2] = pkh(W16); v[4] = pkh(W32); v[8] = pkh(W64);
  const unsigned h128 = pkh(W128);
  hput<0>(0x3c00u); hput<1>(v[1]); hput<2>(v[2]); hput<4>(v[4]); hput<8>(v[8]);
  h4<3, 5, 6, 9>(v[3], v[5], v[6], v[9], v[2], v[4], v[4], v[8], v[1], v[1], v[2], v[1]);
  h4<7, 10, 11, 12>(v[7], v[10], v[11], v[12], v[4], v[8], v[8], v[8], v[3], v[2], v[3], v[4]);
  h4<13, 14, 15, 15>(v[13], v[14], t0, v[15], v[8], v[8], v[1], v[8], v[5], v[6], h128, v[7]);
  v4u vb[4];
#pragma unroll
  for (int k = 0; k < 4; ++k) vb[k] = *(LAS v4u*)(sr + 64 * k);
  mid(2);
  asm volatile("s_mov_b32 m0, %0\n\ts_nop 0" ::"s"(sw) : "memory");
  hput<0>(h128); hput<1>(t0);
  h4<2, 3, 4, 5>(t0, t1, t2, t3, v[2], v[3], v[4], v[5], h128, h128, h128, h128);
#pragma unroll
  for (int k = 0; k < 4; ++k) {
    kacc[0][0] = mfmah(UA[0][k], vb[k], k ? kacc[0][0] : f32x4{0.f, 0.f, 0.f, 0.f});
    kacc[1][0] = mfmah(UA[1][k], vb[k], k ? kacc[1][0] : f32x4{0.f, 0.f, 0.f, 0.f});
  }
  h4<6, 7, 8, 9>(t0, t1, t2, t3, v[6], v[7], v[8], v[9], h128, h128, h128, h128);
  h4<10, 11, 12, 13>(t0, t1, t2, t3, v[10], v[11], v[12], v[13], h128, h128, h128, h128);
  h4<14, 15, 14, 15>(t0, t1, t2, t3, v[14], v[15], v[14], v[15], h128, h128, h128, h128);
#pragma unroll
  for (int k = 0; k < 4; ++k) vb[k] = *(LAS v4u*)(sr + 64 * k);
  SCHED();
#pragma unroll
  for (int k = 0; k < 4; ++k) {
    kacc[0][1] = mfmah(UA[0][k], vb[k], k ? kacc[0][1] : f32x4{0.f, 0.f, 0.f, 0.f});
    kacc[1][1] = mfmah(UA[1][k], vb[k], k ? kacc[1][1] : f32x4{0.f, 0.f, 0.f, 0.f});
  }
  return up;
}
DI void kgen_store(LAS float* pt, const f32x4 (&kacc)[2][2], int lane) {
  const int l15 = lane & 15, g = lane >> 4;
  const int sh = g < 2 ? 3 : 4 - g;
  const int a0 = (l15 << sh) + (g < 2 ? 4 * g : 128 * g);
  const int a1 = g < 3 ? a0 + 2 : a0 + 64;
#pragma unroll
  for (int nh = 0; nh < 2; ++nh) {
    *(LAS f32x2*)(pt + a0 + ((16 * nh) << sh)) = kacc[0][nh].xy;
    *(LAS f32x2*)(pt + a1 + ((16 * nh) << sh)) = kacc[0][nh].zw;
  }
  if (g == 0) {
#pragma unroll
    for (int nh = 0; nh < 2; ++nh)
#pragma unroll
      for (int r = 0; r < 4; ++r) pt[512 + 32 * r + 16 * nh + l15] = kacc[1][nh][r];
  }
  if (lane == 16) { *(LAS f32x4*)(pt + 640) = f32x4{kacc[1][0][0], 0.f, 0.f, 0.f}; }
}

template <int PAR> DI void conv_unit_shared(LAS char* ka, LAS char* ub, f32x4 (&acc)[4]) {
  constexpr int T[4] = {PAR ? 1 : 0, PAR ? 2 : 3, PAR ? 5 : 4, PAR ? 6 : 7};
  v4u A[8], B[8];
#pragma unroll
  for (int i = 0; i <= T[3]; ++i) { A[i] = *(LAS v4u*)(ka + 512 * (7 - i)); B[i] = *(LAS v4u*)(ub + 512 * i); }
  SCHED();
#pragma unroll
  for (int q = 0; q < 4; ++q)
#pragma unroll
    for (int ip = 0; ip <= T[q]; ++ip) acc[q] = mfma(A[T[q] - ip], B[ip], acc[q]);
  SCHED();
}
DI float fadd(float a, float b) { float r; asm("v_add_f32 %0, %1, %2" : "=v"(r) : "v"(a), "v"(b)); return r; }
DI float ffma(float a, float b, float c) { float r; asm("v_fma_f32 %0, %1, %2, %3" : "=v"(r) : "v"(a), "v"(b), "v"(c)); return r; }
DI f32x4 add4(f32x4 a, f32x4 b) { return f32x4{fadd(a.x, b.x), fadd(a.y, b.y), fadd(a.z, b.z), fadd(a.w, b.w)}; }
DI f32x4 fma4(f32x4 a, float b, f32x4 c) { return f32x4{ffma(a.x, b, c.x), ffma(a.y, b, c.y), ffma(a.z, b, c.z), ffma(a.w, b, c.w)}; }
DI int rot12(int v) { return v >= 12 ? v - 12 : v; }
DI void w_loads(v4u (&WF)[5], const char* p, unsigned vo) {
  ld128c<0>(WF[0], vo, p); ld128c<1024>(WF[1], vo, p); ld128c<2048>(WF[2], vo, p); ld128c<3072>(WF[3], vo, p); ld128c<0>(WF[4], vo, p + 4096);
}
DI void a_loads(v4u (&AF)[4], const char* p, const unsigned (&vm)[4]) {
#pragma unroll
  for (int m = 0; m < 4; ++m) ld128c<0>(AF[m], vm[m], p);
}
template <int LIVE, class F> DI void g2_step(f32x4 (&acc)[4][5], v4u (&WF)[5], v4u (&AF)[4], F ld) {
#pragma unroll
  for (int m = 0; m < 4; ++m) PIN(AF[m]);
#pragma unroll
  for (int n = 0; n < 5; ++n) PIN(WF[n]);
#pragma unroll
  for (int e = 0; e < 20; ++e) {
    const int m = e / 5, n = e % 5;
    acc[m][n] = mfma(WF[n], AF[m], m >= LIVE ? f32x4{0.f, 0.f, 0.f, 0.f} : acc[m][n]);
    if (e & 1) { SCHED(); ld(e >> 1); SCHED(); }
  }
}
DI void make_kix(unsigned (&kix)[9], int ts) {
#pragma unroll
  for (int i = 0; i < 9; ++i) {
    const int lg = 2047 - 8 * ts - i;
    const int bits = (int)(pk2(__builtin_fmaxf((float)lg, 128.f), 0.f));
    const int ci = lg < bits - 0x4280 ? lg : bits - 0x4280;
    kix[i] = KTAB + 4 * ci;
    PIN(kix[i]);
  }
}
DI void put_k(LAS char* L, const unsigned (&kix)[9], int ts, int c, unsigned reg) {
  float kv[9];
#pragma unroll
  for (int i = 0; i < 9; ++i) kv[i] = *(LAS float*)(L + kix[i] + c * KP_STRIDE);
  LAS char* q = L + reg + CV_KOFF + ts * 16;
  *(LAS v4u*)(q + CV_KSTR) = v4u{pk2(kv[0], kv[1]), pk2(kv[2], kv[3]), pk2(kv[4], kv[5]), pk2(kv[6], kv[7])};
  *(LAS v4u*)q = v4u{pk2(kv[1], kv[2]), pk2(kv[3], kv[4]), pk2(kv[5], kv[6]), pk2(kv[7], kv[8])};
}
DI void put_u(LAS char* q, v4u u, bool first) {
  *(LAS v4u*)(q + 3 * CV_USTR + 384) = u;
  *(LAS v2u*)(q + 1 * CV_USTR + 392) = v2u{u.x, u.y}; *(LAS v2u*)(q + 1 * CV_USTR + 400) = v2u{u.z, u.w};
  *(LAS unsigned*)(q + 2 * CV_USTR + 388) = u.x; *(LAS v2u*)(q + 2 * CV_USTR + 392) = v2u{u.y, u.z}; *(LAS unsigned*)(q + 2 * CV_USTR + 400) = u.w;
  *(LAS unsigned*)(q + 396) = u.x; *(LAS v2u*)(q + 400) = v2u{u.y, u.z}; *(LAS unsigned*)(q + 408) = u.w;
  if (first) {
    *(LAS v2u*)(q + 384) = v2u{0u, 0u}; *(LAS unsigned*)(q + 392) = 0u;
    *(LAS v2u*)(q + CV_USTR + 384) = v2u{0u, 0u}; *(LAS unsigned*)(q + 2 * CV_USTR + 384) = 0u;
  }
}

__global__ __launch_bounds__(512, 2) void fused_s4_mlp(const char* __restrict__ x, const char* __restrict__ pAre, const char* __restrict__ pAim,
    const char* __restrict__ pBre, const char* __restrict__ pBim, const char* __restrict__ pCre, const char* __restrict__ pCim,
    const char* __restrict__ plogdt, const char* __restrict__ pDskip, const char* __restrict__ w1, const char* __restrict__ b1,
    const char* __restrict__ w2, char* __restrict__ out, char* __restrict__ ws) {
  __shared__ __attribute__((aligned(16))) char smem[163840];
  LAS char* L = (LAS char*)smem;
  const unsigned lds0 = (unsigned)(size_t)L;
  const char* pc0; unsigned codelen;
  asm volatile("s_getpc_b64 %0\n98:\n\ts_mov_b32 %1, 97f-98b\n\ts_max_u32 %1, %1, 99f-98b" : "=s"(pc0), "=&s"(codelen) :: "scc");
  const int b = blockIdx.x, lane = threadIdx.x & 63;
  const int w = RFL(threadIdx.x >> 6), wi = w & 3;
  {
    const unsigned co = (unsigned)(4 * (16 * w + (lane & 15)) + ((b >> 3) & 3)) * 128;
    asm volatile("s_mov_b32 m0, %2\n\ts_nop 0\n\tglobal_load_lds_dword %0, %1" ::"v"((co < codelen ? co : codelen) & ~3u), "s"(pc0), "s"(lds0 + YST + w * 256) : "memory");
  }
  const int xcd = b & 7, j = b >> 3, rg = xcd >> 1, chf = xcd & 1;
  const int ct1 = 8 * chf + (j & 7), rb1 = 4 * rg + (j >> 3);
  const int wid = rb1 * 16 + ct1;
  const int a2 = 4 * chf + (j >> 3), cb2 = j & 7;
  const int l15 = lane & 15, g = lane >> 4, qq = (lane >> 2) & 3, p4 = lane & 3, l31 = lane & 31, h = lane >> 5;
  const int par = j & 1, cbase = 80 * xcd + 5 * (j >> 1);
  const int chA = cbase + 3 * par, chS = cbase + 2;
  const int ts = 64 * wi + lane;
  u64 stv0, stv1;
  ST(0);
  const int row2 = 512 * rg + 64 * a2 + 16 * wi;
  const char* w2b = ws + WS_W2T + (size_t)(cb2 * 48 + 12 * wi) * 5120;
  const char* actb = ws + WS_ACT + (size_t)((32 * rg + 4 * a2) * 48 + 12 * wi) * 1024;
  const int r2 = ((a2 ^ (w >> 2)) & 1) * 6;
#define WL(s) w_loads(WF[s], w2b + rot12(r2 + (s)) * 5120, vo)
#define AL(s) a_loads(AF[s], actb + rot12(r2 + (s)) * 1024, vm)
#define AL1(s, m) ld128c<0>(AF[s][m], vm[m], actb + rot12(r2 + (s)) * 1024)
#define WL1(s, n) ld128c<0>(WF[s][n], vo, w2b + rot12(r2 + (s)) * 5120 + (n) * 1024)
#define NOLD [&](int) {}
#define LATE_LANE() int ln_ = lane; PIN(ln_); const int c15 = ln_ & 15, cg = ln_ >> 4
#define KAO (CV_KOFF + (c15 & 1) * CV_KSTR + 2 * (2072 - 8 * (c15 >> 1) + 8 * cg))
#define UBO ((c15 & 3) * CV_USTR + 2 * (64 * (c15 >> 2) + 8 * cg + 224))
#define YOFF ((unsigned)((c15 >> 2) * 128 + cg * 32 + (c15 & 3) * 8))
#define OROW ((unsigned)(2 * (c15 >> 2) + (c15 & 1) + 8 * ((c15 >> 1) & 1)))
#define ZOFF (lane < 24 ? wi * CV_USTR + lane * 16 : CV_KOFF + wi * CV_KSTR + 4096 + (lane - 24) * 16)
#define ZCOND (lane < 24 || (lane < 31 && wi < 2))

  unsigned voA[4], voB[3];
#pragma unroll
  for (int i = 0; i < 4; ++i) voA[i] = (8 * i + (lane >> 3)) * 1280 + (((lane & 7) ^ (4 * (i & 1) + (lane >> 4))) * 16) - 1024 * i;
#pragma unroll
  for (int i = 0; i < 3; ++i) { const int c = 64 * i + lane; voB[i] = (c / 12) * 3072 + (c % 12) * 16 - 1024 * i; }
  const char* g1a = x + (size_t)(rb1 * 128 + 32 * wi) * 1280;
  const char* g1b = w1 + ct1 * 192 + wi * 49152;
  const unsigned la = lds0 + G1_RING + wi * 4096, lb = lds0 + G1_RING + G1_BOFF + wi * 3072;
  const int r1 = ((j & 7) + (5 * (j >> 3)) / 2 + 5 * chf + rg) % 10;
  auto slab = [&](int s) { const int v = s + r1; return v >= 10 ? v - 10 : v; };
  auto issue = [&](int s) {
    const char* pa = g1a + slab(s) * 128; const char* pb = g1b + (size_t)slab(s) * 64 * 3072;
    dma16(voA[0], pa, la + s * G1_STAGE); dma16o<1024>(voA[1], pa); dma16o<2048>(voA[2], pa); dma16o<3072>(voA[3], pa);
    dma16(voB[0], pb, lb + s * G1_STAGE); dma16o<1024>(voB[1], pb); dma16o<2048>(voB[2], pb);
  };
  unsigned tlo = 0, thi = 0;
  unsigned kix[9];
  if (w < 4) {
    unsigned pr[2][7];
    const bool odd = wi & 1;
    {
      const int kch = chA + (wi >> 1), c0 = odd ? chS : kch;
      const unsigned vo0 = (unsigned)(odd ? chS * 192 + par * 96 + (wi >> 1) * 48 + (lane < 48 ? lane : 47) : kch * 192 + lane) * 2;
      const unsigned vo1 = (unsigned)(kch * 192 + (odd ? 128 : 64) + lane) * 2;
      ld16(pr[0][6], (unsigned)c0 * 2, plogdt);
      ld16(pr[0][0], vo0, pAre); ld16(pr[0][1], vo0, pAim); ld16(pr[0][2], vo0, pBre); ld16(pr[0][3], vo0, pBim); ld16(pr[0][4], vo0, pCre); ld16(pr[0][5], vo0, pCim);
      ld16(pr[1][6], (unsigned)kch * 2, plogdt);
      ld16(pr[1][0], vo1, pAre); ld16(pr[1][1], vo1, pAim); ld16(pr[1][2], vo1, pBre); ld16(pr[1][3], vo1, pBim); ld16(pr[1][4], vo1, pCre); ld16(pr[1][5], vo1, pCim);
    }
    bar();
#pragma unroll
    for (int i = 0; i < 9; ++i) kix[i] = 0;
    waitv<7>();
#pragma unroll
    for (int i = 0; i < 7; ++i) PIN(pr[0][i]);
    ST(1);
    f32x4 ksum[2][2];
#pragma unroll
    for (int i = 0; i < 4; ++i) ksum[i >> 1][i & 1] = f32x4{0.f, 0.f, 0.f, 0.f};
#pragma nounroll
    for (int t = 0; t < 2; ++t) {
      f32x4 kacc[2][2];
      const float scale = kgen_task(pr[0], t == 0 && odd && lane >= 48, lds0 + KG_SCR + wi * KG_WSTR, L + KG_SCR + wi * KG_WSTR + l15 * 272 + g * 16, kacc, [&](int) {});
#pragma unroll
      for (int i = 0; i < 4; ++i) ksum[i >> 1][i & 1] += kacc[i >> 1][i & 1] * scale;
      if (t == 1 || odd) {
        kgen_store((LAS float*)(L + KPART + (t == 0 ? 4 + (wi >> 1) : wi) * KP_STRIDE), ksum, lane);
#pragma unroll
        for (int i = 0; i < 4; ++i) ksum[i >> 1][i & 1] = f32x4{0.f, 0.f, 0.f, 0.f};
      }
      waitv<0>();
#pragma unroll
      for (int i = 0; i < 7; ++i) { PIN(pr[1][i]); pr[0][i] = pr[1][i]; }
    }
    lds_arrive(L, W_KG, lane);
  } else {
    if (w == 4) *(LAS unsigned*)(L + lane * 4) = 0u;
    bar();
    v2u E; v4u xa, xb; v2u xc;
    const int pu = b % 240, pks = pu / 5, pcg = pu % 5;
    const char* xrow = x + (size_t)(8 * wid + 2 * wi) * 1280;
    ld128(xa, h * 1280 + l31 * 16, xrow); ld128(xb, h * 1280 + 512 + l31 * 16, xrow); ld64(xc, h * 1280 + 1024 + l31 * 8, xrow);
    ld64c(E, 0u, ws);
#pragma unroll
    for (int i = 0; i < 2; ++i)
      dma16((unsigned)((lane >> 1) * 1280 + (lane & 1) * 16), w2 + (size_t)pks * 32 * 1280 + (128 * pcg + 32 * wi + 16 * i) * 2, lds0 + W2P + wi * 2048 + i * 1024);
    issue(0);
    make_kix(kix, ts);
    issue(1);
    waitv<14>();
    PIN(E); PIN(xa); PIN(xb); PIN(xc);
    ST(6);
  const u64 tag64 = (((u64)RFL(E.y) << 32) | (unsigned)RFL(E.x)) + 1ull;
  tlo = (unsigned)tag64; thi = (unsigned)(tag64 >> 32);
  if (wi == 0) { lds_set(L, W_TAG, tlo); lds_set(L, W_TAG + 4, thi); lds_set(L, W_TAG + 8, 1); }
  {
    f32x2 v[10] = {unpk(xa.x), unpk(xa.y), unpk(xa.z), unpk(xa.w), unpk(xb.x), unpk(xb.y), unpk(xb.z), unpk(xb.w), unpk(xc.x), unpk(xc.y)};
    f32x2 s2 = ((v[0] + v[1]) + (v[2] + v[3])) + ((v[4] + v[5]) + (v[6] + v[7])) + (v[8] + v[9]);
    const float mean = half_sum(s2.x + s2.y, h) * (1.0f / 640.0f);
    f32x2 q2 = f32x2{0.f, 0.f};
#pragma unroll
    for (int i = 0; i < 10; ++i) { const f32x2 d = v[i] - mean; q2 = __builtin_elementwise_fma(d, d, q2); }
    const float var = rbf(half_sum(q2.x + q2.y, h) * (1.0f / 640.0f));
    const float m = rbf(mean), sd = rbf(__builtin_amdgcn_sqrtf(rbf(var + 1e-5f))), rs = __builtin_amdgcn_rcpf(sd);
    unsigned o[10];
#pragma unroll
    for (int i = 0; i < 10; ++i) { const f32x2 t = (v[i] - m) * rs; o[i] = pk2(t.x, t.y); }
    LAS char* rp = L + LN_ROWS + (2 * wi + h) * LN_PITCH;
    *(LAS v4u*)(rp + l31 * 16) = v4u{o[0], o[1], o[2], o[3]};
    *(LAS v4u*)(rp + 512 + l31 * 16) = v4u{o[4], o[5], o[6], o[7]};
    *(LAS v2u*)(rp + 1024 + l31 * 8) = v2u{o[8], o[9]};
    waitv<0>();
    lds_arrive(L, W_SB, lane);
    issue(2);
    if (l31 == 0) st64c(WS_STATS + (unsigned)(8 * wid + 2 * wi + h) * 8, ws,
                        v2u{(__builtin_bit_cast(unsigned, m) & 0xffff0000u) | (__builtin_bit_cast(unsigned, sd) >> 16), tlo});
  }
#pragma unroll
  for (int i = 0; i < 2; ++i) {
    LAS char* sp = L + W2P + wi * 2048 + i * 1024 + (16 * (g >> 1) + 4 * (g & 1) + qq) * 32 + 8 * p4;
    const v2u lo = trread(sp), hi = trread(sp + 256);
    const int ntg = 8 * pcg + 2 * wi + i;
    st128c((unsigned)lane * 16, ws + WS_W2T + (size_t)(((ntg / 5) * 48 + pks) * 5 + ntg % 5) * 1024, v4u{lo.x, lo.y, hi.x, hi.y});
  }
  lds_wait_ge(L, W_SB, 4);
  {
    LAS char* sp = L + LN_ROWS + qq * LN_PITCH + (160 * wi + 16 * g) * 2 + 8 * p4;
#pragma unroll
    for (int i = 0; i < 3; ++i) {
      const v2u lo = trread(sp + 128 * i), hi = trread(sp + 128 * i + 4 * LN_PITCH);
      const unsigned ch = 64 * i + lane;
      if (i < 2 || lane < 32) st128c((ch / 5) * 20480 + (ch % 5) * 16, ws + WS_XNB + (size_t)wi * 32 * 20480 + wid * 80, v4u{lo.x, lo.y, hi.x, hi.y});
    }
  }
  }
  ST(2);
  int role = w; asm volatile("" : "+s"(role));
  if (role < 4) {
    int lane = __builtin_amdgcn_mbcnt_hi(~0u, __builtin_amdgcn_mbcnt_lo(~0u, 0u)); PIN(lane);
    const int l15 = lane & 15, g = lane >> 4, qq = (lane >> 2) & 3, p4 = lane & 3, l31 = lane & 31, h = lane >> 5;
    const unsigned vo = lane * 16;
    f32x16 acc1[3]; f32x4 csa[3];
#pragma unroll
    for (int n = 0; n < 3; ++n) {
      csa[n] = f32x4{0.f, 0.f, 0.f, 0.f};
#pragma unroll
      for (int i = 0; i < 16; ++i) acc1[n][i] = 0.f;
    }
    const int xrow = (l31 & 17) + 2 * (l15 >> 2) + 8 * ((l15 >> 1) & 1);
    unsigned xo[4], wo[4];
#pragma unroll
    for (int k = 0; k < 4; ++k) {
      const int kr = (k + wi) & 3;
      xo[k] = G1_RING + (32 * wi + xrow) * 128 + (((2 * kr + h) ^ ((xrow >> 1) & 7)) * 16);
      wo[k] = G1_RING + G1_BOFF + (16 * kr + 8 * h + qq) * 192 + 32 * (g & 1) + 8 * p4;
      PIN(xo[k]); PIN(wo[k]);
    }
    const unsigned sel1 = ((l15 ^ g) & 1) ? 0u : 0x3f803f80u;
    const v4u sel = v4u{sel1, sel1, sel1, sel1};
    v4u X[4], W[4][3];
    auto rdw = [&](int s, int k, int n) {
      LAS char* p = L + wo[k] + (s % 3) * G1_STAGE + 64 * n; const v2u lo = trread(p), hi = trread(p + 768); W[k][n] = v4u{lo.x, lo.y, hi.x, hi.y};
    };
    auto rdx = [&](int s, int k) { X[k] = *(LAS v4u*)(L + xo[k] + (s % 3) * G1_STAGE); };
#define MM(k, n) acc1[n] = mfma32(W[k][n], X[k], acc1[n])
#define CS(n) csa[n] = mfma(W[0][n], sel, csa[n])
    v2u gran; unsigned bb[3];
    v4u WF[6][5], AF[6][4];
    const unsigned go = WS_STATS + (unsigned)(rb1 * 128 + 32 * wi + xrow) * 8;
    auto wl1 = [&](int s, int e0) { const int e = 7 * (s - 7) + e0; if (s >= 7 && e0 < 7 && e < 20) ld128c<0>(WF[e / 5][e % 5], vo, w2b + rot12(r2 + e / 5) * 5120 + (e % 5) * 1024); };
    lds_wait_ge(L, W_SB, 4);
    ST(3);
    rdx(0, 0); rdw(0, 0, 0); rdw(0, 0, 1); rdw(0, 0, 2); rdx(0, 1); rdw(0, 1, 0); rdw(0, 1, 1); rdw(0, 1, 2);
#pragma unroll
    for (int s = 0; s < 10; ++s) {
      const bool refill = s < 7;
      const char* pa = g1a + slab(s + 3) * 128; const char* pb = g1b + (size_t)slab(s + 3) * 64 * 3072;
      SCHED();
      MM(0, 0); rdx(s, 2); rdw(s, 2, 0); SCHED();
      MM(0, 1); rdw(s, 2, 1); rdw(s, 2, 2); SCHED();
      MM(0, 2); rdx(s, 3); rdw(s, 3, 0); SCHED();
      CS(0); rdw(s, 3, 1); SCHED();
      CS(1); rdw(s, 3, 2); SCHED();
      CS(2);
      if (s == 0) {} else if (s == 1) lds_wait_ge(L, W_ST, 4); else if (s < 7) waitv<7>(); else if (s == 7) waitv<11>(); else if (s == 8) waitv<11>();
      if (s == 9 && l15 < 2) {
#pragma unroll
        for (int n = 0; n < 3; ++n) *(LAS f32x4*)(L + CSP + wi * 384 + (32 * n + 16 * l15 + 4 * g) * 4) = csa[n];
      }
      lds_set(L, W_RDY + wi * 4, s + 2);
      SCHED();
      MM(1, 0);
      if (refill) { asm volatile("s_waitcnt lgkmcnt(7)" ::: "memory"); dma16(voA[0], pa, la + (s % 3) * G1_STAGE); }
      wl1(s, 0);

      SCHED();
      MM(1, 1); if (refill) dma16o<1024>(voA[1], pa); wl1(s, 1);
      unsigned fl = 0;
      if (s < 9) fl = lw(L, W_RDY + p4 * 4);
      SCHED();
      MM(1, 2); if (refill) dma16o<2048>(voA[2], pa); wl1(s, 2); SCHED();
      MM(2, 0); if (refill) dma16o<3072>(voA[3], pa); wl1(s, 3);
      SCHED();
      if (s == 9) {
        MM(2, 1); wl1(s, 4); SCHED();
        lds_wait_min<4>(L, W_RDY, 11, lane);
        float cs[3];
#pragma unroll
        for (int n = 0; n < 3; ++n) { LAS float* p = (LAS float*)(L + CSP) + 32 * n + l31; cs[n] = fadd(fadd(p[0], p[96]), fadd(p[192], p[288])); }
        MM(2, 2); wl1(s, 5); SCHED();
        waitv<20>(); PIN(gran); PIN(bb[0]); PIN(bb[1]); PIN(bb[2]);
        MM(3, 0); MM(3, 1); MM(3, 2);
        const unsigned keep = h ? 0u : ~0u, nm = (gran.x >> 16) ^ 0x8000u;
        const v4u xe = v4u{(nm | (nm << 16)) & keep, (nm | (gran.x << 16)) & keep, 0u, 0u};
#pragma unroll
        for (int n = 0; n < 3; ++n) {
          const float c0 = bfhi(__builtin_bit_cast(unsigned, cs[n])), r0 = cs[n] - c0, c1 = bfhi(__builtin_bit_cast(unsigned, r0)), r1 = r0 - c1;
          const v4u we = v4u{(__builtin_bit_cast(unsigned, c0) >> 16) | __builtin_bit_cast(unsigned, c1), (__builtin_bit_cast(unsigned, r1) >> 16) | (bb[n] << 16), 0u, 0u};
          acc1[n] = mfma32(we, xe, acc1[n]);
        }
        SCHED();
        break;
      }
      if (__builtin_expect(__builtin_amdgcn_ballot_w64(fl < (unsigned)(s + 2)) != 0ull, 0)) lds_wait_min<4>(L, W_RDY, s + 2, lane);
      FENCE();
      SCHED();
      rdx(s + 1, 0); rdw(s + 1, 0, 0); SCHED();
      MM(2, 1); rdw(s + 1, 0, 1); rdw(s + 1, 0, 2); if (refill) dma16(voB[0], pb, lb + (s % 3) * G1_STAGE); wl1(s, 4); SCHED();
      MM(2, 2); rdx(s + 1, 1); rdw(s + 1, 1, 0); if (refill) dma16o<1024>(voB[1], pb); wl1(s, 5); SCHED();
      MM(3, 0); rdw(s + 1, 1, 1); if (refill) dma16o<2048>(voB[2], pb); wl1(s, 6); SCHED();
      MM(3, 1); rdw(s + 1, 1, 2); wl1(s, 7); SCHED();
      MM(3, 2); wl1(s, 8);
      if (s == 6) { lds_wait_ge(L, W_F1ALL, 4); ld64c(gran, go, ws); ld16(bb[0], l31 * 2, b1 + ct1 * 192); ld16(bb[1], l31 * 2, b1 + ct1 * 192 + 64); ld16(bb[2], l31 * 2, b1 + ct1 * 192 + 128); }
      SCHED();
      if (s == 0) ST(4); if (s == 3) ST(5); if (s == 6) ST(7);
    }
#undef MM
#undef CS
    ST(11);
    const unsigned tlo = RFL(lw(L, W_TAG)), thi = RFL(lw(L, W_TAG + 4));
    {
      const float rs = __builtin_amdgcn_rcpf(bf2f(gran.x)) * 1.44269504088896341f;
      char* ab = ws + WS_ACT + (size_t)((rb1 * 8 + 2 * wi) * 48 + 3 * ct1) * 1024;
      const unsigned ao = (unsigned)((l31 >> 4) * 49152 + h * 256 + l15 * 16);
#pragma unroll
      for (int q = 0; q < 6; ++q) {
        unsigned pk[4];
#pragma unroll
        for (int e = 0; e < 4; ++e) {
          const f32x2 h2 = f32x2{acc1[q >> 1][8 * (q & 1) + 2 * e], acc1[q >> 1][8 * (q & 1) + 2 * e + 1]} * rs;
          const f32x2 s1 = f32x2{__builtin_amdgcn_exp2f(h2.x), __builtin_amdgcn_exp2f(h2.y)} + 1.0f;
          const f32x2 d = __builtin_elementwise_fma(s1, s1, f32x2{1.f, 1.f});
          const f32x2 rr = f32x2{__builtin_amdgcn_rcpf(d.x), __builtin_amdgcn_rcpf(d.y)};
          const f32x2 o = h2 * __builtin_elementwise_fma(rr, f32x2{-1.38629436111989062f, -1.38629436111989062f},
                                                         f32x2{0.69314718055994531f, 0.69314718055994531f});
          pk[e] = pk2(o.x, o.y);
        }
        st128c(ao + q * 512, ab, v4u{pk[0], pk[1], pk[2], pk[3]});
      }
    }
    lds_set(L, W_RDY + wi * 4, 12);
    ST(13);
    __builtin_amdgcn_s_setprio(2);
    LATE_LANE();
    v2u xq[5], dq[5], gr2, f3;
    const unsigned f2o = WS_F2 + (unsigned)((4 * rg + (a2 >> 1)) * 16 + 4 * wi + (ln_ & 3)) * 128;
    unsigned vm[4];
#pragma unroll
    for (int m = 0; m < 4; ++m) vm[m] = vo + ((m + wi) & 3) * 49152;
    v2u f2; v4u ysh;
    {
      f32x4 sacc[4];
#pragma unroll
      for (int i = 0; i < 4; ++i) sacc[i] = f32x4{0.f, 0.f, 0.f, 0.f};
      unsigned kb = CVS + KAO - wi * 128 - 3584 - 64, ubb = CVS + UBO - wi * 128 - 64; PIN(kb); PIN(ubb);
      LAS char* ka = L + kb; LAS char* ub = L + ubb;
      const bool late = RFL(lw(L, W_SB)) < 16;
      if (late) { waitv<0>(); arrive_flag<4>(L, W_F2C, lane, ws, WS_F2 + (unsigned)wid * 128, tlo, thi); }
      lds_wait_ge(L, W_SB, 16);
      if (par) conv_unit_shared<1>(ka + 64, ub + 64, sacc); else conv_unit_shared<0>(ka + 64, ub + 64, sacc);
      if (!late) { waitv<0>(); arrive_flag<4>(L, W_F2C, lane, ws, WS_F2 + (unsigned)wid * 128, tlo, thi); }
      ST(14);
      WL(4); WL(5);
      if (par) conv_unit_shared<1>(ka, ub, sacc); else conv_unit_shared<0>(ka, ub, sacc);
#pragma unroll
      for (int q = 0; q < 4; ++q) *(LAS f32x4*)(L + MXCH + (q * 4 + wi) * 1024 + vo) = sacc[q];
      lds_arrive(L, W_MB, lane);
    {
      const char* xb2 = x + (size_t)row2 * 1280 + cb2 * 160; const unsigned xo2 = OROW * 1280 + cg * 8;
      ld64o<0>(xq[0], xo2, xb2); ld64o<32>(xq[1], xo2, xb2); ld64o<64>(xq[2], xo2, xb2); ld64o<96>(xq[3], xo2, xb2); ld64o<128>(xq[4], xo2, xb2);
      const char* db = pDskip + cb2 * 160;
      ld64o<0>(dq[0], cg * 8, db); ld64o<32>(dq[1], cg * 8, db); ld64o<64>(dq[2], cg * 8, db); ld64o<96>(dq[3], cg * 8, db); ld64o<128>(dq[4], cg * 8, db);
      ld64c(gr2, WS_STATS + (row2 + OROW) * 8, ws);
    }
      lds_wait_ge(L, W_MB, 4);
      LAS char* p = L + MXCH + wi * 4096 + vo;
      const f32x4 s = add4(add4(*(LAS f32x4*)p, *(LAS f32x4*)(p + 1024)), add4(*(LAS f32x4*)(p + 2048), *(LAS f32x4*)(p + 3072)));
      ld64c(f2, f2o, ws);
      const unsigned y0 = pk2(s.x, s.y), y1 = pk2(s.z, s.w);
      *(LAS v2u*)(L + MXCH + wi * 4096 + YOFF) = v2u{y0, y1};
      FENCE();
      ysh = *(LAS v4u*)p;
    }
    ST(15);
    waitv<0>(); PIN(f2);
    f2 = spin64<false>(f2, f2o, ws, tlo, thi);
    lds_set(L, W_F2OK + wi * 4, 1);
    ST(16);
    AL(0); AL(1);
    if (ln_ < 32) st128c(vo, ws + WS_Y + (size_t)chS * 4096 + (2 * wi + ((wi & 1) ^ par)) * 512, ysh);
    f32x4 acc2[4][5];
    PIN(gr2);
    const float rs2 = __builtin_amdgcn_rcpf(bf2f(gr2.x)), m2 = -bfhi(gr2.x) * rs2;
#pragma unroll
    for (int n = 0; n < 5; ++n) {
      PIN(xq[n]); PIN(dq[n]);
      const f32x2 x0 = unpk(xq[n].x), x1 = unpk(xq[n].y), d0 = unpk(dq[n].x), d1 = unpk(dq[n].y);
      acc2[0][n] = f32x4{ffma(d0.x, ffma(x0.x, rs2, m2), x0.x), ffma(d0.y, ffma(x0.y, rs2, m2), x0.y),
                         ffma(d1.x, ffma(x1.x, rs2, m2), x1.x), ffma(d1.y, ffma(x1.y, rs2, m2), x1.y)};
      PIN(acc2[0][n]);
    }
    __builtin_amdgcn_s_setprio(0);
    ST(17);
    waitv<4>(); g2_step<1>(acc2, WF[0], AF[0], [&](int i) { if (i >= 3 && (i & 1)) AL1(2, (i - 3) >> 1); });
    waitv<4>();
    arrive_flag<8>(L, W_F3C, lane, ws, WS_F3 + (unsigned)(xcd * 32 + j) * 128, tlo, thi);
    g2_step<4>(acc2, WF[1], AF[1], [&](int i) { if (i >= 2) AL1(3 + ((i - 2) >> 2), (i - 2) & 3); });
    waitv<8>(); g2_step<4>(acc2, WF[2], AF[2], [&](int i) { if (i < 4) AL1(5, i); });
    ST(20);
    waitv<8>(); g2_step<4>(acc2, WF[3], AF[3], NOLD);
    waitv<4>(); g2_step<4>(acc2, WF[4], AF[4], NOLD);
    lds_wait_ge(L, W_F3C, 8);
    const unsigned f3o = WS_F3 + (unsigned)(cb2 * 32 + 8 * wi + (ln_ & 7)) * 128;
    ld64c(f3, f3o, ws);
    waitv<1>(); g2_step<4>(acc2, WF[5], AF[5], NOLD);
    ST(22);
    waitv<0>(); PIN(f3);
    f3 = spin64<false>(f3, f3o, ws, tlo, thi);
    lds_arrive(L, W_F3ALL, lane);
#pragma unroll
    for (int n = 0; n < 5; ++n) *(LAS f32x4*)(L + FM + wi * 15360 + n * 1024 + vo) = acc2[1][n];
    lds_wait_ge(L, W_F3ALL, 4);
    ST(21);
    v4u yv[3];
    {
      const char* yb = ws + WS_Y + (size_t)cb2 * 80 * 4096 + row2 * 2;
      const unsigned yo = (unsigned)(ln_ >> 1) * 4096 + (ln_ & 1) * 16;
      ld128c<0>(yv[0], yo, yb); ld128c<0>(yv[1], yo, yb + 32 * 4096); ld128c<0>(yv[2], yo & 0xffffu, yb + 64 * 4096);
    }
#pragma unroll
    for (int m = 2; m < 4; ++m)
#pragma unroll
      for (int n = 0; n < 5; ++n) *(LAS f32x4*)(L + FM + wi * 15360 + (m - 1) * 5120 + n * 1024 + vo) = acc2[m][n];
    lds_arrive(L, W_FM, lane);
    lds_wait_ge(L, W_FM, 4);
#pragma unroll
    for (int d = 1; d < 4; ++d)
#pragma unroll
      for (int n = 0; n < 5; ++n) acc2[0][n] = add4(acc2[0][n], *(LAS f32x4*)(L + FM + ((wi - d) & 3) * 15360 + (d - 1) * 5120 + n * 1024 + vo));
    waitv<0>(); PIN(yv[0]); PIN(yv[1]); PIN(yv[2]);
    LAS char* ys = L + YST + wi * 2560;
    *(LAS v4u*)(ys + vo) = yv[0]; *(LAS v4u*)(ys + 1024 + vo) = yv[1];
    if (ln_ < 32) *(LAS v4u*)(ys + 2048 + vo) = yv[2];
    FENCE();
#pragma unroll
    for (int n = 0; n < 5; ++n) {
      const v2u t = trread(ys + n * 512 + ln_ * 8);
      const f32x2 t0 = unpk(t.x), t1 = unpk(t.y);
      *(LAS f32x4*)(L + FM + 61440 + wi * 5120 + n * 1024 + vo) = add4(acc2[0][n], f32x4{t0.x, t0.y, t1.x, t1.y});
    }
    lds_set(L, W_F2OK + wi * 4, 2);
    STEND();
    asm volatile("97:");
    __builtin_amdgcn_endpgm();
  }

  const bool odd = wi & 1;
  const int kch = chA + (wi >> 1);
  const unsigned vo = lane * 16;
  v4u WF[6][5], AF[6][4];
  waitv<6>();
  lds_arrive(L, W_ST, lane);
  waitv<0>();
  arrive_flag8(L, W_F1C, ws, WS_F1 + (unsigned)(lane & 7) * 2048 + wid * 8, tlo, thi);
  ST(8);
  auto reduce = [&](int c, int i) {
    LAS char* pp = L + KPART + 2 * c * KP_STRIDE + i * 16;
    const f32x4 s = add4(*(LAS f32x4*)pp, *(LAS f32x4*)(pp + KP_STRIDE));
    *(LAS f32x4*)(L + KTAB + c * KP_STRIDE + i * 16) = s;
    return s;
  };
  lds_wait_ge(L, W_KG, 4);
  if (ts < 161) { st128c((unsigned)ts * 16, ws + WS_KP + (size_t)wid * KP_STRIDE, __builtin_bit_cast(v4u, reduce(2, ts))); reduce(0, ts); }
  if (ts >= 95) reduce(1, ts - 95);
  if (ts < 3) *(LAS float*)(L + KTAB + ts * KP_STRIDE - 4) = 0.f;
  lds_arrive(L, W_SB, lane);
  waitv<0>();
  arrive_flag(L, W_FKC, lane, ws, WS_FK + (unsigned)wid * 64, tlo, thi);
  ST(9);
  __builtin_amdgcn_s_setprio(0);
  lds_wait_min<4>(L, W_RDY, F1STAGE, lane);
  const unsigned f1o = WS_F1 + (unsigned)xcd * 2048 + ts * 8;
  v2u f1own; ld64c(f1own, f1o, ws);
  waitv<0>(); PIN(f1own);
  f1own = spin64<false>(f1own, f1o, ws, tlo, thi);
  v4u uu[3]; unsigned ulo[3];
#pragma unroll
  for (int r = 0; r < 3; ++r) {
    const int e = 64 * r + lane, tb = 64 * wi + e / 3, c = e % 3;
    ld128c<0>(uu[r], tb * 80 + c * 16, ws + WS_XNB + (size_t)(16 * xcd + (j >> 1)) * 20480 + par * 32);
    ulo[r] = (c == 2 - 2 * par ? CVS : CVA + (c - par) * CV_CH) + tb * 16;
  }
  if (b == 0) arrive_flag(L, W_F1ALL, lane, ws, WS_E, tlo, thi);
  else lds_arrive(L, W_F1ALL, lane);
  ST(4);
  const unsigned fko = WS_FK + (unsigned)(wid ^ 1) * 64;
  v2u fk; ld64c(fk, fko, ws);
  waitv<0>(); PIN(fk); PIN(uu[0]); PIN(uu[1]); PIN(uu[2]);
  fk = spin64<false>(fk, fko, ws, tlo, thi);
  v4u kp; ld128c<0>(kp, (unsigned)(ts < 161 ? ts : 160) * 16, ws + WS_KP + (size_t)(wid ^ 1) * KP_STRIDE);
  lds_wait_ge(L, W_SB, 8);
  waitv<0>(); PIN(kp);
  if (ts < 161) { LAS f32x4* tp = (LAS f32x4*)(L + KTAB + 2 * KP_STRIDE + ts * 16); *tp = add4(*tp, __builtin_bit_cast(f32x4, kp)); }
  lds_arrive(L, W_SB, lane);
  lds_wait_ge(L, W_F1ALL, 4);
  WL(0);
  lds_wait_min<4>(L, W_RDY, 9, lane);
  put_k(L, kix, ts, 0, CVA);
  WL(1);
  lds_wait_min<4>(L, W_RDY, 10, lane);
  put_k(L, kix, ts, 1, CVA + CV_CH);
  if (ZCOND) { *(LAS v4u*)(L + CVA + ZOFF) = v4u{0u, 0u, 0u, 0u}; *(LAS v4u*)(L + CVA + CV_CH + ZOFF) = v4u{0u, 0u, 0u, 0u}; }
  ST(7);
  lds_wait_min<4>(L, W_RDY, 11, lane);
  ST(11);
  put_u(L + ulo[0], uu[0], ts < 3); put_u(L + ulo[1], uu[1], false); put_u(L + ulo[2], uu[2], false);
  if (ZCOND) *(LAS v4u*)(L + CVS + ZOFF) = v4u{0u, 0u, 0u, 0u};
  lds_wait_ge(L, W_SB, 12);
  put_k(L, kix, ts, 2, CVS);
  lds_arrive(L, W_SB, lane);
  WL(2); WL(3); WL(4); WL(5);
  ST(5);
  lds_wait_ge(L, W_SB, 16);
  lds_wait_ge(L, W_RDY + wi * 4, 12);
  ST(13);
  __builtin_amdgcn_s_setprio(SPRIO2);
  LATE_LANE();
  f32x4 cacc[8];
#pragma unroll
  for (int i = 0; i < 8; ++i) cacc[i] = f32x4{0.f, 0.f, 0.f, 0.f};
  {
    const unsigned o1 = CVA + (wi >> 1) * CV_CH - (odd ? 4 * 64 : 0);
    unsigned kb = KAO + o1 - 3584 - 192, ubb = UBO + o1 - 192; PIN(kb); PIN(ubb);
    LAS char* ka = L + kb; LAS char* ub = L + ubb;
    v4u A[8], B[8];
#define LA(i, u) (*(LAS v4u*)(ka + 512 * (7 - (i)) + 64 * (3 - (u))))
#define LB(i, u) (*(LAS v4u*)(ub + 512 * (i) + 64 * (3 - (u))))
    A[7] = LA(7, 0); B[0] = LB(0, 0); A[0] = LA(0, 0); B[7] = LB(7, 0); A[6] = LA(6, 0); B[6] = LB(6, 0); A[1] = LA(1, 0); B[1] = LB(1, 0);
    A[5] = LA(5, 0); B[5] = LB(5, 0); A[2] = LA(2, 0); B[2] = LB(2, 0); A[4] = LA(4, 0); B[4] = LB(4, 0); A[3] = LA(3, 0); B[3] = LB(3, 0);
    SCHED();
#pragma unroll
    for (int u = 0; u < 4; ++u) {
      v4u An[2], Bn[2];
#pragma unroll
      for (int sd = 7; sd >= 0; --sd) {
#pragma unroll
        for (int o = 0; o <= sd; ++o) {
          if (sd + o < 8) cacc[sd + o] = mfma(A[sd], B[o], cacc[sd + o]);
          if (o != sd && sd + o < 8) cacc[sd + o] = mfma(A[o], B[sd], cacc[sd + o]);
        }
        SCHED();
        if (u < 3) {
          if (sd >= 2) { A[sd] = LA(sd, u + 1); B[sd] = LB(sd, u + 1); }
          if (sd == 6 || sd == 5) { An[6 - sd] = LA(6 - sd, u + 1); Bn[6 - sd] = LB(6 - sd, u + 1); }
        }
        SCHED();
      }
      if (u < 3) { A[0] = An[0]; A[1] = An[1]; B[0] = Bn[0]; B[1] = Bn[1]; }
    }
#undef LA
#undef LB
  }
  ST(10);
  unsigned vm[4];
  {
    LAS char* px = L + SXCH + wi * 4096 + vo;
    if (odd) {
#pragma unroll
      for (int i = 0; i < 4; ++i) { *(LAS f32x4*)(px + i * 1024) = cacc[i]; cacc[i] = cacc[4 + i]; }
    } else {
#pragma unroll
      for (int i = 0; i < 4; ++i) *(LAS f32x4*)(px + i * 1024) = cacc[4 + i];
    }
    lds_arrive(L, W_SX + (wi >> 1) * 4, lane);
#pragma unroll
    for (int m = 0; m < 4; ++m) vm[m] = vo + ((m + wi) & 3) * 49152;
    lds_wait_ge(L, W_SX + (wi >> 1) * 4, 2);
    px = L + SXCH + (wi ^ 1) * 4096 + vo;
    v2u yo[4];
#pragma unroll
    for (int i = 0; i < 4; ++i) { const f32x4 s = add4(cacc[i], *(LAS f32x4*)(px + i * 1024)); yo[i] = v2u{pk2(s.x, s.y), pk2(s.z, s.w)}; }
    char* yb = ws + WS_Y + (size_t)kch * 4096 + (odd ? 2048 : 0);
    LAS char* yl = L + SXCH + (wi ^ 1) * 4096;
#pragma unroll
    for (int i = 0; i < 4; ++i) *(LAS v2u*)(yl + i * 512 + YOFF) = yo[i];
    FENCE();
    st128c(vo, yb, *(LAS v4u*)(yl + vo)); st128c(vo, yb + 1024, *(LAS v4u*)(yl + 1024 + vo));
  }
  ST(14);
  lds_wait_ge(L, W_F2OK + wi * 4, 1);
  ST(16);
  AL(0); AL(1);
  ST(17);
  f32x4 acc2[4][5];
  waitv<8>();
  arrive_flag<8>(L, W_F3C, lane, ws, WS_F3 + (unsigned)(xcd * 32 + j) * 128, tlo, thi);
  __builtin_amdgcn_s_setprio(0);
  waitv<4>(); g2_step<0>(acc2, WF[0], AF[0], [&](int i) { if (i >= 3 && (i & 1)) AL1(2, (i - 3) >> 1); });
  waitv<4>(); g2_step<4>(acc2, WF[1], AF[1], [&](int i) { if (i >= 2) AL1(3 + ((i - 2) >> 2), (i - 2) & 3); });
  waitv<8>(); g2_step<4>(acc2, WF[2], AF[2], [&](int i) { if (i < 4) AL1(5, i); });
  ST(20);
  waitv<8>(); g2_step<4>(acc2, WF[3], AF[3], NOLD);
  lds_wait_ge(L, W_F3C, 8);
  waitv<0>();
#pragma unroll
  for (int m = 0; m < 4; ++m) { PIN(AF[4][m]); PIN(AF[5][m]); }
#pragma unroll
  for (int n = 0; n < 5; ++n) { PIN(WF[4][n]); PIN(WF[5][n]); }
#pragma unroll
  for (int mm = 1; mm < 5; ++mm) {
    const int m = mm & 3;
#pragma unroll
    for (int n = 0; n < 5; ++n) {
      acc2[m][n] = mfma(WF[4][n], AF[4][m], acc2[m][n]);
      acc2[m][n] = mfma(WF[5][n], AF[5][m], acc2[m][n]);
      if (mm > 1) *(LAS f32x4*)(L + FS + wi * 15360 + (mm - 2) * 5120 + n * 1024 + vo) = acc2[mm - 1][n];
      SCHED();
    }
  }
  lds_arrive(L, W_FS, lane);
  ST(22);
  lds_wait_ge(L, W_FS, 4);
#pragma unroll
  for (int d = 1; d < 4; ++d)
#pragma unroll
    for (int n = 0; n < 5; ++n) acc2[0][n] += *(LAS f32x4*)(L + FS + ((wi - d) & 3) * 15360 + (d - 1) * 5120 + n * 1024 + vo);
  ST(23);
  lds_wait_ge(L, W_F2OK + wi * 4, 2);
  ST(24);
  {
    char* ob = out + (size_t)row2 * 1280 + cb2 * 160; const unsigned oo = OROW * 1280 + cg * 8;
    v2u o[5];
#pragma unroll
    for (int n = 0; n < 5; ++n) {
      const f32x4 s = acc2[0][n] + *(LAS f32x4*)(L + FM + 61440 + wi * 5120 + n * 1024 + vo);
      o[n] = v2u{pk2(s.x, s.y), pk2(s.z, s.w)};
    }
    st64o<0>(oo, ob, o[0]); st64o<32>(oo, ob, o[1]); st64o<64>(oo, ob, o[2]); st64o<96>(oo, ob, o[3]); st64o<128>(oo, ob, o[4]);
  }
  STEND();
  asm volatile("99:");
}

extern "C" void kernel_launch(void* const* d_in, const int* in_sizes, int n_in, void* d_out, int out_size, void* d_ws, size_t ws_size,
                              hipStream_t stream) {
  fused_s4_mlp<<<256, 512, 0, stream>>>((const char*)d_in[0], (const char*)d_in[1], (const char*)d_in[2], (const char*)d_in[3],
      (const char*)d_in[4], (const char*)d_in[5], (const char*)d_in[6], (const char*)d_in[7], (const char*)d_in[8], (const char*)d_in[10],
      (const char*)d_in[11], (const char*)d_in[12], (char*)d_out, (char*)d_ws);
}
```

```cpp
#include <hip/hip_runtime.h>
#include <stdint.h>

typedef unsigned short u16;
typedef unsigned long long u64;
typedef unsigned v4u __attribute__((ext_vector_type(4)));
typedef unsigned v2u __attribute__((ext_vector_type(2)));
typedef float f32x4 __attribute__((ext_vector_type(4)));
typedef float f32x2 __attribute__((ext_vector_type(2)));
typedef short bf16x8 __attribute__((ext_vector_type(8)));
typedef short s16x4 __attribute__((ext_vector_type(4)));
typedef __bf16 bf16x2_t __attribute__((ext_vector_type(2)));
#define LAS __attribute__((address_space(3)))
#define GAS __attribute__((address_space(1)))
#define DI __device__ __forceinline__

constexpr unsigned WS_E = 0, WS_TMO = 64, WS_F1 = 131072, WS_STATS = 16384, WS_F2 = 32768, WS_F3 = 65536, WS_FK = 98304, WS_STAMP = 262144;
constexpr unsigned WS_XNB = 1u << 20, WS_W2T = 4u << 20, WS_Y = 7u << 20, WS_ACT = 10u << 20, WS_KP = 17u << 20;
constexpr unsigned KP_STRIDE = 2624;
constexpr unsigned SPIN_CAP = 400000;

constexpr unsigned W_F1C = 0, W_F2C = 4, W_F3C = 8, W_KG = 12, W_MB = 16, W_SB = 20, W_SX = 24, W_FM = 32, W_FS = 36, W_F3ALL = 40, W_F1ALL = 44,
                   W_F2OK = 48, W_RDY = 64, W_TAG = 80, W_FKC = 96, W_ST = 100, W_S0 = 104;
constexpr unsigned KTAB = 272;
constexpr unsigned CSP = 8192;
constexpr unsigned CVA = 10240, CV_CH = 27008, CV_USTR = 4640, CV_KOFF = 18560, CV_KSTR = 4224;
constexpr unsigned KG_SCR = CVA, KG_WSTR = 4352, LN_ROWS = KG_SCR + 8 * KG_WSTR, LN_PITCH = 1344, W2P = LN_ROWS + 10752;
constexpr unsigned G1_RING = 64512, G1_STAGE = 28672, G1_BOFF = 16384;
constexpr unsigned CVS = G1_RING, SXCH = CVS + 27136, MXCH = SXCH + 16384;
constexpr unsigned CSF = G1_RING + 3 * G1_STAGE, YST = CSF + 3072;
constexpr unsigned FM = 256, FS = FM + 81920;
static_assert(FS + 61440 <= CSF && CVA + 2 * CV_CH <= G1_RING && W2P + 8192 <= G1_RING && YST + 10240 <= 163840 && MXCH + 16384 <= CSF, "lds map");

#define ROT2 0
#define F1STAGE 5
#define SPRIO2 1
#define F1GAP 16
#define F2GAP 8
#define SPRIO 1
#define ST(i) do {} while (0)
#define STEND() do {} while (0)
#define STARG
#define STPASS

DI float bf2f(unsigned v) { return __builtin_bit_cast(float, v << 16); }
DI float bfhi(unsigned v) { return __builtin_bit_cast(float, v & 0xffff0000u); }
DI unsigned pk2(float lo, float hi) { bf16x2_t r; r.x = (__bf16)lo; r.y = (__bf16)hi; return __builtin_bit_cast(unsigned, r); }
DI float rbf(float f) { return bf2f(pk2(f, f) & 0xffffu); }
DI f32x4 mfma(v4u a, v4u b, f32x4 c) {
  return __builtin_amdgcn_mfma_f32_16x16x32_bf16(__builtin_bit_cast(bf16x8, a), __builtin_bit_cast(bf16x8, b), c, 0, 0, 0);
}
DI v2u trread(LAS char* p) { return __builtin_bit_cast(v2u, __builtin_amdgcn_ds_read_tr16_b64_v4i16((LAS s16x4*)p)); }
template <int N> DI void waitv() { asm volatile("s_waitcnt vmcnt(%0)" ::"i"(N) : "memory"); }
DI void bar() { asm volatile("s_waitcnt lgkmcnt(0)\n\ts_barrier" ::: "memory"); }
#define PIN(x) asm volatile("" : "+v"(x))
#define FENCE() asm volatile("" ::: "memory")
#define SCHED() __builtin_amdgcn_sched_barrier(0)
#define RFL(x) __builtin_amdgcn_readfirstlane(x)

DI void ld16(unsigned& d, unsigned vo, const void* sb) { asm volatile("global_load_ushort %0, %1, %2" : "=v"(d) : "v"(vo), "s"(sb) : "memory"); }
template <int O> DI void ld32(unsigned& d, unsigned vo, const void* sb) { asm volatile("global_load_dword %0, %1, %2 offset:%3" : "=v"(d) : "v"(vo), "s"(sb), "i"(O) : "memory"); }
DI void ld64(v2u& d, unsigned vo, const void* sb) { asm volatile("global_load_dwordx2 %0, %1, %2" : "=v"(d) : "v"(vo), "s"(sb) : "memory"); }
DI void ld128(v4u& d, unsigned vo, const void* sb) { asm volatile("global_load_dwordx4 %0, %1, %2" : "=v"(d) : "v"(vo), "s"(sb) : "memory"); }
DI void ld64c(v2u& d, unsigned vo, const void* sb) { asm volatile("global_load_dwordx2 %0, %1, %2 sc1" : "=v"(d) : "v"(vo), "s"(sb) : "memory"); }
template <int O> DI void ld128c(v4u& d, unsigned vo, const void* sb) { asm volatile("global_load_dwordx4 %0, %1, %2 offset:%3 sc1" : "=v"(d) : "v"(vo), "s"(sb), "i"(O) : "memory"); }
DI void st64c(unsigned vo, void* sb, v2u d) { asm volatile("global_store_dwordx2 %0, %1, %2 sc1" ::"v"(vo), "v"(d), "s"(sb) : "memory"); }
template <int O> DI void ld64o(v2u& d, unsigned vo, const void* sb) { asm volatile("global_load_dwordx2 %0, %1, %2 offset:%3" : "=v"(d) : "v"(vo), "s"(sb), "i"(O) : "memory"); }
template <int O> DI void st64o(unsigned vo, void* sb, v2u d) { asm volatile("global_store_dwordx2 %0, %1, %2 offset:%3" ::"v"(vo), "v"(d), "s"(sb), "i"(O) : "memory"); }
template <int O> DI void st64co(unsigned vo, void* sb, v2u d) { asm volatile("global_store_dwordx2 %0, %1, %2 offset:%3 sc1" ::"v"(vo), "v"(d), "s"(sb), "i"(O) : "memory"); }
DI void st128c(unsigned vo, void* sb, v4u d) { asm volatile("global_store_dwordx4 %0, %1, %2 sc1\n\ts_nop 1" ::"v"(vo), "v"(d), "s"(sb) : "memory"); }
DI void st64u(unsigned vo, void* sb, v2u d) { asm volatile("global_store_dwordx2 %0, %1, %2 sc1 ; CNT" ::"v"(vo), "v"(d), "s"(sb) : "memory"); }
DI void st128u(unsigned vo, void* sb, v4u d) { asm volatile("global_store_dwordx4 %0, %1, %2 sc1 ; CNT\n\ts_nop 1" ::"v"(vo), "v"(d), "s"(sb) : "memory"); }
DI void dma16(unsigned vo, const void* sb, unsigned lds) {
  asm volatile("s_mov_b32 m0, %2\n\ts_nop 0\n\tglobal_load_lds_dwordx4 %0, %1" ::"v"(vo), "s"(sb), "s"(lds) : "memory");
}
template <int O> DI void dma16o(unsigned vo, const void* sb) {
  asm volatile("global_load_lds_dwordx4 %0, %1 offset:%2" ::"v"(vo), "s"(sb), "i"(O) : "memory");
}
template <bool HIONLY> DI v2u spin64(v2u cur, unsigned vo, char* ws, unsigned tlo, unsigned thi) {
  if (__builtin_expect(__builtin_amdgcn_ballot_w64(HIONLY ? (cur.y != tlo) : (cur.x != tlo || cur.y != thi)) != 0ull, 0)) {
#pragma nounroll
    for (unsigned spins = 0; spins < SPIN_CAP; ++spins) {
      __builtin_amdgcn_s_sleep(6);
      asm volatile("global_load_dwordx2 %0, %1, %2 sc1\n\ts_waitcnt vmcnt(0)" : "=&v"(cur) : "v"(vo), "s"(ws) : "memory");
      if (__builtin_amdgcn_ballot_w64(HIONLY ? (cur.y != tlo) : (cur.x != tlo || cur.y != thi)) == 0ull) break;
    }
  }
  return cur;
}
DI unsigned lw(LAS char* L, unsigned off) { return *(volatile LAS unsigned*)(L + off); }
DI void lds_wait_at(unsigned a, unsigned target) {
  unsigned v, cnt; u64 t;
  asm volatile("s_mov_b32 %1, %5\n"
               "1:\n\tds_read_b32 %0, %3\n\ts_sub_u32 %1, %1, 1\n\ts_waitcnt lgkmcnt(0)\n\tv_cmp_gt_u32 vcc, %4, %0\n\t"
               "s_cselect_b64 %2, 0, vcc\n\ts_cmp_lg_u64 %2, 0\n\ts_cbranch_scc1 1b"
               : "=&v"(v), "=&s"(cnt), "=&s"(t) : "v"(a), "s"(target), "i"(4 * SPIN_CAP) : "vcc", "scc", "memory");
}
DI void lds_wait_ge(LAS char* L, unsigned off, unsigned target) { lds_wait_at((unsigned)(size_t)L + off, target); }
template <int N> DI void lds_wait_min(LAS char* L, unsigned off, unsigned target, int lane) {
  lds_wait_at((unsigned)(size_t)L + off + (lane & (N - 1)) * 4, target);
}
DI void lds_set(LAS char* L, unsigned off, unsigned v) { FENCE(); *(volatile LAS unsigned*)(L + off) = v; FENCE(); }
DI void lds_arrive(LAS char* L, unsigned off, int) {
  asm volatile("s_mov_b64 exec, 1\n\tds_add_u32 %0, %1\n\ts_mov_b64 exec, -1" ::"v"((unsigned)(size_t)L + off), "v"(1u) : "memory");
}
template <unsigned N = 4> DI void arrive_flag(LAS char* L, unsigned off, int, char* ws, unsigned fo, unsigned tlo, unsigned thi) {
  unsigned old;
  asm volatile("s_mov_b64 exec, 1\n\tds_add_rtn_u32 %0, %1, %2\n\ts_waitcnt lgkmcnt(0)\n\tv_cmp_eq_u32 vcc, %6, %0\n\t"
               "s_mov_b64 exec, vcc\n\tglobal_store_dwordx2 %3, %4, %5 sc1\n\ts_mov_b64 exec, -1"
               : "=&v"(old) : "v"((unsigned)(size_t)L + off), "v"(1u), "v"(fo), "v"(v2u{tlo, thi}), "s"(ws), "i"(N - 1) : "vcc", "memory");
}
DI void arrive_flag8(LAS char* L, unsigned off, char* ws, unsigned fo, unsigned tlo, unsigned thi) {
  unsigned old;
  asm volatile("s_mov_b64 exec, 1\n\tds_add_rtn_u32 %0, %1, %2\n\ts_waitcnt lgkmcnt(0)\n\tv_cmp_eq_u32 vcc, 3, %0\n\t"
               "s_mul_i32 exec_lo, vcc_lo, 0xff\n\tglobal_store_dwordx2 %3, %4, %5 sc1\n\ts_mov_b64 exec, -1"
               : "=&v"(old) : "v"((unsigned)(size_t)L + off), "v"(1u), "v"(fo), "v"(v2u{tlo, thi}), "s"(ws) : "vcc", "memory");
}
typedef float f32x16 __attribute__((ext_vector_type(16)));
DI f32x16 mfma32(v4u a, v4u b, f32x16 c) {
  return __builtin_amdgcn_mfma_f32_32x32x16_bf16(__builtin_bit_cast(bf16x8, a), __builtin_bit_cast(bf16x8, b), c, 0, 0, 0);
}
template <int C> DI float dpp_add(float v) {
  return v + __builtin_bit_cast(float, __builtin_amdgcn_update_dpp(0, __builtin_bit_cast(int, v), C, 0xf, 0xf, true));
}
DI float rl(float v, int l) { return __builtin_bit_cast(float, __builtin_amdgcn_readlane(__builtin_bit_cast(int, v), l)); }
DI float half_sum(float v, bool upper) {
  v = dpp_add<0xB1>(v); v = dpp_add<0x4E>(v); v = dpp_add<0x141>(v); v = dpp_add<0x140>(v);
  const float a = rl(v, 0) + rl(v, 16), c = rl(v, 32) + rl(v, 48);
  return upper ? c : a;
}
DI f32x2 unpk(unsigned v) { return f32x2{bf2f(v), bfhi(v)}; }
struct c2 { f32x2 r, i; };
DI c2 cmul(c2 a, c2 b) { return c2{__builtin_elementwise_fma(a.r, b.r, -(a.i * b.i)), __builtin_elementwise_fma(a.r, b.i, a.i * b.r)}; }
DI c2 csq(c2 v) { return c2{__builtin_elementwise_fma(v.r, v.r, -(v.i * v.i)), (v.r + v.r) * v.i}; }
DI c2 cexpk(f32x2 e1, f32x2 th, f32x2 tl, float k, float ks) {
  const f32x2 e = e1 * k, t = th * ks;
  const f32x2 mag = f32x2{__builtin_amdgcn_exp2f(e.x), __builtin_amdgcn_exp2f(e.y)};
  const f32x2 fr = __builtin_elementwise_fma(tl, f32x2{ks, ks}, f32x2{__builtin_amdgcn_fractf(t.x), __builtin_amdgcn_fractf(t.y)});
  return c2{f32x2{__builtin_amdgcn_cosf(fr.x), __builtin_amdgcn_cosf(fr.y)} * mag, f32x2{__builtin_amdgcn_sinf(fr.x), __builtin_amdgcn_sinf(fr.y)} * mag};
}
typedef _Float16 f16x8 __attribute__((ext_vector_type(8)));
typedef _Float16 f16x2 __attribute__((ext_vector_type(2)));
DI f32x4 mfmah(v4u a, v4u b, f32x4 c) {
  return __builtin_amdgcn_mfma_f32_16x16x32_f16(__builtin_bit_cast(f16x8, a), __builtin_bit_cast(f16x8, b), c, 0, 0, 0);
}
DI unsigned pkh1(float a, float b) { f16x2 r; r.x = (_Float16)a; r.y = (_Float16)b; return __builtin_bit_cast(unsigned, r); }
DI v2u pkh(c2 v) { return v2u{pkh1(v.r.x, v.i.x), pkh1(v.r.y, v.i.y)}; }
DI v2u pkb(c2 v) { return v2u{pk2(v.r.x, v.i.x), pk2(v.r.y, v.i.y)}; }
#define HMUL(o, i, w) "v_pk_mul_f16 " o ", " i ", " w " op_sel:[1,1] op_sel_hi:[1,0] neg_lo:[0,1]\n\t"
#define HFMA(o, i, w) "v_pk_fma_f16 " o ", " i ", " w ", " o " op_sel_hi:[0,1,1]\n\t"
constexpr int KROW = 528;
template <int R0, int R1> DI void h2(v2u& o0, v2u& o1, v2u i0, v2u i1, v2u w0, v2u w1) {
  unsigned a, b, c, d;
  asm volatile(HMUL("%0", "%4", "%8") HMUL("%1", "%5", "%9") HMUL("%2", "%6", "%10") HMUL("%3", "%7", "%11")
               HFMA("%0", "%4", "%8") HFMA("%1", "%5", "%9") HFMA("%2", "%6", "%10") HFMA("%3", "%7", "%11")
               "ds_write_addtid_b32 %0 offset:%12\n\tds_write_addtid_b32 %1 offset:%13\n\tds_write_addtid_b32 %2 offset:%14\n\tds_write_addtid_b32 %3 offset:%15"
               : "=&v"(a), "=&v"(b), "=&v"(c), "=&v"(d)
               : "v"(i0.x), "v"(i0.y), "v"(i1.x), "v"(i1.y), "v"(w0.x), "v"(w0.y), "v"(w1.x), "v"(w1.y),
                 "i"(R0 * KROW), "i"(R0 * KROW + 256), "i"(R1 * KROW), "i"(R1 * KROW + 256) : "memory");
  o0 = v2u{a, b}; o1 = v2u{c, d};
}
template <int R> DI void hput(v2u v) {
  asm volatile("ds_write_addtid_b32 %0 offset:%2\n\tds_write_addtid_b32 %1 offset:%3" ::"v"(v.x), "v"(v.y), "i"(R * KROW), "i"(R * KROW + 256) : "memory");
}
template <int C> DI unsigned dpp_maxu(unsigned v) { return max(v, (unsigned)__builtin_amdgcn_update_dpp(0, (int)v, C, 0xf, 0xf, true)); }
DI void rd8(v4u (&f)[8], LAS char* sr) {
#pragma unroll
  for (int k = 0; k < 8; ++k) f[k] = *(LAS v4u*)(sr + 64 * k);
}
template <bool H> DI void kmm(f32x4 (&ka)[2][2][2], int nh, const v4u (&UA)[2][8], const v4u (&vb)[8]) {
#pragma unroll
  for (int k = 0; k < 8; ++k)
#pragma unroll
    for (int m = 0; m < 2; ++m) {
      f32x4& a = ka[(k >> 1) & 1][m][nh];
      const f32x4 c = (k == 0 || k == 2) ? f32x4{0.f, 0.f, 0.f, 0.f} : a;
      a = H ? mfmah(UA[m][k], vb[k], c) : mfma(UA[m][k], vb[k], c);
    }
}
DI void kgen_slow(c2 a, c2 W1, c2 W256, c2 W8, LAS char* sr, f32x4 (&ka)[2][2][2]) {
  v4u UA[2][8], vb[8];
  c2 u[8]; u[0] = a;
#pragma unroll
  for (int i = 1; i < 8; ++i) u[i] = cmul(u[i - 1], W1);
  const c2 W512 = csq(W256), W1024 = csq(W512), r8 = cmul(a, W256), r10 = cmul(u[4], W256), r12 = cmul(a, W512), r14 = cmul(r8, W512), r16 = cmul(a, W1024);
  hput<0>(pkb(u[0])); hput<1>(pkb(u[1])); hput<2>(pkb(u[2])); hput<3>(pkb(u[3])); hput<4>(pkb(u[4])); hput<5>(pkb(u[5])); hput<6>(pkb(u[6])); hput<7>(pkb(u[7]));
  hput<8>(pkb(r8)); hput<9>(pkb(cmul(u[2], W256))); hput<10>(pkb(r10)); hput<11>(pkb(cmul(u[6], W256)));
  hput<12>(pkb(r12)); hput<13>(pkb(cmul(u[4], W512))); hput<14>(pkb(r14)); hput<15>(pkb(cmul(r10, W512)));
  rd8(UA[0], sr);
  hput<0>(pkb(r16)); hput<1>(pkb(cmul(r8, W1024))); hput<2>(pkb(cmul(r12, W1024))); hput<3>(pkb(cmul(r14, W1024))); hput<4>(pkb(cmul(r16, W1024)));
  rd8(UA[1], sr);
  c2 v = c2{f32x2{1.f, 1.f}, f32x2{0.f, 0.f}};
#pragma unroll
  for (int nh = 0; nh < 2; ++nh) {
#define VR(r) hput<r>(pkb(v)); v = cmul(v, W8);
    VR(0) VR(1) VR(2) VR(3) VR(4) VR(5) VR(6) VR(7) VR(8) VR(9) VR(10) VR(11) VR(12) VR(13) VR(14) VR(15)
#undef VR
    rd8(vb, sr);
    kmm<false>(ka, nh, UA, vb);
  }
}
DI void kgen(const unsigned (&pr)[7], bool dead, bool upper, unsigned sw, LAS char* sr, f32x4 (&ka)[2][2][2], float (&scale)[2]) {
  const float dt = rbf(__builtin_amdgcn_exp2f(bf2f(pr[6]) * 1.44269504088896341f));
  const f32x2 zr = unpk(pr[0]) * dt, zi = unpk(pr[1]) * dt, dbr = unpk(pr[2]) * dt, dbi = unpk(pr[3]) * dt;
  const f32x2 cr = unpk(dead ? 0u : pr[4]), ci = unpk(dead ? 0u : pr[5]);
  const c2 a = c2{__builtin_elementwise_fma(cr, dbr, -(ci * dbi)), -__builtin_elementwise_fma(cr, dbi, ci * dbr)};
  const f32x2 e1 = zr * 1.44269504088896341f, th = zi * 0.15915494309189535f;
  const f32x2 tl = __builtin_elementwise_fma(zi, f32x2{6.4206383e-9f, 6.4206383e-9f}, __builtin_elementwise_fma(zi, f32x2{0.15915494309189535f, 0.15915494309189535f}, -th));
  const c2 W1 = cexpk(e1, th, tl, 1.f, -1.f), W256 = cexpk(e1, th, tl, 256.f, -256.f), W8 = cexpk(e1, th, tl, 8.f, 8.f);
  asm volatile("s_mov_b32 m0, %0\n\ts_nop 0" ::"s"(sw) : "memory");
  if (__builtin_expect(__builtin_amdgcn_ballot_w64(!(zr.x <= 0.f && zr.y <= 0.f)) != 0ull, 0)) {
    kgen_slow(a, W1, W256, W8, sr, ka);
    scale[0] = scale[1] = 1.f;
    return;
  }
  unsigned am = max(max(__builtin_bit_cast(unsigned, a.r.x) & 0x7fffffffu, __builtin_bit_cast(unsigned, a.r.y) & 0x7fffffffu),
           max(__builtin_bit_cast(unsigned, a.i.x) & 0x7fffffffu, __builtin_bit_cast(unsigned, a.i.y) & 0x7fffffffu));
  am = dpp_maxu<0xB1>(am); am = dpp_maxu<0x4E>(am); am = dpp_maxu<0x141>(am); am = dpp_maxu<0x140>(am);
  const unsigned ex0 = min(max(max((unsigned)__builtin_amdgcn_readlane(am, 0), (unsigned)__builtin_amdgcn_readlane(am, 16)) >> 23, 2u), 252u);
  const unsigned ex1 = min(max(max((unsigned)__builtin_amdgcn_readlane(am, 32), (unsigned)__builtin_amdgcn_readlane(am, 48)) >> 23, 2u), 252u);
  scale[0] = __builtin_bit_cast(float, ex0 << 23); scale[1] = __builtin_bit_cast(float, ex1 << 23);
  const float down = __builtin_bit_cast(float, ((254u - (upper ? ex1 : ex0)) << 23));
  const c2 W2 = csq(W1), W512 = csq(W256), W16 = csq(W8), W4 = csq(W2), W1024 = csq(W512), W32 = csq(W16), W64 = csq(W32), W128 = csq(W64);
  const v2u u0 = pkh(c2{a.r * down, a.i * down}), h1 = pkh(W1), h2_ = pkh(W2), h4 = pkh(W4), h256 = pkh(W256), h512 = pkh(W512), h1024 = pkh(W1024);
  v2u u2, u4, u6, r8, r10, r12, r14, r16, t0, t1;
  v4u UA[2][8], vb[8];
  hput<0>(u0);
  h2<2, 4>(u2, u4, u0, u0, h2_, h4); h2<8, 12>(r8, r12, u0, u0, h256, h512); h2<1, 3>(t0, t1, u0, u2, h1, h1); h2<5, 6>(t0, u6, u4, u4, h1, h2_);
  h2<10, 14>(r10, r14, r8, r8, h4, h512); h2<7, 9>(t0, t1, u6, r8, h1, h2_); h2<11, 13>(t0, t1, r10, r12, h2_, h4);
  h2<15, 15>(r16, t1, u0, r14, h1024, h4);
  rd8(UA[0], sr);
  hput<0>(r16);
  h2<1, 2>(t0, t1, r8, r12, h1024, h1024); h2<3, 4>(t0, t1, r14, r16, h1024, h1024);
  rd8(UA[1], sr);
  v2u v[16];
  v[1] = pkh(W8); v[2] = pkh(W16); v[4] = pkh(W32); v[8] = pkh(W64);
  const v2u h128 = pkh(W128);
  hput<0>(v2u{0x3c00u, 0x3c00u}); hput<1>(v[1]); hput<2>(v[2]); hput<4>(v[4]); hput<8>(v[8]);
  h2<3, 5>(v[3], v[5], v[2], v[4], v[1], v[1]); h2<6, 9>(v[6], v[9], v[4], v[8], v[2], v[1]); h2<7, 10>(v[7], v[10], v[4], v[8], v[3], v[2]);
  h2<11, 12>(v[11], v[12], v[8], v[8], v[3], v[4]); h2<13, 14>(v[13], v[14], v[8], v[8], v[5], v[6]); h2<15, 15>(t0, v[15], v[1], v[8], h128, v[7]);
  rd8(vb, sr);
  hput<0>(h128); hput<1>(t0);
  h2<2, 3>(t0, t1, v[2], v[3], h128, h128); h2<4, 5>(t0, t1, v[4], v[5], h128, h128);
  kmm<true>(ka, 0, UA, vb);
  h2<6, 7>(t0, t1, v[6], v[7], h128, h128); h2<8, 9>(t0, t1, v[8], v[9], h128, h128); h2<10, 11>(t0, t1, v[10], v[11], h128, h128);
  h2<12, 13>(t0, t1, v[12], v[13], h128, h128); h2<14, 15>(t0, t1, v[14], v[15], h128, h128);
  rd8(vb, sr);
  SCHED();
  kmm<true>(ka, 1, UA, vb);
}
DI void kgen_store(LAS float* pt, const f32x4 (&kacc)[2][2], int lane) {
  const int l15 = lane & 15, g = lane >> 4;
  const int sh = g < 2 ? 3 : 4 - g;
  const int a0 = (l15 << sh) + (g < 2 ? 4 * g : 128 * g);
  const int a1 = g < 3 ? a0 + 2 : a0 + 64;
#pragma unroll
  for (int nh = 0; nh < 2; ++nh) {
    *(LAS f32x2*)(pt + a0 + ((16 * nh) << sh)) = kacc[0][nh].xy;
    *(LAS f32x2*)(pt + a1 + ((16 * nh) << sh)) = kacc[0][nh].zw;
  }
  if (g == 0) {
#pragma unroll
    for (int nh = 0; nh < 2; ++nh)
#pragma unroll
      for (int r = 0; r < 4; ++r) pt[512 + 32 * r + 16 * nh + l15] = kacc[1][nh][r];
  }
  if (lane == 16) { *(LAS f32x4*)(pt + 640) = f32x4{kacc[1][0][0], 0.f, 0.f, 0.f}; }
}

template <int PAR> DI void conv_unit_shared(LAS char* ka, LAS char* ub, f32x4 (&acc)[4]) {
  constexpr int T[4] = {PAR ? 1 : 0, PAR ? 2 : 3, PAR ? 5 : 4, PAR ? 6 : 7};
  v4u A[8], B[8];
#pragma unroll
  for (int i = 0; i <= T[3]; ++i) { A[i] = *(LAS v4u*)(ka + 512 * (7 - i)); B[i] = *(LAS v4u*)(ub + 512 * i); }
  SCHED();
#pragma unroll
  for (int q = 0; q < 4; ++q)
#pragma unroll
    for (int ip = 0; ip <= T[q]; ++ip) acc[q] = mfma(A[T[q] - ip], B[ip], acc[q]);
  SCHED();
}
DI float fadd(float a, float b) { float r; asm("v_add_f32 %0, %1, %2" : "=v"(r) : "v"(a), "v"(b)); return r; }
DI float ffma(float a, float b, float c) { float r; asm("v_fma_f32 %0, %1, %2, %3" : "=v"(r) : "v"(a), "v"(b), "v"(c)); return r; }
DI f32x4 add4(f32x4 a, f32x4 b) { return f32x4{fadd(a.x, b.x), fadd(a.y, b.y), fadd(a.z, b.z), fadd(a.w, b.w)}; }
DI f32x4 fma4(f32x4 a, float b, f32x4 c) { return f32x4{ffma(a.x, b, c.x), ffma(a.y, b, c.y), ffma(a.z, b, c.z), ffma(a.w, b, c.w)}; }
DI int rot12(int v) { return v >= 12 ? v - 12 : v; }
DI void w_loads(v4u (&WF)[5], const char* p, unsigned vo) {
  ld128c<0>(WF[0], vo, p); ld128c<1024>(WF[1], vo, p); ld128c<2048>(WF[2], vo, p); ld128c<3072>(WF[3], vo, p); ld128c<0>(WF[4], vo, p + 4096);
}
DI void a_loads(v4u (&AF)[4], const char* p, const unsigned (&vm)[4]) {
#pragma unroll
  for (int m = 0; m < 4; ++m) ld128c<0>(AF[m], vm[m], p);
}
template <int LIVE, class F> DI void g2_step(f32x4 (&acc)[4][5], v4u (&WF)[5], v4u (&AF)[4], F ld) {
#pragma unroll
  for (int m = 0; m < 4; ++m) PIN(AF[m]);
#pragma unroll
  for (int n = 0; n < 5; ++n) PIN(WF[n]);
#pragma unroll
  for (int e = 0; e < 20; ++e) {
    const int m = e / 5, n = e % 5;
    acc[m][n] = mfma(WF[n], AF[m], m >= LIVE ? f32x4{0.f, 0.f, 0.f, 0.f} : acc[m][n]);
    if (e & 1) { SCHED(); ld(e >> 1); SCHED(); }
  }
}
DI void make_kix(unsigned (&kix)[9], int ts) {
#pragma unroll
  for (int i = 0; i < 9; ++i) {
    const int lg = 2047 - 8 * ts - i;
    const int bits = (int)(pk2(__builtin_fmaxf((float)lg, 128.f), 0.f));
    const int ci = lg < bits - 0x4280 ? lg : bits - 0x4280;
    kix[i] = KTAB + 4 * ci;
    PIN(kix[i]);
  }
}
DI void put_k(LAS char* L, const unsigned (&kix)[9], int ts, int c, unsigned reg) {
  float kv[9];
#pragma unroll
  for (int i = 0; i < 9; ++i) kv[i] = *(LAS float*)(L + kix[i] + c * KP_STRIDE);
  LAS char* q = L + reg + CV_KOFF + ts * 16;
  *(LAS v4u*)(q + CV_KSTR) = v4u{pk2(kv[0], kv[1]), pk2(kv[2], kv[3]), pk2(kv[4], kv[5]), pk2(kv[6], kv[7])};
  *(LAS v4u*)q = v4u{pk2(kv[1], kv[2]), pk2(kv[3], kv[4]), pk2(kv[5], kv[6]), pk2(kv[7], kv[8])};
}
DI void put_u(LAS char* q, v4u u, bool first) {
  *(LAS v4u*)(q + 3 * CV_USTR + 384) = u;
  *(LAS v2u*)(q + 1 * CV_USTR + 392) = v2u{u.x, u.y}; *(LAS v2u*)(q + 1 * CV_USTR + 400) = v2u{u.z, u.w};
  *(LAS unsigned*)(q + 2 * CV_USTR + 388) = u.x; *(LAS v2u*)(q + 2 * CV_USTR + 392) = v2u{u.y, u.z}; *(LAS unsigned*)(q + 2 * CV_USTR + 400) = u.w;
  *(LAS unsigned*)(q + 396) = u.x; *(LAS v2u*)(q + 400) = v2u{u.y, u.z}; *(LAS unsigned*)(q + 408) = u.w;
  if (first) {
    *(LAS v2u*)(q + 384) = v2u{0u, 0u}; *(LAS unsigned*)(q + 392) = 0u;
    *(LAS v2u*)(q + CV_USTR + 384) = v2u{0u, 0u}; *(LAS unsigned*)(q + 2 * CV_USTR + 384) = 0u;
  }
}

__global__ __launch_bounds__(512, 2) void fused_s4_mlp(const char* __restrict__ x, const char* __restrict__ pAre, const char* __restrict__ pAim,
    const char* __restrict__ pBre, const char* __restrict__ pBim, const char* __restrict__ pCre, const char* __restrict__ pCim,
    const char* __restrict__ plogdt, const char* __restrict__ pDskip, const char* __restrict__ w1, const char* __restrict__ b1,
    const char* __restrict__ w2, char* __restrict__ out, char* __restrict__ ws) {
  __shared__ __attribute__((aligned(16))) char smem[163840];
  LAS char* L = (LAS char*)smem;
  const unsigned lds0 = (unsigned)(size_t)L;
  const char* pc0; unsigned codelen;
  asm volatile("s_getpc_b64 %0\n98:\n\ts_mov_b32 %1, 97f-98b\n\ts_max_u32 %1, %1, 99f-98b" : "=s"(pc0), "=&s"(codelen) :: "scc");
  const int b = blockIdx.x, lane = threadIdx.x & 63;
  const int w = RFL(threadIdx.x >> 6), wi = w & 3;
  {
    const unsigned co = (unsigned)(4 * (16 * w + (lane & 15)) + ((b >> 3) & 3)) * 128;
    asm volatile("s_mov_b32 m0, %2\n\ts_nop 0\n\tglobal_load_lds_dword %0, %1" ::"v"((co < codelen ? co : codelen) & ~3u), "s"(pc0), "s"(lds0 + YST + w * 256) : "memory");
  }
  const int xcd = b & 7, j = b >> 3, rg = xcd >> 1, chf = xcd & 1;
  const int ct1 = 8 * chf + (j & 7), rb1 = 4 * rg + (j >> 3);
  const int wid = rb1 * 16 + ct1;
  const int a2 = 4 * chf + (j >> 3), cb2 = j & 7;
  const int l15 = lane & 15, g = lane >> 4, qq = (lane >> 2) & 3, p4 = lane & 3, l31 = lane & 31, h = lane >> 5;
  const int par = j & 1, cbase = 80 * xcd + 5 * (j >> 1);
  const int chA = cbase + 3 * par, chS = cbase + 2;
  const int ts = 64 * wi + lane;
  ST(0);
  const int row2 = 512 * rg + 64 * a2 + 16 * wi;
  const char* w2b = ws + WS_W2T + (size_t)(cb2 * 48 + 12 * wi) * 5120;
  const char* actb = ws + WS_ACT + (size_t)((32 * rg + 4 * a2) * 48 + 12 * wi) * 1024;
  const int r2 = ((a2 ^ (w >> 2)) & 1) * 6;
#define WL(s) w_loads(WF[s], w2b + rot12(r2 + (s)) * 5120, vo)
#define AL(s) a_loads(AF[s], actb + rot12(r2 + (s)) * 1024, vm)
#define AL1(s, m) ld128c<0>(AF[s][m], vm[m], actb + rot12(r2 + (s)) * 1024)
#define WL1(s, n) ld128c<0>(WF[s][n], vo, w2b + rot12(r2 + (s)) * 5120 + (n) * 1024)
#define NOLD [&](int) {}
#define LATE_LANE() int ln_ = lane; PIN(ln_); const int c15 = ln_ & 15, cg = ln_ >> 4
#define KAO (CV_KOFF + (c15 & 1) * CV_KSTR + 2 * (2072 - 8 * (c15 >> 1) + 8 * cg))
#define UBO ((c15 & 3) * CV_USTR + 2 * (64 * (c15 >> 2) + 8 * cg + 224))
#define YOFF ((unsigned)((c15 >> 2) * 128 + cg * 32 + (c15 & 3) * 8))
#define OROW ((unsigned)(2 * (c15 >> 2) + (c15 & 1) + 8 * ((c15 >> 1) & 1)))
#define ZOFF (lane < 24 ? wi * CV_USTR + lane * 16 : CV_KOFF + wi * CV_KSTR + 4096 + (lane - 24) * 16)
#define ZCOND (lane < 24 || (lane < 31 && wi < 2))

  unsigned voA[4], voB[3];
#pragma unroll
  for (int i = 0; i < 4; ++i) voA[i] = (8 * i + (lane >> 3)) * 1280 + (((lane & 7) ^ (4 * (i & 1) + (lane >> 4))) * 16) - 1024 * i;
#pragma unroll
  for (int i = 0; i < 3; ++i) { const int c = 64 * i + lane; voB[i] = (c / 12) * 3072 + (c % 12) * 16 - 1024 * i; }
  const char* g1a = x + (size_t)(rb1 * 128 + 32 * wi) * 1280;
  const char* g1b = w1 + ct1 * 192 + wi * 49152;
  const unsigned la = lds0 + G1_RING + wi * 4096, lb = lds0 + G1_RING + G1_BOFF + wi * 3072;
  const int r1 = ((j & 7) + (5 * (j >> 3)) / 2 + 5 * chf + rg) % 10;
  auto slab = [&](int s) { const int v = s + r1; return v >= 10 ? v - 10 : v; };
  auto issue = [&](int s) {
    const char* pa = g1a + slab(s) * 128; const char* pb = g1b + (size_t)slab(s) * 64 * 3072;
    dma16(voA[0], pa, la + s * G1_STAGE); dma16o<1024>(voA[1], pa); dma16o<2048>(voA[2], pa); dma16o<3072>(voA[3], pa);
    dma16(voB[0], pb, lb + s * G1_STAGE); dma16o<1024>(voB[1], pb); dma16o<2048>(voB[2], pb);
  };
  unsigned tlo = 0, thi = 0;
  unsigned kix[9];
  if (w < 4) {
    unsigned pr[7];
    const bool odd = wi & 1;
    {
      const int kch = chA + (wi >> 1);
      const bool shl = odd && h;
      const unsigned vo = (unsigned)(shl ? chS * 192 + par * 96 + (wi >> 1) * 48 + 2 * (l31 < 24 ? l31 : 23) : kch * 192 + (odd ? 128 : 0) + 2 * lane) * 2;
      ld16(pr[6], (unsigned)(shl ? chS : kch) * 2, plogdt);
      ld32<0>(pr[0], vo, pAre); ld32<0>(pr[1], vo, pAim); ld32<0>(pr[2], vo, pBre); ld32<0>(pr[3], vo, pBim); ld32<0>(pr[4], vo, pCre); ld32<0>(pr[5], vo, pCim);
    }
    bar();
#pragma unroll
    for (int i = 0; i < 9; ++i) kix[i] = 0;
    waitv<0>();
#pragma unroll
    for (int i = 0; i < 7; ++i) PIN(pr[i]);
    ST(1);
    f32x4 ka[2][2][2]; float scale[2];
    kgen(pr, odd && lane >= 56, h, lds0 + KG_SCR + wi * 2 * KG_WSTR, L + KG_SCR + wi * 2 * KG_WSTR + l15 * KROW + g * 16, ka, scale);
#pragma unroll
    for (int i = 0; i < 4; ++i) {
      ka[1][i >> 1][i & 1] *= scale[1];
      ka[0][i >> 1][i & 1] = ka[0][i >> 1][i & 1] * scale[0] + (odd ? f32x4{0.f, 0.f, 0.f, 0.f} : ka[1][i >> 1][i & 1]);
    }
    kgen_store((LAS float*)(L + KG_SCR + wi * 2 * KG_WSTR), ka[0], lane);
    if (odd) kgen_store((LAS float*)(L + KG_SCR + wi * 2 * KG_WSTR + KG_WSTR), ka[1], lane);
    lds_arrive(L, W_KG, lane);
  } else {
    if (w == 4) *(LAS unsigned*)(L + lane * 4) = 0u;
    bar();
    v2u E; v4u xa, xb; v2u xc;
    const int pu = b % 240, pks = pu / 5, pcg = pu % 5;
    const char* xrow = x + (size_t)(8 * wid + 2 * wi) * 1280;
    ld128(xa, h * 1280 + l31 * 16, xrow); ld128(xb, h * 1280 + 512 + l31 * 16, xrow); ld64(xc, h * 1280 + 1024 + l31 * 8, xrow);
    ld64c(E, 0u, ws);
#pragma unroll
    for (int i = 0; i < 2; ++i)
      dma16((unsigned)((lane >> 1) * 1280 + (lane & 1) * 16), w2 + (size_t)pks * 32 * 1280 + (128 * pcg + 32 * wi + 16 * i) * 2, lds0 + W2P + wi * 2048 + i * 1024);
    issue(0);
    make_kix(kix, ts);
    waitv<9>();
    PIN(E); PIN(xa); PIN(xb); PIN(xc);
    ST(6);
  const u64 tag64 = (((u64)RFL(E.y) << 32) | (unsigned)RFL(E.x)) + 1ull;
  tlo = (unsigned)tag64; thi = (unsigned)(tag64 >> 32);
  if (wi == 0) { lds_set(L, W_TAG, tlo); lds_set(L, W_TAG + 4, thi); lds_set(L, W_TAG + 8, 1); }
  {
    f32x2 v[10] = {unpk(xa.x), unpk(xa.y), unpk(xa.z), unpk(xa.w), unpk(xb.x), unpk(xb.y), unpk(xb.z), unpk(xb.w), unpk(xc.x), unpk(xc.y)};
    f32x2 s2 = ((v[0] + v[1]) + (v[2] + v[3])) + ((v[4] + v[5]) + (v[6] + v[7])) + (v[8] + v[9]);
    const float mean = half_sum(s2.x + s2.y, h) * (1.0f / 640.0f);
    f32x2 q2 = f32x2{0.f, 0.f};
#pragma unroll
    for (int i = 0; i < 10; ++i) { const f32x2 d = v[i] - mean; q2 = __builtin_elementwise_fma(d, d, q2); }
    const float var = rbf(half_sum(q2.x + q2.y, h) * (1.0f / 640.0f));
    const float m = rbf(mean), sd = rbf(__builtin_amdgcn_sqrtf(rbf(var + 1e-5f))), rs = __builtin_amdgcn_rcpf(sd);
    unsigned o[10];
#pragma unroll
    for (int i = 0; i < 10; ++i) { const f32x2 t = (v[i] - m) * rs; o[i] = pk2(t.x, t.y); }
    LAS char* rp = L + LN_ROWS + (2 * wi + h) * LN_PITCH;
    *(LAS v4u*)(rp + l31 * 16) = v4u{o[0], o[1], o[2], o[3]};
    *(LAS v4u*)(rp + 512 + l31 * 16) = v4u{o[4], o[5], o[6], o[7]};
    *(LAS v2u*)(rp + 1024 + l31 * 8) = v2u{o[8], o[9]};
    lds_arrive(L, W_SB, lane);
    issue(1); issue(2);
    waitv<14>();
    lds_arrive(L, W_S0, lane);
    if (l31 == 0) st64c(WS_STATS + (unsigned)(8 * wid + 2 * wi + h) * 8, ws,
                        v2u{(__builtin_bit_cast(unsigned, m) & 0xffff0000u) | (__builtin_bit_cast(unsigned, sd) >> 16), tlo});
  }
#pragma unroll
  for (int i = 0; i < 2; ++i) {
    LAS char* sp = L + W2P + wi * 2048 + i * 1024 + (16 * (g >> 1) + 4 * (g & 1) + qq) * 32 + 8 * p4;
    const v2u lo = trread(sp), hi = trread(sp + 256);
    const int ntg = 8 * pcg + 2 * wi + i;
    st128c((unsigned)lane * 16, ws + WS_W2T + (size_t)(((ntg / 5) * 48 + pks) * 5 + ntg % 5) * 1024, v4u{lo.x, lo.y, hi.x, hi.y});
  }
  lds_wait_ge(L, W_SB, 4);
  {
    LAS char* sp = L + LN_ROWS + qq * LN_PITCH + (160 * wi + 16 * g) * 2 + 8 * p4;
#pragma unroll
    for (int i = 0; i < 3; ++i) {
      const v2u lo = trread(sp + 128 * i), hi = trread(sp + 128 * i + 4 * LN_PITCH);
      const unsigned ch = 64 * i + lane;
      if (i < 2 || lane < 32) st128c((ch / 5) * 20480 + (ch % 5) * 16, ws + WS_XNB + (size_t)wi * 32 * 20480 + wid * 80, v4u{lo.x, lo.y, hi.x, hi.y});
    }
  }
  }
  ST(2);
  int role = w; asm volatile("" : "+s"(role));
  if (role < 4) {
    int lane = __builtin_amdgcn_mbcnt_hi(~0u, __builtin_amdgcn_mbcnt_lo(~0u, 0u)); PIN(lane);
    const int l15 = lane & 15, g = lane >> 4, qq = (lane >> 2) & 3, p4 = lane & 3, l31 = lane & 31, h = lane >> 5;
    const unsigned vo = lane * 16;
    f32x16 acc1[3]; f32x4 csa[3];
#pragma unroll
    for (int n = 0; n < 3; ++n) {
      csa[n] = f32x4{0.f, 0.f, 0.f, 0.f};
#pragma unroll
      for (int i = 0; i < 16; ++i) acc1[n][i] = 0.f;
    }
    const int xrow = (l31 & 17) + 2 * (l15 >> 2) + 8 * ((l15 >> 1) & 1);
    unsigned xo[4], wo[4];
#pragma unroll
    for (int k = 0; k < 4; ++k) {
      const int kr = (k + wi) & 3;
      xo[k] = G1_RING + (32 * wi + xrow) * 128 + (((2 * kr + h) ^ ((xrow >> 1) & 7)) * 16);
      wo[k] = G1_RING + G1_BOFF + (16 * kr + 8 * h + qq) * 192 + 32 * (g & 1) + 8 * p4;
      PIN(xo[k]); PIN(wo[k]);
    }
    const unsigned sel1 = ((l15 ^ g) & 1) ? 0u : 0x3f803f80u;
    const v4u sel = v4u{sel1, sel1, sel1, sel1};
    v4u X[4], W[4][3];
    auto rdw = [&](int s, int k, int n) {
      LAS char* p = L + wo[k] + (s % 3) * G1_STAGE + 64 * n; const v2u lo = trread(p), hi = trread(p + 768); W[k][n] = v4u{lo.x, lo.y, hi.x, hi.y};
    };
    auto rdx = [&](int s, int k) { X[k] = *(LAS v4u*)(L + xo[k] + (s % 3) * G1_STAGE); };
#define MM(k, n) acc1[n] = mfma32(W[k][n], X[k], acc1[n])
#define CS(n) csa[n] = mfma(W[0][n], sel, csa[n])
    v2u gran; unsigned bb[3];
    v4u WF[6][5], AF[6][4];
    const unsigned go = WS_STATS + (unsigned)(rb1 * 128 + 32 * wi + xrow) * 8;
    auto wl1 = [&](int s, int e0) { const int e = 7 * (s - 7) + e0; if (s >= 7 && e0 < 7 && e < 20) ld128c<0>(WF[e / 5][e % 5], vo, w2b + rot12(r2 + e / 5) * 5120 + (e % 5) * 1024); };
    lds_wait_ge(L, W_S0, 4);
    ST(3);
    rdx(0, 0); rdw(0, 0, 0); rdw(0, 0, 1); rdw(0, 0, 2); rdx(0, 1); rdw(0, 1, 0); rdw(0, 1, 1); rdw(0, 1, 2);
#pragma unroll
    for (int s = 0; s < 10; ++s) {
      const bool refill = s < 7;
      const char* pa = g1a + slab(s + 3) * 128; const char* pb = g1b + (size_t)slab(s + 3) * 64 * 3072;
      SCHED();
      MM(0, 0); rdx(s, 2); rdw(s, 2, 0); SCHED();
      MM(0, 1); rdw(s, 2, 1); rdw(s, 2, 2); SCHED();
      MM(0, 2); rdx(s, 3); rdw(s, 3, 0); SCHED();
      CS(0); rdw(s, 3, 1); SCHED();
      CS(1); rdw(s, 3, 2); SCHED();
      CS(2);
      if (s == 0) lds_wait_ge(L, W_ST, 4); else if (s == 1) {} else if (s < 7) waitv<7>(); else if (s == 7) waitv<11>(); else if (s == 8) waitv<11>();
      if (s == 9 && l15 < 2) {
#pragma unroll
        for (int n = 0; n < 3; ++n) *(LAS f32x4*)(L + CSP + wi * 384 + (32 * n + 16 * l15 + 4 * g) * 4) = csa[n];
      }
      lds_set(L, W_RDY + wi * 4, s + 2);
      SCHED();
      MM(1, 0);
      if (refill) { asm volatile("s_waitcnt lgkmcnt(7)" ::: "memory"); dma16(voA[0], pa, la + (s % 3) * G1_STAGE); }
      wl1(s, 0);

      SCHED();
      MM(1, 1); if (refill) dma16o<1024>(voA[1], pa); wl1(s, 1);
      unsigned fl = 0;
      if (s < 9) fl = lw(L, W_RDY + p4 * 4);
      SCHED();
      MM(1, 2); if (refill) dma16o<2048>(voA[2], pa); wl1(s, 2); SCHED();
      MM(2, 0); if (refill) dma16o<3072>(voA[3], pa); wl1(s, 3);
      SCHED();
      if (s == 9) {
        MM(2, 1); wl1(s, 4); SCHED();
        lds_wait_min<4>(L, W_RDY, 11, lane);
        float cs[3];
#pragma unroll
        for (int n = 0; n < 3; ++n) { LAS float* p = (LAS float*)(L + CSP) + 32 * n + l31; cs[n] = fadd(fadd(p[0], p[96]), fadd(p[192], p[288])); }
        MM(2, 2); wl1(s, 5); SCHED();
        waitv<20>(); PIN(gran); PIN(bb[0]); PIN(bb[1]); PIN(bb[2]);
        MM(3, 0); MM(3, 1); MM(3, 2);
        const unsigned keep = h ? 0u : ~0u, nm = (gran.x >> 16) ^ 0x8000u;
        const v4u xe = v4u{(nm | (nm << 16)) & keep, (nm | (gran.x << 16)) & keep, 0u, 0u};
#pragma unroll
        for (int n = 0; n < 3; ++n) {
          const float c0 = bfhi(__builtin_bit_cast(unsigned, cs[n])), r0 = cs[n] - c0, c1 = bfhi(__builtin_bit_cast(unsigned, r0)), r1 = r0 - c1;
          const v4u we = v4u{(__builtin_bit_cast(unsigned, c0) >> 16) | __builtin_bit_cast(unsigned, c1), (__builtin_bit_cast(unsigned, r1) >> 16) | (bb[n] << 16), 0u, 0u};
          acc1[n] = mfma32(we, xe, acc1[n]);
        }
        SCHED();
        break;
      }
      if (__builtin_expect(__builtin_amdgcn_ballot_w64(fl < (unsigned)(s + 2)) != 0ull, 0)) lds_wait_min<4>(L, W_RDY, s + 2, lane);
      FENCE();
      SCHED();
      rdx(s + 1, 0); rdw(s + 1, 0, 0); SCHED();
      MM(2, 1); rdw(s + 1, 0, 1); rdw(s + 1, 0, 2); if (refill) dma16(voB[0], pb, lb + (s % 3) * G1_STAGE); wl1(s, 4); SCHED();
      MM(2, 2); rdx(s + 1, 1); rdw(s + 1, 1, 0); if (refill) dma16o<1024>(voB[1], pb); wl1(s, 5); SCHED();
      MM(3, 0); rdw(s + 1, 1, 1); if (refill) dma16o<2048>(voB[2], pb); wl1(s, 6); SCHED();
      MM(3, 1); rdw(s + 1, 1, 2); wl1(s, 7); SCHED();
      MM(3, 2); wl1(s, 8);
      if (s == 6) { lds_wait_ge(L, W_F1ALL, 4); ld64c(gran, go, ws); ld16(bb[0], l31 * 2, b1 + ct1 * 192); ld16(bb[1], l31 * 2, b1 + ct1 * 192 + 64); ld16(bb[2], l31 * 2, b1 + ct1 * 192 + 128); }
      SCHED();
      if (s == 0) ST(4); if (s == 3) ST(5); if (s == 6) ST(7);
    }
#undef MM
#undef CS
    ST(11);
    const unsigned tlo = RFL(lw(L, W_TAG)), thi = RFL(lw(L, W_TAG + 4));
    {
      const float rs = __builtin_amdgcn_rcpf(bf2f(gran.x)) * 1.44269504088896341f;
      char* ab = ws + WS_ACT + (size_t)((rb1 * 8 + 2 * wi) * 48 + 3 * ct1) * 1024;
      const unsigned ao = (unsigned)((l31 >> 4) * 49152 + h * 256 + l15 * 16);
#pragma unroll
      for (int q = 0; q < 6; ++q) {
        unsigned pk[4];
#pragma unroll
        for (int e = 0; e < 4; ++e) {
          const f32x2 h2 = f32x2{acc1[q >> 1][8 * (q & 1) + 2 * e], acc1[q >> 1][8 * (q & 1) + 2 * e + 1]} * rs;
          const f32x2 s1 = f32x2{__builtin_amdgcn_exp2f(h2.x), __builtin_amdgcn_exp2f(h2.y)} + 1.0f;
          const f32x2 d = __builtin_elementwise_fma(s1, s1, f32x2{1.f, 1.f});
          const f32x2 rr = f32x2{__builtin_amdgcn_rcpf(d.x), __builtin_amdgcn_rcpf(d.y)};
          const f32x2 o = h2 * __builtin_elementwise_fma(rr, f32x2{-1.38629436111989062f, -1.38629436111989062f},
                                                         f32x2{0.69314718055994531f, 0.69314718055994531f});
          pk[e] = pk2(o.x, o.y);
        }
        st128c(ao + q * 512, ab, v4u{pk[0], pk[1], pk[2], pk[3]});
      }
    }
    lds_set(L, W_RDY + wi * 4, 12);
    ST(13);
    __builtin_amdgcn_s_setprio(2);
    LATE_LANE();
    v2u xq[5], dq[5], gr2, f3;
    const unsigned f2o = WS_F2 + (unsigned)((4 * rg + (a2 >> 1)) * 16 + 4 * wi + (ln_ & 3)) * 128;
    unsigned vm[4];
#pragma unroll
    for (int m = 0; m < 4; ++m) vm[m] = vo + ((m + wi) & 3) * 49152;
    v2u f2; v4u ysh;
    {
      f32x4 sacc[4];
#pragma unroll
      for (int i = 0; i < 4; ++i) sacc[i] = f32x4{0.f, 0.f, 0.f, 0.f};
      unsigned kb = CVS + KAO - wi * 128 - 3584 - 64, ubb = CVS + UBO - wi * 128 - 64; PIN(kb); PIN(ubb);
      LAS char* ka = L + kb; LAS char* ub = L + ubb;
      const bool late = RFL(lw(L, W_SB)) < 16;
      if (late) { waitv<0>(); arrive_flag<4>(L, W_F2C, lane, ws, WS_F2 + (unsigned)wid * 128, tlo, thi); }
      lds_wait_ge(L, W_SB, 16);
      if (par) conv_unit_shared<1>(ka + 64, ub + 64, sacc); else conv_unit_shared<0>(ka + 64, ub + 64, sacc);
      if (!late) { waitv<0>(); arrive_flag<4>(L, W_F2C, lane, ws, WS_F2 + (unsigned)wid * 128, tlo, thi); }
      ST(14);
      WL(4); WL(5);
      if (par) conv_unit_shared<1>(ka, ub, sacc); else conv_unit_shared<0>(ka, ub, sacc);
#pragma unroll
      for (int q = 0; q < 4; ++q) *(LAS f32x4*)(L + MXCH + (q * 4 + wi) * 1024 + vo) = sacc[q];
      lds_arrive(L, W_MB, lane);
    {
      const char* xb2 = x + (size_t)row2 * 1280 + cb2 * 160; const unsigned xo2 = OROW * 1280 + cg * 8;
      ld64o<0>(xq[0], xo2, xb2); ld64o<32>(xq[1], xo2, xb2); ld64o<64>(xq[2], xo2, xb2); ld64o<96>(xq[3], xo2, xb2); ld64o<128>(xq[4], xo2, xb2);
      const char* db = pDskip + cb2 * 160;
      ld64o<0>(dq[0], cg * 8, db); ld64o<32>(dq[1], cg * 8, db); ld64o<64>(dq[2], cg * 8, db); ld64o<96>(dq[3], cg * 8, db); ld64o<128>(dq[4], cg * 8, db);
      ld64c(gr2, WS_STATS + (row2 + OROW) * 8, ws);
    }
      lds_wait_ge(L, W_MB, 4);
      LAS char* p = L + MXCH + wi * 4096 + vo;
      const f32x4 s = add4(add4(*(LAS f32x4*)p, *(LAS f32x4*)(p + 1024)), add4(*(LAS f32x4*)(p + 2048), *(LAS f32x4*)(p + 3072)));
      ld64c(f2, f2o, ws);
      const unsigned y0 = pk2(s.x, s.y), y1 = pk2(s.z, s.w);
      *(LAS v2u*)(L + MXCH + wi * 4096 + YOFF) = v2u{y0, y1};
      FENCE();
      ysh = *(LAS v4u*)p;
    }
    ST(15);
    waitv<0>(); PIN(f2);
    f2 = spin64<false>(f2, f2o, ws, tlo, thi);
    lds_set(L, W_F2OK + wi * 4, 1);
    ST(16);
    AL(0); AL(1);
    if (ln_ < 32) st128c(vo, ws + WS_Y + (size_t)chS * 4096 + (2 * wi + ((wi & 1) ^ par)) * 512, ysh);
    f32x4 acc2[4][5];
    PIN(gr2);
    const float rs2 = __builtin_amdgcn_rcpf(bf2f(gr2.x)), m2 = -bfhi(gr2.x) * rs2;
#pragma unroll
    for (int n = 0; n < 5; ++n) {
      PIN(xq[n]); PIN(dq[n]);
      const f32x2 x0 = unpk(xq[n].x), x1 = unpk(xq[n].y), d0 = unpk(dq[n].x), d1 = unpk(dq[n].y);
      acc2[0][n] = f32x4{ffma(d0.x, ffma(x0.x, rs2, m2), x0.x), ffma(d0.y, ffma(x0.y, rs2, m2), x0.y),
                         ffma(d1.x, ffma(x1.x, rs2, m2), x1.x), ffma(d1.y, ffma(x1.y, rs2, m2), x1.y)};
      PIN(acc2[0][n]);
    }
    __builtin_amdgcn_s_setprio(0);
    ST(17);
    waitv<4>(); g2_step<1>(acc2, WF[0], AF[0], [&](int i) { if (i >= 3 && (i & 1)) AL1(2, (i - 3) >> 1); });
    waitv<4>();
    arrive_flag<8>(L, W_F3C, lane, ws, WS_F3 + (unsigned)(xcd * 32 + j) * 128, tlo, thi);
    g2_step<4>(acc2, WF[1], AF[1], [&](int i) { if (i >= 2) AL1(3 + ((i - 2) >> 2), (i - 2) & 3); });
    waitv<8>(); g2_step<4>(acc2, WF[2], AF[2], [&](int i) { if (i < 4) AL1(5, i); });
    ST(20);
    waitv<8>(); g2_step<4>(acc2, WF[3], AF[3], NOLD);
    waitv<4>(); g2_step<4>(acc2, WF[4], AF[4], NOLD);
    lds_wait_ge(L, W_F3C, 8);
    const unsigned f3o = WS_F3 + (unsigned)(cb2 * 32 + 8 * wi + (ln_ & 7)) * 128;
    ld64c(f3, f3o, ws);
    waitv<1>(); g2_step<4>(acc2, WF[5], AF[5], NOLD);
    ST(22);
    waitv<0>(); PIN(f3);
    f3 = spin64<false>(f3, f3o, ws, tlo, thi);
    lds_arrive(L, W_F3ALL, lane);
#pragma unroll
    for (int n = 0; n < 5; ++n) *(LAS f32x4*)(L + FM + wi * 15360 + n * 1024 + vo) = acc2[1][n];
    lds_wait_ge(L, W_F3ALL, 4);
    ST(21);
    v4u yv[3];
    {
      const char* yb = ws + WS_Y + (size_t)cb2 * 80 * 4096 + row2 * 2;
      const unsigned yo = (unsigned)(ln_ >> 1) * 4096 + (ln_ & 1) * 16;
      ld128c<0>(yv[0], yo, yb); ld128c<0>(yv[1], yo, yb + 32 * 4096); ld128c<0>(yv[2], yo & 0xffffu, yb + 64 * 4096);
    }
#pragma unroll
    for (int m = 2; m < 4; ++m)
#pragma unroll
      for (int n = 0; n < 5; ++n) *(LAS f32x4*)(L + FM + wi * 15360 + (m - 1) * 5120 + n * 1024 + vo) = acc2[m][n];
    lds_arrive(L, W_FM, lane);
    lds_wait_ge(L, W_FM, 4);
#pragma unroll
    for (int d = 1; d < 4; ++d)
#pragma unroll
      for (int n = 0; n < 5; ++n) acc2[0][n] = add4(acc2[0][n], *(LAS f32x4*)(L + FM + ((wi - d) & 3) * 15360 + (d - 1) * 5120 + n * 1024 + vo));
    waitv<0>(); PIN(yv[0]); PIN(yv[1]); PIN(yv[2]);
    LAS char* ys = L + YST + wi * 2560;
    *(LAS v4u*)(ys + vo) = yv[0]; *(LAS v4u*)(ys + 1024 + vo) = yv[1];
    if (ln_ < 32) *(LAS v4u*)(ys + 2048 + vo) = yv[2];
    FENCE();
#pragma unroll
    for (int n = 0; n < 5; ++n) {
      const v2u t = trread(ys + n * 512 + ln_ * 8);
      const f32x2 t0 = unpk(t.x), t1 = unpk(t.y);
      *(LAS f32x4*)(L + FM + 61440 + wi * 5120 + n * 1024 + vo) = add4(acc2[0][n], f32x4{t0.x, t0.y, t1.x, t1.y});
    }
    lds_set(L, W_F2OK + wi * 4, 2);
    STEND();
    asm volatile("97:");
    __builtin_amdgcn_endpgm();
  }

  const bool odd = wi & 1;
  const int kch = chA + (wi >> 1);
  const unsigned vo = lane * 16;
  v4u WF[6][5], AF[6][4];
  waitv<6>();
  lds_arrive(L, W_ST, lane);
  waitv<0>();
  arrive_flag8(L, W_F1C, ws, WS_F1 + (unsigned)(lane & 7) * 2048 + wid * 8, tlo, thi);
  ST(8);
  auto reduce = [&](int c, int i) {
    LAS char* pp = L + KG_SCR + (c < 2 ? 4 * c : 3) * KG_WSTR + i * 16;
    const f32x4 s = add4(*(LAS f32x4*)pp, *(LAS f32x4*)(pp + (c < 2 ? 2 : 4) * KG_WSTR));
    *(LAS f32x4*)(L + KTAB + c * KP_STRIDE + i * 16) = s;
    return s;
  };
  lds_wait_ge(L, W_KG, 4);
  if (ts < 161) { st128c((unsigned)ts * 16, ws + WS_KP + (size_t)wid * KP_STRIDE, __builtin_bit_cast(v4u, reduce(2, ts))); reduce(0, ts); }
  if (ts >= 95) reduce(1, ts - 95);
  if (ts < 3) *(LAS float*)(L + KTAB + ts * KP_STRIDE - 4) = 0.f;
  lds_arrive(L, W_SB, lane);
  waitv<0>();
  arrive_flag(L, W_FKC, lane, ws, WS_FK + (unsigned)wid * 64, tlo, thi);
  ST(9);
  __builtin_amdgcn_s_setprio(0);
  lds_wait_min<4>(L, W_RDY, F1STAGE, lane);
  const unsigned f1o = WS_F1 + (unsigned)xcd * 2048 + ts * 8;
  v2u f1own; ld64c(f1own, f1o, ws);
  waitv<0>(); PIN(f1own);
  f1own = spin64<false>(f1own, f1o, ws, tlo, thi);
  v4u uu[3]; unsigned ulo[3];
#pragma unroll
  for (int r = 0; r < 3; ++r) {
    const int e = 64 * r + lane, tb = 64 * wi + e / 3, c = e % 3;
    ld128c<0>(uu[r], tb * 80 + c * 16, ws + WS_XNB + (size_t)(16 * xcd + (j >> 1)) * 20480 + par * 32);
    ulo[r] = (c == 2 - 2 * par ? CVS : CVA + (c - par) * CV_CH) + tb * 16;
  }
  if (b == 0) arrive_flag(L, W_F1ALL, lane, ws, WS_E, tlo, thi);
  else lds_arrive(L, W_F1ALL, lane);
  ST(4);
  const unsigned fko = WS_FK + (unsigned)(wid ^ 1) * 64;
  v2u fk; ld64c(fk, fko, ws);
  waitv<0>(); PIN(fk); PIN(uu[0]); PIN(uu[1]); PIN(uu[2]);
  fk = spin64<false>(fk, fko, ws, tlo, thi);
  v4u kp; ld128c<0>(kp, (unsigned)(ts < 161 ? ts : 160) * 16, ws + WS_KP + (size_t)(wid ^ 1) * KP_STRIDE);
  lds_wait_ge(L, W_SB, 8);
  waitv<0>(); PIN(kp);
  if (ts < 161) { LAS f32x4* tp = (LAS f32x4*)(L + KTAB + 2 * KP_STRIDE + ts * 16); *tp = add4(*tp, __builtin_bit_cast(f32x4, kp)); }
  lds_arrive(L, W_SB, lane);
  lds_wait_ge(L, W_F1ALL, 4);
  WL(0);
  lds_wait_min<4>(L, W_RDY, 9, lane);
  put_k(L, kix, ts, 0, CVA);
  WL(1);
  lds_wait_min<4>(L, W_RDY, 10, lane);
  put_k(L, kix, ts, 1, CVA + CV_CH);
  if (ZCOND) { *(LAS v4u*)(L + CVA + ZOFF) = v4u{0u, 0u, 0u, 0u}; *(LAS v4u*)(L + CVA + CV_CH + ZOFF) = v4u{0u, 0u, 0u, 0u}; }
  ST(7);
  lds_wait_min<4>(L, W_RDY, 11, lane);
  ST(11);
  put_u(L + ulo[0], uu[0], ts < 3); put_u(L + ulo[1], uu[1], false); put_u(L + ulo[2], uu[2], false);
  if (ZCOND) *(LAS v4u*)(L + CVS + ZOFF) = v4u{0u, 0u, 0u, 0u};
  lds_wait_ge(L, W_SB, 12);
  put_k(L, kix, ts, 2, CVS);
  lds_arrive(L, W_SB, lane);
  WL(2); WL(3); WL(4); WL(5);
  ST(5);
  lds_wait_ge(L, W_SB, 16);
  lds_wait_ge(L, W_RDY + wi * 4, 12);
  ST(13);
  __builtin_amdgcn_s_setprio(SPRIO2);
  LATE_LANE();
  f32x4 cacc[8];
#pragma unroll
  for (int i = 0; i < 8; ++i) cacc[i] = f32x4{0.f, 0.f, 0.f, 0.f};
  {
    const unsigned o1 = CVA + (wi >> 1) * CV_CH - (odd ? 4 * 64 : 0);
    unsigned kb = KAO + o1 - 3584 - 192, ubb = UBO + o1 - 192; PIN(kb); PIN(ubb);
    LAS char* ka = L + kb; LAS char* ub = L + ubb;
    v4u A[8], B[8];
#define LA(i, u) (*(LAS v4u*)(ka + 512 * (7 - (i)) + 64 * (3 - (u))))
#define LB(i, u) (*(LAS v4u*)(ub + 512 * (i) + 64 * (3 - (u))))
    A[7] = LA(7, 0); B[0] = LB(0, 0); A[0] = LA(0, 0); B[7] = LB(7, 0); A[6] = LA(6, 0); B[6] = LB(6, 0); A[1] = LA(1, 0); B[1] = LB(1, 0);
    A[5] = LA(5, 0); B[5] = LB(5, 0); A[2] = LA(2, 0); B[2] = LB(2, 0); A[4] = LA(4, 0); B[4] = LB(4, 0); A[3] = LA(3, 0); B[3] = LB(3, 0);
    SCHED();
#pragma unroll
    for (int u = 0; u < 4; ++u) {
      v4u An[2], Bn[2];
#pragma unroll
      for (int sd = 7; sd >= 0; --sd) {
#pragma unroll
        for (int o = 0; o <= sd; ++o) {
          if (sd + o < 8) cacc[sd + o] = mfma(A[sd], B[o], cacc[sd + o]);
          if (o != sd && sd + o < 8) cacc[sd + o] = mfma(A[o], B[sd], cacc[sd + o]);
        }
        SCHED();
        if (u < 3) {
          if (sd >= 2) { A[sd] = LA(sd, u + 1); B[sd] = LB(sd, u + 1); }
          if (sd == 6 || sd == 5) { An[6 - sd] = LA(6 - sd, u + 1); Bn[6 - sd] = LB(6 - sd, u + 1); }
        }
        SCHED();
      }
      if (u < 3) { A[0] = An[0]; A[1] = An[1]; B[0] = Bn[0]; B[1] = Bn[1]; }
    }
#undef LA
#undef LB
  }
  ST(10);
  unsigned vm[4];
  {
    LAS char* px = L + SXCH + wi * 4096 + vo;
    if (odd) {
#pragma unroll
      for (int i = 0; i < 4; ++i) { *(LAS f32x4*)(px + i * 1024) = cacc[i]; cacc[i] = cacc[4 + i]; }
    } else {
#pragma unroll
      for (int i = 0; i < 4; ++i) *(LAS f32x4*)(px + i * 1024) = cacc[4 + i];
    }
    lds_arrive(L, W_SX + (wi >> 1) * 4, lane);
#pragma unroll
    for (int m = 0; m < 4; ++m) vm[m] = vo + ((m + wi) & 3) * 49152;
    lds_wait_ge(L, W_SX + (wi >> 1) * 4, 2);
    px = L + SXCH + (wi ^ 1) * 4096 + vo;
    v2u yo[4];
#pragma unroll
    for (int i = 0; i < 4; ++i) { const f32x4 s = add4(cacc[i], *(LAS f32x4*)(px + i * 1024)); yo[i] = v2u{pk2(s.x, s.y), pk2(s.z, s.w)}; }
    char* yb = ws + WS_Y + (size_t)kch * 4096 + (odd ? 2048 : 0);
    LAS char* yl = L + SXCH + (wi ^ 1) * 4096;
#pragma unroll
    for (int i = 0; i < 4; ++i) *(LAS v2u*)(yl + i * 512 + YOFF) = yo[i];
    FENCE();
    st128c(vo, yb, *(LAS v4u*)(yl + vo)); st128c(vo, yb + 1024, *(LAS v4u*)(yl + 1024 + vo));
  }
  ST(14);
  lds_wait_ge(L, W_F2OK + wi * 4, 1);
  ST(16);
  AL(0); AL(1);
  ST(17);
  f32x4 acc2[4][5];
  waitv<8>();
  arrive_flag<8>(L, W_F3C, lane, ws, WS_F3 + (unsigned)(xcd * 32 + j) * 128, tlo, thi);
  __builtin_amdgcn_s_setprio(0);
  waitv<4>(); g2_step<0>(acc2, WF[0], AF[0], [&](int i) { if (i >= 3 && (i & 1)) AL1(2, (i - 3) >> 1); });
  waitv<4>(); g2_step<4>(acc2, WF[1], AF[1], [&](int i) { if (i >= 2) AL1(3 + ((i - 2) >> 2), (i - 2) & 3); });
  waitv<8>(); g2_step<4>(acc2, WF[2], AF[2], [&](int i) { if (i < 4) AL1(5, i); });
  ST(20);
  waitv<8>(); g2_step<4>(acc2, WF[3], AF[3], NOLD);
  lds_wait_ge(L, W_F3C, 8);
  waitv<0>();
#pragma unroll
  for (int m = 0; m < 4; ++m) { PIN(AF[4][m]); PIN(AF[5][m]); }
#pragma unroll
  for (int n = 0; n < 5; ++n) { PIN(WF[4][n]); PIN(WF[5][n]); }
#pragma unroll
  for (int mm = 1; mm < 5; ++mm) {
    const int m = mm & 3;
#pragma unroll
    for (int n = 0; n < 5; ++n) {
      acc2[m][n] = mfma(WF[4][n], AF[4][m], acc2[m][n]);
      acc2[m][n] = mfma(WF[5][n], AF[5][m], acc2[m][n]);
      if (mm > 1) *(LAS f32x4*)(L + FS + wi * 15360 + (mm - 2) * 5120 + n * 1024 + vo) = acc2[mm - 1][n];
      SCHED();
    }
  }
  lds_arrive(L, W_FS, lane);
  ST(22);
  lds_wait_ge(L, W_FS, 4);
#pragma unroll
  for (int d = 1; d < 4; ++d)
#pragma unroll
    for (int n = 0; n < 5; ++n) acc2[0][n] += *(LAS f32x4*)(L + FS + ((wi - d) & 3) * 15360 + (d - 1) * 5120 + n * 1024 + vo);
  ST(23);
  lds_wait_ge(L, W_F2OK + wi * 4, 2);
  ST(24);
  {
    char* ob = out + (size_t)row2 * 1280 + cb2 * 160; const unsigned oo = OROW * 1280 + cg * 8;
    v2u o[5];
#pragma unroll
    for (int n = 0; n < 5; ++n) {
      const f32x4 s = acc2[0][n] + *(LAS f32x4*)(L + FM + 61440 + wi * 5120 + n * 1024 + vo);
      o[n] = v2u{pk2(s.x, s.y), pk2(s.z, s.w)};
    }
    st64o<0>(oo, ob, o[0]); st64o<32>(oo, ob, o[1]); st64o<64>(oo, ob, o[2]); st64o<96>(oo, ob, o[3]); st64o<128>(oo, ob, o[4]);
  }
  STEND();
  asm volatile("99:");
}

extern "C" void kernel_launch(void* const* d_in, const int* in_sizes, int n_in, void* d_out, int out_size, void* d_ws, size_t ws_size,
                              hipStream_t stream) {
  fused_s4_mlp<<<256, 512, 0, stream>>>((const char*)d_in[0], (const char*)d_in[1], (const char*)d_in[2], (const char*)d_in[3],
      (const char*)d_in[4], (const char*)d_in[5], (const char*)d_in[6], (const char*)d_in[7], (const char*)d_in[8], (const char*)d_in[10],
      (const char*)d_in[11], (const char*)d_in[12], (char*)d_out, (char*)d_ws);
}
```

```cpp
#include <hip/hip_runtime.h>
#include <stdint.h>

typedef unsigned short u16;
typedef unsigned long long u64;
typedef unsigned v4u __attribute__((ext_vector_type(4)));
typedef unsigned v2u __attribute__((ext_vector_type(2)));
typedef float f32x4 __attribute__((ext_vector_type(4)));
typedef float f32x2 __attribute__((ext_vector_type(2)));
typedef short bf16x8 __attribute__((ext_vector_type(8)));
typedef short s16x4 __attribute__((ext_vector_type(4)));
typedef __bf16 bf16x2_t __attribute__((ext_vector_type(2)));
#define LAS __attribute__((address_space(3)))
#define GAS __attribute__((address_space(1)))
#define DI __device__ __forceinline__

constexpr unsigned WS_E = 0, WS_TMO = 64, WS_F1 = 131072, WS_STATS = 16384, WS_F2 = 32768, WS_F3 = 65536, WS_FK = 98304, WS_STAMP = 262144;
constexpr unsigned WS_XNB = 1u << 20, WS_W2T = 4u << 20, WS_Y = 7u << 20, WS_ACT = 10u << 20, WS_KP = 17u << 20;
constexpr unsigned KP_STRIDE = 2624;
constexpr unsigned SPIN_CAP = 400000;

constexpr unsigned W_F1C = 0, W_F2C = 4, W_F3C = 8, W_KG = 12, W_MB = 16, W_SB = 20, W_SX = 24, W_FM = 32, W_FS = 36, W_F3ALL = 40, W_F1ALL = 44,
                   W_F2OK = 48, W_RDY = 64, W_TAG = 80, W_FKC = 96, W_ST = 100;
constexpr unsigned KTAB = 272;
constexpr unsigned CSP = 8192;
constexpr unsigned CVA = 10240, CV_CH = 27008, CV_USTR = 4640, CV_KOFF = 18560, CV_KSTR = 4224;
constexpr unsigned KG_SCR = CVA, KG_WSTR = 4352, LN_ROWS = KG_SCR + 8 * KG_WSTR, LN_PITCH = 1344, W2P = LN_ROWS + 10752;
constexpr unsigned G1_RING = 64512, G1_STAGE = 28672, G1_BOFF = 16384;
constexpr unsigned CVS = G1_RING, SXCH = CVS + 27136, MXCH = SXCH + 16384;
constexpr unsigned CSF = G1_RING + 3 * G1_STAGE, YST = CSF + 3072;
constexpr unsigned FM = 256, FS = FM + 81920;
static_assert(FS + 61440 <= CSF && CVA + 2 * CV_CH <= G1_RING && W2P + 8192 <= G1_RING && YST + 10240 <= 163840 && MXCH + 16384 <= CSF, "lds map");

#define ROT2 0
#define F1STAGE 5
#define SPRIO2 1
#define F1GAP 16
#define F2GAP 8
#define SPRIO 1
#define ST(i) do {} while (0)
#define STEND() do {} while (0)
#define STARG
#define STPASS

DI float bf2f(unsigned v) { return __builtin_bit_cast(float, v << 16); }
DI float bfhi(unsigned v) { return __builtin_bit_cast(float, v & 0xffff0000u); }
DI unsigned pk2(float lo, float hi) { bf16x2_t r; r.x = (__bf16)lo; r.y = (__bf16)hi; return __builtin_bit_cast(unsigned, r); }
DI float rbf(float f) { return bf2f(pk2(f, f) & 0xffffu); }
DI f32x4 mfma(v4u a, v4u b, f32x4 c) {
  return __builtin_amdgcn_mfma_f32_16x16x32_bf16(__builtin_bit_cast(bf16x8, a), __builtin_bit_cast(bf16x8, b), c, 0, 0, 0);
}
DI v2u trread(LAS char* p) { return __builtin_bit_cast(v2u, __builtin_amdgcn_ds_read_tr16_b64_v4i16((LAS s16x4*)p)); }
template <int N> DI void waitv() { asm volatile("s_waitcnt vmcnt(%0)" ::"i"(N) : "memory"); }
DI void bar() { asm volatile("s_waitcnt lgkmcnt(0)\n\ts_barrier" ::: "memory"); }
#define PIN(x) asm volatile("" : "+v"(x))
#define FENCE() asm volatile("" ::: "memory")
#define SCHED() __builtin_amdgcn_sched_barrier(0)
#define RFL(x) __builtin_amdgcn_readfirstlane(x)

DI void ld16(unsigned& d, unsigned vo, const void* sb) { asm volatile("global_load_ushort %0, %1, %2" : "=v"(d) : "v"(vo), "s"(sb) : "memory"); }
template <int O> DI void ld32(unsigned& d, unsigned vo, const void* sb) { asm volatile("global_load_dword %0, %1, %2 offset:%3" : "=v"(d) : "v"(vo), "s"(sb), "i"(O) : "memory"); }
DI void ld64(v2u& d, unsigned vo, const void* sb) { asm volatile("global_load_dwordx2 %0, %1, %2" : "=v"(d) : "v"(vo), "s"(sb) : "memory"); }
DI void ld128(v4u& d, unsigned vo, const void* sb) { asm volatile("global_load_dwordx4 %0, %1, %2" : "=v"(d) : "v"(vo), "s"(sb) : "memory"); }
DI void ld64c(v2u& d, unsigned vo, const void* sb) { asm volatile("global_load_dwordx2 %0, %1, %2 sc1" : "=v"(d) : "v"(vo), "s"(sb) : "memory"); }
template <int O> DI void ld128c(v4u& d, unsigned vo, const void* sb) { asm volatile("global_load_dwordx4 %0, %1, %2 offset:%3 sc1" : "=v"(d) : "v"(vo), "s"(sb), "i"(O) : "memory"); }
DI void st64c(unsigned vo, void* sb, v2u d) { asm volatile("global_store_dwordx2 %0, %1, %2 sc1" ::"v"(vo), "v"(d), "s"(sb) : "memory"); }
template <int O> DI void ld64o(v2u& d, unsigned vo, const void* sb) { asm volatile("global_load_dwordx2 %0, %1, %2 offset:%3" : "=v"(d) : "v"(vo), "s"(sb), "i"(O) : "memory"); }
template <int O> DI void st64o(unsigned vo, void* sb, v2u d) { asm volatile("global_store_dwordx2 %0, %1, %2 offset:%3" ::"v"(vo), "v"(d), "s"(sb), "i"(O) : "memory"); }
template <int O> DI void st64co(unsigned vo, void* sb, v2u d) { asm volatile("global_store_dwordx2 %0, %1, %2 offset:%3 sc1" ::"v"(vo), "v"(d), "s"(sb), "i"(O) : "memory"); }
DI void st128c(unsigned vo, void* sb, v4u d) { asm volatile("global_store_dwordx4 %0, %1, %2 sc1\n\ts_nop 1" ::"v"(vo), "v"(d), "s"(sb) : "memory"); }
DI void st64u(unsigned vo, void* sb, v2u d) { asm volatile("global_store_dwordx2 %0, %1, %2 sc1 ; CNT" ::"v"(vo), "v"(d), "s"(sb) : "memory"); }
DI void st128u(unsigned vo, void* sb, v4u d) { asm volatile("global_store_dwordx4 %0, %1, %2 sc1 ; CNT\n\ts_nop 1" ::"v"(vo), "v"(d), "s"(sb) : "memory"); }
DI void dma16(unsigned vo, const void* sb, unsigned lds) {
  asm volatile("s_mov_b32 m0, %2\n\ts_nop 0\n\tglobal_load_lds_dwordx4 %0, %1" ::"v"(vo), "s"(sb), "s"(lds) : "memory");
}
template <int O> DI void dma16o(unsigned vo, const void* sb) {
  asm volatile("global_load_lds_dwordx4 %0, %1 offset:%2" ::"v"(vo), "s"(sb), "i"(O) : "memory");
}
template <bool HIONLY> DI v2u spin64(v2u cur, unsigned vo, char* ws, unsigned tlo, unsigned thi) {
  if (__builtin_expect(__builtin_amdgcn_ballot_w64(HIONLY ? (cur.y != tlo) : (cur.x != tlo || cur.y != thi)) != 0ull, 0)) {
#pragma nounroll
    for (unsigned spins = 0; spins < SPIN_CAP; ++spins) {
      __builtin_amdgcn_s_sleep(6);
      asm volatile("global_load_dwordx2 %0, %1, %2 sc1\n\ts_waitcnt vmcnt(0)" : "=&v"(cur) : "v"(vo), "s"(ws) : "memory");
      if (__builtin_amdgcn_ballot_w64(HIONLY ? (cur.y != tlo) : (cur.x != tlo || cur.y != thi)) == 0ull) break;
    }
  }
  return cur;
}
DI unsigned lw(LAS char* L, unsigned off) { return *(volatile LAS unsigned*)(L + off); }
DI void lds_wait_at(unsigned a, unsigned target) {
  unsigned v, cnt; u64 t;
  asm volatile("s_mov_b32 %1, %5\n"
               "1:\n\tds_read_b32 %0, %3\n\ts_sub_u32 %1, %1, 1\n\ts_waitcnt lgkmcnt(0)\n\tv_cmp_gt_u32 vcc, %4, %0\n\t"
               "s_cselect_b64 %2, 0, vcc\n\ts_cmp_lg_u64 %2, 0\n\ts_cbranch_scc1 1b"
               : "=&v"(v), "=&s"(cnt), "=&s"(t) : "v"(a), "s"(target), "i"(4 * SPIN_CAP) : "vcc", "scc", "memory");
}
DI void lds_wait_ge(LAS char* L, unsigned off, unsigned target) { lds_wait_at((unsigned)(size_t)L + off, target); }
template <int N> DI void lds_wait_min(LAS char* L, unsigned off, unsigned target, int lane) {
  lds_wait_at((unsigned)(size_t)L + off + (lane & (N - 1)) * 4, target);
}
DI void lds_set(LAS char* L, unsigned off, unsigned v) { FENCE(); *(volatile LAS unsigned*)(L + off) = v; FENCE(); }
DI void lds_arrive(LAS char* L, unsigned off, int) {
  asm volatile("s_mov_b64 exec, 1\n\tds_add_u32 %0, %1\n\ts_mov_b64 exec, -1" ::"v"((unsigned)(size_t)L + off), "v"(1u) : "memory");
}
template <unsigned N = 4> DI void arrive_flag(LAS char* L, unsigned off, int, char* ws, unsigned fo, unsigned tlo, unsigned thi) {
  unsigned old;
  asm volatile("s_mov_b64 exec, 1\n\tds_add_rtn_u32 %0, %1, %2\n\ts_waitcnt lgkmcnt(0)\n\tv_cmp_eq_u32 vcc, %6, %0\n\t"
               "s_mov_b64 exec, vcc\n\tglobal_store_dwordx2 %3, %4, %5 sc1\n\ts_mov_b64 exec, -1"
               : "=&v"(old) : "v"((unsigned)(size_t)L + off), "v"(1u), "v"(fo), "v"(v2u{tlo, thi}), "s"(ws), "i"(N - 1) : "vcc", "memory");
}
DI void arrive_flag8(LAS char* L, unsigned off, char* ws, unsigned fo, unsigned tlo, unsigned thi) {
  unsigned old;
  asm volatile("s_mov_b64 exec, 1\n\tds_add_rtn_u32 %0, %1, %2\n\ts_waitcnt lgkmcnt(0)\n\tv_cmp_eq_u32 vcc, 3, %0\n\t"
               "s_mul_i32 exec_lo, vcc_lo, 0xff\n\tglobal_store_dwordx2 %3, %4, %5 sc1\n\ts_mov_b64 exec, -1"
               : "=&v"(old) : "v"((unsigned)(size_t)L + off), "v"(1u), "v"(fo), "v"(v2u{tlo, thi}), "s"(ws) : "vcc", "memory");
}
typedef float f32x16 __attribute__((ext_vector_type(16)));
DI f32x16 mfma32(v4u a, v4u b, f32x16 c) {
  return __builtin_amdgcn_mfma_f32_32x32x16_bf16(__builtin_bit_cast(bf16x8, a), __builtin_bit_cast(bf16x8, b), c, 0, 0, 0);
}
template <int C> DI float dpp_add(float v) {
  return v + __builtin_bit_cast(float, __builtin_amdgcn_update_dpp(0, __builtin_bit_cast(int, v), C, 0xf, 0xf, true));
}
DI float rl(float v, int l) { return __builtin_bit_cast(float, __builtin_amdgcn_readlane(__builtin_bit_cast(int, v), l)); }
DI float half_sum(float v, bool upper) {
  v = dpp_add<0xB1>(v); v = dpp_add<0x4E>(v); v = dpp_add<0x141>(v); v = dpp_add<0x140>(v);
  const float a = rl(v, 0) + rl(v, 16), c = rl(v, 32) + rl(v, 48);
  return upper ? c : a;
}
DI f32x2 unpk(unsigned v) { return f32x2{bf2f(v), bfhi(v)}; }
struct c2 { f32x2 r, i; };
DI c2 cmul(c2 a, c2 b) { return c2{__builtin_elementwise_fma(a.r, b.r, -(a.i * b.i)), __builtin_elementwise_fma(a.r, b.i, a.i * b.r)}; }
DI c2 csq(c2 v) { return c2{__builtin_elementwise_fma(v.r, v.r, -(v.i * v.i)), (v.r + v.r) * v.i}; }
DI c2 cexpk(f32x2 e1, f32x2 th, f32x2 tl, float k, float ks) {
  const f32x2 e = e1 * k, t = th * ks;
  const f32x2 mag = f32x2{__builtin_amdgcn_exp2f(e.x), __builtin_amdgcn_exp2f(e.y)};
  const f32x2 fr = __builtin_elementwise_fma(tl, f32x2{ks, ks}, f32x2{__builtin_amdgcn_fractf(t.x), __builtin_amdgcn_fractf(t.y)});
  return c2{f32x2{__builtin_amdgcn_cosf(fr.x), __builtin_amdgcn_cosf(fr.y)} * mag, f32x2{__builtin_amdgcn_sinf(fr.x), __builtin_amdgcn_sinf(fr.y)} * mag};
}
typedef _Float16 f16x8 __attribute__((ext_vector_type(8)));
typedef _Float16 f16x2 __attribute__((ext_vector_type(2)));
DI f32x4 mfmah(v4u a, v4u b, f32x4 c) {
  return __builtin_amdgcn_mfma_f32_16x16x32_f16(__builtin_bit_cast(f16x8, a), __builtin_bit_cast(f16x8, b), c, 0, 0, 0);
}
DI unsigned pkh1(float a, float b) { f16x2 r; r.x = (_Float16)a; r.y = (_Float16)b; return __builtin_bit_cast(unsigned, r); }
DI v2u pkh(c2 v) { return v2u{pkh1(v.r.x, v.i.x), pkh1(v.r.y, v.i.y)}; }
DI v2u pkb(c2 v) { return v2u{pk2(v.r.x, v.i.x), pk2(v.r.y, v.i.y)}; }
#define HMUL(o, i, w) "v_pk_mul_f16 " o ", " i ", " w " op_sel:[1,1] op_sel_hi:[1,0] neg_lo:[0,1]\n\t"
#define HFMA(o, i, w) "v_pk_fma_f16 " o ", " i ", " w ", " o " op_sel_hi:[0,1,1]\n\t"
constexpr int KROW = 528;
template <int R0, int R1> DI void h2(v2u& o0, v2u& o1, v2u i0, v2u i1, v2u w0, v2u w1) {
  unsigned a, b, c, d;
  asm volatile(HMUL("%0", "%4", "%8") HMUL("%1", "%5", "%9") HMUL("%2", "%6", "%10") HMUL("%3", "%7", "%11")
               HFMA("%0", "%4", "%8") HFMA("%1", "%5", "%9") HFMA("%2", "%6", "%10") HFMA("%3", "%7", "%11")
               "ds_write_addtid_b32 %0 offset:%12\n\tds_write_addtid_b32 %1 offset:%13\n\tds_write_addtid_b32 %2 offset:%14\n\tds_write_addtid_b32 %3 offset:%15"
               : "=&v"(a), "=&v"(b), "=&v"(c), "=&v"(d)
               : "v"(i0.x), "v"(i0.y), "v"(i1.x), "v"(i1.y), "v"(w0.x), "v"(w0.y), "v"(w1.x), "v"(w1.y),
                 "i"(R0 * KROW), "i"(R0 * KROW + 256), "i"(R1 * KROW), "i"(R1 * KROW + 256) : "memory");
  o0 = v2u{a, b}; o1 = v2u{c, d};
}
template <int R> DI void hput(v2u v) {
  asm volatile("ds_write_addtid_b32 %0 offset:%2\n\tds_write_addtid_b32 %1 offset:%3" ::"v"(v.x), "v"(v.y), "i"(R * KROW), "i"(R * KROW + 256) : "memory");
}
template <int C> DI unsigned dpp_maxu(unsigned v) { return max(v, (unsigned)__builtin_amdgcn_update_dpp(0, (int)v, C, 0xf, 0xf, true)); }
DI void rd8(v4u (&f)[8], LAS char* sr) {
#pragma unroll
  for (int k = 0; k < 8; ++k) f[k] = *(LAS v4u*)(sr + 64 * k);
}
template <bool H> DI void kmm(f32x4 (&ka)[2][2][2], int nh, const v4u (&UA)[2][8], const v4u (&vb)[8]) {
#pragma unroll
  for (int k = 0; k < 8; ++k)
#pragma unroll
    for (int m = 0; m < 2; ++m) {
      f32x4& a = ka[(k >> 1) & 1][m][nh];
      const f32x4 c = (k == 0 || k == 2) ? f32x4{0.f, 0.f, 0.f, 0.f} : a;
      a = H ? mfmah(UA[m][k], vb[k], c) : mfma(UA[m][k], vb[k], c);
    }
}
DI void kgen_slow(c2 a, c2 W1, c2 W256, c2 W8, LAS char* sr, f32x4 (&ka)[2][2][2]) {
  v4u UA[2][8], vb[8];
  c2 u[8]; u[0] = a;
#pragma unroll
  for (int i = 1; i < 8; ++i) u[i] = cmul(u[i - 1], W1);
  const c2 W512 = csq(W256), W1024 = csq(W512), r8 = cmul(a, W256), r10 = cmul(u[4], W256), r12 = cmul(a, W512), r14 = cmul(r8, W512), r16 = cmul(a, W1024);
  hput<0>(pkb(u[0])); hput<1>(pkb(u[1])); hput<2>(pkb(u[2])); hput<3>(pkb(u[3])); hput<4>(pkb(u[4])); hput<5>(pkb(u[5])); hput<6>(pkb(u[6])); hput<7>(pkb(u[7]));
  hput<8>(pkb(r8)); hput<9>(pkb(cmul(u[2], W256))); hput<10>(pkb(r10)); hput<11>(pkb(cmul(u[6], W256)));
  hput<12>(pkb(r12)); hput<13>(pkb(cmul(u[4], W512))); hput<14>(pkb(r14)); hput<15>(pkb(cmul(r10, W512)));
  rd8(UA[0], sr);
  hput<0>(pkb(r16)); hput<1>(pkb(cmul(r8, W1024))); hput<2>(pkb(cmul(r12, W1024))); hput<3>(pkb(cmul(r14, W1024))); hput<4>(pkb(cmul(r16, W1024)));
  rd8(UA[1], sr);
  c2 v = c2{f32x2{1.f, 1.f}, f32x2{0.f, 0.f}};
#pragma unroll
  for (int nh = 0; nh < 2; ++nh) {
#define VR(r) hput<r>(pkb(v)); v = cmul(v, W8);
    VR(0) VR(1) VR(2) VR(3) VR(4) VR(5) VR(6) VR(7) VR(8) VR(9) VR(10) VR(11) VR(12) VR(13) VR(14) VR(15)
#undef VR
    rd8(vb, sr);
    kmm<false>(ka, nh, UA, vb);
  }
}
DI void kgen(const unsigned (&pr)[7], bool dead, bool upper, unsigned sw, LAS char* sr, f32x4 (&ka)[2][2][2], float (&scale)[2]) {
  const float dt = rbf(__builtin_amdgcn_exp2f(bf2f(pr[6]) * 1.44269504088896341f));
  const f32x2 zr = unpk(pr[0]) * dt, zi = unpk(pr[1]) * dt, dbr = unpk(pr[2]) * dt, dbi = unpk(pr[3]) * dt;
  const f32x2 cr = unpk(dead ? 0u : pr[4]), ci = unpk(dead ? 0u : pr[5]);
  const c2 a = c2{__builtin_elementwise_fma(cr, dbr, -(ci * dbi)), -__builtin_elementwise_fma(cr, dbi, ci * dbr)};
  const f32x2 e1 = zr * 1.44269504088896341f, th = zi * 0.15915494309189535f;
  const f32x2 tl = __builtin_elementwise_fma(zi, f32x2{6.4206383e-9f, 6.4206383e-9f}, __builtin_elementwise_fma(zi, f32x2{0.15915494309189535f, 0.15915494309189535f}, -th));
  const c2 W1 = cexpk(e1, th, tl, 1.f, -1.f), W256 = cexpk(e1, th, tl, 256.f, -256.f), W8 = cexpk(e1, th, tl, 8.f, 8.f);
  asm volatile("s_mov_b32 m0, %0\n\ts_nop 0" ::"s"(sw) : "memory");
  if (__builtin_expect(__builtin_amdgcn_ballot_w64(!(zr.x <= 0.f && zr.y <= 0.f)) != 0ull, 0)) {
    kgen_slow(a, W1, W256, W8, sr, ka);
    scale[0] = scale[1] = 1.f;
    return;
  }
  unsigned am = max(max(__builtin_bit_cast(unsigned, a.r.x) & 0x7fffffffu, __builtin_bit_cast(unsigned, a.r.y) & 0x7fffffffu),
           max(__builtin_bit_cast(unsigned, a.i.x) & 0x7fffffffu, __builtin_bit_cast(unsigned, a.i.y) & 0x7fffffffu));
  am = dpp_maxu<0xB1>(am); am = dpp_maxu<0x4E>(am); am = dpp_maxu<0x141>(am); am = dpp_maxu<0x140>(am);
  const unsigned ex0 = min(max(max((unsigned)__builtin_amdgcn_readlane(am, 0), (unsigned)__builtin_amdgcn_readlane(am, 16)) >> 23, 2u), 252u);
  const unsigned ex1 = min(max(max((unsigned)__builtin_amdgcn_readlane(am, 32), (unsigned)__builtin_amdgcn_readlane(am, 48)) >> 23, 2u), 252u);
  scale[0] = __builtin_bit_cast(float, ex0 << 23); scale[1] = __builtin_bit_cast(float, ex1 << 23);
  const float down = __builtin_bit_cast(float, ((254u - (upper ? ex1 : ex0)) << 23));
  const c2 W2 = csq(W1), W512 = csq(W256), W16 = csq(W8), W4 = csq(W2), W1024 = csq(W512), W32 = csq(W16), W64 = csq(W32), W128 = csq(W64);
  const v2u u0 = pkh(c2{a.r * down, a.i * down}), h1 = pkh(W1), h2_ = pkh(W2), h4 = pkh(W4), h256 = pkh(W256), h512 = pkh(W512), h1024 = pkh(W1024);
  v2u u2, u4, u6, r8, r10, r12, r14, r16, t0, t1;
  v4u UA[2][8], vb[8];
  hput<0>(u0);
  h2<2, 4>(u2, u4, u0, u0, h2_, h4); h2<8, 12>(r8, r12, u0, u0, h256, h512); h2<1, 3>(t0, t1, u0, u2, h1, h1); h2<5, 6>(t0, u6, u4, u4, h1, h2_);
  h2<10, 14>(r10, r14, r8, r8, h4, h512); h2<7, 9>(t0, t1, u6, r8, h1, h2_); h2<11, 13>(t0, t1, r10, r12, h2_, h4);
  h2<15, 15>(r16, t1, u0, r14, h1024, h4);
  rd8(UA[0], sr);
  hput<0>(r16);
  h2<1, 2>(t0, t1, r8, r12, h1024, h1024); h2<3, 4>(t0, t1, r14, r16, h1024, h1024);
  rd8(UA[1], sr);
  v2u v[16];
  v[1] = pkh(W8); v[2] = pkh(W16); v[4] = pkh(W32); v[8] = pkh(W64);
  const v2u h128 = pkh(W128);
  hput<0>(v2u{0x3c00u, 0x3c00u}); hput<1>(v[1]); hput<2>(v[2]); hput<4>(v[4]); hput<8>(v[8]);
  h2<3, 5>(v[3], v[5], v[2], v[4], v[1], v[1]); h2<6, 9>(v[6], v[9], v[4], v[8], v[2], v[1]); h2<7, 10>(v[7], v[10], v[4], v[8], v[3], v[2]);
  h2<11, 12>(v[11], v[12], v[8], v[8], v[3], v[4]); h2<13, 14>(v[13], v[14], v[8], v[8], v[5], v[6]); h2<15, 15>(t0, v[15], v[1], v[8], h128, v[7]);
  rd8(vb, sr);
  hput<0>(h128); hput<1>(t0);
  h2<2, 3>(t0, t1, v[2], v[3], h128, h128); h2<4, 5>(t0, t1, v[4], v[5], h128, h128);
  kmm<true>(ka, 0, UA, vb);
  h2<6, 7>(t0, t1, v[6], v[7], h128, h128); h2<8, 9>(t0, t1, v[8], v[9], h128, h128); h2<10, 11>(t0, t1, v[10], v[11], h128, h128);
  h2<12, 13>(t0, t1, v[12], v[13], h128, h128); h2<14, 15>(t0, t1, v[14], v[15], h128, h128);
  rd8(vb, sr);
  SCHED();
  kmm<true>(ka, 1, UA, vb);
}
DI void kgen_store(LAS float* pt, const f32x4 (&kacc)[2][2], int lane) {
  const int l15 = lane & 15, g = lane >> 4;
  const int sh = g < 2 ? 3 : 4 - g;
  const int a0 = (l15 << sh) + (g < 2 ? 4 * g : 128 * g);
  const int a1 = g < 3 ? a0 + 2 : a0 + 64;
#pragma unroll
  for (int nh = 0; nh < 2; ++nh) {
    *(LAS f32x2*)(pt + a0 + ((16 * nh) << sh)) = kacc[0][nh].xy;
    *(LAS f32x2*)(pt + a1 + ((16 * nh) << sh)) = kacc[0][nh].zw;
  }
  if (g == 0) {
#pragma unroll
    for (int nh = 0; nh < 2; ++nh)
#pragma unroll
      for (int r = 0; r < 4; ++r) pt[512 + 32 * r + 16 * nh + l15] = kacc[1][nh][r];
  }
  if (lane == 16) { *(LAS f32x4*)(pt + 640) = f32x4{kacc[1][0][0], 0.f, 0.f, 0.f}; }
}

template <int PAR> DI void conv_unit_shared(LAS char* ka, LAS char* ub, f32x4 (&acc)[4]) {
  constexpr int T[4] = {PAR ? 1 : 0, PAR ? 2 : 3, PAR ? 5 : 4, PAR ? 6 : 7};
  v4u A[8], B[8];
#pragma unroll
  for (int i = 0; i <= T[3]; ++i) { A[i] = *(LAS v4u*)(ka + 512 * (7 - i)); B[i] = *(LAS v4u*)(ub + 512 * i); }
  SCHED();
#pragma unroll
  for (int q = 0; q < 4; ++q)
#pragma unroll
    for (int ip = 0; ip <= T[q]; ++ip) acc[q] = mfma(A[T[q] - ip], B[ip], acc[q]);
  SCHED();
}
DI float fadd(float a, float b) { float r; asm("v_add_f32 %0, %1, %2" : "=v"(r) : "v"(a), "v"(b)); return r; }
DI float ffma(float a, float b, float c) { float r; asm("v_fma_f32 %0, %1, %2, %3" : "=v"(r) : "v"(a), "v"(b), "v"(c)); return r; }
DI f32x4 add4(f32x4 a, f32x4 b) { return f32x4{fadd(a.x, b.x), fadd(a.y, b.y), fadd(a.z, b.z), fadd(a.w, b.w)}; }
DI f32x4 fma4(f32x4 a, float b, f32x4 c) { return f32x4{ffma(a.x, b, c.x), ffma(a.y, b, c.y), ffma(a.z, b, c.z), ffma(a.w, b, c.w)}; }
DI int rot12(int v) { return v >= 12 ? v - 12 : v; }
DI void w_loads(v4u (&WF)[5], const char* p, unsigned vo) {
  ld128c<0>(WF[0], vo, p); ld128c<1024>(WF[1], vo, p); ld128c<2048>(WF[2], vo, p); ld128c<3072>(WF[3], vo, p); ld128c<0>(WF[4], vo, p + 4096);
}
DI void a_loads(v4u (&AF)[4], const char* p, const unsigned (&vm)[4]) {
#pragma unroll
  for (int m = 0; m < 4; ++m) ld128c<0>(AF[m], vm[m], p);
}
template <int LIVE, class F> DI void g2_step(f32x4 (&acc)[4][5], v4u (&WF)[5], v4u (&AF)[4], F ld) {
#pragma unroll
  for (int m = 0; m < 4; ++m) PIN(AF[m]);
#pragma unroll
  for (int n = 0; n < 5; ++n) PIN(WF[n]);
#pragma unroll
  for (int e = 0; e < 20; ++e) {
    const int m = e / 5, n = e % 5;
    acc[m][n] = mfma(WF[n], AF[m], m >= LIVE ? f32x4{0.f, 0.f, 0.f, 0.f} : acc[m][n]);
    if (e & 1) { SCHED(); ld(e >> 1); SCHED(); }
  }
}
DI void make_kix(unsigned (&kix)[9], int ts) {
#pragma unroll
  for (int i = 0; i < 9; ++i) {
    const int lg = 2047 - 8 * ts - i;
    const int bits = (int)(pk2(__builtin_fmaxf((float)lg, 128.f), 0.f));
    const int ci = lg < bits - 0x4280 ? lg : bits - 0x4280;
    kix[i] = KTAB + 4 * ci;
    PIN(kix[i]);
  }
}
DI void put_k(LAS char* L, const unsigned (&kix)[9], int ts, int c, unsigned reg) {
  float kv[9];
#pragma unroll
  for (int i = 0; i < 9; ++i) kv[i] = *(LAS float*)(L + kix[i] + c * KP_STRIDE);
  LAS char* q = L + reg + CV_KOFF + ts * 16;
  *(LAS v4u*)(q + CV_KSTR) = v4u{pk2(kv[0], kv[1]), pk2(kv[2], kv[3]), pk2(kv[4], kv[5]), pk2(kv[6], kv[7])};
  *(LAS v4u*)q = v4u{pk2(kv[1], kv[2]), pk2(kv[3], kv[4]), pk2(kv[5], kv[6]), pk2(kv[7], kv[8])};
}
DI void put_u(LAS char* q, v4u u, bool first) {
  *(LAS v4u*)(q + 3 * CV_USTR + 384) = u;
  *(LAS v2u*)(q + 1 * CV_USTR + 392) = v2u{u.x, u.y}; *(LAS v2u*)(q + 1 * CV_USTR + 400) = v2u{u.z, u.w};
  *(LAS unsigned*)(q + 2 * CV_USTR + 388) = u.x; *(LAS v2u*)(q + 2 * CV_USTR + 392) = v2u{u.y, u.z}; *(LAS unsigned*)(q + 2 * CV_USTR + 400) = u.w;
  *(LAS unsigned*)(q + 396) = u.x; *(LAS v2u*)(q + 400) = v2u{u.y, u.z}; *(LAS unsigned*)(q + 408) = u.w;
  if (first) {
    *(LAS v2u*)(q + 384) = v2u{0u, 0u}; *(LAS unsigned*)(q + 392) = 0u;
    *(LAS v2u*)(q + CV_USTR + 384) = v2u{0u, 0u}; *(LAS unsigned*)(q + 2 * CV_USTR + 384) = 0u;
  }
}

__global__ __launch_bounds__(512, 2) void fused_s4_mlp(const char* __restrict__ x, const char* __restrict__ pAre, const char* __restrict__ pAim,
    const char* __restrict__ pBre, const char* __restrict__ pBim, const char* __restrict__ pCre, const char* __restrict__ pCim,
    const char* __restrict__ plogdt, const char* __restrict__ pDskip, const char* __restrict__ w1, const char* __restrict__ b1,
    const char* __restrict__ w2, char* __restrict__ out, char* __restrict__ ws) {
  __shared__ __attribute__((aligned(16))) char smem[163840];
  LAS char* L = (LAS char*)smem;
  const unsigned lds0 = (unsigned)(size_t)L;
  const char* pc0; unsigned codelen;
  asm volatile("s_getpc_b64 %0\n98:\n\ts_mov_b32 %1, 97f-98b\n\ts_max_u32 %1, %1, 99f-98b" : "=s"(pc0), "=&s"(codelen) :: "scc");
  const int b = blockIdx.x, lane = threadIdx.x & 63;
  const int w = RFL(threadIdx.x >> 6), wi = w & 3;
  {
    const unsigned co = (unsigned)(4 * (16 * w + (lane & 15)) + ((b >> 3) & 3)) * 128;
    asm volatile("s_mov_b32 m0, %2\n\ts_nop 0\n\tglobal_load_lds_dword %0, %1" ::"v"((co < codelen ? co : codelen) & ~3u), "s"(pc0), "s"(lds0 + YST + w * 256) : "memory");
  }
  const int xcd = b & 7, j = b >> 3, rg = xcd >> 1, chf = xcd & 1;
  const int ct1 = 8 * chf + (j & 7), rb1 = 4 * rg + (j >> 3);
  const int wid = rb1 * 16 + ct1;
  const int a2 = 4 * chf + (j >> 3), cb2 = j & 7;
  const int l15 = lane & 15, g = lane >> 4, qq = (lane >> 2) & 3, p4 = lane & 3, l31 = lane & 31, h = lane >> 5;
  const int par = j & 1, cbase = 80 * xcd + 5 * (j >> 1);
  const int chA = cbase + 3 * par, chS = cbase + 2;
  const int ts = 64 * wi + lane;
  ST(0);
  const int row2 = 512 * rg + 64 * a2 + 16 * wi;
  const char* w2b = ws + WS_W2T + (size_t)(cb2 * 48 + 12 * wi) * 5120;
  const char* actb = ws + WS_ACT + (size_t)((32 * rg + 4 * a2) * 48 + 12 * wi) * 1024;
  const int r2 = ((a2 ^ (w >> 2)) & 1) * 6;
#define WL(s) w_loads(WF[s], w2b + rot12(r2 + (s)) * 5120, vo)
#define AL(s) a_loads(AF[s], actb + rot12(r2 + (s)) * 1024, vm)
#define AL1(s, m) ld128c<0>(AF[s][m], vm[m], actb + rot12(r2 + (s)) * 1024)
#define WL1(s, n) ld128c<0>(WF[s][n], vo, w2b + rot12(r2 + (s)) * 5120 + (n) * 1024)
#define NOLD [&](int) {}
#define LATE_LANE() int ln_ = lane; PIN(ln_); const int c15 = ln_ & 15, cg = ln_ >> 4
#define KAO (CV_KOFF + (c15 & 1) * CV_KSTR + 2 * (2072 - 8 * (c15 >> 1) + 8 * cg))
#define UBO ((c15 & 3) * CV_USTR + 2 * (64 * (c15 >> 2) + 8 * cg + 224))
#define YOFF ((unsigned)((c15 >> 2) * 128 + cg * 32 + (c15 & 3) * 8))
#define OROW ((unsigned)(2 * (c15 >> 2) + (c15 & 1) + 8 * ((c15 >> 1) & 1)))
#define ZOFF (lane < 24 ? wi * CV_USTR + lane * 16 : CV_KOFF + wi * CV_KSTR + 4096 + (lane - 24) * 16)
#define ZCOND (lane < 24 || (lane < 31 && wi < 2))

  unsigned voA[4], voB[3];
#pragma unroll
  for (int i = 0; i < 4; ++i) voA[i] = (8 * i + (lane >> 3)) * 1280 + (((lane & 7) ^ (4 * (i & 1) + (lane >> 4))) * 16) - 1024 * i;
#pragma unroll
  for (int i = 0; i < 3; ++i) { const int c = 64 * i + lane; voB[i] = (c / 12) * 3072 + (c % 12) * 16 - 1024 * i; }
  const char* g1a = x + (size_t)(rb1 * 128 + 32 * wi) * 1280;
  const char* g1b = w1 + ct1 * 192 + wi * 49152;
  const unsigned la = lds0 + G1_RING + wi * 4096, lb = lds0 + G1_RING + G1_BOFF + wi * 3072;
  const int r1 = ((j & 7) + (5 * (j >> 3)) / 2 + 5 * chf + rg) % 10;
  auto slab = [&](int s) { const int v = s + r1; return v >= 10 ? v - 10 : v; };
  auto issue = [&](int s) {
    const char* pa = g1a + slab(s) * 128; const char* pb = g1b + (size_t)slab(s) * 64 * 3072;
    dma16(voA[0], pa, la + s * G1_STAGE); dma16o<1024>(voA[1], pa); dma16o<2048>(voA[2], pa); dma16o<3072>(voA[3], pa);
    dma16(voB[0], pb, lb + s * G1_STAGE); dma16o<1024>(voB[1], pb); dma16o<2048>(voB[2], pb);
  };
  unsigned tlo = 0, thi = 0;
  unsigned kix[9];
  if (w < 4) {
    unsigned pr[7];
    const bool odd = wi & 1;
    {
      const int kch = chA + (wi >> 1);
      const bool shl = odd && h;
      const unsigned vo = (unsigned)(shl ? chS * 192 + par * 96 + (wi >> 1) * 48 + 2 * (l31 < 24 ? l31 : 23) : kch * 192 + (odd ? 128 : 0) + 2 * lane) * 2;
      ld16(pr[6], (unsigned)(shl ? chS : kch) * 2, plogdt);
      ld32<0>(pr[0], vo, pAre); ld32<0>(pr[1], vo, pAim); ld32<0>(pr[2], vo, pBre); ld32<0>(pr[3], vo, pBim); ld32<0>(pr[4], vo, pCre); ld32<0>(pr[5], vo, pCim);
    }
    bar();
#pragma unroll
    for (int i = 0; i < 9; ++i) kix[i] = 0;
    waitv<0>();
#pragma unroll
    for (int i = 0; i < 7; ++i) PIN(pr[i]);
    ST(1);
    f32x4 ka[2][2][2]; float scale[2];
    kgen(pr, odd && lane >= 56, h, lds0 + KG_SCR + wi * 2 * KG_WSTR, L + KG_SCR + wi * 2 * KG_WSTR + l15 * KROW + g * 16, ka, scale);
#pragma unroll
    for (int i = 0; i < 4; ++i) {
      ka[1][i >> 1][i & 1] *= scale[1];
      ka[0][i >> 1][i & 1] = ka[0][i >> 1][i & 1] * scale[0] + (odd ? f32x4{0.f, 0.f, 0.f, 0.f} : ka[1][i >> 1][i & 1]);
    }
    kgen_store((LAS float*)(L + KG_SCR + wi * 2 * KG_WSTR), ka[0], lane);
    if (odd) kgen_store((LAS float*)(L + KG_SCR + wi * 2 * KG_WSTR + KG_WSTR), ka[1], lane);
    lds_arrive(L, W_KG, lane);
  } else {
    if (w == 4) *(LAS unsigned*)(L + lane * 4) = 0u;
    bar();
    v2u E; v4u xa, xb; v2u xc;
    const int pu = b % 240, pks = pu / 5, pcg = pu % 5;
    const char* xrow = x + (size_t)(8 * wid + 2 * wi) * 1280;
    ld128(xa, h * 1280 + l31 * 16, xrow); ld128(xb, h * 1280 + 512 + l31 * 16, xrow); ld64(xc, h * 1280 + 1024 + l31 * 8, xrow);
    ld64c(E, 0u, ws);
#pragma unroll
    for (int i = 0; i < 2; ++i)
      dma16((unsigned)((lane >> 1) * 1280 + (lane & 1) * 16), w2 + (size_t)pks * 32 * 1280 + (128 * pcg + 32 * wi + 16 * i) * 2, lds0 + W2P + wi * 2048 + i * 1024);
    issue(0);
    make_kix(kix, ts);
    issue(1);
    waitv<14>();
    PIN(E); PIN(xa); PIN(xb); PIN(xc);
    ST(6);
  const u64 tag64 = (((u64)RFL(E.y) << 32) | (unsigned)RFL(E.x)) + 1ull;
  tlo = (unsigned)tag64; thi = (unsigned)(tag64 >> 32);
  if (wi == 0) { lds_set(L, W_TAG, tlo); lds_set(L, W_TAG + 4, thi); lds_set(L, W_TAG + 8, 1); }
  {
    f32x2 v[10] = {unpk(xa.x), unpk(xa.y), unpk(xa.z), unpk(xa.w), unpk(xb.x), unpk(xb.y), unpk(xb.z), unpk(xb.w), unpk(xc.x), unpk(xc.y)};
    f32x2 s2 = ((v[0] + v[1]) + (v[2] + v[3])) + ((v[4] + v[5]) + (v[6] + v[7])) + (v[8] + v[9]);
    const float mean = half_sum(s2.x + s2.y, h) * (1.0f / 640.0f);
    f32x2 q2 = f32x2{0.f, 0.f};
#pragma unroll
    for (int i = 0; i < 10; ++i) { const f32x2 d = v[i] - mean; q2 = __builtin_elementwise_fma(d, d, q2); }
    const float var = rbf(half_sum(q2.x + q2.y, h) * (1.0f / 640.0f));
    const float m = rbf(mean), sd = rbf(__builtin_amdgcn_sqrtf(rbf(var + 1e-5f))), rs = __builtin_amdgcn_rcpf(sd);
    unsigned o[10];
#pragma unroll
    for (int i = 0; i < 10; ++i) { const f32x2 t = (v[i] - m) * rs; o[i] = pk2(t.x, t.y); }
    LAS char* rp = L + LN_ROWS + (2 * wi + h) * LN_PITCH;
    *(LAS v4u*)(rp + l31 * 16) = v4u{o[0], o[1], o[2], o[3]};
    *(LAS v4u*)(rp + 512 + l31 * 16) = v4u{o[4], o[5], o[6], o[7]};
    *(LAS v2u*)(rp + 1024 + l31 * 8) = v2u{o[8], o[9]};
    waitv<0>();
    lds_arrive(L, W_SB, lane);
    issue(2);
    if (l31 == 0) st64c(WS_STATS + (unsigned)(8 * wid + 2 * wi + h) * 8, ws,
                        v2u{(__builtin_bit_cast(unsigned, m) & 0xffff0000u) | (__builtin_bit_cast(unsigned, sd) >> 16), tlo});
  }
#pragma unroll
  for (int i = 0; i < 2; ++i) {
    LAS char* sp = L + W2P + wi * 2048 + i * 1024 + (16 * (g >> 1) + 4 * (g & 1) + qq) * 32 + 8 * p4;
    const v2u lo = trread(sp), hi = trread(sp + 256);
    const int ntg = 8 * pcg + 2 * wi + i;
    st128c((unsigned)lane * 16, ws + WS_W2T + (size_t)(((ntg / 5) * 48 + pks) * 5 + ntg % 5) * 1024, v4u{lo.x, lo.y, hi.x, hi.y});
  }
  lds_wait_ge(L, W_SB, 4);
  {
    LAS char* sp = L + LN_ROWS + qq * LN_PITCH + (160 * wi + 16 * g) * 2 + 8 * p4;
#pragma unroll
    for (int i = 0; i < 3; ++i) {
      const v2u lo = trread(sp + 128 * i), hi = trread(sp + 128 * i + 4 * LN_PITCH);
      const unsigned ch = 64 * i + lane;
      if (i < 2 || lane < 32) st128c((ch / 5) * 20480 + (ch % 5) * 16, ws + WS_XNB + (size_t)wi * 32 * 20480 + wid * 80, v4u{lo.x, lo.y, hi.x, hi.y});
    }
  }
  }
  ST(2);
  int role = w; asm volatile("" : "+s"(role));
  if (role < 4) {
    int lane = __builtin_amdgcn_mbcnt_hi(~0u, __builtin_amdgcn_mbcnt_lo(~0u, 0u)); PIN(lane);
    const int l15 = lane & 15, g = lane >> 4, qq = (lane >> 2) & 3, p4 = lane & 3, l31 = lane & 31, h = lane >> 5;
    const unsigned vo = lane * 16;
    f32x16 acc1[3]; f32x4 csa[3];
#pragma unroll
    for (int n = 0; n < 3; ++n) {
      csa[n] = f32x4{0.f, 0.f, 0.f, 0.f};
#pragma unroll
      for (int i = 0; i < 16; ++i) acc1[n][i] = 0.f;
    }
    const int xrow = (l31 & 17) + 2 * (l15 >> 2) + 8 * ((l15 >> 1) & 1);
    unsigned xo[4], wo[4];
#pragma unroll
    for (int k = 0; k < 4; ++k) {
      const int kr = (k + wi) & 3;
      xo[k] = G1_RING + (32 * wi + xrow) * 128 + (((2 * kr + h) ^ ((xrow >> 1) & 7)) * 16);
      wo[k] = G1_RING + G1_BOFF + (16 * kr + 8 * h + qq) * 192 + 32 * (g & 1) + 8 * p4;
      PIN(xo[k]); PIN(wo[k]);
    }
    const unsigned sel1 = ((l15 ^ g) & 1) ? 0u : 0x3f803f80u;
    const v4u sel = v4u{sel1, sel1, sel1, sel1};
    v4u X[4], W[4][3];
    auto rdw = [&](int s, int k, int n) {
      LAS char* p = L + wo[k] + (s % 3) * G1_STAGE + 64 * n; const v2u lo = trread(p), hi = trread(p + 768); W[k][n] = v4u{lo.x, lo.y, hi.x, hi.y};
    };
    auto rdx = [&](int s, int k) { X[k] = *(LAS v4u*)(L + xo[k] + (s % 3) * G1_STAGE); };
#define MM(k, n) acc1[n] = mfma32(W[k][n], X[k], acc1[n])
#define CS(n) csa[n] = mfma(W[0][n], sel, csa[n])
    v2u gran; unsigned bb[3];
    v4u WF[6][5], AF[6][4];
    const unsigned go = WS_STATS + (unsigned)(rb1 * 128 + 32 * wi + xrow) * 8;
    auto wl1 = [&](int s, int e0) { const int e = 7 * (s - 7) + e0; if (s >= 7 && e0 < 7 && e < 20) ld128c<0>(WF[e / 5][e % 5], vo, w2b + rot12(r2 + e / 5) * 5120 + (e % 5) * 1024); };
    lds_wait_ge(L, W_SB, 4);
    ST(3);
    rdx(0, 0); rdw(0, 0, 0); rdw(0, 0, 1); rdw(0, 0, 2); rdx(0, 1); rdw(0, 1, 0); rdw(0, 1, 1); rdw(0, 1, 2);
#pragma unroll
    for (int s = 0; s < 10; ++s) {
      const bool refill = s < 7;
      const char* pa = g1a + slab(s + 3) * 128; const char* pb = g1b + (size_t)slab(s + 3) * 64 * 3072;
      SCHED();
      MM(0, 0); rdx(s, 2); rdw(s, 2, 0); SCHED();
      MM(0, 1); rdw(s, 2, 1); rdw(s, 2, 2); SCHED();
      MM(0, 2); rdx(s, 3); rdw(s, 3, 0); SCHED();
      CS(0); rdw(s, 3, 1); SCHED();
      CS(1); rdw(s, 3, 2); SCHED();
      CS(2);
      if (s == 0) {} else if (s == 1) lds_wait_ge(L, W_ST, 4); else if (s < 7) waitv<7>(); else if (s == 7) waitv<11>(); else if (s == 8) waitv<11>();
      if (s == 9 && l15 < 2) {
#pragma unroll
        for (int n = 0; n < 3; ++n) *(LAS f32x4*)(L + CSP + wi * 384 + (32 * n + 16 * l15 + 4 * g) * 4) = csa[n];
      }
      lds_set(L, W_RDY + wi * 4, s + 2);
      SCHED();
      MM(1, 0);
      if (refill) { asm volatile("s_waitcnt lgkmcnt(7)" ::: "memory"); dma16(voA[0], pa, la + (s % 3) * G1_STAGE); }
      wl1(s, 0);

      SCHED();
      MM(1, 1); if (refill) dma16o<1024>(voA[1], pa); wl1(s, 1);
      unsigned fl = 0;
      if (s < 9) fl = lw(L, W_RDY + p4 * 4);
      SCHED();
      MM(1, 2); if (refill) dma16o<2048>(voA[2], pa); wl1(s, 2); SCHED();
      MM(2, 0); if (refill) dma16o<3072>(voA[3], pa); wl1(s, 3);
      SCHED();
      if (s == 9) {
        MM(2, 1); wl1(s, 4); SCHED();
        lds_wait_min<4>(L, W_RDY, 11, lane);
        float cs[3];
#pragma unroll
        for (int n = 0; n < 3; ++n) { LAS float* p = (LAS float*)(L + CSP) + 32 * n + l31; cs[n] = fadd(fadd(p[0], p[96]), fadd(p[192], p[288])); }
        MM(2, 2); wl1(s, 5); SCHED();
        waitv<20>(); PIN(gran); PIN(bb[0]); PIN(bb[1]); PIN(bb[2]);
        MM(3, 0); MM(3, 1); MM(3, 2);
        const unsigned keep = h ? 0u : ~0u, nm = (gran.x >> 16) ^ 0x8000u;
        const v4u xe = v4u{(nm | (nm << 16)) & keep, (nm | (gran.x << 16)) & keep, 0u, 0u};
#pragma unroll
        for (int n = 0; n < 3; ++n) {
          const float c0 = bfhi(__builtin_bit_cast(unsigned, cs[n])), r0 = cs[n] - c0, c1 = bfhi(__builtin_bit_cast(unsigned, r0)), r1 = r0 - c1;
          const v4u we = v4u{(__builtin_bit_cast(unsigned, c0) >> 16) | __builtin_bit_cast(unsigned, c1), (__builtin_bit_cast(unsigned, r1) >> 16) | (bb[n] << 16), 0u, 0u};
          acc1[n] = mfma32(we, xe, acc1[n]);
        }
        SCHED();
        break;
      }
      if (__builtin_expect(__builtin_amdgcn_ballot_w64(fl < (unsigned)(s + 2)) != 0ull, 0)) lds_wait_min<4>(L, W_RDY, s + 2, lane);
      FENCE();
      SCHED();
      rdx(s + 1, 0); rdw(s + 1, 0, 0); SCHED();
      MM(2, 1); rdw(s + 1, 0, 1); rdw(s + 1, 0, 2); if (refill) dma16(voB[0], pb, lb + (s % 3) * G1_STAGE); wl1(s, 4); SCHED();
      MM(2, 2); rdx(s + 1, 1); rdw(s + 1, 1, 0); if (refill) dma16o<1024>(voB[1], pb); wl1(s, 5); SCHED();
      MM(3, 0); rdw(s + 1, 1, 1); if (refill) dma16o<2048>(voB[2], pb); wl1(s, 6); SCHED();
      MM(3, 1); rdw(s + 1, 1, 2); wl1(s, 7); SCHED();
      MM(3, 2); wl1(s, 8);
      if (s == 6) { lds_wait_ge(L, W_F1ALL, 4); ld64c(gran, go, ws); ld16(bb[0], l31 * 2, b1 + ct1 * 192); ld16(bb[1], l31 * 2, b1 + ct1 * 192 + 64); ld16(bb[2], l31 * 2, b1 + ct1 * 192 + 128); }
      SCHED();
      if (s == 0) ST(4); if (s == 3) ST(5); if (s == 6) ST(7);
    }
#undef MM
#undef CS
    ST(11);
    const unsigned tlo = RFL(lw(L, W_TAG)), thi = RFL(lw(L, W_TAG + 4));
    {
      const float rs = __builtin_amdgcn_rcpf(bf2f(gran.x)) * 1.44269504088896341f;
      char* ab = ws + WS_ACT + (size_t)((rb1 * 8 + 2 * wi) * 48 + 3 * ct1) * 1024;
      const unsigned ao = (unsigned)((l31 >> 4) * 49152 + h * 256 + l15 * 16);
#pragma unroll
      for (int q = 0; q < 6; ++q) {
        unsigned pk[4];
#pragma unroll
        for (int e = 0; e < 4; ++e) {
          const f32x2 h2 = f32x2{acc1[q >> 1][8 * (q & 1) + 2 * e], acc1[q >> 1][8 * (q & 1) + 2 * e + 1]} * rs;
          const f32x2 s1 = f32x2{__builtin_amdgcn_exp2f(h2.x), __builtin_amdgcn_exp2f(h2.y)} + 1.0f;
          const f32x2 d = __builtin_elementwise_fma(s1, s1, f32x2{1.f, 1.f});
          const f32x2 rr = f32x2{__builtin_amdgcn_rcpf(d.x), __builtin_amdgcn_rcpf(d.y)};
          const f32x2 o = h2 * __builtin_elementwise_fma(rr, f32x2{-1.38629436111989062f, -1.38629436111989062f},
                                                         f32x2{0.69314718055994531f, 0.69314718055994531f});
          pk[e] = pk2(o.x, o.y);
        }
        st128c(ao + q * 512, ab, v4u{pk[0], pk[1], pk[2], pk[3]});
      }
    }
    lds_set(L, W_RDY + wi * 4, 12);
    ST(13);
    __builtin_amdgcn_s_setprio(2);
    LATE_LANE();
    v2u xq[5], dq[5], gr2, f3;
    const unsigned f2o = WS_F2 + (unsigned)((4 * rg + (a2 >> 1)) * 16 + 4 * wi + (ln_ & 3)) * 128;
    unsigned vm[4];
#pragma unroll
    for (int m = 0; m < 4; ++m) vm[m] = vo + ((m + wi) & 3) * 49152;
    v2u f2; v4u ysh;
    {
      f32x4 sacc[4];
#pragma unroll
      for (int i = 0; i < 4; ++i) sacc[i] = f32x4{0.f, 0.f, 0.f, 0.f};
      unsigned kb = CVS + KAO - wi * 128 - 3584 - 64, ubb = CVS + UBO - wi * 128 - 64; PIN(kb); PIN(ubb);
      LAS char* ka = L + kb; LAS char* ub = L + ubb;
      const bool late = RFL(lw(L, W_SB)) < 16;
      if (late) { waitv<0>(); arrive_flag<4>(L, W_F2C, lane, ws, WS_F2 + (unsigned)wid * 128, tlo, thi); }
      lds_wait_ge(L, W_SB, 16);
      if (par) conv_unit_shared<1>(ka + 64, ub + 64, sacc); else conv_unit_shared<0>(ka + 64, ub + 64, sacc);
      if (!late) { waitv<0>(); arrive_flag<4>(L, W_F2C, lane, ws, WS_F2 + (unsigned)wid * 128, tlo, thi); }
      ST(14);
      WL(4); WL(5);
      if (par) conv_unit_shared<1>(ka, ub, sacc); else conv_unit_shared<0>(ka, ub, sacc);
#pragma unroll
      for (int q = 0; q < 4; ++q) *(LAS f32x4*)(L + MXCH + (q * 4 + wi) * 1024 + vo) = sacc[q];
      lds_arrive(L, W_MB, lane);
    {
      const char* xb2 = x + (size_t)row2 * 1280 + cb2 * 160; const unsigned xo2 = OROW * 1280 + cg * 8;
      ld64o<0>(xq[0], xo2, xb2); ld64o<32>(xq[1], xo2, xb2); ld64o<64>(xq[2], xo2, xb2); ld64o<96>(xq[3], xo2, xb2); ld64o<128>(xq[4], xo2, xb2);
      const char* db = pDskip + cb2 * 160;
      ld64o<0>(dq[0], cg * 8, db); ld64o<32>(dq[1], cg * 8, db); ld64o<64>(dq[2], cg * 8, db); ld64o<96>(dq[3], cg * 8, db); ld64o<128>(dq[4], cg * 8, db);
      ld64c(gr2, WS_STATS + (row2 + OROW) * 8, ws);
    }
      lds_wait_ge(L, W_MB, 4);
      LAS char* p = L + MXCH + wi * 4096 + vo;
      const f32x4 s = add4(add4(*(LAS f32x4*)p, *(LAS f32x4*)(p + 1024)), add4(*(LAS f32x4*)(p + 2048), *(LAS f32x4*)(p + 3072)));
      ld64c(f2, f2o, ws);
      const unsigned y0 = pk2(s.x, s.y), y1 = pk2(s.z, s.w);
      *(LAS v2u*)(L + MXCH + wi * 4096 + YOFF) = v2u{y0, y1};
      FENCE();
      ysh = *(LAS v4u*)p;
    }
    ST(15);
    waitv<0>(); PIN(f2);
    f2 = spin64<false>(f2, f2o, ws, tlo, thi);
    lds_set(L, W_F2OK + wi * 4, 1);
    ST(16);
    AL(0); AL(1);
    if (ln_ < 32) st128c(vo, ws + WS_Y + (size_t)chS * 4096 + (2 * wi + ((wi & 1) ^ par)) * 512, ysh);
    f32x4 acc2[4][5];
    PIN(gr2);
    const float rs2 = __builtin_amdgcn_rcpf(bf2f(gr2.x)), m2 = -bfhi(gr2.x) * rs2;
#pragma unroll
    for (int n = 0; n < 5; ++n) {
      PIN(xq[n]); PIN(dq[n]);
      const f32x2 x0 = unpk(xq[n].x), x1 = unpk(xq[n].y), d0 = unpk(dq[n].x), d1 = unpk(dq[n].y);
      acc2[0][n] = f32x4{ffma(d0.x, ffma(x0.x, rs2, m2), x0.x), ffma(d0.y, ffma(x0.y, rs2, m2), x0.y),
                         ffma(d1.x, ffma(x1.x, rs2, m2), x1.x), ffma(d1.y, ffma(x1.y, rs2, m2), x1.y)};
      PIN(acc2[0][n]);
    }
    __builtin_amdgcn_s_setprio(0);
    ST(17);
    waitv<4>(); g2_step<1>(acc2, WF[0], AF[0], [&](int i) { if (i >= 3 && (i & 1)) AL1(2, (i - 3) >> 1); });
    waitv<4>();
    arrive_flag<8>(L, W_F3C, lane, ws, WS_F3 + (unsigned)(xcd * 32 + j) * 128, tlo, thi);
    g2_step<4>(acc2, WF[1], AF[1], [&](int i) { if (i >= 2) AL1(3 + ((i - 2) >> 2), (i - 2) & 3); });
    waitv<8>(); g2_step<4>(acc2, WF[2], AF[2], [&](int i) { if (i < 4) AL1(5, i); });
    ST(20);
    waitv<8>(); g2_step<4>(acc2, WF[3], AF[3], NOLD);
    waitv<4>(); g2_step<4>(acc2, WF[4], AF[4], NOLD);
    lds_wait_ge(L, W_F3C, 8);
    const unsigned f3o = WS_F3 + (unsigned)(cb2 * 32 + 8 * wi + (ln_ & 7)) * 128;
    ld64c(f3, f3o, ws);
    waitv<1>(); g2_step<4>(acc2, WF[5], AF[5], NOLD);
    ST(22);
#pragma unroll
    for (int m = 1; m < 4; ++m)
#pragma unroll
      for (int n = 0; n < 5; ++n) *(LAS f32x4*)(L + FM + wi * 15360 + (m - 1) * 5120 + n * 1024 + vo) = acc2[m][n];
    waitv<0>(); PIN(f3);
    f3 = spin64<false>(f3, f3o, ws, tlo, thi);
    lds_arrive(L, W_F3ALL, lane);
    lds_wait_ge(L, W_F3ALL, 4);
    ST(21);
    v4u yv[3];
    {
      const char* yb = ws + WS_Y + (size_t)cb2 * 80 * 4096 + row2 * 2;
      const unsigned yo = (unsigned)(ln_ >> 1) * 4096 + (ln_ & 1) * 16;
      ld128c<0>(yv[0], yo, yb); ld128c<0>(yv[1], yo, yb + 32 * 4096); ld128c<0>(yv[2], yo & 0xffffu, yb + 64 * 4096);
    }
#pragma unroll
    for (int n = 0; n < 5; ++n) {
#pragma unroll
      for (int d = 1; d < 4; ++d) acc2[0][n] = add4(acc2[0][n], *(LAS f32x4*)(L + FM + ((wi - d) & 3) * 15360 + (d - 1) * 5120 + n * 1024 + vo));
      *(LAS f32x4*)(L + FM + 61440 + wi * 5120 + n * 1024 + vo) = acc2[0][n];
    }
    waitv<0>(); PIN(yv[0]); PIN(yv[1]); PIN(yv[2]);
    LAS char* ys = L + YST + wi * 2560;
    *(LAS v4u*)(ys + vo) = yv[0]; *(LAS v4u*)(ys + 1024 + vo) = yv[1];
    if (ln_ < 32) *(LAS v4u*)(ys + 2048 + vo) = yv[2];
    lds_set(L, W_F2OK + wi * 4, 2);
    STEND();
    asm volatile("97:");
    __builtin_amdgcn_endpgm();
  }

  const bool odd = wi & 1;
  const int kch = chA + (wi >> 1);
  const unsigned vo = lane * 16;
  v4u WF[6][5], AF[6][4];
  waitv<6>();
  lds_arrive(L, W_ST, lane);
  waitv<0>();
  arrive_flag8(L, W_F1C, ws, WS_F1 + (unsigned)(lane & 7) * 2048 + wid * 8, tlo, thi);
  ST(8);
  auto reduce = [&](int c, int i) {
    LAS char* pp = L + KG_SCR + (c < 2 ? 4 * c : 3) * KG_WSTR + i * 16;
    const f32x4 s = add4(*(LAS f32x4*)pp, *(LAS f32x4*)(pp + (c < 2 ? 2 : 4) * KG_WSTR));
    *(LAS f32x4*)(L + KTAB + c * KP_STRIDE + i * 16) = s;
    return s;
  };
  lds_wait_ge(L, W_KG, 4);
  if (ts < 161) { st128c((unsigned)ts * 16, ws + WS_KP + (size_t)wid * KP_STRIDE, __builtin_bit_cast(v4u, reduce(2, ts))); reduce(0, ts); }
  if (ts >= 95) reduce(1, ts - 95);
  if (ts < 3) *(LAS float*)(L + KTAB + ts * KP_STRIDE - 4) = 0.f;
  lds_arrive(L, W_SB, lane);
  waitv<0>();
  arrive_flag(L, W_FKC, lane, ws, WS_FK + (unsigned)wid * 64, tlo, thi);
  ST(9);
  __builtin_amdgcn_s_setprio(0);
  lds_wait_min<4>(L, W_RDY, F1STAGE, lane);
  const unsigned f1o = WS_F1 + (unsigned)xcd * 2048 + ts * 8;
  v2u f1own; ld64c(f1own, f1o, ws);
  waitv<0>(); PIN(f1own);
  f1own = spin64<false>(f1own, f1o, ws, tlo, thi);
  v4u uu[3]; unsigned ulo[3];
#pragma unroll
  for (int r = 0; r < 3; ++r) {
    const int e = 64 * r + lane, tb = 64 * wi + e / 3, c = e % 3;
    ld128c<0>(uu[r], tb * 80 + c * 16, ws + WS_XNB + (size_t)(16 * xcd + (j >> 1)) * 20480 + par * 32);
    ulo[r] = (c == 2 - 2 * par ? CVS : CVA + (c - par) * CV_CH) + tb * 16;
  }
  if (b == 0) arrive_flag(L, W_F1ALL, lane, ws, WS_E, tlo, thi);
  else lds_arrive(L, W_F1ALL, lane);
  ST(4);
  const unsigned fko = WS_FK + (unsigned)(wid ^ 1) * 64;
  v2u fk; ld64c(fk, fko, ws);
  waitv<0>(); PIN(fk); PIN(uu[0]); PIN(uu[1]); PIN(uu[2]);
  fk = spin64<false>(fk, fko, ws, tlo, thi);
  v4u kp; ld128c<0>(kp, (unsigned)(ts < 161 ? ts : 160) * 16, ws + WS_KP + (size_t)(wid ^ 1) * KP_STRIDE);
  lds_wait_ge(L, W_SB, 8);
  waitv<0>(); PIN(kp);
  if (ts < 161) { LAS f32x4* tp = (LAS f32x4*)(L + KTAB + 2 * KP_STRIDE + ts * 16); *tp = add4(*tp, __builtin_bit_cast(f32x4, kp)); }
  lds_arrive(L, W_SB, lane);
  lds_wait_ge(L, W_F1ALL, 4);
  WL(0);
  lds_wait_min<4>(L, W_RDY, 9, lane);
  put_k(L, kix, ts, 0, CVA);
  WL(1);
  lds_wait_min<4>(L, W_RDY, 10, lane);
  put_k(L, kix, ts, 1, CVA + CV_CH);
  if (ZCOND) { *(LAS v4u*)(L + CVA + ZOFF) = v4u{0u, 0u, 0u, 0u}; *(LAS v4u*)(L + CVA + CV_CH + ZOFF) = v4u{0u, 0u, 0u, 0u}; }
  ST(7);
  lds_wait_min<4>(L, W_RDY, 11, lane);
  ST(11);
  put_u(L + ulo[0], uu[0], ts < 3); put_u(L + ulo[1], uu[1], false); put_u(L + ulo[2], uu[2], false);
  if (ZCOND) *(LAS v4u*)(L + CVS + ZOFF) = v4u{0u, 0u, 0u, 0u};
  lds_wait_ge(L, W_SB, 12);
  put_k(L, kix, ts, 2, CVS);
  lds_arrive(L, W_SB, lane);
  WL(2); WL(3); WL(4); WL(5);
  ST(5);
  lds_wait_ge(L, W_SB, 16);
  lds_wait_ge(L, W_RDY + wi * 4, 12);
  ST(13);
  __builtin_amdgcn_s_setprio(SPRIO2);
  LATE_LANE();
  f32x4 cacc[8];
#pragma unroll
  for (int i = 0; i < 8; ++i) cacc[i] = f32x4{0.f, 0.f, 0.f, 0.f};
  {
    const unsigned o1 = CVA + (wi >> 1) * CV_CH - (odd ? 4 * 64 : 0);
    unsigned kb = KAO + o1 - 3584 - 192, ubb = UBO + o1 - 192; PIN(kb); PIN(ubb);
    LAS char* ka = L + kb; LAS char* ub = L + ubb;
    v4u A[8], B[8];
#define LA(i, u) (*(LAS v4u*)(ka + 512 * (7 - (i)) + 64 * (3 - (u))))
#define LB(i, u) (*(LAS v4u*)(ub + 512 * (i) + 64 * (3 - (u))))
    A[7] = LA(7, 0); B[0] = LB(0, 0); A[0] = LA(0, 0); B[7] = LB(7, 0); A[6] = LA(6, 0); B[6] = LB(6, 0); A[1] = LA(1, 0); B[1] = LB(1, 0);
    A[5] = LA(5, 0); B[5] = LB(5, 0); A[2] = LA(2, 0); B[2] = LB(2, 0); A[4] = LA(4, 0); B[4] = LB(4, 0); A[3] = LA(3, 0); B[3] = LB(3, 0);
    SCHED();
#pragma unroll
    for (int u = 0; u < 4; ++u) {
      v4u An[2], Bn[2];
#pragma unroll
      for (int sd = 7; sd >= 0; --sd) {
#pragma unroll
        for (int o = 0; o <= sd; ++o) {
          if (sd + o < 8) cacc[sd + o] = mfma(A[sd], B[o], cacc[sd + o]);
          if (o != sd && sd + o < 8) cacc[sd + o] = mfma(A[o], B[sd], cacc[sd + o]);
        }
        SCHED();
        if (u < 3) {
          if (sd >= 2) { A[sd] = LA(sd, u + 1); B[sd] = LB(sd, u + 1); }
          if (sd == 6 || sd == 5) { An[6 - sd] = LA(6 - sd, u + 1); Bn[6 - sd] = LB(6 - sd, u + 1); }
        }
        SCHED();
      }
      if (u < 3) { A[0] = An[0]; A[1] = An[1]; B[0] = Bn[0]; B[1] = Bn[1]; }
    }
#undef LA
#undef LB
  }
  ST(10);
  unsigned vm[4];
  {
    LAS char* px = L + SXCH + wi * 4096 + vo;
    if (odd) {
#pragma unroll
      for (int i = 0; i < 4; ++i) { *(LAS f32x4*)(px + i * 1024) = cacc[i]; cacc[i] = cacc[4 + i]; }
    } else {
#pragma unroll
      for (int i = 0; i < 4; ++i) *(LAS f32x4*)(px + i * 1024) = cacc[4 + i];
    }
    lds_arrive(L, W_SX + (wi >> 1) * 4, lane);
#pragma unroll
    for (int m = 0; m < 4; ++m) vm[m] = vo + ((m + wi) & 3) * 49152;
    lds_wait_ge(L, W_SX + (wi >> 1) * 4, 2);
    px = L + SXCH + (wi ^ 1) * 4096 + vo;
    v2u yo[4];
#pragma unroll
    for (int i = 0; i < 4; ++i) { const f32x4 s = add4(cacc[i], *(LAS f32x4*)(px + i * 1024)); yo[i] = v2u{pk2(s.x, s.y), pk2(s.z, s.w)}; }
    char* yb = ws + WS_Y + (size_t)kch * 4096 + (odd ? 2048 : 0);
    LAS char* yl = L + SXCH + (wi ^ 1) * 4096;
#pragma unroll
    for (int i = 0; i < 4; ++i) *(LAS v2u*)(yl + i * 512 + YOFF) = yo[i];
    FENCE();
    st128c(vo, yb, *(LAS v4u*)(yl + vo)); st128c(vo, yb + 1024, *(LAS v4u*)(yl + 1024 + vo));
  }
  ST(14);
  lds_wait_ge(L, W_F2OK + wi * 4, 1);
  ST(16);
  AL(0); AL(1);
  ST(17);
  f32x4 acc2[4][5];
  waitv<8>();
  arrive_flag<8>(L, W_F3C, lane, ws, WS_F3 + (unsigned)(xcd * 32 + j) * 128, tlo, thi);
  __builtin_amdgcn_s_setprio(0);
  waitv<4>(); g2_step<0>(acc2, WF[0], AF[0], [&](int i) { if (i >= 3 && (i & 1)) AL1(2, (i - 3) >> 1); });
  waitv<4>(); g2_step<4>(acc2, WF[1], AF[1], [&](int i) { if (i >= 2) AL1(3 + ((i - 2) >> 2), (i - 2) & 3); });
  waitv<8>(); g2_step<4>(acc2, WF[2], AF[2], [&](int i) { if (i < 4) AL1(5, i); });
  ST(20);
  waitv<8>(); g2_step<4>(acc2, WF[3], AF[3], NOLD);
  lds_wait_ge(L, W_F3C, 8);
  waitv<0>();
#pragma unroll
  for (int m = 0; m < 4; ++m) { PIN(AF[4][m]); PIN(AF[5][m]); }
#pragma unroll
  for (int n = 0; n < 5; ++n) { PIN(WF[4][n]); PIN(WF[5][n]); }
#pragma unroll
  for (int mm = 1; mm < 5; ++mm) {
    const int m = mm & 3;
#pragma unroll
    for (int n = 0; n < 5; ++n) {
      acc2[m][n] = mfma(WF[4][n], AF[4][m], acc2[m][n]);
      acc2[m][n] = mfma(WF[5][n], AF[5][m], acc2[m][n]);
      if (mm > 1) *(LAS f32x4*)(L + FS + wi * 15360 + (mm - 2) * 5120 + n * 1024 + vo) = acc2[mm - 1][n];
      SCHED();
    }
  }
  lds_arrive(L, W_FS, lane);
  ST(22);
  lds_wait_ge(L, W_FS, 4);
#pragma unroll
  for (int d = 1; d < 4; ++d)
#pragma unroll
    for (int n = 0; n < 5; ++n) acc2[0][n] += *(LAS f32x4*)(L + FS + ((wi - d) & 3) * 15360 + (d - 1) * 5120 + n * 1024 + vo);
  ST(23);
  lds_wait_ge(L, W_F2OK + wi * 4, 2);
  ST(24);
  {
    char* ob = out + (size_t)row2 * 1280 + cb2 * 160; const unsigned oo = OROW * 1280 + cg * 8;
    v2u o[5];
#pragma unroll
    for (int n = 0; n < 5; ++n) {
      const v2u t = trread(L + YST + wi * 2560 + n * 512 + ln_ * 8);
      const f32x4 s = acc2[0][n] + *(LAS f32x4*)(L + FM + 61440 + wi * 5120 + n * 1024 + vo);
      const f32x2 s0 = s.xy + unpk(t.x), s1 = s.zw + unpk(t.y);
      o[n] = v2u{pk2(s0.x, s0.y), pk2(s1.x, s1.y)};
    }
    st64o<0>(oo, ob, o[0]); st64o<32>(oo, ob, o[1]); st64o<64>(oo, ob, o[2]); st64o<96>(oo, ob, o[3]); st64o<128>(oo, ob, o[4]);
  }
  STEND();
  asm volatile("99:");
}

extern "C" void kernel_launch(void* const* d_in, const int* in_sizes, int n_in, void* d_out, int out_size, void* d_ws, size_t ws_size,
                              hipStream_t stream) {
  fused_s4_mlp<<<256, 512, 0, stream>>>((const char*)d_in[0], (const char*)d_in[1], (const char*)d_in[2], (const char*)d_in[3],
      (const char*)d_in[4], (const char*)d_in[5], (const char*)d_in[6], (const char*)d_in[7], (const char*)d_in[8], (const char*)d_in[10],
      (const char*)d_in[11], (const char*)d_in[12], (char*)d_out, (char*)d_ws);
}
```

```cpp
#include <hip/hip_runtime.h>
#include <stdint.h>

typedef unsigned short u16;
typedef unsigned long long u64;
typedef unsigned v4u __attribute__((ext_vector_type(4)));
typedef unsigned v2u __attribute__((ext_vector_type(2)));
typedef float f32x4 __attribute__((ext_vector_type(4)));
typedef float f32x2 __attribute__((ext_vector_type(2)));
typedef short bf16x8 __attribute__((ext_vector_type(8)));
typedef short s16x4 __attribute__((ext_vector_type(4)));
typedef __bf16 bf16x2_t __attribute__((ext_vector_type(2)));
#define LAS __attribute__((address_space(3)))
#define GAS __attribute__((address_space(1)))
#define DI __device__ __forceinline__

constexpr unsigned WS_E = 0, WS_TMO = 64, WS_F1 = 131072, WS_STATS = 16384, WS_F2 = 32768, WS_F3 = 65536, WS_FK = 98304, WS_STAMP = 262144;
constexpr unsigned WS_XNB = 1u << 20, WS_W2T = 4u << 20, WS_Y = 7u << 20, WS_ACT = 10u << 20, WS_KP = 17u << 20;
constexpr unsigned KP_STRIDE = 2624;
constexpr unsigned SPIN_CAP = 400000;

constexpr unsigned W_F1C = 0, W_F2C = 4, W_F3C = 8, W_KG = 12, W_MB = 16, W_SB = 20, W_SX = 24, W_FM = 32, W_FS = 36, W_F3ALL = 40, W_F1ALL = 44,
                   W_F2OK = 48, W_RDY = 64, W_TAG = 80, W_FKC = 96, W_ST = 100;
constexpr unsigned KTAB = 272;
constexpr unsigned CSP = 8192;
constexpr unsigned CVA = 10240, CV_CH = 27008, CV_USTR = 4640, CV_KOFF = 18560, CV_KSTR = 4224;
constexpr unsigned KG_SCR = CVA, KG_WSTR = 4352, LN_ROWS = KG_SCR + 8 * KG_WSTR, LN_PITCH = 1344, W2P = LN_ROWS + 10752;
constexpr unsigned G1_RING = 64512, G1_STAGE = 28672, G1_BOFF = 16384;
constexpr unsigned CVS = G1_RING, SXCH = CVS + 27136, MXCH = SXCH + 16384;
constexpr unsigned CSF = G1_RING + 3 * G1_STAGE, YST = CSF + 3072;
constexpr unsigned FM = 256, FS = FM + 81920;
static_assert(FS + 61440 <= CSF && CVA + 2 * CV_CH <= G1_RING && W2P + 8192 <= G1_RING && YST + 10240 <= 163840 && MXCH + 16384 <= CSF, "lds map");

#define ROT2 0
#define F1STAGE 5
#define SPRIO2 1
#define F1GAP 16
#define F2GAP 8
#define SPRIO 1
#define STAMP
#define ST(i) do { if ((i) == 0) { stv0 = __builtin_amdgcn_s_memrealtime(); stv1 = stv0; } if ((i) == 1 && (w < 4)) { asm volatile("s_memrealtime %0\n\ts_waitcnt lgkmcnt(0)" : "=s"(stv1) :: "memory"); } } while (0)
#define STEND() do { while (__builtin_amdgcn_s_memrealtime() - stv1 < 4000) __builtin_amdgcn_s_sleep(2); } while (0)

DI float bf2f(unsigned v) { return __builtin_bit_cast(float, v << 16); }
DI float bfhi(unsigned v) { return __builtin_bit_cast(float, v & 0xffff0000u); }
DI unsigned pk2(float lo, float hi) { bf16x2_t r; r.x = (__bf16)lo; r.y = (__bf16)hi; return __builtin_bit_cast(unsigned, r); }
DI float rbf(float f) { return bf2f(pk2(f, f) & 0xffffu); }
DI f32x4 mfma(v4u a, v4u b, f32x4 c) {
  return __builtin_amdgcn_mfma_f32_16x16x32_bf16(__builtin_bit_cast(bf16x8, a), __builtin_bit_cast(bf16x8, b), c, 0, 0, 0);
}
DI v2u trread(LAS char* p) { return __builtin_bit_cast(v2u, __builtin_amdgcn_ds_read_tr16_b64_v4i16((LAS s16x4*)p)); }
template <int N> DI void waitv() { asm volatile("s_waitcnt vmcnt(%0)" ::"i"(N) : "memory"); }
DI void bar() { asm volatile("s_waitcnt lgkmcnt(0)\n\ts_barrier" ::: "memory"); }
#define PIN(x) asm volatile("" : "+v"(x))
#define FENCE() asm volatile("" ::: "memory")
#define SCHED() __builtin_amdgcn_sched_barrier(0)
#define RFL(x) __builtin_amdgcn_readfirstlane(x)

DI void ld16(unsigned& d, unsigned vo, const void* sb) { asm volatile("global_load_ushort %0, %1, %2" : "=v"(d) : "v"(vo), "s"(sb) : "memory"); }
template <int O> DI void ld32(unsigned& d, unsigned vo, const void* sb) { asm volatile("global_load_dword %0, %1, %2 offset:%3" : "=v"(d) : "v"(vo), "s"(sb), "i"(O) : "memory"); }
DI void ld64(v2u& d, unsigned vo, const void* sb) { asm volatile("global_load_dwordx2 %0, %1, %2" : "=v"(d) : "v"(vo), "s"(sb) : "memory"); }
DI void ld128(v4u& d, unsigned vo, const void* sb) { asm volatile("global_load_dwordx4 %0, %1, %2" : "=v"(d) : "v"(vo), "s"(sb) : "memory"); }
DI void ld64c(v2u& d, unsigned vo, const void* sb) { asm volatile("global_load_dwordx2 %0, %1, %2 sc1" : "=v"(d) : "v"(vo), "s"(sb) : "memory"); }
template <int O> DI void ld128c(v4u& d, unsigned vo, const void* sb) { asm volatile("global_load_dwordx4 %0, %1, %2 offset:%3 sc1" : "=v"(d) : "v"(vo), "s"(sb), "i"(O) : "memory"); }
DI void st64c(unsigned vo, void* sb, v2u d) { asm volatile("global_store_dwordx2 %0, %1, %2 sc1" ::"v"(vo), "v"(d), "s"(sb) : "memory"); }
template <int O> DI void ld64o(v2u& d, unsigned vo, const void* sb) { asm volatile("global_load_dwordx2 %0, %1, %2 offset:%3" : "=v"(d) : "v"(vo), "s"(sb), "i"(O) : "memory"); }
template <int O> DI void st64o(unsigned vo, void* sb, v2u d) { asm volatile("global_store_dwordx2 %0, %1, %2 offset:%3" ::"v"(vo), "v"(d), "s"(sb), "i"(O) : "memory"); }
template <int O> DI void st64co(unsigned vo, void* sb, v2u d) { asm volatile("global_store_dwordx2 %0, %1, %2 offset:%3 sc1" ::"v"(vo), "v"(d), "s"(sb), "i"(O) : "memory"); }
DI void st128c(unsigned vo, void* sb, v4u d) { asm volatile("global_store_dwordx4 %0, %1, %2 sc1\n\ts_nop 1" ::"v"(vo), "v"(d), "s"(sb) : "memory"); }
DI void st64u(unsigned vo, void* sb, v2u d) { asm volatile("global_store_dwordx2 %0, %1, %2 sc1 ; CNT" ::"v"(vo), "v"(d), "s"(sb) : "memory"); }
DI void st128u(unsigned vo, void* sb, v4u d) { asm volatile("global_store_dwordx4 %0, %1, %2 sc1 ; CNT\n\ts_nop 1" ::"v"(vo), "v"(d), "s"(sb) : "memory"); }
DI void dma16(unsigned vo, const void* sb, unsigned lds) {
  asm volatile("s_mov_b32 m0, %2\n\ts_nop 0\n\tglobal_load_lds_dwordx4 %0, %1" ::"v"(vo), "s"(sb), "s"(lds) : "memory");
}
template <int O> DI void dma16o(unsigned vo, const void* sb) {
  asm volatile("global_load_lds_dwordx4 %0, %1 offset:%2" ::"v"(vo), "s"(sb), "i"(O) : "memory");
}
template <bool HIONLY> DI v2u spin64(v2u cur, unsigned vo, char* ws, unsigned tlo, unsigned thi) {
  if (__builtin_expect(__builtin_amdgcn_ballot_w64(HIONLY ? (cur.y != tlo) : (cur.x != tlo || cur.y != thi)) != 0ull, 0)) {
#pragma nounroll
    for (unsigned spins = 0; spins < SPIN_CAP; ++spins) {
      __builtin_amdgcn_s_sleep(6);
      asm volatile("global_load_dwordx2 %0, %1, %2 sc1\n\ts_waitcnt vmcnt(0)" : "=&v"(cur) : "v"(vo), "s"(ws) : "memory");
      if (__builtin_amdgcn_ballot_w64(HIONLY ? (cur.y != tlo) : (cur.x != tlo || cur.y != thi)) == 0ull) break;
    }
  }
  return cur;
}
DI unsigned lw(LAS char* L, unsigned off) { return *(volatile LAS unsigned*)(L + off); }
DI void lds_wait_at(unsigned a, unsigned target) {
  unsigned v, cnt; u64 t;
  asm volatile("s_mov_b32 %1, %5\n"
               "1:\n\tds_read_b32 %0, %3\n\ts_sub_u32 %1, %1, 1\n\ts_waitcnt lgkmcnt(0)\n\tv_cmp_gt_u32 vcc, %4, %0\n\t"
               "s_cselect_b64 %2, 0, vcc\n\ts_cmp_lg_u64 %2, 0\n\ts_cbranch_scc1 1b"
               : "=&v"(v), "=&s"(cnt), "=&s"(t) : "v"(a), "s"(target), "i"(4 * SPIN_CAP) : "vcc", "scc", "memory");
}
DI void lds_wait_ge(LAS char* L, unsigned off, unsigned target) { lds_wait_at((unsigned)(size_t)L + off, target); }
template <int N> DI void lds_wait_min(LAS char* L, unsigned off, unsigned target, int lane) {
  lds_wait_at((unsigned)(size_t)L + off + (lane & (N - 1)) * 4, target);
}
DI void lds_set(LAS char* L, unsigned off, unsigned v) { FENCE(); *(volatile LAS unsigned*)(L + off) = v; FENCE(); }
DI void lds_arrive(LAS char* L, unsigned off, int) {
  asm volatile("s_mov_b64 exec, 1\n\tds_add_u32 %0, %1\n\ts_mov_b64 exec, -1" ::"v"((unsigned)(size_t)L + off), "v"(1u) : "memory");
}
template <unsigned N = 4> DI void arrive_flag(LAS char* L, unsigned off, int, char* ws, unsigned fo, unsigned tlo, unsigned thi) {
  unsigned old;
  asm volatile("s_mov_b64 exec, 1\n\tds_add_rtn_u32 %0, %1, %2\n\ts_waitcnt lgkmcnt(0)\n\tv_cmp_eq_u32 vcc, %6, %0\n\t"
               "s_mov_b64 exec, vcc\n\tglobal_store_dwordx2 %3, %4, %5 sc1\n\ts_mov_b64 exec, -1"
               : "=&v"(old) : "v"((unsigned)(size_t)L + off), "v"(1u), "v"(fo), "v"(v2u{tlo, thi}), "s"(ws), "i"(N - 1) : "vcc", "memory");
}
DI void arrive_flag8(LAS char* L, unsigned off, char* ws, unsigned fo, unsigned tlo, unsigned thi) {
  unsigned old;
  asm volatile("s_mov_b64 exec, 1\n\tds_add_rtn_u32 %0, %1, %2\n\ts_waitcnt lgkmcnt(0)\n\tv_cmp_eq_u32 vcc, 3, %0\n\t"
               "s_mul_i32 exec_lo, vcc_lo, 0xff\n\tglobal_store_dwordx2 %3, %4, %5 sc1\n\ts_mov_b64 exec, -1"
               : "=&v"(old) : "v"((unsigned)(size_t)L + off), "v"(1u), "v"(fo), "v"(v2u{tlo, thi}), "s"(ws) : "vcc", "memory");
}
typedef float f32x16 __attribute__((ext_vector_type(16)));
DI f32x16 mfma32(v4u a, v4u b, f32x16 c) {
  return __builtin_amdgcn_mfma_f32_32x32x16_bf16(__builtin_bit_cast(bf16x8, a), __builtin_bit_cast(bf16x8, b), c, 0, 0, 0);
}
template <int C> DI float dpp_add(float v) {
  return v + __builtin_bit_cast(float, __builtin_amdgcn_update_dpp(0, __builtin_bit_cast(int, v), C, 0xf, 0xf, true));
}
DI float rl(float v, int l) { return __builtin_bit_cast(float, __builtin_amdgcn_readlane(__builtin_bit_cast(int, v), l)); }
DI float half_sum(float v, bool upper) {
  v = dpp_add<0xB1>(v); v = dpp_add<0x4E>(v); v = dpp_add<0x141>(v); v = dpp_add<0x140>(v);
  const float a = rl(v, 0) + rl(v, 16), c = rl(v, 32) + rl(v, 48);
  return upper ? c : a;
}
DI f32x2 unpk(unsigned v) { return f32x2{bf2f(v), bfhi(v)}; }
struct c2 { f32x2 r, i; };
DI c2 cmul(c2 a, c2 b) { return c2{__builtin_elementwise_fma(a.r, b.r, -(a.i * b.i)), __builtin_elementwise_fma(a.r, b.i, a.i * b.r)}; }
DI c2 csq(c2 v) { return c2{__builtin_elementwise_fma(v.r, v.r, -(v.i * v.i)), (v.r + v.r) * v.i}; }
DI c2 cexpk(f32x2 e1, f32x2 th, f32x2 tl, float k, float ks) {
  const f32x2 e = e1 * k, t = th * ks;
  const f32x2 mag = f32x2{__builtin_amdgcn_exp2f(e.x), __builtin_amdgcn_exp2f(e.y)};
  const f32x2 fr = __builtin_elementwise_fma(tl, f32x2{ks, ks}, f32x2{__builtin_amdgcn_fractf(t.x), __builtin_amdgcn_fractf(t.y)});
  return c2{f32x2{__builtin_amdgcn_cosf(fr.x), __builtin_amdgcn_cosf(fr.y)} * mag, f32x2{__builtin_amdgcn_sinf(fr.x), __builtin_amdgcn_sinf(fr.y)} * mag};
}
typedef _Float16 f16x8 __attribute__((ext_vector_type(8)));
typedef _Float16 f16x2 __attribute__((ext_vector_type(2)));
DI f32x4 mfmah(v4u a, v4u b, f32x4 c) {
  return __builtin_amdgcn_mfma_f32_16x16x32_f16(__builtin_bit_cast(f16x8, a), __builtin_bit_cast(f16x8, b), c, 0, 0, 0);
}
DI unsigned pkh1(float a, float b) { f16x2 r; r.x = (_Float16)a; r.y = (_Float16)b; return __builtin_bit_cast(unsigned, r); }
DI v2u pkh(c2 v) { return v2u{pkh1(v.r.x, v.i.x), pkh1(v.r.y, v.i.y)}; }
DI v2u pkb(c2 v) { return v2u{pk2(v.r.x, v.i.x), pk2(v.r.y, v.i.y)}; }
#define HMUL(o, i, w) "v_pk_mul_f16 " o ", " i ", " w " op_sel:[1,1] op_sel_hi:[1,0] neg_lo:[0,1]\n\t"
#define HFMA(o, i, w) "v_pk_fma_f16 " o ", " i ", " w ", " o " op_sel_hi:[0,1,1]\n\t"
constexpr int KROW = 528;
template <int R0, int R1> DI void h2(v2u& o0, v2u& o1, v2u i0, v2u i1, v2u w0, v2u w1) {
  unsigned a, b, c, d;
  asm volatile(HMUL("%0", "%4", "%8") HMUL("%1", "%5", "%9") HMUL("%2", "%6", "%10") HMUL("%3", "%7", "%11")
               HFMA("%0", "%4", "%8") HFMA("%1", "%5", "%9") HFMA("%2", "%6", "%10") HFMA("%3", "%7", "%11")
               "ds_write_addtid_b32 %0 offset:%12\n\tds_write_addtid_b32 %1 offset:%13\n\tds_write_addtid_b32 %2 offset:%14\n\tds_write_addtid_b32 %3 offset:%15"
               : "=&v"(a), "=&v"(b), "=&v"(c), "=&v"(d)
               : "v"(i0.x), "v"(i0.y), "v"(i1.x), "v"(i1.y), "v"(w0.x), "v"(w0.y), "v"(w1.x), "v"(w1.y),
                 "i"(R0 * KROW), "i"(R0 * KROW + 256), "i"(R1 * KROW), "i"(R1 * KROW + 256) : "memory");
  o0 = v2u{a, b}; o1 = v2u{c, d};
}
template <int R> DI void hput(v2u v) {
  asm volatile("ds_write_addtid_b32 %0 offset:%2\n\tds_write_addtid_b32 %1 offset:%3" ::"v"(v.x), "v"(v.y), "i"(R * KROW), "i"(R * KROW + 256) : "memory");
}
template <int C> DI unsigned dpp_maxu(unsigned v) { return max(v, (unsigned)__builtin_amdgcn_update_dpp(0, (int)v, C, 0xf, 0xf, true)); }
DI void rd8(v4u (&f)[8], LAS char* sr) {
#pragma unroll
  for (int k = 0; k < 8; ++k) f[k] = *(LAS v4u*)(sr + 64 * k);
}
template <bool H> DI void kmm(f32x4 (&ka)[2][2][2], int nh, const v4u (&UA)[2][8], const v4u (&vb)[8]) {
#pragma unroll
  for (int k = 0; k < 8; ++k)
#pragma unroll
    for (int m = 0; m < 2; ++m) {
      f32x4& a = ka[(k >> 1) & 1][m][nh];
      const f32x4 c = (k == 0 || k == 2) ? f32x4{0.f, 0.f, 0.f, 0.f} : a;
      a = H ? mfmah(UA[m][k], vb[k], c) : mfma(UA[m][k], vb[k], c);
    }
}
DI void kgen_slow(c2 a, c2 W1, c2 W256, c2 W8, LAS char* sr, f32x4 (&ka)[2][2][2]) {
  v4u UA[2][8], vb[8];
  c2 u[8]; u[0] = a;
#pragma unroll
  for (int i = 1; i < 8; ++i) u[i] = cmul(u[i - 1], W1);
  const c2 W512 = csq(W256), W1024 = csq(W512), r8 = cmul(a, W256), r10 = cmul(u[4], W256), r12 = cmul(a, W512), r14 = cmul(r8, W512), r16 = cmul(a, W1024);
  hput<0>(pkb(u[0])); hput<1>(pkb(u[1])); hput<2>(pkb(u[2])); hput<3>(pkb(u[3])); hput<4>(pkb(u[4])); hput<5>(pkb(u[5])); hput<6>(pkb(u[6])); hput<7>(pkb(u[7]));
  hput<8>(pkb(r8)); hput<9>(pkb(cmul(u[2], W256))); hput<10>(pkb(r10)); hput<11>(pkb(cmul(u[6], W256)));
  hput<12>(pkb(r12)); hput<13>(pkb(cmul(u[4], W512))); hput<14>(pkb(r14)); hput<15>(pkb(cmul(r10, W512)));
  rd8(UA[0], sr);
  hput<0>(pkb(r16)); hput<1>(pkb(cmul(r8, W1024))); hput<2>(pkb(cmul(r12, W1024))); hput<3>(pkb(cmul(r14, W1024))); hput<4>(pkb(cmul(r16, W1024)));
  rd8(UA[1], sr);
  c2 v = c2{f32x2{1.f, 1.f}, f32x2{0.f, 0.f}};
#pragma unroll
  for (int nh = 0; nh < 2; ++nh) {
#define VR(r) hput<r>(pkb(v)); v = cmul(v, W8);
    VR(0) VR(1) VR(2) VR(3) VR(4) VR(5) VR(6) VR(7) VR(8) VR(9) VR(10) VR(11) VR(12) VR(13) VR(14) VR(15)
#undef VR
    rd8(vb, sr);
    kmm<false>(ka, nh, UA, vb);
  }
}
DI void kgen(const unsigned (&pr)[7], bool dead, bool upper, unsigned sw, LAS char* sr, f32x4 (&ka)[2][2][2], float (&scale)[2]) {
  const float dt = rbf(__builtin_amdgcn_exp2f(bf2f(pr[6]) * 1.44269504088896341f));
  const f32x2 zr = unpk(pr[0]) * dt, zi = unpk(pr[1]) * dt, dbr = unpk(pr[2]) * dt, dbi = unpk(pr[3]) * dt;
  const f32x2 cr = unpk(dead ? 0u : pr[4]), ci = unpk(dead ? 0u : pr[5]);
  const c2 a = c2{__builtin_elementwise_fma(cr, dbr, -(ci * dbi)), -__builtin_elementwise_fma(cr, dbi, ci * dbr)};
  const f32x2 e1 = zr * 1.44269504088896341f, th = zi * 0.15915494309189535f;
  const f32x2 tl = __builtin_elementwise_fma(zi, f32x2{6.4206383e-9f, 6.4206383e-9f}, __builtin_elementwise_fma(zi, f32x2{0.15915494309189535f, 0.15915494309189535f}, -th));
  const c2 W1 = cexpk(e1, th, tl, 1.f, -1.f), W256 = cexpk(e1, th, tl, 256.f, -256.f), W8 = cexpk(e1, th, tl, 8.f, 8.f);
  asm volatile("s_mov_b32 m0, %0\n\ts_nop 0" ::"s"(sw) : "memory");
  if (__builtin_expect(__builtin_amdgcn_ballot_w64(!(zr.x <= 0.f && zr.y <= 0.f)) != 0ull, 0)) {
    kgen_slow(a, W1, W256, W8, sr, ka);
    scale[0] = scale[1] = 1.f;
    return;
  }
  unsigned am = max(max(__builtin_bit_cast(unsigned, a.r.x) & 0x7fffffffu, __builtin_bit_cast(unsigned, a.r.y) & 0x7fffffffu),
           max(__builtin_bit_cast(unsigned, a.i.x) & 0x7fffffffu, __builtin_bit_cast(unsigned, a.i.y) & 0x7fffffffu));
  am = dpp_maxu<0xB1>(am); am = dpp_maxu<0x4E>(am); am = dpp_maxu<0x141>(am); am = dpp_maxu<0x140>(am);
  const unsigned ex0 = min(max(max((unsigned)__builtin_amdgcn_readlane(am, 0), (unsigned)__builtin_amdgcn_readlane(am, 16)) >> 23, 2u), 252u);
  const unsigned ex1 = min(max(max((unsigned)__builtin_amdgcn_readlane(am, 32), (unsigned)__builtin_amdgcn_readlane(am, 48)) >> 23, 2u), 252u);
  scale[0] = __builtin_bit_cast(float, ex0 << 23); scale[1] = __builtin_bit_cast(float, ex1 << 23);
  const float down = __builtin_bit_cast(float, ((254u - (upper ? ex1 : ex0)) << 23));
  const c2 W2 = csq(W1), W512 = csq(W256), W16 = csq(W8), W4 = csq(W2), W1024 = csq(W512), W32 = csq(W16), W64 = csq(W32), W128 = csq(W64);
  const v2u u0 = pkh(c2{a.r * down, a.i * down}), h1 = pkh(W1), h2_ = pkh(W2), h4 = pkh(W4), h256 = pkh(W256), h512 = pkh(W512), h1024 = pkh(W1024);
  v2u u2, u4, u6, r8, r10, r12, r14, r16, t0, t1;
  v4u UA[2][8], vb[8];
  hput<0>(u0);
  h2<2, 4>(u2, u4, u0, u0, h2_, h4); h2<8, 12>(r8, r12, u0, u0, h256, h512); h2<1, 3>(t0, t1, u0, u2, h1, h1); h2<5, 6>(t0, u6, u4, u4, h1, h2_);
  h2<10, 14>(r10, r14, r8, r8, h4, h512); h2<7, 9>(t0, t1, u6, r8, h1, h2_); h2<11, 13>(t0, t1, r10, r12, h2_, h4);
  h2<15, 15>(r16, t1, u0, r14, h1024, h4);
  rd8(UA[0], sr);
  hput<0>(r16);
  h2<1, 2>(t0, t1, r8, r12, h1024, h1024); h2<3, 4>(t0, t1, r14, r16, h1024, h1024);
  rd8(UA[1], sr);
  v2u v[16];
  v[1] = pkh(W8); v[2] = pkh(W16); v[4] = pkh(W32); v[8] = pkh(W64);
  const v2u h128 = pkh(W128);
  hput<0>(v2u{0x3c00u, 0x3c00u}); hput<1>(v[1]); hput<2>(v[2]); hput<4>(v[4]); hput<8>(v[8]);
  h2<3, 5>(v[3], v[5], v[2], v[4], v[1], v[1]); h2<6, 9>(v[6], v[9], v[4], v[8], v[2], v[1]); h2<7, 10>(v[7], v[10], v[4], v[8], v[3], v[2]);
  h2<11, 12>(v[11], v[12], v[8], v[8], v[3], v[4]); h2<13, 14>(v[13], v[14], v[8], v[8], v[5], v[6]); h2<15, 15>(t0, v[15], v[1], v[8], h128, v[7]);
  rd8(vb, sr);
  hput<0>(h128); hput<1>(t0);
  h2<2, 3>(t0, t1, v[2], v[3], h128, h128); h2<4, 5>(t0, t1, v[4], v[5], h128, h128);
  kmm<true>(ka, 0, UA, vb);
  h2<6, 7>(t0, t1, v[6], v[7], h128, h128); h2<8, 9>(t0, t1, v[8], v[9], h128, h128); h2<10, 11>(t0, t1, v[10], v[11], h128, h128);
  h2<12, 13>(t0, t1, v[12], v[13], h128, h128); h2<14, 15>(t0, t1, v[14], v[15], h128, h128);
  rd8(vb, sr);
  SCHED();
  kmm<true>(ka, 1, UA, vb);
}
DI void kgen_store(LAS float* pt, const f32x4 (&kacc)[2][2], int lane) {
  const int l15 = lane & 15, g = lane >> 4;
  const int sh = g < 2 ? 3 : 4 - g;
  const int a0 = (l15 << sh) + (g < 2 ? 4 * g : 128 * g);
  const int a1 = g < 3 ? a0 + 2 : a0 + 64;
#pragma unroll
  for (int nh = 0; nh < 2; ++nh) {
    *(LAS f32x2*)(pt + a0 + ((16 * nh) << sh)) = kacc[0][nh].xy;
    *(LAS f32x2*)(pt + a1 + ((16 * nh) << sh)) = kacc[0][nh].zw;
  }
  if (g == 0) {
#pragma unroll
    for (int nh = 0; nh < 2; ++nh)
#pragma unroll
      for (int r = 0; r < 4; ++r) pt[512 + 32 * r + 16 * nh + l15] = kacc[1][nh][r];
  }
  if (lane == 16) { *(LAS f32x4*)(pt + 640) = f32x4{kacc[1][0][0], 0.f, 0.f, 0.f}; }
}

template <int PAR> DI void conv_unit_shared(LAS char* ka, LAS char* ub, f32x4 (&acc)[4]) {
  constexpr int T[4] = {PAR ? 1 : 0, PAR ? 2 : 3, PAR ? 5 : 4, PAR ? 6 : 7};
  v4u A[8], B[8];
#pragma unroll
  for (int i = 0; i <= T[3]; ++i) { A[i] = *(LAS v4u*)(ka + 512 * (7 - i)); B[i] = *(LAS v4u*)(ub + 512 * i); }
  SCHED();
#pragma unroll
  for (int q = 0; q < 4; ++q)
#pragma unroll
    for (int ip = 0; ip <= T[q]; ++ip) acc[q] = mfma(A[T[q] - ip], B[ip], acc[q]);
  SCHED();
}
DI float fadd(float a, float b) { float r; asm("v_add_f32 %0, %1, %2" : "=v"(r) : "v"(a), "v"(b)); return r; }
DI float ffma(float a, float b, float c) { float r; asm("v_fma_f32 %0, %1, %2, %3" : "=v"(r) : "v"(a), "v"(b), "v"(c)); return r; }
DI f32x4 add4(f32x4 a, f32x4 b) { return f32x4{fadd(a.x, b.x), fadd(a.y, b.y), fadd(a.z, b.z), fadd(a.w, b.w)}; }
DI f32x4 fma4(f32x4 a, float b, f32x4 c) { return f32x4{ffma(a.x, b, c.x), ffma(a.y, b, c.y), ffma(a.z, b, c.z), ffma(a.w, b, c.w)}; }
DI int rot12(int v) { return v >= 12 ? v - 12 : v; }
DI void w_loads(v4u (&WF)[5], const char* p, unsigned vo) {
  ld128c<0>(WF[0], vo, p); ld128c<1024>(WF[1], vo, p); ld128c<2048>(WF[2], vo, p); ld128c<3072>(WF[3], vo, p); ld128c<0>(WF[4], vo, p + 4096);
}
DI void a_loads(v4u (&AF)[4], const char* p, const unsigned (&vm)[4]) {
#pragma unroll
  for (int m = 0; m < 4; ++m) ld128c<0>(AF[m], vm[m], p);
}
template <int LIVE, class F> DI void g2_step(f32x4 (&acc)[4][5], v4u (&WF)[5], v4u (&AF)[4], F ld) {
#pragma unroll
  for (int m = 0; m < 4; ++m) PIN(AF[m]);
#pragma unroll
  for (int n = 0; n < 5; ++n) PIN(WF[n]);
#pragma unroll
  for (int e = 0; e < 20; ++e) {
    const int m = e / 5, n = e % 5;
    acc[m][n] = mfma(WF[n], AF[m], m >= LIVE ? f32x4{0.f, 0.f, 0.f, 0.f} : acc[m][n]);
    if (e & 1) { SCHED(); ld(e >> 1); SCHED(); }
  }
}
DI void make_kix(unsigned (&kix)[9], int ts) {
#pragma unroll
  for (int i = 0; i < 9; ++i) {
    const int lg = 2047 - 8 * ts - i;
    const int bits = (int)(pk2(__builtin_fmaxf((float)lg, 128.f), 0.f));
    const int ci = lg < bits - 0x4280 ? lg : bits - 0x4280;
    kix[i] = KTAB + 4 * ci;
    PIN(kix[i]);
  }
}
DI void put_k(LAS char* L, const unsigned (&kix)[9], int ts, int c, unsigned reg) {
  float kv[9];
#pragma unroll
  for (int i = 0; i < 9; ++i) kv[i] = *(LAS float*)(L + kix[i] + c * KP_STRIDE);
  LAS char* q = L + reg + CV_KOFF + ts * 16;
  *(LAS v4u*)(q + CV_KSTR) = v4u{pk2(kv[0], kv[1]), pk2(kv[2], kv[3]), pk2(kv[4], kv[5]), pk2(kv[6], kv[7])};
  *(LAS v4u*)q = v4u{pk2(kv[1], kv[2]), pk2(kv[3], kv[4]), pk2(kv[5], kv[6]), pk2(kv[7], kv[8])};
}
DI void put_u(LAS char* q, v4u u, bool first) {
  *(LAS v4u*)(q + 3 * CV_USTR + 384) = u;
  *(LAS v2u*)(q + 1 * CV_USTR + 392) = v2u{u.x, u.y}; *(LAS v2u*)(q + 1 * CV_USTR + 400) = v2u{u.z, u.w};
  *(LAS unsigned*)(q + 2 * CV_USTR + 388) = u.x; *(LAS v2u*)(q + 2 * CV_USTR + 392) = v2u{u.y, u.z}; *(LAS unsigned*)(q + 2 * CV_USTR + 400) = u.w;
  *(LAS unsigned*)(q + 396) = u.x; *(LAS v2u*)(q + 400) = v2u{u.y, u.z}; *(LAS unsigned*)(q + 408) = u.w;
  if (first) {
    *(LAS v2u*)(q + 384) = v2u{0u, 0u}; *(LAS unsigned*)(q + 392) = 0u;
    *(LAS v2u*)(q + CV_USTR + 384) = v2u{0u, 0u}; *(LAS unsigned*)(q + 2 * CV_USTR + 384) = 0u;
  }
}

__global__ __launch_bounds__(512, 2) void fused_s4_mlp(const char* __restrict__ x, const char* __restrict__ pAre, const char* __restrict__ pAim,
    const char* __restrict__ pBre, const char* __restrict__ pBim, const char* __restrict__ pCre, const char* __restrict__ pCim,
    const char* __restrict__ plogdt, const char* __restrict__ pDskip, const char* __restrict__ w1, const char* __restrict__ b1,
    const char* __restrict__ w2, char* __restrict__ out, char* __restrict__ ws) {
  __shared__ __attribute__((aligned(16))) char smem[163840];
  LAS char* L = (LAS char*)smem;
  const unsigned lds0 = (unsigned)(size_t)L;
  const char* pc0; unsigned codelen;
  asm volatile("s_getpc_b64 %0\n98:\n\ts_mov_b32 %1, 97f-98b\n\ts_max_u32 %1, %1, 99f-98b" : "=s"(pc0), "=&s"(codelen) :: "scc");
  const int b = blockIdx.x, lane = threadIdx.x & 63;
  const int w = RFL(threadIdx.x >> 6), wi = w & 3;
  {
    const unsigned co = (unsigned)(4 * (16 * w + (lane & 15)) + ((b >> 3) & 3)) * 128;
    asm volatile("s_mov_b32 m0, %2\n\ts_nop 0\n\tglobal_load_lds_dword %0, %1" ::"v"((co < codelen ? co : codelen) & ~3u), "s"(pc0), "s"(lds0 + YST + w * 256) : "memory");
  }
  const int xcd = b & 7, j = b >> 3, rg = xcd >> 1, chf = xcd & 1;
  const int ct1 = 8 * chf + (j & 7), rb1 = 4 * rg + (j >> 3);
  const int wid = rb1 * 16 + ct1;
  const int a2 = 4 * chf + (j >> 3), cb2 = j & 7;
  const int l15 = lane & 15, g = lane >> 4, qq = (lane >> 2) & 3, p4 = lane & 3, l31 = lane & 31, h = lane >> 5;
  const int par = j & 1, cbase = 80 * xcd + 5 * (j >> 1);
  const int chA = cbase + 3 * par, chS = cbase + 2;
  const int ts = 64 * wi + lane;
  u64 stv0, stv1;
  ST(0);
  const int row2 = 512 * rg + 64 * a2 + 16 * wi;
  const char* w2b = ws + WS_W2T + (size_t)(cb2 * 48 + 12 * wi) * 5120;
  const char* actb = ws + WS_ACT + (size_t)((32 * rg + 4 * a2) * 48 + 12 * wi) * 1024;
  const int r2 = ((a2 ^ (w >> 2)) & 1) * 6;
#define WL(s) w_loads(WF[s], w2b + rot12(r2 + (s)) * 5120, vo)
#define AL(s) a_loads(AF[s], actb + rot12(r2 + (s)) * 1024, vm)
#define AL1(s, m) ld128c<0>(AF[s][m], vm[m], actb + rot12(r2 + (s)) * 1024)
#define WL1(s, n) ld128c<0>(WF[s][n], vo, w2b + rot12(r2 + (s)) * 5120 + (n) * 1024)
#define NOLD [&](int) {}
#define LATE_LANE() int ln_ = lane; PIN(ln_); const int c15 = ln_ & 15, cg = ln_ >> 4
#define KAO (CV_KOFF + (c15 & 1) * CV_KSTR + 2 * (2072 - 8 * (c15 >> 1) + 8 * cg))
#define UBO ((c15 & 3) * CV_USTR + 2 * (64 * (c15 >> 2) + 8 * cg + 224))
#define YOFF ((unsigned)((c15 >> 2) * 128 + cg * 32 + (c15 & 3) * 8))
#define OROW ((unsigned)(2 * (c15 >> 2) + (c15 & 1) + 8 * ((c15 >> 1) & 1)))
#define ZOFF (lane < 24 ? wi * CV_USTR + lane * 16 : CV_KOFF + wi * CV_KSTR + 4096 + (lane - 24) * 16)
#define ZCOND (lane < 24 || (lane < 31 && wi < 2))

  unsigned voA[4], voB[3];
#pragma unroll
  for (int i = 0; i < 4; ++i) voA[i] = (8 * i + (lane >> 3)) * 1280 + (((lane & 7) ^ (4 * (i & 1) + (lane >> 4))) * 16) - 1024 * i;
#pragma unroll
  for (int i = 0; i < 3; ++i) { const int c = 64 * i + lane; voB[i] = (c / 12) * 3072 + (c % 12) * 16 - 1024 * i; }
  const char* g1a = x + (size_t)(rb1 * 128 + 32 * wi) * 1280;
  const char* g1b = w1 + ct1 * 192 + wi * 49152;
  const unsigned la = lds0 + G1_RING + wi * 4096, lb = lds0 + G1_RING + G1_BOFF + wi * 3072;
  const int r1 = ((j & 7) + (5 * (j >> 3)) / 2 + 5 * chf + rg) % 10;
  auto slab = [&](int s) { const int v = s + r1; return v >= 10 ? v - 10 : v; };
  auto issue = [&](int s) {
    const char* pa = g1a + slab(s) * 128; const char* pb = g1b + (size_t)slab(s) * 64 * 3072;
    dma16(voA[0], pa, la + s * G1_STAGE); dma16o<1024>(voA[1], pa); dma16o<2048>(voA[2], pa); dma16o<3072>(voA[3], pa);
    dma16(voB[0], pb, lb + s * G1_STAGE); dma16o<1024>(voB[1], pb); dma16o<2048>(voB[2], pb);
  };
  unsigned tlo = 0, thi = 0;
  unsigned kix[9];
  if (w < 4) {
    unsigned pr[7];
    const bool odd = wi & 1;
    {
      const int kch = chA + (wi >> 1);
      const bool shl = odd && h;
      const unsigned vo = (unsigned)(shl ? chS * 192 + par * 96 + (wi >> 1) * 48 + 2 * (l31 < 24 ? l31 : 23) : kch * 192 + (odd ? 128 : 0) + 2 * lane) * 2;
      ld16(pr[6], (unsigned)(shl ? chS : kch) * 2, plogdt);
      ld32<0>(pr[0], vo, pAre); ld32<0>(pr[1], vo, pAim); ld32<0>(pr[2], vo, pBre); ld32<0>(pr[3], vo, pBim); ld32<0>(pr[4], vo, pCre); ld32<0>(pr[5], vo, pCim);
    }
    bar();
#pragma unroll
    for (int i = 0; i < 9; ++i) kix[i] = 0;
    waitv<0>();
#pragma unroll
    for (int i = 0; i < 7; ++i) PIN(pr[i]);
    ST(1);
    f32x4 ka[2][2][2]; float scale[2];
    kgen(pr, odd && lane >= 56, h, lds0 + KG_SCR + wi * 2 * KG_WSTR, L + KG_SCR + wi * 2 * KG_WSTR + l15 * KROW + g * 16, ka, scale);
#pragma unroll
    for (int i = 0; i < 4; ++i) {
      ka[1][i >> 1][i & 1] *= scale[1];
      ka[0][i >> 1][i & 1] = ka[0][i >> 1][i & 1] * scale[0] + (odd ? f32x4{0.f, 0.f, 0.f, 0.f} : ka[1][i >> 1][i & 1]);
    }
    kgen_store((LAS float*)(L + KG_SCR + wi * 2 * KG_WSTR), ka[0], lane);
    if (odd) kgen_store((LAS float*)(L + KG_SCR + wi * 2 * KG_WSTR + KG_WSTR), ka[1], lane);
    lds_arrive(L, W_KG, lane);
  } else {
    if (w == 4) *(LAS unsigned*)(L + lane * 4) = 0u;
    bar();
    v2u E; v4u xa, xb; v2u xc;
    const int pu = b % 240, pks = pu / 5, pcg = pu % 5;
    const char* xrow = x + (size_t)(8 * wid + 2 * wi) * 1280;
    ld128(xa, h * 1280 + l31 * 16, xrow); ld128(xb, h * 1280 + 512 + l31 * 16, xrow); ld64(xc, h * 1280 + 1024 + l31 * 8, xrow);
    ld64c(E, 0u, ws);
#pragma unroll
    for (int i = 0; i < 2; ++i)
      dma16((unsigned)((lane >> 1) * 1280 + (lane & 1) * 16), w2 + (size_t)pks * 32 * 1280 + (128 * pcg + 32 * wi + 16 * i) * 2, lds0 + W2P + wi * 2048 + i * 1024);
    issue(0);
    make_kix(kix, ts);
    issue(1);
    waitv<14>();
    PIN(E); PIN(xa); PIN(xb); PIN(xc);
    ST(6);
  const u64 tag64 = (((u64)RFL(E.y) << 32) | (unsigned)RFL(E.x)) + 1ull;
  tlo = (unsigned)tag64; thi = (unsigned)(tag64 >> 32);
  if (wi == 0) { lds_set(L, W_TAG, tlo); lds_set(L, W_TAG + 4, thi); lds_set(L, W_TAG + 8, 1); }
  {
    f32x2 v[10] = {unpk(xa.x), unpk(xa.y), unpk(xa.z), unpk(xa.w), unpk(xb.x), unpk(xb.y), unpk(xb.z), unpk(xb.w), unpk(xc.x), unpk(xc.y)};
    f32x2 s2 = ((v[0] + v[1]) + (v[2] + v[3])) + ((v[4] + v[5]) + (v[6] + v[7])) + (v[8] + v[9]);
    const float mean = half_sum(s2.x + s2.y, h) * (1.0f / 640.0f);
    f32x2 q2 = f32x2{0.f, 0.f};
#pragma unroll
    for (int i = 0; i < 10; ++i) { const f32x2 d = v[i] - mean; q2 = __builtin_elementwise_fma(d, d, q2); }
    const float var = rbf(half_sum(q2.x + q2.y, h) * (1.0f / 640.0f));
    const float m = rbf(mean), sd = rbf(__builtin_amdgcn_sqrtf(rbf(var + 1e-5f))), rs = __builtin_amdgcn_rcpf(sd);
    unsigned o[10];
#pragma unroll
    for (int i = 0; i < 10; ++i) { const f32x2 t = (v[i] - m) * rs; o[i] = pk2(t.x, t.y); }
    LAS char* rp = L + LN_ROWS + (2 * wi + h) * LN_PITCH;
    *(LAS v4u*)(rp + l31 * 16) = v4u{o[0], o[1], o[2], o[3]};
    *(LAS v4u*)(rp + 512 + l31 * 16) = v4u{o[4], o[5], o[6], o[7]};
    *(LAS v2u*)(rp + 1024 + l31 * 8) = v2u{o[8], o[9]};
    waitv<0>();
    lds_arrive(L, W_SB, lane);
    issue(2);
    if (l31 == 0) st64c(WS_STATS + (unsigned)(8 * wid + 2 * wi + h) * 8, ws,
                        v2u{(__builtin_bit_cast(unsigned, m) & 0xffff0000u) | (__builtin_bit_cast(unsigned, sd) >> 16), tlo});
  }
#pragma unroll
  for (int i = 0; i < 2; ++i) {
    LAS char* sp = L + W2P + wi * 2048 + i * 1024 + (16 * (g >> 1) + 4 * (g & 1) + qq) * 32 + 8 * p4;
    const v2u lo = trread(sp), hi = trread(sp + 256);
    const int ntg = 8 * pcg + 2 * wi + i;
    st128c((unsigned)lane * 16, ws + WS_W2T + (size_t)(((ntg / 5) * 48 + pks) * 5 + ntg % 5) * 1024, v4u{lo.x, lo.y, hi.x, hi.y});
  }
  lds_wait_ge(L, W_SB, 4);
  {
    LAS char* sp = L + LN_ROWS + qq * LN_PITCH + (160 * wi + 16 * g) * 2 + 8 * p4;
#pragma unroll
    for (int i = 0; i < 3; ++i) {
      const v2u lo = trread(sp + 128 * i), hi = trread(sp + 128 * i + 4 * LN_PITCH);
      const unsigned ch = 64 * i + lane;
      if (i < 2 || lane < 32) st128c((ch / 5) * 20480 + (ch % 5) * 16, ws + WS_XNB + (size_t)wi * 32 * 20480 + wid * 80, v4u{lo.x, lo.y, hi.x, hi.y});
    }
  }
  }
  ST(2);
  int role = w; asm volatile("" : "+s"(role));
  if (role < 4) {
    int lane = __builtin_amdgcn_mbcnt_hi(~0u, __builtin_amdgcn_mbcnt_lo(~0u, 0u)); PIN(lane);
    const int l15 = lane & 15, g = lane >> 4, qq = (lane >> 2) & 3, p4 = lane & 3, l31 = lane & 31, h = lane >> 5;
    const unsigned vo = lane * 16;
    f32x16 acc1[3]; f32x4 csa[3];
#pragma unroll
    for (int n = 0; n < 3; ++n) {
      csa[n] = f32x4{0.f, 0.f, 0.f, 0.f};
#pragma unroll
      for (int i = 0; i < 16; ++i) acc1[n][i] = 0.f;
    }
    const int xrow = (l31 & 17) + 2 * (l15 >> 2) + 8 * ((l15 >> 1) & 1);
    unsigned xo[4], wo[4];
#pragma unroll
    for (int k = 0; k < 4; ++k) {
      const int kr = (k + wi) & 3;
      xo[k] = G1_RING + (32 * wi + xrow) * 128 + (((2 * kr + h) ^ ((xrow >> 1) & 7)) * 16);
      wo[k] = G1_RING + G1_BOFF + (16 * kr + 8 * h + qq) * 192 + 32 * (g & 1) + 8 * p4;
      PIN(xo[k]); PIN(wo[k]);
    }
    const unsigned sel1 = ((l15 ^ g) & 1) ? 0u : 0x3f803f80u;
    const v4u sel = v4u{sel1, sel1, sel1, sel1};
    v4u X[4], W[4][3];
    auto rdw = [&](int s, int k, int n) {
      LAS char* p = L + wo[k] + (s % 3) * G1_STAGE + 64 * n; const v2u lo = trread(p), hi = trread(p + 768); W[k][n] = v4u{lo.x, lo.y, hi.x, hi.y};
    };
    auto rdx = [&](int s, int k) { X[k] = *(LAS v4u*)(L + xo[k] + (s % 3) * G1_STAGE); };
#define MM(k, n) acc1[n] = mfma32(W[k][n], X[k], acc1[n])
#define CS(n) csa[n] = mfma(W[0][n], sel, csa[n])
    v2u gran; unsigned bb[3];
    v4u WF[6][5], AF[6][4];
    const unsigned go = WS_STATS + (unsigned)(rb1 * 128 + 32 * wi + xrow) * 8;
    auto wl1 = [&](int s, int e0) { const int e = 7 * (s - 7) + e0; if (s >= 7 && e0 < 7 && e < 20) ld128c<0>(WF[e / 5][e % 5], vo, w2b + rot12(r2 + e / 5) * 5120 + (e % 5) * 1024); };
    lds_wait_ge(L, W_SB, 4);
    ST(3);
    rdx(0, 0); rdw(0, 0, 0); rdw(0, 0, 1); rdw(0, 0, 2); rdx(0, 1); rdw(0, 1, 0); rdw(0, 1, 1); rdw(0, 1, 2);
#pragma unroll
    for (int s = 0; s < 10; ++s) {
      const bool refill = s < 7;
      const char* pa = g1a + slab(s + 3) * 128; const char* pb = g1b + (size_t)slab(s + 3) * 64 * 3072;
      SCHED();
      MM(0, 0); rdx(s, 2); rdw(s, 2, 0); SCHED();
      MM(0, 1); rdw(s, 2, 1); rdw(s, 2, 2); SCHED();
      MM(0, 2); rdx(s, 3); rdw(s, 3, 0); SCHED();
      CS(0); rdw(s, 3, 1); SCHED();
      CS(1); rdw(s, 3, 2); SCHED();
      CS(2);
      if (s == 0) {} else if (s == 1) lds_wait_ge(L, W_ST, 4); else if (s < 7) waitv<7>(); else if (s == 7) waitv<11>(); else if (s == 8) waitv<11>();
      if (s == 9 && l15 < 2) {
#pragma unroll
        for (int n = 0; n < 3; ++n) *(LAS f32x4*)(L + CSP + wi * 384 + (32 * n + 16 * l15 + 4 * g) * 4) = csa[n];
      }
      lds_set(L, W_RDY + wi * 4, s + 2);
      SCHED();
      MM(1, 0);
      if (refill) { asm volatile("s_waitcnt lgkmcnt(7)" ::: "memory"); dma16(voA[0], pa, la + (s % 3) * G1_STAGE); }
      wl1(s, 0);

      SCHED();
      MM(1, 1); if (refill) dma16o<1024>(voA[1], pa); wl1(s, 1);
      unsigned fl = 0;
      if (s < 9) fl = lw(L, W_RDY + p4 * 4);
      SCHED();
      MM(1, 2); if (refill) dma16o<2048>(voA[2], pa); wl1(s, 2); SCHED();
      MM(2, 0); if (refill) dma16o<3072>(voA[3], pa); wl1(s, 3);
      SCHED();
      if (s == 9) {
        MM(2, 1); wl1(s, 4); SCHED();
        lds_wait_min<4>(L, W_RDY, 11, lane);
        float cs[3];
#pragma unroll
        for (int n = 0; n < 3; ++n) { LAS float* p = (LAS float*)(L + CSP) + 32 * n + l31; cs[n] = fadd(fadd(p[0], p[96]), fadd(p[192], p[288])); }
        MM(2, 2); wl1(s, 5); SCHED();
        waitv<20>(); PIN(gran); PIN(bb[0]); PIN(bb[1]); PIN(bb[2]);
        MM(3, 0); MM(3, 1); MM(3, 2);
        const unsigned keep = h ? 0u : ~0u, nm = (gran.x >> 16) ^ 0x8000u;
        const v4u xe = v4u{(nm | (nm << 16)) & keep, (nm | (gran.x << 16)) & keep, 0u, 0u};
#pragma unroll
        for (int n = 0; n < 3; ++n) {
          const float c0 = bfhi(__builtin_bit_cast(unsigned, cs[n])), r0 = cs[n] - c0, c1 = bfhi(__builtin_bit_cast(unsigned, r0)), r1 = r0 - c1;
          const v4u we = v4u{(__builtin_bit_cast(unsigned, c0) >> 16) | __builtin_bit_cast(unsigned, c1), (__builtin_bit_cast(unsigned, r1) >> 16) | (bb[n] << 16), 0u, 0u};
          acc1[n] = mfma32(we, xe, acc1[n]);
        }
        SCHED();
        break;
      }
      if (__builtin_expect(__builtin_amdgcn_ballot_w64(fl < (unsigned)(s + 2)) != 0ull, 0)) lds_wait_min<4>(L, W_RDY, s + 2, lane);
      FENCE();
      SCHED();
      rdx(s + 1, 0); rdw(s + 1, 0, 0); SCHED();
      MM(2, 1); rdw(s + 1, 0, 1); rdw(s + 1, 0, 2); if (refill) dma16(voB[0], pb, lb + (s % 3) * G1_STAGE); wl1(s, 4); SCHED();
      MM(2, 2); rdx(s + 1, 1); rdw(s + 1, 1, 0); if (refill) dma16o<1024>(voB[1], pb); wl1(s, 5); SCHED();
      MM(3, 0); rdw(s + 1, 1, 1); if (refill) dma16o<2048>(voB[2], pb); wl1(s, 6); SCHED();
      MM(3, 1); rdw(s + 1, 1, 2); wl1(s, 7); SCHED();
      MM(3, 2); wl1(s, 8);
      if (s == 6) { lds_wait_ge(L, W_F1ALL, 4); ld64c(gran, go, ws); ld16(bb[0], l31 * 2, b1 + ct1 * 192); ld16(bb[1], l31 * 2, b1 + ct1 * 192 + 64); ld16(bb[2], l31 * 2, b1 + ct1 * 192 + 128); }
      SCHED();
      if (s == 0) ST(4); if (s == 3) ST(5); if (s == 6) ST(7);
    }
#undef MM
#undef CS
    ST(11);
    const unsigned tlo = RFL(lw(L, W_TAG)), thi = RFL(lw(L, W_TAG + 4));
    {
      const float rs = __builtin_amdgcn_rcpf(bf2f(gran.x)) * 1.44269504088896341f;
      char* ab = ws + WS_ACT + (size_t)((rb1 * 8 + 2 * wi) * 48 + 3 * ct1) * 1024;
      const unsigned ao = (unsigned)((l31 >> 4) * 49152 + h * 256 + l15 * 16);
#pragma unroll
      for (int q = 0; q < 6; ++q) {
        unsigned pk[4];
#pragma unroll
        for (int e = 0; e < 4; ++e) {
          const f32x2 h2 = f32x2{acc1[q >> 1][8 * (q & 1) + 2 * e], acc1[q >> 1][8 * (q & 1) + 2 * e + 1]} * rs;
          const f32x2 s1 = f32x2{__builtin_amdgcn_exp2f(h2.x), __builtin_amdgcn_exp2f(h2.y)} + 1.0f;
          const f32x2 d = __builtin_elementwise_fma(s1, s1, f32x2{1.f, 1.f});
          const f32x2 rr = f32x2{__builtin_amdgcn_rcpf(d.x), __builtin_amdgcn_rcpf(d.y)};
          const f32x2 o = h2 * __builtin_elementwise_fma(rr, f32x2{-1.38629436111989062f, -1.38629436111989062f},
                                                         f32x2{0.69314718055994531f, 0.69314718055994531f});
          pk[e] = pk2(o.x, o.y);
        }
        st128c(ao + q * 512, ab, v4u{pk[0], pk[1], pk[2], pk[3]});
      }
    }
    lds_set(L, W_RDY + wi * 4, 12);
    ST(13);
    __builtin_amdgcn_s_setprio(2);
    LATE_LANE();
    v2u xq[5], dq[5], gr2, f3;
    const unsigned f2o = WS_F2 + (unsigned)((4 * rg + (a2 >> 1)) * 16 + 4 * wi + (ln_ & 3)) * 128;
    unsigned vm[4];
#pragma unroll
    for (int m = 0; m < 4; ++m) vm[m] = vo + ((m + wi) & 3) * 49152;
    v2u f2; v4u ysh;
    {
      f32x4 sacc[4];
#pragma unroll
      for (int i = 0; i < 4; ++i) sacc[i] = f32x4{0.f, 0.f, 0.f, 0.f};
      unsigned kb = CVS + KAO - wi * 128 - 3584 - 64, ubb = CVS + UBO - wi * 128 - 64; PIN(kb); PIN(ubb);
      LAS char* ka = L + kb; LAS char* ub = L + ubb;
      const bool late = RFL(lw(L, W_SB)) < 16;
      if (late) { waitv<0>(); arrive_flag<4>(L, W_F2C, lane, ws, WS_F2 + (unsigned)wid * 128, tlo, thi); }
      lds_wait_ge(L, W_SB, 16);
      if (par) conv_unit_shared<1>(ka + 64, ub + 64, sacc); else conv_unit_shared<0>(ka + 64, ub + 64, sacc);
      if (!late) { waitv<0>(); arrive_flag<4>(L, W_F2C, lane, ws, WS_F2 + (unsigned)wid * 128, tlo, thi); }
      ST(14);
      WL(4); WL(5);
      if (par) conv_unit_shared<1>(ka, ub, sacc); else conv_unit_shared<0>(ka, ub, sacc);
#pragma unroll
      for (int q = 0; q < 4; ++q) *(LAS f32x4*)(L + MXCH + (q * 4 + wi) * 1024 + vo) = sacc[q];
      lds_arrive(L, W_MB, lane);
    {
      const char* xb2 = x + (size_t)row2 * 1280 + cb2 * 160; const unsigned xo2 = OROW * 1280 + cg * 8;
      ld64o<0>(xq[0], xo2, xb2); ld64o<32>(xq[1], xo2, xb2); ld64o<64>(xq[2], xo2, xb2); ld64o<96>(xq[3], xo2, xb2); ld64o<128>(xq[4], xo2, xb2);
      const char* db = pDskip + cb2 * 160;
      ld64o<0>(dq[0], cg * 8, db); ld64o<32>(dq[1], cg * 8, db); ld64o<64>(dq[2], cg * 8, db); ld64o<96>(dq[3], cg * 8, db); ld64o<128>(dq[4], cg * 8, db);
      ld64c(gr2, WS_STATS + (row2 + OROW) * 8, ws);
    }
      lds_wait_ge(L, W_MB, 4);
      LAS char* p = L + MXCH + wi * 4096 + vo;
      const f32x4 s = add4(add4(*(LAS f32x4*)p, *(LAS f32x4*)(p + 1024)), add4(*(LAS f32x4*)(p + 2048), *(LAS f32x4*)(p + 3072)));
      ld64c(f2, f2o, ws);
      const unsigned y0 = pk2(s.x, s.y), y1 = pk2(s.z, s.w);
      *(LAS v2u*)(L + MXCH + wi * 4096 + YOFF) = v2u{y0, y1};
      FENCE();
      ysh = *(LAS v4u*)p;
    }
    ST(15);
    waitv<0>(); PIN(f2);
    f2 = spin64<false>(f2, f2o, ws, tlo, thi);
    lds_set(L, W_F2OK + wi * 4, 1);
    ST(16);
    AL(0); AL(1);
    if (ln_ < 32) st128c(vo, ws + WS_Y + (size_t)chS * 4096 + (2 * wi + ((wi & 1) ^ par)) * 512, ysh);
    f32x4 acc2[4][5];
    PIN(gr2);
    const float rs2 = __builtin_amdgcn_rcpf(bf2f(gr2.x)), m2 = -bfhi(gr2.x) * rs2;
#pragma unroll
    for (int n = 0; n < 5; ++n) {
      PIN(xq[n]); PIN(dq[n]);
      const f32x2 x0 = unpk(xq[n].x), x1 = unpk(xq[n].y), d0 = unpk(dq[n].x), d1 = unpk(dq[n].y);
      acc2[0][n] = f32x4{ffma(d0.x, ffma(x0.x, rs2, m2), x0.x), ffma(d0.y, ffma(x0.y, rs2, m2), x0.y),
                         ffma(d1.x, ffma(x1.x, rs2, m2), x1.x), ffma(d1.y, ffma(x1.y, rs2, m2), x1.y)};
      PIN(acc2[0][n]);
    }
    __builtin_amdgcn_s_setprio(0);
    ST(17);
    waitv<4>(); g2_step<1>(acc2, WF[0], AF[0], [&](int i) { if (i >= 3 && (i & 1)) AL1(2, (i - 3) >> 1); });
    waitv<4>();
    arrive_flag<8>(L, W_F3C, lane, ws, WS_F3 + (unsigned)(xcd * 32 + j) * 128, tlo, thi);
    g2_step<4>(acc2, WF[1], AF[1], [&](int i) { if (i >= 2) AL1(3 + ((i - 2) >> 2), (i - 2) & 3); });
    waitv<8>(); g2_step<4>(acc2, WF[2], AF[2], [&](int i) { if (i < 4) AL1(5, i); });
    ST(20);
    waitv<8>(); g2_step<4>(acc2, WF[3], AF[3], NOLD);
    waitv<4>(); g2_step<4>(acc2, WF[4], AF[4], NOLD);
    lds_wait_ge(L, W_F3C, 8);
    const unsigned f3o = WS_F3 + (unsigned)(cb2 * 32 + 8 * wi + (ln_ & 7)) * 128;
    ld64c(f3, f3o, ws);
    waitv<1>(); g2_step<4>(acc2, WF[5], AF[5], NOLD);
    ST(22);
#pragma unroll
    for (int m = 1; m < 4; ++m)
#pragma unroll
      for (int n = 0; n < 5; ++n) *(LAS f32x4*)(L + FM + wi * 15360 + (m - 1) * 5120 + n * 1024 + vo) = acc2[m][n];
    waitv<0>(); PIN(f3);
    f3 = spin64<false>(f3, f3o, ws, tlo, thi);
    lds_arrive(L, W_F3ALL, lane);
    lds_wait_ge(L, W_F3ALL, 4);
    ST(21);
    v4u yv[3];
    {
      const char* yb = ws + WS_Y + (size_t)cb2 * 80 * 4096 + row2 * 2;
      const unsigned yo = (unsigned)(ln_ >> 1) * 4096 + (ln_ & 1) * 16;
      ld128c<0>(yv[0], yo, yb); ld128c<0>(yv[1], yo, yb + 32 * 4096); ld128c<0>(yv[2], yo & 0xffffu, yb + 64 * 4096);
    }
#pragma unroll
    for (int n = 0; n < 5; ++n) {
#pragma unroll
      for (int d = 1; d < 4; ++d) acc2[0][n] = add4(acc2[0][n], *(LAS f32x4*)(L + FM + ((wi - d) & 3) * 15360 + (d - 1) * 5120 + n * 1024 + vo));
      *(LAS f32x4*)(L + FM + 61440 + wi * 5120 + n * 1024 + vo) = acc2[0][n];
    }
    waitv<0>(); PIN(yv[0]); PIN(yv[1]); PIN(yv[2]);
    LAS char* ys = L + YST + wi * 2560;
    *(LAS v4u*)(ys + vo) = yv[0]; *(LAS v4u*)(ys + 1024 + vo) = yv[1];
    if (ln_ < 32) *(LAS v4u*)(ys + 2048 + vo) = yv[2];
    lds_set(L, W_F2OK + wi * 4, 2);
    STEND();
    asm volatile("97:");
    __builtin_amdgcn_endpgm();
  }

  const bool odd = wi & 1;
  const int kch = chA + (wi >> 1);
  const unsigned vo = lane * 16;
  v4u WF[6][5], AF[6][4];
  waitv<6>();
  lds_arrive(L, W_ST, lane);
  waitv<0>();
  arrive_flag8(L, W_F1C, ws, WS_F1 + (unsigned)(lane & 7) * 2048 + wid * 8, tlo, thi);
  ST(8);
  auto reduce = [&](int c, int i) {
    LAS char* pp = L + KG_SCR + (c < 2 ? 4 * c : 3) * KG_WSTR + i * 16;
    const f32x4 s = add4(*(LAS f32x4*)pp, *(LAS f32x4*)(pp + (c < 2 ? 2 : 4) * KG_WSTR));
    *(LAS f32x4*)(L + KTAB + c * KP_STRIDE + i * 16) = s;
    return s;
  };
  lds_wait_ge(L, W_KG, 4);
  if (ts < 161) { st128c((unsigned)ts * 16, ws + WS_KP + (size_t)wid * KP_STRIDE, __builtin_bit_cast(v4u, reduce(2, ts))); reduce(0, ts); }
  if (ts >= 95) reduce(1, ts - 95);
  if (ts < 3) *(LAS float*)(L + KTAB + ts * KP_STRIDE - 4) = 0.f;
  lds_arrive(L, W_SB, lane);
  waitv<0>();
  arrive_flag(L, W_FKC, lane, ws, WS_FK + (unsigned)wid * 64, tlo, thi);
  ST(9);
  __builtin_amdgcn_s_setprio(0);
  lds_wait_min<4>(L, W_RDY, F1STAGE, lane);
  const unsigned f1o = WS_F1 + (unsigned)xcd * 2048 + ts * 8;
  v2u f1own; ld64c(f1own, f1o, ws);
  waitv<0>(); PIN(f1own);
  f1own = spin64<false>(f1own, f1o, ws, tlo, thi);
  v4u uu[3]; unsigned ulo[3];
#pragma unroll
  for (int r = 0; r < 3; ++r) {
    const int e = 64 * r + lane, tb = 64 * wi + e / 3, c = e % 3;
    ld128c<0>(uu[r], tb * 80 + c * 16, ws + WS_XNB + (size_t)(16 * xcd + (j >> 1)) * 20480 + par * 32);
    ulo[r] = (c == 2 - 2 * par ? CVS : CVA + (c - par) * CV_CH) + tb * 16;
  }
  if (b == 0) arrive_flag(L, W_F1ALL, lane, ws, WS_E, tlo, thi);
  else lds_arrive(L, W_F1ALL, lane);
  ST(4);
  const unsigned fko = WS_FK + (unsigned)(wid ^ 1) * 64;
  v2u fk; ld64c(fk, fko, ws);
  waitv<0>(); PIN(fk); PIN(uu[0]); PIN(uu[1]); PIN(uu[2]);
  fk = spin64<false>(fk, fko, ws, tlo, thi);
  v4u kp; ld128c<0>(kp, (unsigned)(ts < 161 ? ts : 160) * 16, ws + WS_KP + (size_t)(wid ^ 1) * KP_STRIDE);
  lds_wait_ge(L, W_SB, 8);
  waitv<0>(); PIN(kp);
  if (ts < 161) { LAS f32x4* tp = (LAS f32x4*)(L + KTAB + 2 * KP_STRIDE + ts * 16); *tp = add4(*tp, __builtin_bit_cast(f32x4, kp)); }
  lds_arrive(L, W_SB, lane);
  lds_wait_ge(L, W_F1ALL, 4);
  WL(0);
  lds_wait_min<4>(L, W_RDY, 9, lane);
  put_k(L, kix, ts, 0, CVA);
  WL(1);
  lds_wait_min<4>(L, W_RDY, 10, lane);
  put_k(L, kix, ts, 1, CVA + CV_CH);
  if (ZCOND) { *(LAS v4u*)(L + CVA + ZOFF) = v4u{0u, 0u, 0u, 0u}; *(LAS v4u*)(L + CVA + CV_CH + ZOFF) = v4u{0u, 0u, 0u, 0u}; }
  ST(7);
  lds_wait_min<4>(L, W_RDY, 11, lane);
  ST(11);
  put_u(L + ulo[0], uu[0], ts < 3); put_u(L + ulo[1], uu[1], false); put_u(L + ulo[2], uu[2], false);
  if (ZCOND) *(LAS v4u*)(L + CVS + ZOFF) = v4u{0u, 0u, 0u, 0u};
  lds_wait_ge(L, W_SB, 12);
  put_k(L, kix, ts, 2, CVS);
  lds_arrive(L, W_SB, lane);
  WL(2); WL(3); WL(4); WL(5);
  ST(5);
  lds_wait_ge(L, W_SB, 16);
  lds_wait_ge(L, W_RDY + wi * 4, 12);
  ST(13);
  __builtin_amdgcn_s_setprio(SPRIO2);
  LATE_LANE();
  f32x4 cacc[8];
#pragma unroll
  for (int i = 0; i < 8; ++i) cacc[i] = f32x4{0.f, 0.f, 0.f, 0.f};
  {
    const unsigned o1 = CVA + (wi >> 1) * CV_CH - (odd ? 4 * 64 : 0);
    unsigned kb = KAO + o1 - 3584 - 192, ubb = UBO + o1 - 192; PIN(kb); PIN(ubb);
    LAS char* ka = L + kb; LAS char* ub = L + ubb;
    v4u A[8], B[8];
#define LA(i, u) (*(LAS v4u*)(ka + 512 * (7 - (i)) + 64 * (3 - (u))))
#define LB(i, u) (*(LAS v4u*)(ub + 512 * (i) + 64 * (3 - (u))))
    A[7] = LA(7, 0); B[0] = LB(0, 0); A[0] = LA(0, 0); B[7] = LB(7, 0); A[6] = LA(6, 0); B[6] = LB(6, 0); A[1] = LA(1, 0); B[1] = LB(1, 0);
    A[5] = LA(5, 0); B[5] = LB(5, 0); A[2] = LA(2, 0); B[2] = LB(2, 0); A[4] = LA(4, 0); B[4] = LB(4, 0); A[3] = LA(3, 0); B[3] = LB(3, 0);
    SCHED();
#pragma unroll
    for (int u = 0; u < 4; ++u) {
      v4u An[2], Bn[2];
#pragma unroll
      for (int sd = 7; sd >= 0; --sd) {
#pragma unroll
        for (int o = 0; o <= sd; ++o) {
          if (sd + o < 8) cacc[sd + o] = mfma(A[sd], B[o], cacc[sd + o]);
          if (o != sd && sd + o < 8) cacc[sd + o] = mfma(A[o], B[sd], cacc[sd + o]);
        }
        SCHED();
        if (u < 3) {
          if (sd >= 2) { A[sd] = LA(sd, u + 1); B[sd] = LB(sd, u + 1); }
          if (sd == 6 || sd == 5) { An[6 - sd] = LA(6 - sd, u + 1); Bn[6 - sd] = LB(6 - sd, u + 1); }
        }
        SCHED();
      }
      if (u < 3) { A[0] = An[0]; A[1] = An[1]; B[0] = Bn[0]; B[1] = Bn[1]; }
    }
#undef LA
#undef LB
  }
  ST(10);
  unsigned vm[4];
  {
    LAS char* px = L + SXCH + wi * 4096 + vo;
    if (odd) {
#pragma unroll
      for (int i = 0; i < 4; ++i) { *(LAS f32x4*)(px + i * 1024) = cacc[i]; cacc[i] = cacc[4 + i]; }
    } else {
#pragma unroll
      for (int i = 0; i < 4; ++i) *(LAS f32x4*)(px + i * 1024) = cacc[4 + i];
    }
    lds_arrive(L, W_SX + (wi >> 1) * 4, lane);
#pragma unroll
    for (int m = 0; m < 4; ++m) vm[m] = vo + ((m + wi) & 3) * 49152;
    lds_wait_ge(L, W_SX + (wi >> 1) * 4, 2);
    px = L + SXCH + (wi ^ 1) * 4096 + vo;
    v2u yo[4];
#pragma unroll
    for (int i = 0; i < 4; ++i) { const f32x4 s = add4(cacc[i], *(LAS f32x4*)(px + i * 1024)); yo[i] = v2u{pk2(s.x, s.y), pk2(s.z, s.w)}; }
    char* yb = ws + WS_Y + (size_t)kch * 4096 + (odd ? 2048 : 0);
    LAS char* yl = L + SXCH + (wi ^ 1) * 4096;
#pragma unroll
    for (int i = 0; i < 4; ++i) *(LAS v2u*)(yl + i * 512 + YOFF) = yo[i];
    FENCE();
    st128c(vo, yb, *(LAS v4u*)(yl + vo)); st128c(vo, yb + 1024, *(LAS v4u*)(yl + 1024 + vo));
  }
  ST(14);
  lds_wait_ge(L, W_F2OK + wi * 4, 1);
  ST(16);
  AL(0); AL(1);
  ST(17);
  f32x4 acc2[4][5];
  waitv<8>();
  arrive_flag<8>(L, W_F3C, lane, ws, WS_F3 + (unsigned)(xcd * 32 + j) * 128, tlo, thi);
  __builtin_amdgcn_s_setprio(0);
  waitv<4>(); g2_step<0>(acc2, WF[0], AF[0], [&](int i) { if (i >= 3 && (i & 1)) AL1(2, (i - 3) >> 1); });
  waitv<4>(); g2_step<4>(acc2, WF[1], AF[1], [&](int i) { if (i >= 2) AL1(3 + ((i - 2) >> 2), (i - 2) & 3); });
  waitv<8>(); g2_step<4>(acc2, WF[2], AF[2], [&](int i) { if (i < 4) AL1(5, i); });
  ST(20);
  waitv<8>(); g2_step<4>(acc2, WF[3], AF[3], NOLD);
  lds_wait_ge(L, W_F3C, 8);
  waitv<0>();
#pragma unroll
  for (int m = 0; m < 4; ++m) { PIN(AF[4][m]); PIN(AF[5][m]); }
#pragma unroll
  for (int n = 0; n < 5; ++n) { PIN(WF[4][n]); PIN(WF[5][n]); }
#pragma unroll
  for (int mm = 1; mm < 5; ++mm) {
    const int m = mm & 3;
#pragma unroll
    for (int n = 0; n < 5; ++n) {
      acc2[m][n] = mfma(WF[4][n], AF[4][m], acc2[m][n]);
      acc2[m][n] = mfma(WF[5][n], AF[5][m], acc2[m][n]);
      if (mm > 1) *(LAS f32x4*)(L + FS + wi * 15360 + (mm - 2) * 5120 + n * 1024 + vo) = acc2[mm - 1][n];
      SCHED();
    }
  }
  lds_arrive(L, W_FS, lane);
  ST(22);
  lds_wait_ge(L, W_FS, 4);
#pragma unroll
  for (int d = 1; d < 4; ++d)
#pragma unroll
    for (int n = 0; n < 5; ++n) acc2[0][n] += *(LAS f32x4*)(L + FS + ((wi - d) & 3) * 15360 + (d - 1) * 5120 + n * 1024 + vo);
  ST(23);
  lds_wait_ge(L, W_F2OK + wi * 4, 2);
  ST(24);
  {
    char* ob = out + (size_t)row2 * 1280 + cb2 * 160; const unsigned oo = OROW * 1280 + cg * 8;
    v2u o[5];
#pragma unroll
    for (int n = 0; n < 5; ++n) {
      const v2u t = trread(L + YST + wi * 2560 + n * 512 + ln_ * 8);
      const f32x4 s = acc2[0][n] + *(LAS f32x4*)(L + FM + 61440 + wi * 5120 + n * 1024 + vo);
      const f32x2 s0 = s.xy + unpk(t.x), s1 = s.zw + unpk(t.y);
      o[n] = v2u{pk2(s0.x, s0.y), pk2(s1.x, s1.y)};
    }
    st64o<0>(oo, ob, o[0]); st64o<32>(oo, ob, o[1]); st64o<64>(oo, ob, o[2]); st64o<96>(oo, ob, o[3]); st64o<128>(oo, ob, o[4]);
  }
  STEND();
  asm volatile("99:");
}

extern "C" void kernel_launch(void* const* d_in, const int* in_sizes, int n_in, void* d_out, int out_size, void* d_ws, size_t ws_size,
                              hipStream_t stream) {
  fused_s4_mlp<<<256, 512, 0, stream>>>((const char*)d_in[0], (const char*)d_in[1], (const char*)d_in[2], (const char*)d_in[3],
      (const char*)d_in[4], (const char*)d_in[5], (const char*)d_in[6], (const char*)d_in[7], (const char*)d_in[8], (const char*)d_in[10],
      (const char*)d_in[11], (const char*)d_in[12], (char*)d_out, (char*)d_ws);
}
```

```cpp
#include <hip/hip_runtime.h>
#include <stdint.h>

typedef unsigned short u16;
typedef unsigned long long u64;
typedef unsigned v4u __attribute__((ext_vector_type(4)));
typedef unsigned v2u __attribute__((ext_vector_type(2)));
typedef float f32x4 __attribute__((ext_vector_type(4)));
typedef float f32x2 __attribute__((ext_vector_type(2)));
typedef short bf16x8 __attribute__((ext_vector_type(8)));
typedef short s16x4 __attribute__((ext_vector_type(4)));
typedef __bf16 bf16x2_t __attribute__((ext_vector_type(2)));
#define LAS __attribute__((address_space(3)))
#define GAS __attribute__((address_space(1)))
#define DI __device__ __forceinline__

constexpr unsigned WS_E = 0, WS_TMO = 64, WS_F1 = 131072, WS_STATS = 16384, WS_F2 = 32768, WS_F3 = 65536, WS_FK = 98304, WS_STAMP = 262144;
constexpr unsigned WS_XNB = 1u << 20, WS_W2T = 4u << 20, WS_Y = 7u << 20, WS_ACT = 10u << 20, WS_KP = 17u << 20;
constexpr unsigned KP_STRIDE = 2624;
constexpr unsigned SPIN_CAP = 400000;

constexpr unsigned W_F1C = 0, W_F2C = 4, W_F3C = 8, W_KG = 12, W_MB = 16, W_SB = 20, W_SX = 24, W_FM = 32, W_FS = 36, W_F3ALL = 40, W_F1ALL = 44,
                   W_F2OK = 48, W_RDY = 64, W_TAG = 80, W_FKC = 96, W_ST = 100;
constexpr unsigned KTAB = 272;
constexpr unsigned CSP = 8192;
constexpr unsigned CVA = 10240, CV_CH = 27008, CV_USTR = 4640, CV_KOFF = 18560, CV_KSTR = 4224;
constexpr unsigned KG_SCR = CVA, KG_WSTR = 4352, LN_ROWS = KG_SCR + 8 * KG_WSTR, LN_PITCH = 1344, W2P = LN_ROWS + 10752;
constexpr unsigned G1_RING = 64512, G1_STAGE = 28672, G1_BOFF = 16384;
constexpr unsigned CVS = G1_RING, SXCH = CVS + 27136, MXCH = SXCH + 16384;
constexpr unsigned CSF = G1_RING + 3 * G1_STAGE, YST = CSF + 3072;
constexpr unsigned FM = 256, FS = FM + 81920;
static_assert(FS + 61440 <= CSF && CVA + 2 * CV_CH <= G1_RING && W2P + 8192 <= G1_RING && YST + 10240 <= 163840 && MXCH + 16384 <= CSF, "lds map");

#define ROT2 0
#define F1STAGE 5
#define SPRIO2 1
#define F1GAP 16
#define F2GAP 8
#define SPRIO 1
#define ST(i) do {} while (0)
#define STEND() do {} while (0)
#define STARG
#define STPASS

DI float bf2f(unsigned v) { return __builtin_bit_cast(float, v << 16); }
DI float bfhi(unsigned v) { return __builtin_bit_cast(float, v & 0xffff0000u); }
DI unsigned pk2(float lo, float hi) { bf16x2_t r; r.x = (__bf16)lo; r.y = (__bf16)hi; return __builtin_bit_cast(unsigned, r); }
DI float rbf(float f) { return bf2f(pk2(f, f) & 0xffffu); }
DI f32x4 mfma(v4u a, v4u b, f32x4 c) {
  return __builtin_amdgcn_mfma_f32_16x16x32_bf16(__builtin_bit_cast(bf16x8, a), __builtin_bit_cast(bf16x8, b), c, 0, 0, 0);
}
DI v2u trread(LAS char* p) { return __builtin_bit_cast(v2u, __builtin_amdgcn_ds_read_tr16_b64_v4i16((LAS s16x4*)p)); }
template <int N> DI void waitv() { asm volatile("s_waitcnt vmcnt(%0)" ::"i"(N) : "memory"); }
DI void bar() { asm volatile("s_waitcnt lgkmcnt(0)\n\ts_barrier" ::: "memory"); }
#define PIN(x) asm volatile("" : "+v"(x))
#define FENCE() asm volatile("" ::: "memory")
#define SCHED() __builtin_amdgcn_sched_barrier(0)
#define RFL(x) __builtin_amdgcn_readfirstlane(x)

DI void ld16(unsigned& d, unsigned vo, const void* sb) { asm volatile("global_load_ushort %0, %1, %2" : "=v"(d) : "v"(vo), "s"(sb) : "memory"); }
template <int O> DI void ld32(unsigned& d, unsigned vo, const void* sb) { asm volatile("global_load_dword %0, %1, %2 offset:%3" : "=v"(d) : "v"(vo), "s"(sb), "i"(O) : "memory"); }
DI void ld32c(unsigned& d, unsigned vo, const void* sb) { asm volatile("global_load_dword %0, %1, %2 sc1" : "=v"(d) : "v"(vo), "s"(sb) : "memory"); }
DI void ld64(v2u& d, unsigned vo, const void* sb) { asm volatile("global_load_dwordx2 %0, %1, %2" : "=v"(d) : "v"(vo), "s"(sb) : "memory"); }
DI void ld128(v4u& d, unsigned vo, const void* sb) { asm volatile("global_load_dwordx4 %0, %1, %2" : "=v"(d) : "v"(vo), "s"(sb) : "memory"); }
DI void ld64c(v2u& d, unsigned vo, const void* sb) { asm volatile("global_load_dwordx2 %0, %1, %2 sc1" : "=v"(d) : "v"(vo), "s"(sb) : "memory"); }
template <int O> DI void ld128c(v4u& d, unsigned vo, const void* sb) { asm volatile("global_load_dwordx4 %0, %1, %2 offset:%3 sc1" : "=v"(d) : "v"(vo), "s"(sb), "i"(O) : "memory"); }
DI void st64c(unsigned vo, void* sb, v2u d) { asm volatile("global_store_dwordx2 %0, %1, %2 sc1" ::"v"(vo), "v"(d), "s"(sb) : "memory"); }
template <int O> DI void ld64o(v2u& d, unsigned vo, const void* sb) { asm volatile("global_load_dwordx2 %0, %1, %2 offset:%3" : "=v"(d) : "v"(vo), "s"(sb), "i"(O) : "memory"); }
template <int O> DI void st64o(unsigned vo, void* sb, v2u d) { asm volatile("global_store_dwordx2 %0, %1, %2 offset:%3" ::"v"(vo), "v"(d), "s"(sb), "i"(O) : "memory"); }
template <int O> DI void st64co(unsigned vo, void* sb, v2u d) { asm volatile("global_store_dwordx2 %0, %1, %2 offset:%3 sc1" ::"v"(vo), "v"(d), "s"(sb), "i"(O) : "memory"); }
DI void st128c(unsigned vo, void* sb, v4u d) { asm volatile("global_store_dwordx4 %0, %1, %2 sc1\n\ts_nop 1" ::"v"(vo), "v"(d), "s"(sb) : "memory"); }
DI void st64u(unsigned vo, void* sb, v2u d) { asm volatile("global_store_dwordx2 %0, %1, %2 sc1 ; CNT" ::"v"(vo), "v"(d), "s"(sb) : "memory"); }
DI void st128u(unsigned vo, void* sb, v4u d) { asm volatile("global_store_dwordx4 %0, %1, %2 sc1 ; CNT\n\ts_nop 1" ::"v"(vo), "v"(d), "s"(sb) : "memory"); }
DI void dma16(unsigned vo, const void* sb, unsigned lds) {
  asm volatile("s_mov_b32 m0, %2\n\ts_nop 0\n\tglobal_load_lds_dwordx4 %0, %1" ::"v"(vo), "s"(sb), "s"(lds) : "memory");
}
template <int O> DI void dma16o(unsigned vo, const void* sb) {
  asm volatile("global_load_lds_dwordx4 %0, %1 offset:%2" ::"v"(vo), "s"(sb), "i"(O) : "memory");
}
template <bool HIONLY> DI v2u spin64(v2u cur, unsigned vo, char* ws, unsigned tlo, unsigned thi) {
  if (__builtin_expect(__builtin_amdgcn_ballot_w64(HIONLY ? (cur.y != tlo) : (cur.x != tlo || cur.y != thi)) != 0ull, 0)) {
#pragma nounroll
    for (unsigned spins = 0; spins < SPIN_CAP; ++spins) {
      __builtin_amdgcn_s_sleep(6);
      asm volatile("global_load_dwordx2 %0, %1, %2 sc1\n\ts_waitcnt vmcnt(0)" : "=&v"(cur) : "v"(vo), "s"(ws) : "memory");
      if (__builtin_amdgcn_ballot_w64(HIONLY ? (cur.y != tlo) : (cur.x != tlo || cur.y != thi)) == 0ull) break;
    }
  }
  return cur;
}
DI unsigned lw(LAS char* L, unsigned off) { return *(volatile LAS unsigned*)(L + off); }
DI void lds_wait_at(unsigned a, unsigned target) {
  unsigned v, cnt; u64 t;
  asm volatile("s_mov_b32 %1, %5\n"
               "1:\n\tds_read_b32 %0, %3\n\ts_sub_u32 %1, %1, 1\n\ts_waitcnt lgkmcnt(0)\n\tv_cmp_gt_u32 vcc, %4, %0\n\t"
               "s_cselect_b64 %2, 0, vcc\n\ts_cmp_lg_u64 %2, 0\n\ts_cbranch_scc1 1b"
               : "=&v"(v), "=&s"(cnt), "=&s"(t) : "v"(a), "s"(target), "i"(4 * SPIN_CAP) : "vcc", "scc", "memory");
}
DI void lds_wait_ge(LAS char* L, unsigned off, unsigned target) { lds_wait_at((unsigned)(size_t)L + off, target); }
template <int N> DI void lds_wait_min(LAS char* L, unsigned off, unsigned target, int lane) {
  lds_wait_at((unsigned)(size_t)L + off + (lane & (N - 1)) * 4, target);
}
DI void lds_set(LAS char* L, unsigned off, unsigned v) { FENCE(); *(volatile LAS unsigned*)(L + off) = v; FENCE(); }
DI void lds_arrive(LAS char* L, unsigned off, int) {
  asm volatile("s_mov_b64 exec, 1\n\tds_add_u32 %0, %1\n\ts_mov_b64 exec, -1" ::"v"((unsigned)(size_t)L + off), "v"(1u) : "memory");
}
template <unsigned N = 4> DI void arrive_flag(LAS char* L, unsigned off, int, char* ws, unsigned fo, unsigned tlo, unsigned thi) {
  unsigned old;
  asm volatile("s_mov_b64 exec, 1\n\tds_add_rtn_u32 %0, %1, %2\n\ts_waitcnt lgkmcnt(0)\n\tv_cmp_eq_u32 vcc, %6, %0\n\t"
               "s_mov_b64 exec, vcc\n\tglobal_store_dwordx2 %3, %4, %5 sc1\n\ts_mov_b64 exec, -1"
               : "=&v"(old) : "v"((unsigned)(size_t)L + off), "v"(1u), "v"(fo), "v"(v2u{tlo, thi}), "s"(ws), "i"(N - 1) : "vcc", "memory");
}
DI void arrive_flag8(LAS char* L, unsigned off, char* ws, unsigned fo, unsigned tlo, unsigned thi) {
  unsigned old;
  asm volatile("s_mov_b64 exec, 1\n\tds_add_rtn_u32 %0, %1, %2\n\ts_waitcnt lgkmcnt(0)\n\tv_cmp_eq_u32 vcc, 3, %0\n\t"
               "s_mul_i32 exec_lo, vcc_lo, 0xff\n\tglobal_store_dwordx2 %3, %4, %5 sc1\n\ts_mov_b64 exec, -1"
               : "=&v"(old) : "v"((unsigned)(size_t)L + off), "v"(1u), "v"(fo), "v"(v2u{tlo, thi}), "s"(ws) : "vcc", "memory");
}
typedef float f32x16 __attribute__((ext_vector_type(16)));
DI f32x16 mfma32(v4u a, v4u b, f32x16 c) {
  return __builtin_amdgcn_mfma_f32_32x32x16_bf16(__builtin_bit_cast(bf16x8, a), __builtin_bit_cast(bf16x8, b), c, 0, 0, 0);
}
template <int C> DI float dpp_add(float v) {
  return v + __builtin_bit_cast(float, __builtin_amdgcn_update_dpp(0, __builtin_bit_cast(int, v), C, 0xf, 0xf, true));
}
DI float rl(float v, int l) { return __builtin_bit_cast(float, __builtin_amdgcn_readlane(__builtin_bit_cast(int, v), l)); }
DI float half_sum(float v, bool upper) {
  v = dpp_add<0xB1>(v); v = dpp_add<0x4E>(v); v = dpp_add<0x141>(v); v = dpp_add<0x140>(v);
  const float a = rl(v, 0) + rl(v, 16), c = rl(v, 32) + rl(v, 48);
  return upper ? c : a;
}
DI f32x2 unpk(unsigned v) { return f32x2{bf2f(v), bfhi(v)}; }
struct c2 { f32x2 r, i; };
DI c2 cmul(c2 a, c2 b) { return c2{__builtin_elementwise_fma(a.r, b.r, -(a.i * b.i)), __builtin_elementwise_fma(a.r, b.i, a.i * b.r)}; }
DI c2 csq(c2 v) { return c2{__builtin_elementwise_fma(v.r, v.r, -(v.i * v.i)), (v.r + v.r) * v.i}; }
DI c2 cexpk(f32x2 e1, f32x2 th, f32x2 tl, float k, float ks) {
  const f32x2 e = e1 * k, t = th * ks;
  const f32x2 mag = f32x2{__builtin_amdgcn_exp2f(e.x), __builtin_amdgcn_exp2f(e.y)};
  const f32x2 fr = __builtin_elementwise_fma(tl, f32x2{ks, ks}, f32x2{__builtin_amdgcn_fractf(t.x), __builtin_amdgcn_fractf(t.y)});
  return c2{f32x2{__builtin_amdgcn_cosf(fr.x), __builtin_amdgcn_cosf(fr.y)} * mag, f32x2{__builtin_amdgcn_sinf(fr.x), __builtin_amdgcn_sinf(fr.y)} * mag};
}
typedef _Float16 f16x8 __attribute__((ext_vector_type(8)));
typedef _Float16 f16x2 __attribute__((ext_vector_type(2)));
DI f32x4 mfmah(v4u a, v4u b, f32x4 c) {
  return __builtin_amdgcn_mfma_f32_16x16x32_f16(__builtin_bit_cast(f16x8, a), __builtin_bit_cast(f16x8, b), c, 0, 0, 0);
}
DI unsigned pkh1(float a, float b) { f16x2 r; r.x = (_Float16)a; r.y = (_Float16)b; return __builtin_bit_cast(unsigned, r); }
DI v2u pkh(c2 v) { return v2u{pkh1(v.r.x, v.i.x), pkh1(v.r.y, v.i.y)}; }
DI v2u pkb(c2 v) { return v2u{pk2(v.r.x, v.i.x), pk2(v.r.y, v.i.y)}; }
#define HMUL(o, i, w) "v_pk_mul_f16 " o ", " i ", " w " op_sel:[1,1] op_sel_hi:[1,0] neg_lo:[0,1]\n\t"
#define HFMA(o, i, w) "v_pk_fma_f16 " o ", " i ", " w ", " o " op_sel_hi:[0,1,1]\n\t"
constexpr int KROW = 528;
template <int R0, int R1> DI void h2(v2u& o0, v2u& o1, v2u i0, v2u i1, v2u w0, v2u w1) {
  unsigned a, b, c, d;
  asm volatile(HMUL("%0", "%4", "%8") HMUL("%1", "%5", "%9") HMUL("%2", "%6", "%10") HMUL("%3", "%7", "%11")
               HFMA("%0", "%4", "%8") HFMA("%1", "%5", "%9") HFMA("%2", "%6", "%10") HFMA("%3", "%7", "%11")
               "ds_write_addtid_b32 %0 offset:%12\n\tds_write_addtid_b32 %1 offset:%13\n\tds_write_addtid_b32 %2 offset:%14\n\tds_write_addtid_b32 %3 offset:%15"
               : "=&v"(a), "=&v"(b), "=&v"(c), "=&v"(d)
               : "v"(i0.x), "v"(i0.y), "v"(i1.x), "v"(i1.y), "v"(w0.x), "v"(w0.y), "v"(w1.x), "v"(w1.y),
                 "i"(R0 * KROW), "i"(R0 * KROW + 256), "i"(R1 * KROW), "i"(R1 * KROW + 256) : "memory");
  o0 = v2u{a, b}; o1 = v2u{c, d};
}
template <int R> DI void hput(v2u v) {
  asm volatile("ds_write_addtid_b32 %0 offset:%2\n\tds_write_addtid_b32 %1 offset:%3" ::"v"(v.x), "v"(v.y), "i"(R * KROW), "i"(R * KROW + 256) : "memory");
}
template <int C> DI unsigned dpp_maxu(unsigned v) { return max(v, (unsigned)__builtin_amdgcn_update_dpp(0, (int)v, C, 0xf, 0xf, true)); }
DI void rd8(v4u (&f)[8], LAS char* sr) {
#pragma unroll
  for (int k = 0; k < 8; ++k) f[k] = *(LAS v4u*)(sr + 64 * k);
}
template <bool H> DI void kmm(f32x4 (&ka)[2][2][2], int nh, const v4u (&UA)[2][8], const v4u (&vb)[8]) {
#pragma unroll
  for (int k = 0; k < 8; ++k)
#pragma unroll
    for (int m = 0; m < 2; ++m) {
      f32x4& a = ka[(k >> 1) & 1][m][nh];
      const f32x4 c = (k == 0 || k == 2) ? f32x4{0.f, 0.f, 0.f, 0.f} : a;
      a = H ? mfmah(UA[m][k], vb[k], c) : mfma(UA[m][k], vb[k], c);
    }
}
DI void kgen_slow(c2 a, c2 W1, c2 W256, c2 W8, LAS char* sr, f32x4 (&ka)[2][2][2]) {
  v4u UA[2][8], vb[8];
  c2 u[8]; u[0] = a;
#pragma unroll
  for (int i = 1; i < 8; ++i) u[i] = cmul(u[i - 1], W1);
  const c2 W512 = csq(W256), W1024 = csq(W512), r8 = cmul(a, W256), r10 = cmul(u[4], W256), r12 = cmul(a, W512), r14 = cmul(r8, W512), r16 = cmul(a, W1024);
  hput<0>(pkb(u[0])); hput<1>(pkb(u[1])); hput<2>(pkb(u[2])); hput<3>(pkb(u[3])); hput<4>(pkb(u[4])); hput<5>(pkb(u[5])); hput<6>(pkb(u[6])); hput<7>(pkb(u[7]));
  hput<8>(pkb(r8)); hput<9>(pkb(cmul(u[2], W256))); hput<10>(pkb(r10)); hput<11>(pkb(cmul(u[6], W256)));
  hput<12>(pkb(r12)); hput<13>(pkb(cmul(u[4], W512))); hput<14>(pkb(r14)); hput<15>(pkb(cmul(r10, W512)));
  rd8(UA[0], sr);
  hput<0>(pkb(r16)); hput<1>(pkb(cmul(r8, W1024))); hput<2>(pkb(cmul(r12, W1024))); hput<3>(pkb(cmul(r14, W1024))); hput<4>(pkb(cmul(r16, W1024)));
  rd8(UA[1], sr);
  c2 v = c2{f32x2{1.f, 1.f}, f32x2{0.f, 0.f}};
#pragma unroll
  for (int nh = 0; nh < 2; ++nh) {
#define VR(r) hput<r>(pkb(v)); v = cmul(v, W8);
    VR(0) VR(1) VR(2) VR(3) VR(4) VR(5) VR(6) VR(7) VR(8) VR(9) VR(10) VR(11) VR(12) VR(13) VR(14) VR(15)
#undef VR
    rd8(vb, sr);
    kmm<false>(ka, nh, UA, vb);
  }
}
DI void kgen(const unsigned (&pr)[7], bool dead, bool upper, unsigned sw, LAS char* sr, f32x4 (&ka)[2][2][2], float (&scale)[2]) {
  const float dt = rbf(__builtin_amdgcn_exp2f(bf2f(pr[6]) * 1.44269504088896341f));
  const f32x2 zr = unpk(pr[0]) * dt, zi = unpk(pr[1]) * dt, dbr = unpk(pr[2]) * dt, dbi = unpk(pr[3]) * dt;
  const f32x2 cr = unpk(dead ? 0u : pr[4]), ci = unpk(dead ? 0u : pr[5]);
  const c2 a = c2{__builtin_elementwise_fma(cr, dbr, -(ci * dbi)), -__builtin_elementwise_fma(cr, dbi, ci * dbr)};
  const f32x2 e1 = zr * 1.44269504088896341f, th = zi * 0.15915494309189535f;
  const f32x2 tl = __builtin_elementwise_fma(zi, f32x2{6.4206383e-9f, 6.4206383e-9f}, __builtin_elementwise_fma(zi, f32x2{0.15915494309189535f, 0.15915494309189535f}, -th));
  const c2 W1 = cexpk(e1, th, tl, 1.f, -1.f), W256 = cexpk(e1, th, tl, 256.f, -256.f), W8 = cexpk(e1, th, tl, 8.f, 8.f);
  asm volatile("s_mov_b32 m0, %0\n\ts_nop 0" ::"s"(sw) : "memory");
  if (__builtin_expect(__builtin_amdgcn_ballot_w64(!(zr.x <= 0.f && zr.y <= 0.f)) != 0ull, 0)) {
    kgen_slow(a, W1, W256, W8, sr, ka);
    scale[0] = scale[1] = 1.f;
    return;
  }
  unsigned am = max(max(__builtin_bit_cast(unsigned, a.r.x) & 0x7fffffffu, __builtin_bit_cast(unsigned, a.r.y) & 0x7fffffffu),
           max(__builtin_bit_cast(unsigned, a.i.x) & 0x7fffffffu, __builtin_bit_cast(unsigned, a.i.y) & 0x7fffffffu));
  am = dpp_maxu<0xB1>(am); am = dpp_maxu<0x4E>(am); am = dpp_maxu<0x141>(am); am = dpp_maxu<0x140>(am);
  const unsigned ex0 = min(max(max((unsigned)__builtin_amdgcn_readlane(am, 0), (unsigned)__builtin_amdgcn_readlane(am, 16)) >> 23, 2u), 252u);
  const unsigned ex1 = min(max(max((unsigned)__builtin_amdgcn_readlane(am, 32), (unsigned)__builtin_amdgcn_readlane(am, 48)) >> 23, 2u), 252u);
  scale[0] = __builtin_bit_cast(float, ex0 << 23); scale[1] = __builtin_bit_cast(float, ex1 << 23);
  const float down = __builtin_bit_cast(float, ((254u - (upper ? ex1 : ex0)) << 23));
  const c2 W2 = csq(W1), W512 = csq(W256), W16 = csq(W8), W4 = csq(W2), W1024 = csq(W512), W32 = csq(W16), W64 = csq(W32), W128 = csq(W64);
  const v2u u0 = pkh(c2{a.r * down, a.i * down}), h1 = pkh(W1), h2_ = pkh(W2), h4 = pkh(W4), h256 = pkh(W256), h512 = pkh(W512), h1024 = pkh(W1024);
  v2u u2, u4, u6, r8, r10, r12, r14, r16, t0, t1;
  v4u UA[2][8], vb[8];
  hput<0>(u0);
  h2<2, 4>(u2, u4, u0, u0, h2_, h4); h2<8, 12>(r8, r12, u0, u0, h256, h512); h2<1, 3>(t0, t1, u0, u2, h1, h1); h2<5, 6>(t0, u6, u4, u4, h1, h2_);
  h2<10, 14>(r10, r14, r8, r8, h4, h512); h2<7, 9>(t0, t1, u6, r8, h1, h2_); h2<11, 13>(t0, t1, r10, r12, h2_, h4);
  h2<15, 15>(r16, t1, u0, r14, h1024, h4);
  rd8(UA[0], sr);
  hput<0>(r16);
  h2<1, 2>(t0, t1, r8, r12, h1024, h1024); h2<3, 4>(t0, t1, r14, r16, h1024, h1024);
  rd8(UA[1], sr);
  v2u v[16];
  v[1] = pkh(W8); v[2] = pkh(W16); v[4] = pkh(W32); v[8] = pkh(W64);
  const v2u h128 = pkh(W128);
  hput<0>(v2u{0x3c00u, 0x3c00u}); hput<1>(v[1]); hput<2>(v[2]); hput<4>(v[4]); hput<8>(v[8]);
  h2<3, 5>(v[3], v[5], v[2], v[4], v[1], v[1]); h2<6, 9>(v[6], v[9], v[4], v[8], v[2], v[1]); h2<7, 10>(v[7], v[10], v[4], v[8], v[3], v[2]);
  h2<11, 12>(v[11], v[12], v[8], v[8], v[3], v[4]); h2<13, 14>(v[13], v[14], v[8], v[8], v[5], v[6]); h2<15, 15>(t0, v[15], v[1], v[8], h128, v[7]);
  rd8(vb, sr);
  hput<0>(h128); hput<1>(t0);
  h2<2, 3>(t0, t1, v[2], v[3], h128, h128); h2<4, 5>(t0, t1, v[4], v[5], h128, h128);
  kmm<true>(ka, 0, UA, vb);
  h2<6, 7>(t0, t1, v[6], v[7], h128, h128); h2<8, 9>(t0, t1, v[8], v[9], h128, h128); h2<10, 11>(t0, t1, v[10], v[11], h128, h128);
  h2<12, 13>(t0, t1, v[12], v[13], h128, h128); h2<14, 15>(t0, t1, v[14], v[15], h128, h128);
  rd8(vb, sr);
  SCHED();
  kmm<true>(ka, 1, UA, vb);
}
DI void kgen_store(LAS float* pt, const f32x4 (&kacc)[2][2], int lane) {
  const int l15 = lane & 15, g = lane >> 4;
  const int sh = g < 2 ? 3 : 4 - g;
  const int a0 = (l15 << sh) + (g < 2 ? 4 * g : 128 * g);
  const int a1 = g < 3 ? a0 + 2 : a0 + 64;
#pragma unroll
  for (int nh = 0; nh < 2; ++nh) {
    *(LAS f32x2*)(pt + a0 + ((16 * nh) << sh)) = kacc[0][nh].xy;
    *(LAS f32x2*)(pt + a1 + ((16 * nh) << sh)) = kacc[0][nh].zw;
  }
  if (g == 0) {
#pragma unroll
    for (int nh = 0; nh < 2; ++nh)
#pragma unroll
      for (int r = 0; r < 4; ++r) pt[512 + 32 * r + 16 * nh + l15] = kacc[1][nh][r];
  }
  if (lane == 16) { *(LAS f32x4*)(pt + 640) = f32x4{kacc[1][0][0], 0.f, 0.f, 0.f}; }
}

template <int PAR> DI void conv_unit_shared(LAS char* ka, LAS char* ub, f32x4 (&acc)[4]) {
  constexpr int T[4] = {PAR ? 1 : 0, PAR ? 2 : 3, PAR ? 5 : 4, PAR ? 6 : 7};
  v4u A[8], B[8];
#pragma unroll
  for (int i = 0; i <= T[3]; ++i) { A[i] = *(LAS v4u*)(ka + 512 * (7 - i)); B[i] = *(LAS v4u*)(ub + 512 * i); }
  SCHED();
#pragma unroll
  for (int q = 0; q < 4; ++q)
#pragma unroll
    for (int ip = 0; ip <= T[q]; ++ip) acc[q] = mfma(A[T[q] - ip], B[ip], acc[q]);
  SCHED();
}
DI float fadd(float a, float b) { float r; asm("v_add_f32 %0, %1, %2" : "=v"(r) : "v"(a), "v"(b)); return r; }
DI float ffma(float a, float b, float c) { float r; asm("v_fma_f32 %0, %1, %2, %3" : "=v"(r) : "v"(a), "v"(b), "v"(c)); return r; }
DI f32x4 add4(f32x4 a, f32x4 b) { return f32x4{fadd(a.x, b.x), fadd(a.y, b.y), fadd(a.z, b.z), fadd(a.w, b.w)}; }
DI f32x4 fma4(f32x4 a, float b, f32x4 c) { return f32x4{ffma(a.x, b, c.x), ffma(a.y, b, c.y), ffma(a.z, b, c.z), ffma(a.w, b, c.w)}; }
DI int rot12(int v) { return v >= 12 ? v - 12 : v; }
DI void w_loads(v4u (&WF)[5], const char* p, unsigned vo) {
  ld128c<0>(WF[0], vo, p); ld128c<1024>(WF[1], vo, p); ld128c<2048>(WF[2], vo, p); ld128c<3072>(WF[3], vo, p); ld128c<0>(WF[4], vo, p + 4096);
}
DI void a_loads(v4u (&AF)[4], const char* p, const unsigned (&vm)[4]) {
#pragma unroll
  for (int m = 0; m < 4; ++m) ld128c<0>(AF[m], vm[m], p);
}
template <int LIVE, class F> DI void g2_step(f32x4 (&acc)[4][5], v4u (&WF)[5], v4u (&AF)[4], F ld) {
#pragma unroll
  for (int m = 0; m < 4; ++m) PIN(AF[m]);
#pragma unroll
  for (int n = 0; n < 5; ++n) PIN(WF[n]);
#pragma unroll
  for (int e = 0; e < 20; ++e) {
    const int m = e / 5, n = e % 5;
    acc[m][n] = mfma(WF[n], AF[m], m >= LIVE ? f32x4{0.f, 0.f, 0.f, 0.f} : acc[m][n]);
    if (e & 1) { SCHED(); ld(e >> 1); SCHED(); }
  }
}
DI void make_kix(unsigned (&kix)[9], int ts) {
#pragma unroll
  for (int i = 0; i < 9; ++i) {
    const int lg = 2047 - 8 * ts - i;
    const int bits = (int)(pk2(__builtin_fmaxf((float)lg, 128.f), 0.f));
    const int ci = lg < bits - 0x4280 ? lg : bits - 0x4280;
    kix[i] = KTAB + 4 * ci;
    PIN(kix[i]);
  }
}
DI void put_k(LAS char* L, const unsigned (&kix)[9], int ts, int c, unsigned reg) {
  float kv[9];
#pragma unroll
  for (int i = 0; i < 9; ++i) kv[i] = *(LAS float*)(L + kix[i] + c * KP_STRIDE);
  LAS char* q = L + reg + CV_KOFF + ts * 16;
  *(LAS v4u*)(q + CV_KSTR) = v4u{pk2(kv[0], kv[1]), pk2(kv[2], kv[3]), pk2(kv[4], kv[5]), pk2(kv[6], kv[7])};
  *(LAS v4u*)q = v4u{pk2(kv[1], kv[2]), pk2(kv[3], kv[4]), pk2(kv[5], kv[6]), pk2(kv[7], kv[8])};
}
DI void put_u(LAS char* q, v4u u, bool first) {
  *(LAS v4u*)(q + 3 * CV_USTR + 384) = u;
  *(LAS v2u*)(q + 1 * CV_USTR + 392) = v2u{u.x, u.y}; *(LAS v2u*)(q + 1 * CV_USTR + 400) = v2u{u.z, u.w};
  *(LAS unsigned*)(q + 2 * CV_USTR + 388) = u.x; *(LAS v2u*)(q + 2 * CV_USTR + 392) = v2u{u.y, u.z}; *(LAS unsigned*)(q + 2 * CV_USTR + 400) = u.w;
  *(LAS unsigned*)(q + 396) = u.x; *(LAS v2u*)(q + 400) = v2u{u.y, u.z}; *(LAS unsigned*)(q + 408) = u.w;
  if (first) {
    *(LAS v2u*)(q + 384) = v2u{0u, 0u}; *(LAS unsigned*)(q + 392) = 0u;
    *(LAS v2u*)(q + CV_USTR + 384) = v2u{0u, 0u}; *(LAS unsigned*)(q + 2 * CV_USTR + 384) = 0u;
  }
}

__global__ __launch_bounds__(512, 2) void fused_s4_mlp(const char* __restrict__ x, const char* __restrict__ pAre, const char* __restrict__ pAim,
    const char* __restrict__ pBre, const char* __restrict__ pBim, const char* __restrict__ pCre, const char* __restrict__ pCim,
    const char* __restrict__ plogdt, const char* __restrict__ pDskip, const char* __restrict__ w1, const char* __restrict__ b1,
    const char* __restrict__ w2, char* __restrict__ out, char* __restrict__ ws) {
  __shared__ __attribute__((aligned(16))) char smem[163840];
  LAS char* L = (LAS char*)smem;
  const unsigned lds0 = (unsigned)(size_t)L;
  const char* pc0; unsigned codelen;
  asm volatile("s_getpc_b64 %0\n98:\n\ts_mov_b32 %1, 97f-98b\n\ts_max_u32 %1, %1, 99f-98b" : "=s"(pc0), "=&s"(codelen) :: "scc");
  const int b = blockIdx.x, lane = threadIdx.x & 63;
  const int w = RFL(threadIdx.x >> 6), wi = w & 3;
  {
    const unsigned co = (unsigned)(4 * (16 * w + (lane & 15)) + ((b >> 3) & 3)) * 128;
    asm volatile("s_mov_b32 m0, %2\n\ts_nop 0\n\tglobal_load_lds_dword %0, %1" ::"v"((co < codelen ? co : codelen) & ~3u), "s"(pc0), "s"(lds0 + YST + w * 256) : "memory");
  }
  const int xcd = b & 7, j = b >> 3, rg = xcd >> 1, chf = xcd & 1;
  const int ct1 = 8 * chf + (j & 7), rb1 = 4 * rg + (j >> 3);
  const int wid = rb1 * 16 + ct1;
  const int a2 = 4 * chf + (j >> 3), cb2 = j & 7;
  const int l15 = lane & 15, g = lane >> 4, qq = (lane >> 2) & 3, p4 = lane & 3, l31 = lane & 31, h = lane >> 5;
  const int par = j & 1, cbase = 80 * xcd + 5 * (j >> 1);
  const int chA = cbase + 3 * par, chS = cbase + 2;
  const int ts = 64 * wi + lane;
  ST(0);
  unsigned pr[7]; v4u xa, xb;
  {
    const bool odd = wi & 1, shl = odd && h, mw = w < 4;
    const int kch = chA + (wi >> 1);
    unsigned p0 = (kch * 192 + (wi & 1) * 128 + 2 * lane) * 2, p1 = (chS * 192 + par * 96 + (wi >> 1) * 48 + 2 * min(l31, 23)) * 2, x0 = h * 1280 + l31 * 8;
    PIN(p0); PIN(p1); PIN(x0);
    const unsigned po = shl ? p1 : p0, mm = mw ? ~0u : 0u;
    const unsigned xo = (x0 + l31 * 8) & ~mm, vo = (po & mm) | ((x0 + 1024) & ~mm), v1 = po & mm;
    asm volatile("" ::"s"(x), "s"(pAre), "s"(pAim), "s"(pBre), "s"(pBim), "s"(pCre), "s"(pCim), "s"(plogdt), "s"(ws));
    const char* xrow = x + (size_t)(8 * wid + 2 * wi) * 1280;
    ld16(pr[6], (unsigned)(shl ? chS : kch) * 2, plogdt);
    ld32<0>(pr[0], vo, mw ? pAre : xrow); ld32<0>(pr[1], vo, mw ? pAim : xrow + 4);
    ld32c(pr[2], v1, mw ? pBre : ws); ld32c(pr[3], v1, mw ? pBim : ws + 4);
    ld32<0>(pr[4], po, pCre); ld32<0>(pr[5], po, pCim);
    ld128(xa, xo, xrow); ld128(xb, xo, xrow + 512);
  }
  const int row2 = 512 * rg + 64 * a2 + 16 * wi;
  const char* w2b = ws + WS_W2T + (size_t)(cb2 * 48 + 12 * wi) * 5120;
  const char* actb = ws + WS_ACT + (size_t)((32 * rg + 4 * a2) * 48 + 12 * wi) * 1024;
  const int r2 = ((a2 ^ (w >> 2)) & 1) * 6;
#define WL(s) w_loads(WF[s], w2b + rot12(r2 + (s)) * 5120, vo)
#define AL(s) a_loads(AF[s], actb + rot12(r2 + (s)) * 1024, vm)
#define AL1(s, m) ld128c<0>(AF[s][m], vm[m], actb + rot12(r2 + (s)) * 1024)
#define WL1(s, n) ld128c<0>(WF[s][n], vo, w2b + rot12(r2 + (s)) * 5120 + (n) * 1024)
#define NOLD [&](int) {}
#define LATE_LANE() int ln_ = lane; PIN(ln_); const int c15 = ln_ & 15, cg = ln_ >> 4
#define KAO (CV_KOFF + (c15 & 1) * CV_KSTR + 2 * (2072 - 8 * (c15 >> 1) + 8 * cg))
#define UBO ((c15 & 3) * CV_USTR + 2 * (64 * (c15 >> 2) + 8 * cg + 224))
#define YOFF ((unsigned)((c15 >> 2) * 128 + cg * 32 + (c15 & 3) * 8))
#define OROW ((unsigned)(2 * (c15 >> 2) + (c15 & 1) + 8 * ((c15 >> 1) & 1)))
#define ZOFF (lane < 24 ? wi * CV_USTR + lane * 16 : CV_KOFF + wi * CV_KSTR + 4096 + (lane - 24) * 16)
#define ZCOND (lane < 24 || (lane < 31 && wi < 2))

  unsigned voA[4], voB[3];
#pragma unroll
  for (int i = 0; i < 4; ++i) voA[i] = (8 * i + (lane >> 3)) * 1280 + (((lane & 7) ^ (4 * (i & 1) + (lane >> 4))) * 16) - 1024 * i;
#pragma unroll
  for (int i = 0; i < 3; ++i) { const int c = 64 * i + lane; voB[i] = (c / 12) * 3072 + (c % 12) * 16 - 1024 * i; }
  const char* g1a = x + (size_t)(rb1 * 128 + 32 * wi) * 1280;
  const char* g1b = w1 + ct1 * 192 + wi * 49152;
  const unsigned la = lds0 + G1_RING + wi * 4096, lb = lds0 + G1_RING + G1_BOFF + wi * 3072;
  const int r1 = ((j & 7) + (5 * (j >> 3)) / 2 + 5 * chf + rg) % 10;
  auto slab = [&](int s) { const int v = s + r1; return v >= 10 ? v - 10 : v; };
  auto issue = [&](int s) {
    const char* pa = g1a + slab(s) * 128; const char* pb = g1b + (size_t)slab(s) * 64 * 3072;
    dma16(voA[0], pa, la + s * G1_STAGE); dma16o<1024>(voA[1], pa); dma16o<2048>(voA[2], pa); dma16o<3072>(voA[3], pa);
    dma16(voB[0], pb, lb + s * G1_STAGE); dma16o<1024>(voB[1], pb); dma16o<2048>(voB[2], pb);
  };
  unsigned tlo = 0, thi = 0;
  unsigned kix[9];
  const int pu = b % 240, pks = pu / 5, pcg = pu % 5;
  if (w < 4) {
    bar();
#pragma unroll
    for (int i = 0; i < 9; ++i) kix[i] = 0;
    waitv<0>();
  } else {
    if (w == 4) *(LAS unsigned*)(L + lane * 4) = 0u;
    bar();
#pragma unroll
    for (int i = 0; i < 2; ++i)
      dma16((unsigned)((lane >> 1) * 1280 + (lane & 1) * 16), w2 + (size_t)pks * 32 * 1280 + (128 * pcg + 32 * wi + 16 * i) * 2, lds0 + W2P + wi * 2048 + i * 1024);
    issue(0);
    make_kix(kix, ts);
    issue(1);
    waitv<14>();
  }
  asm volatile("; AUDITCLR");
  PIN(xa); PIN(xb);
#pragma unroll
  for (int i = 0; i < 7; ++i) PIN(pr[i]);
  ST(1);
  int role0 = w; asm volatile("" : "+s"(role0));
  if (role0 < 4) {
    const bool odd = wi & 1;
    f32x4 ka[2][2][2]; float scale[2];
    kgen(pr, odd && lane >= 56, h, lds0 + KG_SCR + wi * 2 * KG_WSTR, L + KG_SCR + wi * 2 * KG_WSTR + l15 * KROW + g * 16, ka, scale);
#pragma unroll
    for (int i = 0; i < 4; ++i) {
      ka[1][i >> 1][i & 1] *= scale[1];
      ka[0][i >> 1][i & 1] = ka[0][i >> 1][i & 1] * scale[0] + (odd ? f32x4{0.f, 0.f, 0.f, 0.f} : ka[1][i >> 1][i & 1]);
    }
    kgen_store((LAS float*)(L + KG_SCR + wi * 2 * KG_WSTR), ka[0], lane);
    if (odd) kgen_store((LAS float*)(L + KG_SCR + wi * 2 * KG_WSTR + KG_WSTR), ka[1], lane);
    lds_arrive(L, W_KG, lane);
  } else {
    const v2u xc = v2u{pr[0], pr[1]}, E = v2u{pr[2], pr[3]};
    ST(6);
  const u64 tag64 = (((u64)RFL(E.y) << 32) | (unsigned)RFL(E.x)) + 1ull;
  tlo = (unsigned)tag64; thi = (unsigned)(tag64 >> 32);
  if (wi == 0) { lds_set(L, W_TAG, tlo); lds_set(L, W_TAG + 4, thi); lds_set(L, W_TAG + 8, 1); }
  {
    f32x2 v[10] = {unpk(xa.x), unpk(xa.y), unpk(xa.z), unpk(xa.w), unpk(xb.x), unpk(xb.y), unpk(xb.z), unpk(xb.w), unpk(xc.x), unpk(xc.y)};
    f32x2 s2 = ((v[0] + v[1]) + (v[2] + v[3])) + ((v[4] + v[5]) + (v[6] + v[7])) + (v[8] + v[9]);
    const float mean = half_sum(s2.x + s2.y, h) * (1.0f / 640.0f);
    f32x2 q2 = f32x2{0.f, 0.f};
#pragma unroll
    for (int i = 0; i < 10; ++i) { const f32x2 d = v[i] - mean; q2 = __builtin_elementwise_fma(d, d, q2); }
    const float var = rbf(half_sum(q2.x + q2.y, h) * (1.0f / 640.0f));
    const float m = rbf(mean), sd = rbf(__builtin_amdgcn_sqrtf(rbf(var + 1e-5f))), rs = __builtin_amdgcn_rcpf(sd);
    unsigned o[10];
#pragma unroll
    for (int i = 0; i < 10; ++i) { const f32x2 t = (v[i] - m) * rs; o[i] = pk2(t.x, t.y); }
    LAS char* rp = L + LN_ROWS + (2 * wi + h) * LN_PITCH;
    *(LAS v4u*)(rp + l31 * 16) = v4u{o[0], o[1], o[2], o[3]};
    *(LAS v4u*)(rp + 512 + l31 * 16) = v4u{o[4], o[5], o[6], o[7]};
    *(LAS v2u*)(rp + 1024 + l31 * 8) = v2u{o[8], o[9]};
    waitv<0>();
    lds_arrive(L, W_SB, lane);
    issue(2);
    if (l31 == 0) st64c(WS_STATS + (unsigned)(8 * wid + 2 * wi + h) * 8, ws,
                        v2u{(__builtin_bit_cast(unsigned, m) & 0xffff0000u) | (__builtin_bit_cast(unsigned, sd) >> 16), tlo});
  }
#pragma unroll
  for (int i = 0; i < 2; ++i) {
    LAS char* sp = L + W2P + wi * 2048 + i * 1024 + (16 * (g >> 1) + 4 * (g & 1) + qq) * 32 + 8 * p4;
    const v2u lo = trread(sp), hi = trread(sp + 256);
    const int ntg = 8 * pcg + 2 * wi + i;
    st128c((unsigned)lane * 16, ws + WS_W2T + (size_t)(((ntg / 5) * 48 + pks) * 5 + ntg % 5) * 1024, v4u{lo.x, lo.y, hi.x, hi.y});
  }
  lds_wait_ge(L, W_SB, 4);
  {
    LAS char* sp = L + LN_ROWS + qq * LN_PITCH + (160 * wi + 16 * g) * 2 + 8 * p4;
#pragma unroll
    for (int i = 0; i < 3; ++i) {
      const v2u lo = trread(sp + 128 * i), hi = trread(sp + 128 * i + 4 * LN_PITCH);
      const unsigned ch = 64 * i + lane;
      if (i < 2 || lane < 32) st128c((ch / 5) * 20480 + (ch % 5) * 16, ws + WS_XNB + (size_t)wi * 32 * 20480 + wid * 80, v4u{lo.x, lo.y, hi.x, hi.y});
    }
  }
  }
  asm volatile("; AUDITCLR");
  ST(2);
  int role = w; asm volatile("" : "+s"(role));
  if (role < 4) {
    int lane = __builtin_amdgcn_mbcnt_hi(~0u, __builtin_amdgcn_mbcnt_lo(~0u, 0u)); PIN(lane);
    const int l15 = lane & 15, g = lane >> 4, qq = (lane >> 2) & 3, p4 = lane & 3, l31 = lane & 31, h = lane >> 5;
    const unsigned vo = lane * 16;
    f32x16 acc1[3]; f32x4 csa[3];
#pragma unroll
    for (int n = 0; n < 3; ++n) {
      csa[n] = f32x4{0.f, 0.f, 0.f, 0.f};
#pragma unroll
      for (int i = 0; i < 16; ++i) acc1[n][i] = 0.f;
    }
    const int xrow = (l31 & 17) + 2 * (l15 >> 2) + 8 * ((l15 >> 1) & 1);
    unsigned xo[4], wo[4];
#pragma unroll
    for (int k = 0; k < 4; ++k) {
      const int kr = (k + wi) & 3;
      xo[k] = G1_RING + (32 * wi + xrow) * 128 + (((2 * kr + h) ^ ((xrow >> 1) & 7)) * 16);
      wo[k] = G1_RING + G1_BOFF + (16 * kr + 8 * h + qq) * 192 + 32 * (g & 1) + 8 * p4;
      PIN(xo[k]); PIN(wo[k]);
    }
    const unsigned sel1 = ((l15 ^ g) & 1) ? 0u : 0x3f803f80u;
    const v4u sel = v4u{sel1, sel1, sel1, sel1};
    v4u X[4], W[4][3];
    auto rdw = [&](int s, int k, int n) {
      LAS char* p = L + wo[k] + (s % 3) * G1_STAGE + 64 * n; const v2u lo = trread(p), hi = trread(p + 768); W[k][n] = v4u{lo.x, lo.y, hi.x, hi.y};
    };
    auto rdx = [&](int s, int k) { X[k] = *(LAS v4u*)(L + xo[k] + (s % 3) * G1_STAGE); };
#define MM(k, n) acc1[n] = mfma32(W[k][n], X[k], acc1[n])
#define CS(n) csa[n] = mfma(W[0][n], sel, csa[n])
    v2u gran; unsigned bb[3];
    v4u WF[6][5], AF[6][4];
    const unsigned go = WS_STATS + (unsigned)(rb1 * 128 + 32 * wi + xrow) * 8;
    auto wl1 = [&](int s, int e0) { const int e = 7 * (s - 7) + e0; if (s >= 7 && e0 < 7 && e < 20) ld128c<0>(WF[e / 5][e % 5], vo, w2b + rot12(r2 + e / 5) * 5120 + (e % 5) * 1024); };
    lds_wait_ge(L, W_SB, 4);
    ST(3);
    rdx(0, 0); rdw(0, 0, 0); rdw(0, 0, 1); rdw(0, 0, 2); rdx(0, 1); rdw(0, 1, 0); rdw(0, 1, 1); rdw(0, 1, 2);
#pragma unroll
    for (int s = 0; s < 10; ++s) {
      const bool refill = s < 7;
      const char* pa = g1a + slab(s + 3) * 128; const char* pb = g1b + (size_t)slab(s + 3) * 64 * 3072;
      SCHED();
      MM(0, 0); rdx(s, 2); rdw(s, 2, 0); SCHED();
      MM(0, 1); rdw(s, 2, 1); rdw(s, 2, 2); SCHED();
      MM(0, 2); rdx(s, 3); rdw(s, 3, 0); SCHED();
      CS(0); rdw(s, 3, 1); SCHED();
      CS(1); rdw(s, 3, 2); SCHED();
      CS(2);
      if (s == 0) {} else if (s == 1) lds_wait_ge(L, W_ST, 4); else if (s < 7) waitv<7>(); else if (s == 7) waitv<11>(); else if (s == 8) waitv<11>();
      if (s == 9 && l15 < 2) {
#pragma unroll
        for (int n = 0; n < 3; ++n) *(LAS f32x4*)(L + CSP + wi * 384 + (32 * n + 16 * l15 + 4 * g) * 4) = csa[n];
      }
      lds_set(L, W_RDY + wi * 4, s + 2);
      SCHED();
      MM(1, 0);
      if (refill) { asm volatile("s_waitcnt lgkmcnt(7)" ::: "memory"); dma16(voA[0], pa, la + (s % 3) * G1_STAGE); }
      wl1(s, 0);

      SCHED();
      MM(1, 1); if (refill) dma16o<1024>(voA[1], pa); wl1(s, 1);
      unsigned fl = 0;
      if (s < 9) fl = lw(L, W_RDY + p4 * 4);
      SCHED();
      MM(1, 2); if (refill) dma16o<2048>(voA[2], pa); wl1(s, 2); SCHED();
      MM(2, 0); if (refill) dma16o<3072>(voA[3], pa); wl1(s, 3);
      SCHED();
      if (s == 9) {
        MM(2, 1); wl1(s, 4); SCHED();
        lds_wait_min<4>(L, W_RDY, 11, lane);
        float cs[3];
#pragma unroll
        for (int n = 0; n < 3; ++n) { LAS float* p = (LAS float*)(L + CSP) + 32 * n + l31; cs[n] = fadd(fadd(p[0], p[96]), fadd(p[192], p[288])); }
        MM(2, 2); wl1(s, 5); SCHED();
        waitv<20>(); PIN(gran); PIN(bb[0]); PIN(bb[1]); PIN(bb[2]);
        MM(3, 0); MM(3, 1); MM(3, 2);
        const unsigned keep = h ? 0u : ~0u, nm = (gran.x >> 16) ^ 0x8000u;
        const v4u xe = v4u{(nm | (nm << 16)) & keep, (nm | (gran.x << 16)) & keep, 0u, 0u};
#pragma unroll
        for (int n = 0; n < 3; ++n) {
          const float c0 = bfhi(__builtin_bit_cast(unsigned, cs[n])), r0 = cs[n] - c0, c1 = bfhi(__builtin_bit_cast(unsigned, r0)), r1 = r0 - c1;
          const v4u we = v4u{(__builtin_bit_cast(unsigned, c0) >> 16) | __builtin_bit_cast(unsigned, c1), (__builtin_bit_cast(unsigned, r1) >> 16) | (bb[n] << 16), 0u, 0u};
          acc1[n] = mfma32(we, xe, acc1[n]);
        }
        SCHED();
        break;
      }
      if (__builtin_expect(__builtin_amdgcn_ballot_w64(fl < (unsigned)(s + 2)) != 0ull, 0)) lds_wait_min<4>(L, W_RDY, s + 2, lane);
      FENCE();
      SCHED();
      rdx(s + 1, 0); rdw(s + 1, 0, 0); SCHED();
      MM(2, 1); rdw(s + 1, 0, 1); rdw(s + 1, 0, 2); if (refill) dma16(voB[0], pb, lb + (s % 3) * G1_STAGE); wl1(s, 4); SCHED();
      MM(2, 2); rdx(s + 1, 1); rdw(s + 1, 1, 0); if (refill) dma16o<1024>(voB[1], pb); wl1(s, 5); SCHED();
      MM(3, 0); rdw(s + 1, 1, 1); if (refill) dma16o<2048>(voB[2], pb); wl1(s, 6); SCHED();
      MM(3, 1); rdw(s + 1, 1, 2); wl1(s, 7); SCHED();
      MM(3, 2); wl1(s, 8);
      if (s == 6) { lds_wait_ge(L, W_F1ALL, 4); ld64c(gran, go, ws); ld16(bb[0], l31 * 2, b1 + ct1 * 192); ld16(bb[1], l31 * 2, b1 + ct1 * 192 + 64); ld16(bb[2], l31 * 2, b1 + ct1 * 192 + 128); }
      SCHED();
      if (s == 0) ST(4); if (s == 3) ST(5); if (s == 6) ST(7);
    }
#undef MM
#undef CS
    ST(11);
    const unsigned tlo = RFL(lw(L, W_TAG)), thi = RFL(lw(L, W_TAG + 4));
    {
      const float rs = __builtin_amdgcn_rcpf(bf2f(gran.x)) * 1.44269504088896341f;
      char* ab = ws + WS_ACT + (size_t)((rb1 * 8 + 2 * wi) * 48 + 3 * ct1) * 1024;
      const unsigned ao = (unsigned)((l31 >> 4) * 49152 + h * 256 + l15 * 16);
#pragma unroll
      for (int q = 0; q < 6; ++q) {
        unsigned pk[4];
#pragma unroll
        for (int e = 0; e < 4; ++e) {
          const f32x2 h2 = f32x2{acc1[q >> 1][8 * (q & 1) + 2 * e], acc1[q >> 1][8 * (q & 1) + 2 * e + 1]} * rs;
          const f32x2 s1 = f32x2{__builtin_amdgcn_exp2f(h2.x), __builtin_amdgcn_exp2f(h2.y)} + 1.0f;
          const f32x2 d = __builtin_elementwise_fma(s1, s1, f32x2{1.f, 1.f});
          const f32x2 rr = f32x2{__builtin_amdgcn_rcpf(d.x), __builtin_amdgcn_rcpf(d.y)};
          const f32x2 o = h2 * __builtin_elementwise_fma(rr, f32x2{-1.38629436111989062f, -1.38629436111989062f},
                                                         f32x2{0.69314718055994531f, 0.69314718055994531f});
          pk[e] = pk2(o.x, o.y);
        }
        st128c(ao + q * 512, ab, v4u{pk[0], pk[1], pk[2], pk[3]});
      }
    }
    lds_set(L, W_RDY + wi * 4, 12);
    ST(13);
    __builtin_amdgcn_s_setprio(2);
    LATE_LANE();
    v2u xq[5], dq[5], gr2, f3;
    const unsigned f2o = WS_F2 + (unsigned)((4 * rg + (a2 >> 1)) * 16 + 4 * wi + (ln_ & 3)) * 128;
    unsigned vm[4];
#pragma unroll
    for (int m = 0; m < 4; ++m) vm[m] = vo + ((m + wi) & 3) * 49152;
    v2u f2; v4u ysh;
    {
      f32x4 sacc[4];
#pragma unroll
      for (int i = 0; i < 4; ++i) sacc[i] = f32x4{0.f, 0.f, 0.f, 0.f};
      unsigned kb = CVS + KAO - wi * 128 - 3584 - 64, ubb = CVS + UBO - wi * 128 - 64; PIN(kb); PIN(ubb);
      LAS char* ka = L + kb; LAS char* ub = L + ubb;
      const bool late = RFL(lw(L, W_SB)) < 16;
      if (late) { waitv<0>(); arrive_flag<4>(L, W_F2C, lane, ws, WS_F2 + (unsigned)wid * 128, tlo, thi); }
      lds_wait_ge(L, W_SB, 16);
      if (par) conv_unit_shared<1>(ka + 64, ub + 64, sacc); else conv_unit_shared<0>(ka + 64, ub + 64, sacc);
      if (!late) { waitv<0>(); arrive_flag<4>(L, W_F2C, lane, ws, WS_F2 + (unsigned)wid * 128, tlo, thi); }
      ST(14);
      WL(4); WL(5);
      if (par) conv_unit_shared<1>(ka, ub, sacc); else conv_unit_shared<0>(ka, ub, sacc);
#pragma unroll
      for (int q = 0; q < 4; ++q) *(LAS f32x4*)(L + MXCH + (q * 4 + wi) * 1024 + vo) = sacc[q];
      lds_arrive(L, W_MB, lane);
    {
      const char* xb2 = x + (size_t)row2 * 1280 + cb2 * 160; const unsigned xo2 = OROW * 1280 + cg * 8;
      ld64o<0>(xq[0], xo2, xb2); ld64o<32>(xq[1], xo2, xb2); ld64o<64>(xq[2], xo2, xb2); ld64o<96>(xq[3], xo2, xb2); ld64o<128>(xq[4], xo2, xb2);
      const char* db = pDskip + cb2 * 160;
      ld64o<0>(dq[0], cg * 8, db); ld64o<32>(dq[1], cg * 8, db); ld64o<64>(dq[2], cg * 8, db); ld64o<96>(dq[3], cg * 8, db); ld64o<128>(dq[4], cg * 8, db);
      ld64c(gr2, WS_STATS + (row2 + OROW) * 8, ws);
    }
      lds_wait_ge(L, W_MB, 4);
      LAS char* p = L + MXCH + wi * 4096 + vo;
      const f32x4 s = add4(add4(*(LAS f32x4*)p, *(LAS f32x4*)(p + 1024)), add4(*(LAS f32x4*)(p + 2048), *(LAS f32x4*)(p + 3072)));
      ld64c(f2, f2o, ws);
      const unsigned y0 = pk2(s.x, s.y), y1 = pk2(s.z, s.w);
      *(LAS v2u*)(L + MXCH + wi * 4096 + YOFF) = v2u{y0, y1};
      FENCE();
      ysh = *(LAS v4u*)p;
    }
    ST(15);
    waitv<0>(); PIN(f2);
    f2 = spin64<false>(f2, f2o, ws, tlo, thi);
    lds_set(L, W_F2OK + wi * 4, 1);
    ST(16);
    AL(0); AL(1);
    if (ln_ < 32) st128c(vo, ws + WS_Y + (size_t)chS * 4096 + (2 * wi + ((wi & 1) ^ par)) * 512, ysh);
    f32x4 acc2[4][5];
    PIN(gr2);
    const float rs2 = __builtin_amdgcn_rcpf(bf2f(gr2.x)), m2 = -bfhi(gr2.x) * rs2;
#pragma unroll
    for (int n = 0; n < 5; ++n) {
      PIN(xq[n]); PIN(dq[n]);
      const f32x2 x0 = unpk(xq[n].x), x1 = unpk(xq[n].y), d0 = unpk(dq[n].x), d1 = unpk(dq[n].y);
      acc2[0][n] = f32x4{ffma(d0.x, ffma(x0.x, rs2, m2), x0.x), ffma(d0.y, ffma(x0.y, rs2, m2), x0.y),
                         ffma(d1.x, ffma(x1.x, rs2, m2), x1.x), ffma(d1.y, ffma(x1.y, rs2, m2), x1.y)};
      PIN(acc2[0][n]);
    }
    __builtin_amdgcn_s_setprio(0);
    ST(17);
    waitv<4>(); g2_step<1>(acc2, WF[0], AF[0], [&](int i) { if (i >= 3 && (i & 1)) AL1(2, (i - 3) >> 1); });
    waitv<4>();
    arrive_flag<8>(L, W_F3C, lane, ws, WS_F3 + (unsigned)(xcd * 32 + j) * 128, tlo, thi);
    g2_step<4>(acc2, WF[1], AF[1], [&](int i) { if (i >= 2) AL1(3 + ((i - 2) >> 2), (i - 2) & 3); });
    waitv<8>(); g2_step<4>(acc2, WF[2], AF[2], [&](int i) { if (i < 4) AL1(5, i); });
    ST(20);
    waitv<8>(); g2_step<4>(acc2, WF[3], AF[3], NOLD);
    waitv<4>(); g2_step<4>(acc2, WF[4], AF[4], NOLD);
    lds_wait_ge(L, W_F3C, 8);
    const unsigned f3o = WS_F3 + (unsigned)(cb2 * 32 + 8 * wi + (ln_ & 7)) * 128;
    ld64c(f3, f3o, ws);
    waitv<1>(); g2_step<4>(acc2, WF[5], AF[5], NOLD);
    ST(22);
#pragma unroll
    for (int m = 1; m < 4; ++m)
#pragma unroll
      for (int n = 0; n < 5; ++n) *(LAS f32x4*)(L + FM + wi * 15360 + (m - 1) * 5120 + n * 1024 + vo) = acc2[m][n];
    waitv<0>(); PIN(f3);
    f3 = spin64<false>(f3, f3o, ws, tlo, thi);
    lds_arrive(L, W_F3ALL, lane);
    lds_wait_ge(L, W_F3ALL, 4);
    ST(21);
    v4u yv[3];
    {
      const char* yb = ws + WS_Y + (size_t)cb2 * 80 * 4096 + row2 * 2;
      const unsigned yo = (unsigned)(ln_ >> 1) * 4096 + (ln_ & 1) * 16;
      ld128c<0>(yv[0], yo, yb); ld128c<0>(yv[1], yo, yb + 32 * 4096); ld128c<0>(yv[2], yo & 0xffffu, yb + 64 * 4096);
    }
#pragma unroll
    for (int n = 0; n < 5; ++n) {
#pragma unroll
      for (int d = 1; d < 4; ++d) acc2[0][n] = add4(acc2[0][n], *(LAS f32x4*)(L + FM + ((wi - d) & 3) * 15360 + (d - 1) * 5120 + n * 1024 + vo));
      *(LAS f32x4*)(L + FM + 61440 + wi * 5120 + n * 1024 + vo) = acc2[0][n];
    }
    waitv<0>(); PIN(yv[0]); PIN(yv[1]); PIN(yv[2]);
    LAS char* ys = L + YST + wi * 2560;
    *(LAS v4u*)(ys + vo) = yv[0]; *(LAS v4u*)(ys + 1024 + vo) = yv[1];
    if (ln_ < 32) *(LAS v4u*)(ys + 2048 + vo) = yv[2];
    lds_set(L, W_F2OK + wi * 4, 2);
    STEND();
    asm volatile("97:");
    __builtin_amdgcn_endpgm();
  }

  const bool odd = wi & 1;
  const int kch = chA + (wi >> 1);
  const unsigned vo = lane * 16;
  v4u WF[6][5], AF[6][4];
  waitv<6>();
  lds_arrive(L, W_ST, lane);
  waitv<0>();
  arrive_flag8(L, W_F1C, ws, WS_F1 + (unsigned)(lane & 7) * 2048 + wid * 8, tlo, thi);
  ST(8);
  auto reduce = [&](int c, int i) {
    LAS char* pp = L + KG_SCR + (c < 2 ? 4 * c : 3) * KG_WSTR + i * 16;
    const f32x4 s = add4(*(LAS f32x4*)pp, *(LAS f32x4*)(pp + (c < 2 ? 2 : 4) * KG_WSTR));
    *(LAS f32x4*)(L + KTAB + c * KP_STRIDE + i * 16) = s;
    return s;
  };
  lds_wait_ge(L, W_KG, 4);
  if (ts < 161) { st128c((unsigned)ts * 16, ws + WS_KP + (size_t)wid * KP_STRIDE, __builtin_bit_cast(v4u, reduce(2, ts))); reduce(0, ts); }
  if (ts >= 95) reduce(1, ts - 95);
  if (ts < 3) *(LAS float*)(L + KTAB + ts * KP_STRIDE - 4) = 0.f;
  lds_arrive(L, W_SB, lane);
  waitv<0>();
  arrive_flag(L, W_FKC, lane, ws, WS_FK + (unsigned)wid * 64, tlo, thi);
  ST(9);
  __builtin_amdgcn_s_setprio(0);
  lds_wait_min<4>(L, W_RDY, F1STAGE, lane);
  const unsigned f1o = WS_F1 + (unsigned)xcd * 2048 + ts * 8;
  v2u f1own; ld64c(f1own, f1o, ws);
  waitv<0>(); PIN(f1own);
  f1own = spin64<false>(f1own, f1o, ws, tlo, thi);
  v4u uu[3]; unsigned ulo[3];
#pragma unroll
  for (int r = 0; r < 3; ++r) {
    const int e = 64 * r + lane, tb = 64 * wi + e / 3, c = e % 3;
    ld128c<0>(uu[r], tb * 80 + c * 16, ws + WS_XNB + (size_t)(16 * xcd + (j >> 1)) * 20480 + par * 32);
    ulo[r] = (c == 2 - 2 * par ? CVS : CVA + (c - par) * CV_CH) + tb * 16;
  }
  if (b == 0) arrive_flag(L, W_F1ALL, lane, ws, WS_E, tlo, thi);
  else lds_arrive(L, W_F1ALL, lane);
  ST(4);
  const unsigned fko = WS_FK + (unsigned)(wid ^ 1) * 64;
  v2u fk; ld64c(fk, fko, ws);
  waitv<0>(); PIN(fk); PIN(uu[0]); PIN(uu[1]); PIN(uu[2]);
  fk = spin64<false>(fk, fko, ws, tlo, thi);
  v4u kp; ld128c<0>(kp, (unsigned)(ts < 161 ? ts : 160) * 16, ws + WS_KP + (size_t)(wid ^ 1) * KP_STRIDE);
  lds_wait_ge(L, W_SB, 8);
  waitv<0>(); PIN(kp);
  if (ts < 161) { LAS f32x4* tp = (LAS f32x4*)(L + KTAB + 2 * KP_STRIDE + ts * 16); *tp = add4(*tp, __builtin_bit_cast(f32x4, kp)); }
  lds_arrive(L, W_SB, lane);
  lds_wait_ge(L, W_F1ALL, 4);
  WL(0);
  lds_wait_min<4>(L, W_RDY, 9, lane);
  put_k(L, kix, ts, 0, CVA);
  WL(1);
  lds_wait_min<4>(L, W_RDY, 10, lane);
  put_k(L, kix, ts, 1, CVA + CV_CH);
  if (ZCOND) { *(LAS v4u*)(L + CVA + ZOFF) = v4u{0u, 0u, 0u, 0u}; *(LAS v4u*)(L + CVA + CV_CH + ZOFF) = v4u{0u, 0u, 0u, 0u}; }
  ST(7);
  lds_wait_min<4>(L, W_RDY, 11, lane);
  ST(11);
  put_u(L + ulo[0], uu[0], ts < 3); put_u(L + ulo[1], uu[1], false); put_u(L + ulo[2], uu[2], false);
  if (ZCOND) *(LAS v4u*)(L + CVS + ZOFF) = v4u{0u, 0u, 0u, 0u};
  lds_wait_ge(L, W_SB, 12);
  put_k(L, kix, ts, 2, CVS);
  lds_arrive(L, W_SB, lane);
  WL(2); WL(3); WL(4); WL(5);
  ST(5);
  lds_wait_ge(L, W_SB, 16);
  lds_wait_ge(L, W_RDY + wi * 4, 12);
  ST(13);
  __builtin_amdgcn_s_setprio(SPRIO2);
  LATE_LANE();
  f32x4 cacc[8];
#pragma unroll
  for (int i = 0; i < 8; ++i) cacc[i] = f32x4{0.f, 0.f, 0.f, 0.f};
  {
    const unsigned o1 = CVA + (wi >> 1) * CV_CH - (odd ? 4 * 64 : 0);
    unsigned kb = KAO + o1 - 3584 - 192, ubb = UBO + o1 - 192; PIN(kb); PIN(ubb);
    LAS char* ka = L + kb; LAS char* ub = L + ubb;
    v4u A[8], B[8];
#define LA(i, u) (*(LAS v4u*)(ka + 512 * (7 - (i)) + 64 * (3 - (u))))
#define LB(i, u) (*(LAS v4u*)(ub + 512 * (i) + 64 * (3 - (u))))
    A[7] = LA(7, 0); B[0] = LB(0, 0); A[0] = LA(0, 0); B[7] = LB(7, 0); A[6] = LA(6, 0); B[6] = LB(6, 0); A[1] = LA(1, 0); B[1] = LB(1, 0);
    A[5] = LA(5, 0); B[5] = LB(5, 0); A[2] = LA(2, 0); B[2] = LB(2, 0); A[4] = LA(4, 0); B[4] = LB(4, 0); A[3] = LA(3, 0); B[3] = LB(3, 0);
    SCHED();
#pragma unroll
    for (int u = 0; u < 4; ++u) {
      v4u An[2], Bn[2];
#pragma unroll
      for (int sd = 7; sd >= 0; --sd) {
#pragma unroll
        for (int o = 0; o <= sd; ++o) {
          if (sd + o < 8) cacc[sd + o] = mfma(A[sd], B[o], cacc[sd + o]);
          if (o != sd && sd + o < 8) cacc[sd + o] = mfma(A[o], B[sd], cacc[sd + o]);
        }
        SCHED();
        if (u < 3) {
          if (sd >= 2) { A[sd] = LA(sd, u + 1); B[sd] = LB(sd, u + 1); }
          if (sd == 6 || sd == 5) { An[6 - sd] = LA(6 - sd, u + 1); Bn[6 - sd] = LB(6 - sd, u + 1); }
        }
        SCHED();
      }
      if (u < 3) { A[0] = An[0]; A[1] = An[1]; B[0] = Bn[0]; B[1] = Bn[1]; }
    }
#undef LA
#undef LB
  }
  ST(10);
  unsigned vm[4];
  {
    LAS char* px = L + SXCH + wi * 4096 + vo;
    if (odd) {
#pragma unroll
      for (int i = 0; i < 4; ++i) { *(LAS f32x4*)(px + i * 1024) = cacc[i]; cacc[i] = cacc[4 + i]; }
    } else {
#pragma unroll
      for (int i = 0; i < 4; ++i) *(LAS f32x4*)(px + i * 1024) = cacc[4 + i];
    }
    lds_arrive(L, W_SX + (wi >> 1) * 4, lane);
#pragma unroll
    for (int m = 0; m < 4; ++m) vm[m] = vo + ((m + wi) & 3) * 49152;
    lds_wait_ge(L, W_SX + (wi >> 1) * 4, 2);
    px = L + SXCH + (wi ^ 1) * 4096 + vo;
    v2u yo[4];
#pragma unroll
    for (int i = 0; i < 4; ++i) { const f32x4 s = add4(cacc[i], *(LAS f32x4*)(px + i * 1024)); yo[i] = v2u{pk2(s.x, s.y), pk2(s.z, s.w)}; }
    char* yb = ws + WS_Y + (size_t)kch * 4096 + (odd ? 2048 : 0);
    LAS char* yl = L + SXCH + (wi ^ 1) * 4096;
#pragma unroll
    for (int i = 0; i < 4; ++i) *(LAS v2u*)(yl + i * 512 + YOFF) = yo[i];
    FENCE();
    st128c(vo, yb, *(LAS v4u*)(yl + vo)); st128c(vo, yb + 1024, *(LAS v4u*)(yl + 1024 + vo));
  }
  ST(14);
  lds_wait_ge(L, W_F2OK + wi * 4, 1);
  ST(16);
  AL(0); AL(1);
  ST(17);
  f32x4 acc2[4][5];
  waitv<8>();
  arrive_flag<8>(L, W_F3C, lane, ws, WS_F3 + (unsigned)(xcd * 32 + j) * 128, tlo, thi);
  __builtin_amdgcn_s_setprio(0);
  waitv<4>(); g2_step<0>(acc2, WF[0], AF[0], [&](int i) { if (i >= 3 && (i & 1)) AL1(2, (i - 3) >> 1); });
  waitv<4>(); g2_step<4>(acc2, WF[1], AF[1], [&](int i) { if (i >= 2) AL1(3 + ((i - 2) >> 2), (i - 2) & 3); });
  waitv<8>(); g2_step<4>(acc2, WF[2], AF[2], [&](int i) { if (i < 4) AL1(5, i); });
  ST(20);
  waitv<8>(); g2_step<4>(acc2, WF[3], AF[3], NOLD);
  lds_wait_ge(L, W_F3C, 8);
  waitv<0>();
#pragma unroll
  for (int m = 0; m < 4; ++m) { PIN(AF[4][m]); PIN(AF[5][m]); }
#pragma unroll
  for (int n = 0; n < 5; ++n) { PIN(WF[4][n]); PIN(WF[5][n]); }
#pragma unroll
  for (int mm = 1; mm < 5; ++mm) {
    const int m = mm & 3;
#pragma unroll
    for (int n = 0; n < 5; ++n) {
      acc2[m][n] = mfma(WF[4][n], AF[4][m], acc2[m][n]);
      acc2[m][n] = mfma(WF[5][n], AF[5][m], acc2[m][n]);
      if (mm > 1) *(LAS f32x4*)(L + FS + wi * 15360 + (mm - 2) * 5120 + n * 1024 + vo) = acc2[mm - 1][n];
      SCHED();
    }
  }
  lds_arrive(L, W_FS, lane);
  ST(22);
  lds_wait_ge(L, W_FS, 4);
#pragma unroll
  for (int d = 1; d < 4; ++d)
#pragma unroll
    for (int n = 0; n < 5; ++n) acc2[0][n] += *(LAS f32x4*)(L + FS + ((wi - d) & 3) * 15360 + (d - 1) * 5120 + n * 1024 + vo);
  ST(23);
  lds_wait_ge(L, W_F2OK + wi * 4, 2);
  ST(24);
  {
    char* ob = out + (size_t)row2 * 1280 + cb2 * 160; const unsigned oo = OROW * 1280 + cg * 8;
    v2u o[5];
#pragma unroll
    for (int n = 0; n < 5; ++n) {
      const v2u t = trread(L + YST + wi * 2560 + n * 512 + ln_ * 8);
      const f32x4 s = acc2[0][n] + *(LAS f32x4*)(L + FM + 61440 + wi * 5120 + n * 1024 + vo);
      const f32x2 s0 = s.xy + unpk(t.x), s1 = s.zw + unpk(t.y);
      o[n] = v2u{pk2(s0.x, s0.y), pk2(s1.x, s1.y)};
    }
    st64o<0>(oo, ob, o[0]); st64o<32>(oo, ob, o[1]); st64o<64>(oo, ob, o[2]); st64o<96>(oo, ob, o[3]); st64o<128>(oo, ob, o[4]);
  }
  STEND();
  asm volatile("99:");
}

extern "C" void kernel_launch(void* const* d_in, const int* in_sizes, int n_in, void* d_out, int out_size, void* d_ws, size_t ws_size,
                              hipStream_t stream) {
  fused_s4_mlp<<<256, 512, 0, stream>>>((const char*)d_in[0], (const char*)d_in[1], (const char*)d_in[2], (const char*)d_in[3],
      (const char*)d_in[4], (const char*)d_in[5], (const char*)d_in[6], (const char*)d_in[7], (const char*)d_in[8], (const char*)d_in[10],
      (const char*)d_in[11], (const char*)d_in[12], (char*)d_out, (char*)d_ws);
}
```

```cpp
#include <hip/hip_runtime.h>
#include <stdint.h>

typedef unsigned short u16;
typedef unsigned long long u64;
typedef unsigned v4u __attribute__((ext_vector_type(4)));
typedef unsigned v2u __attribute__((ext_vector_type(2)));
typedef float f32x4 __attribute__((ext_vector_type(4)));
typedef float f32x2 __attribute__((ext_vector_type(2)));
typedef short bf16x8 __attribute__((ext_vector_type(8)));
typedef short s16x4 __attribute__((ext_vector_type(4)));
typedef __bf16 bf16x2_t __attribute__((ext_vector_type(2)));
#define LAS __attribute__((address_space(3)))
#define GAS __attribute__((address_space(1)))
#define DI __device__ __forceinline__

constexpr unsigned WS_E = 0, WS_TMO = 64, WS_F1 = 131072, WS_STATS = 16384, WS_F2 = 32768, WS_F3 = 65536, WS_FK = 98304, WS_STAMP = 262144;
constexpr unsigned WS_XNB = 1u << 20, WS_W2T = 4u << 20, WS_Y = 7u << 20, WS_ACT = 10u << 20, WS_KP = 17u << 20;
constexpr unsigned KP_STRIDE = 2624;
constexpr unsigned SPIN_CAP = 400000;

constexpr unsigned W_F1C = 0, W_F2C = 4, W_F3C = 8, W_KG = 12, W_MB = 16, W_SB = 20, W_SX = 24, W_FM = 32, W_FS = 36, W_F3ALL = 40, W_F1ALL = 44,
                   W_F2OK = 48, W_RDY = 64, W_TAG = 80, W_FKC = 96, W_ST = 100;
constexpr unsigned KTAB = 272;
constexpr unsigned CSP = 8192;
constexpr unsigned CVA = 10240, CV_CH = 27008, CV_USTR = 4640, CV_KOFF = 18560, CV_KSTR = 4224;
constexpr unsigned KG_SCR = CVA, KG_WSTR = 4352, LN_ROWS = KG_SCR + 8 * KG_WSTR, LN_PITCH = 1344, W2P = LN_ROWS + 10752;
constexpr unsigned G1_RING = 64512, G1_STAGE = 28672, G1_BOFF = 16384;
constexpr unsigned CVS = G1_RING, SXCH = CVS + 27136, MXCH = SXCH + 16384;
constexpr unsigned CSF = G1_RING + 3 * G1_STAGE, YST = CSF + 3072;
constexpr unsigned FM = 256, FS = FM + 81920;
static_assert(FS + 61440 <= CSF && CVA + 2 * CV_CH <= G1_RING && W2P + 8192 <= G1_RING && YST + 10240 <= 163840 && MXCH + 16384 <= CSF, "lds map");

#define ROT2 0
#define F1STAGE 5
#define SPRIO2 1
#define F1GAP 16
#define F2GAP 8
#define SPRIO 1
#define ST(i) do {} while (0)
#define STEND() do {} while (0)
#define STARG
#define STPASS

DI float bf2f(unsigned v) { return __builtin_bit_cast(float, v << 16); }
DI float bfhi(unsigned v) { return __builtin_bit_cast(float, v & 0xffff0000u); }
DI unsigned pk2(float lo, float hi) { bf16x2_t r; r.x = (__bf16)lo; r.y = (__bf16)hi; return __builtin_bit_cast(unsigned, r); }
DI float rbf(float f) { return bf2f(pk2(f, f) & 0xffffu); }
DI f32x4 mfma(v4u a, v4u b, f32x4 c) {
  return __builtin_amdgcn_mfma_f32_16x16x32_bf16(__builtin_bit_cast(bf16x8, a), __builtin_bit_cast(bf16x8, b), c, 0, 0, 0);
}
DI v2u trread(LAS char* p) { return __builtin_bit_cast(v2u, __builtin_amdgcn_ds_read_tr16_b64_v4i16((LAS s16x4*)p)); }
template <int N> DI void waitv() { asm volatile("s_waitcnt vmcnt(%0)" ::"i"(N) : "memory"); }
DI void bar() { asm volatile("s_waitcnt lgkmcnt(0)\n\ts_barrier" ::: "memory"); }
#define PIN(x) asm volatile("" : "+v"(x))
#define FENCE() asm volatile("" ::: "memory")
#define SCHED() __builtin_amdgcn_sched_barrier(0)
#define RFL(x) __builtin_amdgcn_readfirstlane(x)

DI void ld16(unsigned& d, unsigned vo, const void* sb) { asm volatile("global_load_ushort %0, %1, %2" : "=v"(d) : "v"(vo), "s"(sb) : "memory"); }
template <int O> DI void ld32(unsigned& d, unsigned vo, const void* sb) { asm volatile("global_load_dword %0, %1, %2 offset:%3" : "=v"(d) : "v"(vo), "s"(sb), "i"(O) : "memory"); }
DI void ld32c(unsigned& d, unsigned vo, const void* sb) { asm volatile("global_load_dword %0, %1, %2 sc1" : "=v"(d) : "v"(vo), "s"(sb) : "memory"); }
DI void ld64(v2u& d, unsigned vo, const void* sb) { asm volatile("global_load_dwordx2 %0, %1, %2" : "=v"(d) : "v"(vo), "s"(sb) : "memory"); }
DI void ld128(v4u& d, unsigned vo, const void* sb) { asm volatile("global_load_dwordx4 %0, %1, %2" : "=v"(d) : "v"(vo), "s"(sb) : "memory"); }
DI void ld64c(v2u& d, unsigned vo, const void* sb) { asm volatile("global_load_dwordx2 %0, %1, %2 sc1" : "=v"(d) : "v"(vo), "s"(sb) : "memory"); }
template <int O> DI void ld128c(v4u& d, unsigned vo, const void* sb) { asm volatile("global_load_dwordx4 %0, %1, %2 offset:%3 sc1" : "=v"(d) : "v"(vo), "s"(sb), "i"(O) : "memory"); }
DI void st64c(unsigned vo, void* sb, v2u d) { asm volatile("global_store_dwordx2 %0, %1, %2 sc1" ::"v"(vo), "v"(d), "s"(sb) : "memory"); }
template <int O> DI void ld64o(v2u& d, unsigned vo, const void* sb) { asm volatile("global_load_dwordx2 %0, %1, %2 offset:%3" : "=v"(d) : "v"(vo), "s"(sb), "i"(O) : "memory"); }
template <int O> DI void st64o(unsigned vo, void* sb, v2u d) { asm volatile("global_store_dwordx2 %0, %1, %2 offset:%3" ::"v"(vo), "v"(d), "s"(sb), "i"(O) : "memory"); }
template <int O> DI void st64co(unsigned vo, void* sb, v2u d) { asm volatile("global_store_dwordx2 %0, %1, %2 offset:%3 sc1" ::"v"(vo), "v"(d), "s"(sb), "i"(O) : "memory"); }
DI void st128c(unsigned vo, void* sb, v4u d) { asm volatile("global_store_dwordx4 %0, %1, %2 sc1\n\ts_nop 1" ::"v"(vo), "v"(d), "s"(sb) : "memory"); }
DI void st64u(unsigned vo, void* sb, v2u d) { asm volatile("global_store_dwordx2 %0, %1, %2 sc1 ; CNT" ::"v"(vo), "v"(d), "s"(sb) : "memory"); }
DI void st128u(unsigned vo, void* sb, v4u d) { asm volatile("global_store_dwordx4 %0, %1, %2 sc1 ; CNT\n\ts_nop 1" ::"v"(vo), "v"(d), "s"(sb) : "memory"); }
DI void dma16(unsigned vo, const void* sb, unsigned lds) {
  asm volatile("s_mov_b32 m0, %2\n\ts_nop 0\n\tglobal_load_lds_dwordx4 %0, %1" ::"v"(vo), "s"(sb), "s"(lds) : "memory");
}
template <int O> DI void dma16o(unsigned vo, const void* sb) {
  asm volatile("global_load_lds_dwordx4 %0, %1 offset:%2" ::"v"(vo), "s"(sb), "i"(O) : "memory");
}
template <bool HIONLY> DI v2u spin64(v2u cur, unsigned vo, char* ws, unsigned tlo, unsigned thi) {
  if (__builtin_expect(__builtin_amdgcn_ballot_w64(HIONLY ? (cur.y != tlo) : (cur.x != tlo || cur.y != thi)) != 0ull, 0)) {
#pragma nounroll
    for (unsigned spins = 0; spins < SPIN_CAP; ++spins) {
      __builtin_amdgcn_s_sleep(6);
      asm volatile("global_load_dwordx2 %0, %1, %2 sc1\n\ts_waitcnt vmcnt(0)" : "=&v"(cur) : "v"(vo), "s"(ws) : "memory");
      if (__builtin_amdgcn_ballot_w64(HIONLY ? (cur.y != tlo) : (cur.x != tlo || cur.y != thi)) == 0ull) break;
    }
  }
  return cur;
}
DI unsigned lw(LAS char* L, unsigned off) { return *(volatile LAS unsigned*)(L + off); }
DI void lds_wait_at(unsigned a, unsigned target) {
  unsigned v, cnt; u64 t;
  asm volatile("s_mov_b32 %1, %5\n"
               "1:\n\tds_read_b32 %0, %3\n\ts_sub_u32 %1, %1, 1\n\ts_waitcnt lgkmcnt(0)\n\tv_cmp_gt_u32 vcc, %4, %0\n\t"
               "s_cselect_b64 %2, 0, vcc\n\ts_cmp_lg_u64 %2, 0\n\ts_cbranch_scc1 1b"
               : "=&v"(v), "=&s"(cnt), "=&s"(t) : "v"(a), "s"(target), "i"(4 * SPIN_CAP) : "vcc", "scc", "memory");
}
DI void lds_wait_ge(LAS char* L, unsigned off, unsigned target) { lds_wait_at((unsigned)(size_t)L + off, target); }
template <int N> DI void lds_wait_min(LAS char* L, unsigned off, unsigned target, int lane) {
  lds_wait_at((unsigned)(size_t)L + off + (lane & (N - 1)) * 4, target);
}
DI void lds_set(LAS char* L, unsigned off, unsigned v) { FENCE(); *(volatile LAS unsigned*)(L + off) = v; FENCE(); }
DI void lds_arrive(LAS char* L, unsigned off, int) {
  asm volatile("s_mov_b64 exec, 1\n\tds_add_u32 %0, %1\n\ts_mov_b64 exec, -1" ::"v"((unsigned)(size_t)L + off), "v"(1u) : "memory");
}
template <unsigned N = 4> DI void arrive_flag(LAS char* L, unsigned off, int, char* ws, unsigned fo, unsigned tlo, unsigned thi) {
  unsigned old;
  asm volatile("s_mov_b64 exec, 1\n\tds_add_rtn_u32 %0, %1, %2\n\ts_waitcnt lgkmcnt(0)\n\tv_cmp_eq_u32 vcc, %6, %0\n\t"
               "s_mov_b64 exec, vcc\n\tglobal_store_dwordx2 %3, %4, %5 sc1\n\ts_mov_b64 exec, -1"
               : "=&v"(old) : "v"((unsigned)(size_t)L + off), "v"(1u), "v"(fo), "v"(v2u{tlo, thi}), "s"(ws), "i"(N - 1) : "vcc", "memory");
}
DI void arrive_flag8(LAS char* L, unsigned off, char* ws, unsigned fo, unsigned tlo, unsigned thi) {
  unsigned old;
  asm volatile("s_mov_b64 exec, 1\n\tds_add_rtn_u32 %0, %1, %2\n\ts_waitcnt lgkmcnt(0)\n\tv_cmp_eq_u32 vcc, 3, %0\n\t"
               "s_mul_i32 exec_lo, vcc_lo, 0xff\n\tglobal_store_dwordx2 %3, %4, %5 sc1\n\ts_mov_b64 exec, -1"
               : "=&v"(old) : "v"((unsigned)(size_t)L + off), "v"(1u), "v"(fo), "v"(v2u{tlo, thi}), "s"(ws) : "vcc", "memory");
}
typedef float f32x16 __attribute__((ext_vector_type(16)));
DI f32x16 mfma32(v4u a, v4u b, f32x16 c) {
  return __builtin_amdgcn_mfma_f32_32x32x16_bf16(__builtin_bit_cast(bf16x8, a), __builtin_bit_cast(bf16x8, b), c, 0, 0, 0);
}
template <int C> DI float dpp_add(float v) {
  return v + __builtin_bit_cast(float, __builtin_amdgcn_update_dpp(0, __builtin_bit_cast(int, v), C, 0xf, 0xf, true));
}
DI float rl(float v, int l) { return __builtin_bit_cast(float, __builtin_amdgcn_readlane(__builtin_bit_cast(int, v), l)); }
DI float half_sum(float v, bool upper) {
  v = dpp_add<0xB1>(v); v = dpp_add<0x4E>(v); v = dpp_add<0x141>(v); v = dpp_add<0x140>(v);
  const float a = rl(v, 0) + rl(v, 16), c = rl(v, 32) + rl(v, 48);
  return upper ? c : a;
}
DI f32x2 unpk(unsigned v) { return f32x2{bf2f(v), bfhi(v)}; }
struct c2 { f32x2 r, i; };
DI c2 cmul(c2 a, c2 b) { return c2{__builtin_elementwise_fma(a.r, b.r, -(a.i * b.i)), __builtin_elementwise_fma(a.r, b.i, a.i * b.r)}; }
DI c2 csq(c2 v) { return c2{__builtin_elementwise_fma(v.r, v.r, -(v.i * v.i)), (v.r + v.r) * v.i}; }
DI c2 cexpk(f32x2 e1, f32x2 th, f32x2 tl, float k, float ks) {
  const f32x2 e = e1 * k, t = th * ks;
  const f32x2 mag = f32x2{__builtin_amdgcn_exp2f(e.x), __builtin_amdgcn_exp2f(e.y)};
  const f32x2 fr = __builtin_elementwise_fma(tl, f32x2{ks, ks}, f32x2{__builtin_amdgcn_fractf(t.x), __builtin_amdgcn_fractf(t.y)});
  return c2{f32x2{__builtin_amdgcn_cosf(fr.x), __builtin_amdgcn_cosf(fr.y)} * mag, f32x2{__builtin_amdgcn_sinf(fr.x), __builtin_amdgcn_sinf(fr.y)} * mag};
}
typedef _Float16 f16x8 __attribute__((ext_vector_type(8)));
typedef _Float16 f16x2 __attribute__((ext_vector_type(2)));
DI f32x4 mfmah(v4u a, v4u b, f32x4 c) {
  return __builtin_amdgcn_mfma_f32_16x16x32_f16(__builtin_bit_cast(f16x8, a), __builtin_bit_cast(f16x8, b), c, 0, 0, 0);
}
DI unsigned pkh1(float a, float b) { f16x2 r; r.x = (_Float16)a; r.y = (_Float16)b; return __builtin_bit_cast(unsigned, r); }
DI v2u pkh(c2 v) { return v2u{pkh1(v.r.x, v.i.x), pkh1(v.r.y, v.i.y)}; }
DI v2u pkb(c2 v) { return v2u{pk2(v.r.x, v.i.x), pk2(v.r.y, v.i.y)}; }
#define HMUL(o, i, w) "v_pk_mul_f16 " o ", " i ", " w " op_sel:[1,1] op_sel_hi:[1,0] neg_lo:[0,1]\n\t"
#define HFMA(o, i, w) "v_pk_fma_f16 " o ", " i ", " w ", " o " op_sel_hi:[0,1,1]\n\t"
constexpr int KROW = 528;
template <int R0, int R1> DI void h2(v2u& o0, v2u& o1, v2u i0, v2u i1, v2u w0, v2u w1) {
  unsigned a, b, c, d;
  asm volatile(HMUL("%0", "%4", "%8") HMUL("%1", "%5", "%9") HMUL("%2", "%6", "%10") HMUL("%3", "%7", "%11")
               HFMA("%0", "%4", "%8") HFMA("%1", "%5", "%9") HFMA("%2", "%6", "%10") HFMA("%3", "%7", "%11")
               "ds_write_addtid_b32 %0 offset:%12\n\tds_write_addtid_b32 %1 offset:%13\n\tds_write_addtid_b32 %2 offset:%14\n\tds_write_addtid_b32 %3 offset:%15"
               : "=&v"(a), "=&v"(b), "=&v"(c), "=&v"(d)
               : "v"(i0.x), "v"(i0.y), "v"(i1.x), "v"(i1.y), "v"(w0.x), "v"(w0.y), "v"(w1.x), "v"(w1.y),
                 "i"(R0 * KROW), "i"(R0 * KROW + 256), "i"(R1 * KROW), "i"(R1 * KROW + 256) : "memory");
  o0 = v2u{a, b}; o1 = v2u{c, d};
}
template <int R> DI void hput(v2u v) {
  asm volatile("ds_write_addtid_b32 %0 offset:%2\n\tds_write_addtid_b32 %1 offset:%3" ::"v"(v.x), "v"(v.y), "i"(R * KROW), "i"(R * KROW + 256) : "memory");
}
template <int C> DI unsigned dpp_maxu(unsigned v) { return max(v, (unsigned)__builtin_amdgcn_update_dpp(0, (int)v, C, 0xf, 0xf, true)); }
DI void rd8(v4u (&f)[8], LAS char* sr) {
#pragma unroll
  for (int k = 0; k < 8; ++k) f[k] = *(LAS v4u*)(sr + 64 * k);
}
template <bool H> DI void kmm(f32x4 (&ka)[2][2][2], int nh, const v4u (&UA)[2][8], const v4u (&vb)[8]) {
#pragma unroll
  for (int k = 0; k < 8; ++k)
#pragma unroll
    for (int m = 0; m < 2; ++m) {
      f32x4& a = ka[(k >> 1) & 1][m][nh];
      const f32x4 c = (k == 0 || k == 2) ? f32x4{0.f, 0.f, 0.f, 0.f} : a;
      a = H ? mfmah(UA[m][k], vb[k], c) : mfma(UA[m][k], vb[k], c);
    }
}
DI void kgen_slow(c2 a, c2 W1, c2 W256, c2 W8, LAS char* sr, f32x4 (&ka)[2][2][2]) {
  v4u UA[2][8], vb[8];
  c2 u[8]; u[0] = a;
#pragma unroll
  for (int i = 1; i < 8; ++i) u[i] = cmul(u[i - 1], W1);
  const c2 W512 = csq(W256), W1024 = csq(W512), r8 = cmul(a, W256), r10 = cmul(u[4], W256), r12 = cmul(a, W512), r14 = cmul(r8, W512), r16 = cmul(a, W1024);
  hput<0>(pkb(u[0])); hput<1>(pkb(u[1])); hput<2>(pkb(u[2])); hput<3>(pkb(u[3])); hput<4>(pkb(u[4])); hput<5>(pkb(u[5])); hput<6>(pkb(u[6])); hput<7>(pkb(u[7]));
  hput<8>(pkb(r8)); hput<9>(pkb(cmul(u[2], W256))); hput<10>(pkb(r10)); hput<11>(pkb(cmul(u[6], W256)));
  hput<12>(pkb(r12)); hput<13>(pkb(cmul(u[4], W512))); hput<14>(pkb(r14)); hput<15>(pkb(cmul(r10, W512)));
  rd8(UA[0], sr);
  hput<0>(pkb(r16)); hput<1>(pkb(cmul(r8, W1024))); hput<2>(pkb(cmul(r12, W1024))); hput<3>(pkb(cmul(r14, W1024))); hput<4>(pkb(cmul(r16, W1024)));
  rd8(UA[1], sr);
  c2 v = c2{f32x2{1.f, 1.f}, f32x2{0.f, 0.f}};
#pragma unroll
  for (int nh = 0; nh < 2; ++nh) {
#define VR(r) hput<r>(pkb(v)); v = cmul(v, W8);
    VR(0) VR(1) VR(2) VR(3) VR(4) VR(5) VR(6) VR(7) VR(8) VR(9) VR(10) VR(11) VR(12) VR(13) VR(14) VR(15)
#undef VR
    rd8(vb, sr);
    kmm<false>(ka, nh, UA, vb);
  }
}
DI void kgen(const unsigned (&pr)[7], bool dead, bool upper, unsigned sw, LAS char* sr, f32x4 (&ka)[2][2][2], float (&scale)[2]) {
  const float dt = rbf(__builtin_amdgcn_exp2f(bf2f(pr[6]) * 1.44269504088896341f));
  const f32x2 zr = unpk(pr[0]) * dt, zi = unpk(pr[1]) * dt, dbr = unpk(pr[2]) * dt, dbi = unpk(pr[3]) * dt;
  const f32x2 cr = unpk(dead ? 0u : pr[4]), ci = unpk(dead ? 0u : pr[5]);
  const c2 a = c2{__builtin_elementwise_fma(cr, dbr, -(ci * dbi)), -__builtin_elementwise_fma(cr, dbi, ci * dbr)};
  const f32x2 e1 = zr * 1.44269504088896341f, th = zi * 0.15915494309189535f;
  const f32x2 tl = __builtin_elementwise_fma(zi, f32x2{6.4206383e-9f, 6.4206383e-9f}, __builtin_elementwise_fma(zi, f32x2{0.15915494309189535f, 0.15915494309189535f}, -th));
  const c2 W1 = cexpk(e1, th, tl, 1.f, -1.f), W256 = cexpk(e1, th, tl, 256.f, -256.f), W8 = cexpk(e1, th, tl, 8.f, 8.f);
  asm volatile("s_mov_b32 m0, %0\n\ts_nop 0" ::"s"(sw) : "memory");
  if (__builtin_expect(__builtin_amdgcn_ballot_w64(!(zr.x <= 0.f && zr.y <= 0.f)) != 0ull, 0)) {
    kgen_slow(a, W1, W256, W8, sr, ka);
    scale[0] = scale[1] = 1.f;
    return;
  }
  unsigned am = max(max(__builtin_bit_cast(unsigned, a.r.x) & 0x7fffffffu, __builtin_bit_cast(unsigned, a.r.y) & 0x7fffffffu),
           max(__builtin_bit_cast(unsigned, a.i.x) & 0x7fffffffu, __builtin_bit_cast(unsigned, a.i.y) & 0x7fffffffu));
  am = dpp_maxu<0xB1>(am); am = dpp_maxu<0x4E>(am); am = dpp_maxu<0x141>(am); am = dpp_maxu<0x140>(am);
  const unsigned ex0 = min(max(max((unsigned)__builtin_amdgcn_readlane(am, 0), (unsigned)__builtin_amdgcn_readlane(am, 16)) >> 23, 2u), 252u);
  const unsigned ex1 = min(max(max((unsigned)__builtin_amdgcn_readlane(am, 32), (unsigned)__builtin_amdgcn_readlane(am, 48)) >> 23, 2u), 252u);
  scale[0] = __builtin_bit_cast(float, ex0 << 23); scale[1] = __builtin_bit_cast(float, ex1 << 23);
  const float down = __builtin_bit_cast(float, ((254u - (upper ? ex1 : ex0)) << 23));
  const c2 W2 = csq(W1), W512 = csq(W256), W16 = csq(W8), W4 = csq(W2), W1024 = csq(W512), W32 = csq(W16), W64 = csq(W32), W128 = csq(W64);
  const v2u u0 = pkh(c2{a.r * down, a.i * down}), h1 = pkh(W1), h2_ = pkh(W2), h4 = pkh(W4), h256 = pkh(W256), h512 = pkh(W512), h1024 = pkh(W1024);
  v2u u2, u4, u6, r8, r10, r12, r14, r16, t0, t1;
  v4u UA[2][8], vb[8];
  hput<0>(u0);
  h2<2, 4>(u2, u4, u0, u0, h2_, h4); h2<8, 12>(r8, r12, u0, u0, h256, h512); h2<1, 3>(t0, t1, u0, u2, h1, h1); h2<5, 6>(t0, u6, u4, u4, h1, h2_);
  h2<10, 14>(r10, r14, r8, r8, h4, h512); h2<7, 9>(t0, t1, u6, r8, h1, h2_); h2<11, 13>(t0, t1, r10, r12, h2_, h4);
  h2<15, 15>(r16, t1, u0, r14, h1024, h4);
  rd8(UA[0], sr);
  hput<0>(r16);
  h2<1, 2>(t0, t1, r8, r12, h1024, h1024); h2<3, 4>(t0, t1, r14, r16, h1024, h1024);
  rd8(UA[1], sr);
  v2u v[16];
  v[1] = pkh(W8); v[2] = pkh(W16); v[4] = pkh(W32); v[8] = pkh(W64);
  const v2u h128 = pkh(W128);
  hput<0>(v2u{0x3c00u, 0x3c00u}); hput<1>(v[1]); hput<2>(v[2]); hput<4>(v[4]); hput<8>(v[8]);
  h2<3, 5>(v[3], v[5], v[2], v[4], v[1], v[1]); h2<6, 9>(v[6], v[9], v[4], v[8], v[2], v[1]); h2<7, 10>(v[7], v[10], v[4], v[8], v[3], v[2]);
  h2<11, 12>(v[11], v[12], v[8], v[8], v[3], v[4]); h2<13, 14>(v[13], v[14], v[8], v[8], v[5], v[6]); h2<15, 15>(t0, v[15], v[1], v[8], h128, v[7]);
  rd8(vb, sr);
  hput<0>(h128); hput<1>(t0);
  h2<2, 3>(t0, t1, v[2], v[3], h128, h128); h2<4, 5>(t0, t1, v[4], v[5], h128, h128);
  kmm<true>(ka, 0, UA, vb);
  h2<6, 7>(t0, t1, v[6], v[7], h128, h128); h2<8, 9>(t0, t1, v[8], v[9], h128, h128); h2<10, 11>(t0, t1, v[10], v[11], h128, h128);
  h2<12, 13>(t0, t1, v[12], v[13], h128, h128); h2<14, 15>(t0, t1, v[14], v[15], h128, h128);
  rd8(vb, sr);
  SCHED();
  kmm<true>(ka, 1, UA, vb);
}
DI void kgen_store(LAS float* pt, const f32x4 (&kacc)[2][2], int lane) {
  const int l15 = lane & 15, g = lane >> 4;
  const int sh = g < 2 ? 3 : 4 - g;
  const int a0 = (l15 << sh) + (g < 2 ? 4 * g : 128 * g);
  const int a1 = g < 3 ? a0 + 2 : a0 + 64;
#pragma unroll
  for (int nh = 0; nh < 2; ++nh) {
    *(LAS f32x2*)(pt + a0 + ((16 * nh) << sh)) = kacc[0][nh].xy;
    *(LAS f32x2*)(pt + a1 + ((16 * nh) << sh)) = kacc[0][nh].zw;
  }
  if (g == 0) {
#pragma unroll
    for (int nh = 0; nh < 2; ++nh)
#pragma unroll
      for (int r = 0; r < 4; ++r) pt[512 + 32 * r + 16 * nh + l15] = kacc[1][nh][r];
  }
  if (lane == 16) { *(LAS f32x4*)(pt + 640) = f32x4{kacc[1][0][0], 0.f, 0.f, 0.f}; }
}

template <int PAR> DI void conv_unit_shared(LAS char* ka, LAS char* ub, f32x4 (&acc)[4]) {
  constexpr int T[4] = {PAR ? 1 : 0, PAR ? 2 : 3, PAR ? 5 : 4, PAR ? 6 : 7};
  v4u A[8], B[8];
#pragma unroll
  for (int i = 0; i <= T[3]; ++i) { A[i] = *(LAS v4u*)(ka + 512 * (7 - i)); B[i] = *(LAS v4u*)(ub + 512 * i); }
  SCHED();
#pragma unroll
  for (int q = 0; q < 4; ++q)
#pragma unroll
    for (int ip = 0; ip <= T[q]; ++ip) acc[q] = mfma(A[T[q] - ip], B[ip], acc[q]);
  SCHED();
}
DI float fadd(float a, float b) { float r; asm("v_add_f32 %0, %1, %2" : "=v"(r) : "v"(a), "v"(b)); return r; }
DI float ffma(float a, float b, float c) { float r; asm("v_fma_f32 %0, %1, %2, %3" : "=v"(r) : "v"(a), "v"(b), "v"(c)); return r; }
DI f32x4 add4(f32x4 a, f32x4 b) { return f32x4{fadd(a.x, b.x), fadd(a.y, b.y), fadd(a.z, b.z), fadd(a.w, b.w)}; }
DI f32x4 fma4(f32x4 a, float b, f32x4 c) { return f32x4{ffma(a.x, b, c.x), ffma(a.y, b, c.y), ffma(a.z, b, c.z), ffma(a.w, b, c.w)}; }
DI int rot12(int v) { return v >= 12 ? v - 12 : v; }
DI void w_loads(v4u (&WF)[5], const char* p, unsigned vo) {
  ld128c<0>(WF[0], vo, p); ld128c<1024>(WF[1], vo, p); ld128c<2048>(WF[2], vo, p); ld128c<3072>(WF[3], vo, p); ld128c<0>(WF[4], vo, p + 4096);
}
DI void a_loads(v4u (&AF)[4], const char* p, const unsigned (&vm)[4]) {
#pragma unroll
  for (int m = 0; m < 4; ++m) ld128c<0>(AF[m], vm[m], p);
}
template <int LIVE, class F> DI void g2_step(f32x4 (&acc)[4][5], v4u (&WF)[5], v4u (&AF)[4], F ld) {
#pragma unroll
  for (int m = 0; m < 4; ++m) PIN(AF[m]);
#pragma unroll
  for (int n = 0; n < 5; ++n) PIN(WF[n]);
#pragma unroll
  for (int e = 0; e < 20; ++e) {
    const int m = e / 5, n = e % 5;
    acc[m][n] = mfma(WF[n], AF[m], m >= LIVE ? f32x4{0.f, 0.f, 0.f, 0.f} : acc[m][n]);
    if (e & 1) { SCHED(); ld(e >> 1); SCHED(); }
  }
}
DI void make_kix(unsigned (&kix)[9], int ts) {
#pragma unroll
  for (int i = 0; i < 9; ++i) {
    const int lg = 2047 - 8 * ts - i;
    const int bits = (int)(pk2(__builtin_fmaxf((float)lg, 128.f), 0.f));
    const int ci = lg < bits - 0x4280 ? lg : bits - 0x4280;
    kix[i] = KTAB + 4 * ci;
    PIN(kix[i]);
  }
}
DI void put_k(LAS char* L, const unsigned (&kix)[9], int ts, int c, unsigned reg) {
  float kv[9];
#pragma unroll
  for (int i = 0; i < 9; ++i) kv[i] = *(LAS float*)(L + kix[i] + c * KP_STRIDE);
  LAS char* q = L + reg + CV_KOFF + ts * 16;
  *(LAS v4u*)(q + CV_KSTR) = v4u{pk2(kv[0], kv[1]), pk2(kv[2], kv[3]), pk2(kv[4], kv[5]), pk2(kv[6], kv[7])};
  *(LAS v4u*)q = v4u{pk2(kv[1], kv[2]), pk2(kv[3], kv[4]), pk2(kv[5], kv[6]), pk2(kv[7], kv[8])};
}
DI void put_u(LAS char* q, v4u u, bool first) {
  *(LAS v4u*)(q + 3 * CV_USTR + 384) = u;
  *(LAS v2u*)(q + 1 * CV_USTR + 392) = v2u{u.x, u.y}; *(LAS v2u*)(q + 1 * CV_USTR + 400) = v2u{u.z, u.w};
  *(LAS unsigned*)(q + 2 * CV_USTR + 388) = u.x; *(LAS v2u*)(q + 2 * CV_USTR + 392) = v2u{u.y, u.z}; *(LAS unsigned*)(q + 2 * CV_USTR + 400) = u.w;
  *(LAS unsigned*)(q + 396) = u.x; *(LAS v2u*)(q + 400) = v2u{u.y, u.z}; *(LAS unsigned*)(q + 408) = u.w;
  if (first) {
    *(LAS v2u*)(q + 384) = v2u{0u, 0u}; *(LAS unsigned*)(q + 392) = 0u;
    *(LAS v2u*)(q + CV_USTR + 384) = v2u{0u, 0u}; *(LAS unsigned*)(q + 2 * CV_USTR + 384) = 0u;
  }
}

__global__ __launch_bounds__(512, 2) void fused_s4_mlp(const char* __restrict__ x, const char* __restrict__ pAre, const char* __restrict__ pAim,
    const char* __restrict__ pBre, const char* __restrict__ pBim, const char* __restrict__ pCre, const char* __restrict__ pCim,
    const char* __restrict__ plogdt, const char* __restrict__ pDskip, const char* __restrict__ w1, const char* __restrict__ b1,
    const char* __restrict__ w2, char* __restrict__ out, char* __restrict__ ws) {
  __shared__ __attribute__((aligned(16))) char smem[163840];
  LAS char* L = (LAS char*)smem;
  const unsigned lds0 = (unsigned)(size_t)L;
  const char* pc0; unsigned codelen;
  asm volatile("s_getpc_b64 %0\n98:\n\ts_mov_b32 %1, 97f-98b\n\ts_max_u32 %1, %1, 99f-98b" : "=s"(pc0), "=&s"(codelen) :: "scc");
  const int b = blockIdx.x, lane = threadIdx.x & 63;
  const int w = RFL(threadIdx.x >> 6), wi = w & 3;
  {
    const unsigned co = (unsigned)(4 * (16 * w + (lane & 15)) + ((b >> 3) & 3)) * 128;
    asm volatile("s_mov_b32 m0, %2\n\ts_nop 0\n\tglobal_load_lds_dword %0, %1" ::"v"((co < codelen ? co : codelen) & ~3u), "s"(pc0), "s"(lds0 + YST + w * 256) : "memory");
  }
  const int xcd = b & 7, j = b >> 3, rg = xcd >> 1, chf = xcd & 1;
  const int ct1 = 8 * chf + (j & 7), rb1 = 4 * rg + (j >> 3);
  const int wid = rb1 * 16 + ct1;
  const int a2 = 4 * chf + (j >> 3), cb2 = j & 7;
  const int l15 = lane & 15, g = lane >> 4, qq = (lane >> 2) & 3, p4 = lane & 3, l31 = lane & 31, h = lane >> 5;
  const int par = j & 1, cbase = 80 * xcd + 5 * (j >> 1);
  const int chA = cbase + 3 * par, chS = cbase + 2;
  const int ts = 64 * wi + lane;
  ST(0);
  unsigned pr[7]; v4u xa, xb;
  {
    const bool odd = wi & 1, shl = odd && h, mw = w < 4;
    const int kch = chA + (wi >> 1);
    unsigned p0 = (kch * 192 + (wi & 1) * 128 + 2 * lane) * 2, p1 = (chS * 192 + par * 96 + (wi >> 1) * 48 + 2 * min(l31, 23)) * 2, x0 = h * 1280 + l31 * 8;
    PIN(p0); PIN(p1); PIN(x0);
    const unsigned po = shl ? p1 : p0, mm = mw ? ~0u : 0u;
    const unsigned xo = (x0 + l31 * 8) & ~mm, vo = (po & mm) | ((x0 + 1024) & ~mm), v1 = po & mm;
    asm volatile("" ::"s"(x), "s"(pAre), "s"(pAim), "s"(pBre), "s"(pBim), "s"(pCre), "s"(pCim), "s"(plogdt), "s"(ws));
    const char* xrow = x + (size_t)(8 * wid + 2 * wi) * 1280;
    ld16(pr[6], (unsigned)(shl ? chS : kch) * 2, plogdt);
    ld32<0>(pr[0], vo, mw ? pAre : xrow); ld32<0>(pr[1], vo, mw ? pAim : xrow + 4);
    ld32c(pr[2], v1, mw ? pBre : ws); ld32c(pr[3], v1, mw ? pBim : ws + 4);
    ld32<0>(pr[4], po, pCre); ld32<0>(pr[5], po, pCim);
    ld128(xa, xo, xrow); ld128(xb, xo, xrow + 512);
  }
  const int row2 = 512 * rg + 64 * a2 + 16 * wi;
  const char* w2b = ws + WS_W2T + (size_t)(cb2 * 48 + 12 * wi) * 5120;
  const char* actb = ws + WS_ACT + (size_t)((32 * rg + 4 * a2) * 48 + 12 * wi) * 1024;
  const int r2 = ((a2 ^ (w >> 2)) & 1) * 6;
#define WL(s) w_loads(WF[s], w2b + rot12(r2 + (s)) * 5120, vo)
#define AL(s) a_loads(AF[s], actb + rot12(r2 + (s)) * 1024, vm)
#define AL1(s, m) ld128c<0>(AF[s][m], vm[m], actb + rot12(r2 + (s)) * 1024)
#define WL1(s, n) ld128c<0>(WF[s][n], vo, w2b + rot12(r2 + (s)) * 5120 + (n) * 1024)
#define NOLD [&](int) {}
#define LATE_LANE() int ln_ = lane; PIN(ln_); const int c15 = ln_ & 15, cg = ln_ >> 4
#define KAO (CV_KOFF + (c15 & 1) * CV_KSTR + 2 * (2072 - 8 * (c15 >> 1) + 8 * cg))
#define UBO ((c15 & 3) * CV_USTR + 2 * (64 * (c15 >> 2) + 8 * cg + 224))
#define YOFF ((unsigned)((c15 >> 2) * 128 + cg * 32 + (c15 & 3) * 8))
#define OROW ((unsigned)(2 * (c15 >> 2) + (c15 & 1) + 8 * ((c15 >> 1) & 1)))
#define ZOFF (lane < 24 ? wi * CV_USTR + lane * 16 : CV_KOFF + wi * CV_KSTR + 4096 + (lane - 24) * 16)
#define ZCOND (lane < 24 || (lane < 31 && wi < 2))

  unsigned voA[4], voB[3];
#pragma unroll
  for (int i = 0; i < 4; ++i) voA[i] = (8 * i + (lane >> 3)) * 1280 + (((lane & 7) ^ (4 * (i & 1) + (lane >> 4))) * 16) - 1024 * i;
#pragma unroll
  for (int i = 0; i < 3; ++i) { const int c = 64 * i + lane; voB[i] = (c / 12) * 3072 + (c % 12) * 16 - 1024 * i; }
  const char* g1a = x + (size_t)(rb1 * 128 + 32 * wi) * 1280;
  const char* g1b = w1 + ct1 * 192 + wi * 49152;
  const unsigned la = lds0 + G1_RING + wi * 4096, lb = lds0 + G1_RING + G1_BOFF + wi * 3072;
  const int r1 = ((j & 7) + (5 * (j >> 3)) / 2 + 5 * chf + rg) % 10;
  auto slab = [&](int s) { const int v = s + r1; return v >= 10 ? v - 10 : v; };
  auto issue = [&](int s) {
    const char* pa = g1a + slab(s) * 128; const char* pb = g1b + (size_t)slab(s) * 64 * 3072;
    dma16(voA[0], pa, la + s * G1_STAGE); dma16o<1024>(voA[1], pa); dma16o<2048>(voA[2], pa); dma16o<3072>(voA[3], pa);
    dma16(voB[0], pb, lb + s * G1_STAGE); dma16o<1024>(voB[1], pb); dma16o<2048>(voB[2], pb);
  };
  unsigned tlo = 0, thi = 0;
  unsigned kix[9];
  const int pu = b % 240, pks = pu / 5, pcg = pu % 5;
  if (w < 4) {
    bar();
#pragma unroll
    for (int i = 0; i < 9; ++i) kix[i] = 0;
    waitv<0>();
  } else {
    if (w == 4) *(LAS unsigned*)(L + lane * 4) = 0u;
    bar();
#pragma unroll
    for (int i = 0; i < 2; ++i)
      dma16((unsigned)((lane >> 1) * 1280 + (lane & 1) * 16), w2 + (size_t)pks * 32 * 1280 + (128 * pcg + 32 * wi + 16 * i) * 2, lds0 + W2P + wi * 2048 + i * 1024);
    issue(0);
    make_kix(kix, ts);
    issue(1);
    waitv<14>();
  }
  asm volatile("; AUDITCLR");
  PIN(xa); PIN(xb);
#pragma unroll
  for (int i = 0; i < 7; ++i) PIN(pr[i]);
  ST(1);
  int role0 = w; asm volatile("" : "+s"(role0));
  if (role0 < 4) {
    const bool odd = wi & 1;
    f32x4 ka[2][2][2]; float scale[2];
    kgen(pr, odd && lane >= 56, h, lds0 + KG_SCR + wi * 2 * KG_WSTR, L + KG_SCR + wi * 2 * KG_WSTR + l15 * KROW + g * 16, ka, scale);
#pragma unroll
    for (int i = 0; i < 4; ++i) {
      ka[1][i >> 1][i & 1] *= scale[1];
      ka[0][i >> 1][i & 1] = ka[0][i >> 1][i & 1] * scale[0] + (odd ? f32x4{0.f, 0.f, 0.f, 0.f} : ka[1][i >> 1][i & 1]);
    }
    kgen_store((LAS float*)(L + KG_SCR + wi * 2 * KG_WSTR), ka[0], lane);
    if (odd) kgen_store((LAS float*)(L + KG_SCR + wi * 2 * KG_WSTR + KG_WSTR), ka[1], lane);
    lds_arrive(L, W_KG, lane);
  } else {
    const v2u xc = v2u{pr[0], pr[1]}, E = v2u{pr[2], pr[3]};
    ST(6);
  const u64 tag64 = (((u64)RFL(E.y) << 32) | (unsigned)RFL(E.x)) + 1ull;
  tlo = (unsigned)tag64; thi = (unsigned)(tag64 >> 32);
  if (wi == 0) { lds_set(L, W_TAG, tlo); lds_set(L, W_TAG + 4, thi); lds_set(L, W_TAG + 8, 1); }
  {
    f32x2 v[10] = {unpk(xa.x), unpk(xa.y), unpk(xa.z), unpk(xa.w), unpk(xb.x), unpk(xb.y), unpk(xb.z), unpk(xb.w), unpk(xc.x), unpk(xc.y)};
    f32x2 s2 = ((v[0] + v[1]) + (v[2] + v[3])) + ((v[4] + v[5]) + (v[6] + v[7])) + (v[8] + v[9]);
    const float mean = half_sum(s2.x + s2.y, h) * (1.0f / 640.0f);
    f32x2 q2 = f32x2{0.f, 0.f};
#pragma unroll
    for (int i = 0; i < 10; ++i) { const f32x2 d = v[i] - mean; q2 = __builtin_elementwise_fma(d, d, q2); }
    const float var = rbf(half_sum(q2.x + q2.y, h) * (1.0f / 640.0f));
    const float m = rbf(mean), sd = rbf(__builtin_amdgcn_sqrtf(rbf(var + 1e-5f))), rs = __builtin_amdgcn_rcpf(sd);
    unsigned o[10];
#pragma unroll
    for (int i = 0; i < 10; ++i) { const f32x2 t = (v[i] - m) * rs; o[i] = pk2(t.x, t.y); }
    LAS char* rp = L + LN_ROWS + (2 * wi + h) * LN_PITCH;
    *(LAS v4u*)(rp + l31 * 16) = v4u{o[0], o[1], o[2], o[3]};
    *(LAS v4u*)(rp + 512 + l31 * 16) = v4u{o[4], o[5], o[6], o[7]};
    *(LAS v2u*)(rp + 1024 + l31 * 8) = v2u{o[8], o[9]};
    waitv<0>();
    lds_arrive(L, W_SB, lane);
    issue(2);
    if (l31 == 0) st64c(WS_STATS + (unsigned)(8 * wid + 2 * wi + h) * 8, ws,
                        v2u{(__builtin_bit_cast(unsigned, m) & 0xffff0000u) | (__builtin_bit_cast(unsigned, sd) >> 16), tlo});
  }
#pragma unroll
  for (int i = 0; i < 2; ++i) {
    LAS char* sp = L + W2P + wi * 2048 + i * 1024 + (16 * (g >> 1) + 4 * (g & 1) + qq) * 32 + 8 * p4;
    const v2u lo = trread(sp), hi = trread(sp + 256);
    const int ntg = 8 * pcg + 2 * wi + i;
    st128c((unsigned)lane * 16, ws + WS_W2T + (size_t)(((ntg / 5) * 48 + pks) * 5 + ntg % 5) * 1024, v4u{lo.x, lo.y, hi.x, hi.y});
  }
  lds_wait_ge(L, W_SB, 4);
  {
    LAS char* sp = L + LN_ROWS + qq * LN_PITCH + (160 * wi + 16 * g) * 2 + 8 * p4;
#pragma unroll
    for (int i = 0; i < 3; ++i) {
      const v2u lo = trread(sp + 128 * i), hi = trread(sp + 128 * i + 4 * LN_PITCH);
      const unsigned ch = 64 * i + lane;
      if (i < 2 || lane < 32) st128c((ch / 5) * 20480 + (ch % 5) * 16, ws + WS_XNB + (size_t)wi * 32 * 20480 + wid * 80, v4u{lo.x, lo.y, hi.x, hi.y});
    }
  }
  }
  asm volatile("; AUDITCLR");
  ST(2);
  int role = w; asm volatile("" : "+s"(role));
  if (role < 4) {
    int lane = __builtin_amdgcn_mbcnt_hi(~0u, __builtin_amdgcn_mbcnt_lo(~0u, 0u)); PIN(lane);
    const int l15 = lane & 15, g = lane >> 4, qq = (lane >> 2) & 3, p4 = lane & 3, l31 = lane & 31, h = lane >> 5;
    const unsigned vo = lane * 16;
    f32x16 acc1[3]; f32x4 csa[3];
#pragma unroll
    for (int n = 0; n < 3; ++n) {
      csa[n] = f32x4{0.f, 0.f, 0.f, 0.f};
#pragma unroll
      for (int i = 0; i < 16; ++i) acc1[n][i] = 0.f;
    }
    const int xrow = (l31 & 17) + 2 * (l15 >> 2) + 8 * ((l15 >> 1) & 1);
    unsigned xo[4], wo[4];
#pragma unroll
    for (int k = 0; k < 4; ++k) {
      const int kr = (k + wi) & 3;
      xo[k] = G1_RING + (32 * wi + xrow) * 128 + (((2 * kr + h) ^ ((xrow >> 1) & 7)) * 16);
      wo[k] = G1_RING + G1_BOFF + (16 * kr + 8 * h + qq) * 192 + 32 * (g & 1) + 8 * p4;
      PIN(xo[k]); PIN(wo[k]);
    }
    const unsigned sel1 = ((l15 ^ g) & 1) ? 0u : 0x3f803f80u;
    const v4u sel = v4u{sel1, sel1, sel1, sel1};
    v4u X[4], W[4][3];
    auto rdw = [&](int s, int k, int n) {
      LAS char* p = L + wo[k] + (s % 3) * G1_STAGE + 64 * n; const v2u lo = trread(p), hi = trread(p + 768); W[k][n] = v4u{lo.x, lo.y, hi.x, hi.y};
    };
    auto rdx = [&](int s, int k) { X[k] = *(LAS v4u*)(L + xo[k] + (s % 3) * G1_STAGE); };
#define MM(k, n) acc1[n] = mfma32(W[k][n], X[k], acc1[n])
#define CS(n) csa[n] = mfma(W[0][n], sel, csa[n])
    v2u gran; unsigned bb[3];
    v4u WF[6][5], AF[6][4];
    const unsigned go = WS_STATS + (unsigned)(rb1 * 128 + 32 * wi + xrow) * 8;
    auto wl1 = [&](int s, int e0) { const int e = 7 * (s - 7) + e0; if (s >= 7 && e0 < 7 && e < 20) ld128c<0>(WF[e / 5][e % 5], vo, w2b + rot12(r2 + e / 5) * 5120 + (e % 5) * 1024); };
    lds_wait_ge(L, W_SB, 4);
    ST(3);
    rdx(0, 0); rdw(0, 0, 0); rdw(0, 0, 1); rdw(0, 0, 2); rdx(0, 1); rdw(0, 1, 0); rdw(0, 1, 1); rdw(0, 1, 2);
#pragma unroll
    for (int s = 0; s < 10; ++s) {
      const bool refill = s < 7;
      const char* pa = g1a + slab(s + 3) * 128; const char* pb = g1b + (size_t)slab(s + 3) * 64 * 3072;
      SCHED();
      MM(0, 0); rdx(s, 2); rdw(s, 2, 0); SCHED();
      MM(0, 1); rdw(s, 2, 1); rdw(s, 2, 2); SCHED();
      MM(0, 2); rdx(s, 3); rdw(s, 3, 0); SCHED();
      CS(0); rdw(s, 3, 1); SCHED();
      CS(1); rdw(s, 3, 2); SCHED();
      CS(2);
      if (s == 0) {} else if (s == 1) lds_wait_ge(L, W_ST, 4); else if (s < 7) waitv<7>(); else if (s == 7) waitv<11>(); else if (s == 8) waitv<11>();
      if (s == 9 && l15 < 2) {
#pragma unroll
        for (int n = 0; n < 3; ++n) *(LAS f32x4*)(L + CSP + wi * 384 + (32 * n + 16 * l15 + 4 * g) * 4) = csa[n];
      }
      lds_set(L, W_RDY + wi * 4, s + 2);
      SCHED();
      MM(1, 0);
      if (refill) { asm volatile("s_waitcnt lgkmcnt(7)" ::: "memory"); dma16(voA[0], pa, la + (s % 3) * G1_STAGE); }
      wl1(s, 0);

      SCHED();
      MM(1, 1); if (refill) dma16o<1024>(voA[1], pa); wl1(s, 1);
      unsigned fl = 0;
      if (s < 9) fl = lw(L, W_RDY + p4 * 4);
      SCHED();
      MM(1, 2); if (refill) dma16o<2048>(voA[2], pa); wl1(s, 2); SCHED();
      MM(2, 0); if (refill) dma16o<3072>(voA[3], pa); wl1(s, 3);
      SCHED();
      if (s == 9) {
        MM(2, 1); wl1(s, 4); SCHED();
        lds_wait_min<4>(L, W_RDY, 11, lane);
        float cs[3];
#pragma unroll
        for (int n = 0; n < 3; ++n) { LAS float* p = (LAS float*)(L + CSP) + 32 * n + l31; cs[n] = fadd(fadd(p[0], p[96]), fadd(p[192], p[288])); }
        MM(2, 2); wl1(s, 5); SCHED();
        waitv<20>(); PIN(gran); PIN(bb[0]); PIN(bb[1]); PIN(bb[2]);
        MM(3, 0); MM(3, 1); MM(3, 2);
        const unsigned keep = h ? 0u : ~0u, nm = (gran.x >> 16) ^ 0x8000u;
        const v4u xe = v4u{(nm | (nm << 16)) & keep, (nm | (gran.x << 16)) & keep, 0u, 0u};
#pragma unroll
        for (int n = 0; n < 3; ++n) {
          const float c0 = bfhi(__builtin_bit_cast(unsigned, cs[n])), r0 = cs[n] - c0, c1 = bfhi(__builtin_bit_cast(unsigned, r0)), r1 = r0 - c1;
          const v4u we = v4u{(__builtin_bit_cast(unsigned, c0) >> 16) | __builtin_bit_cast(unsigned, c1), (__builtin_bit_cast(unsigned, r1) >> 16) | (bb[n] << 16), 0u, 0u};
          acc1[n] = mfma32(we, xe, acc1[n]);
        }
        SCHED();
        break;
      }
      if (__builtin_expect(__builtin_amdgcn_ballot_w64(fl < (unsigned)(s + 2)) != 0ull, 0)) lds_wait_min<4>(L, W_RDY, s + 2, lane);
      FENCE();
      SCHED();
      rdx(s + 1, 0); rdw(s + 1, 0, 0); SCHED();
      MM(2, 1); rdw(s + 1, 0, 1); rdw(s + 1, 0, 2); if (refill) dma16(voB[0], pb, lb + (s % 3) * G1_STAGE); wl1(s, 4); SCHED();
      MM(2, 2); rdx(s + 1, 1); rdw(s + 1, 1, 0); if (refill) dma16o<1024>(voB[1], pb); wl1(s, 5); SCHED();
      MM(3, 0); rdw(s + 1, 1, 1); if (refill) dma16o<2048>(voB[2], pb); wl1(s, 6); SCHED();
      MM(3, 1); rdw(s + 1, 1, 2); wl1(s, 7); SCHED();
      MM(3, 2); wl1(s, 8);
      if (s == 6) { lds_wait_ge(L, W_F1ALL, 4); ld64c(gran, go, ws); ld16(bb[0], l31 * 2, b1 + ct1 * 192); ld16(bb[1], l31 * 2, b1 + ct1 * 192 + 64); ld16(bb[2], l31 * 2, b1 + ct1 * 192 + 128); }
      SCHED();
      if (s == 0) ST(4); if (s == 3) ST(5); if (s == 6) ST(7);
    }
#undef MM
#undef CS
    ST(11);
    const unsigned tlo = RFL(lw(L, W_TAG)), thi = RFL(lw(L, W_TAG + 4));
    {
      const float rs = __builtin_amdgcn_rcpf(bf2f(gran.x)) * 1.44269504088896341f;
      char* ab = ws + WS_ACT + (size_t)((rb1 * 8 + 2 * wi) * 48 + 3 * ct1) * 1024;
      const unsigned ao = (unsigned)((l31 >> 4) * 49152 + h * 256 + l15 * 16);
#pragma unroll
      for (int q = 0; q < 6; ++q) {
        unsigned pk[4];
#pragma unroll
        for (int e = 0; e < 4; ++e) {
          const f32x2 h2 = f32x2{acc1[q >> 1][8 * (q & 1) + 2 * e], acc1[q >> 1][8 * (q & 1) + 2 * e + 1]} * rs;
          const f32x2 s1 = f32x2{__builtin_amdgcn_exp2f(h2.x), __builtin_amdgcn_exp2f(h2.y)} + 1.0f;
          const f32x2 d = __builtin_elementwise_fma(s1, s1, f32x2{1.f, 1.f});
          const f32x2 rr = f32x2{__builtin_amdgcn_rcpf(d.x), __builtin_amdgcn_rcpf(d.y)};
          const f32x2 o = h2 * __builtin_elementwise_fma(rr, f32x2{-1.38629436111989062f, -1.38629436111989062f},
                                                         f32x2{0.69314718055994531f, 0.69314718055994531f});
          pk[e] = pk2(o.x, o.y);
        }
        st128c(ao + q * 512, ab, v4u{pk[0], pk[1], pk[2], pk[3]});
      }
    }
    lds_set(L, W_RDY + wi * 4, 12);
    ST(13);
    __builtin_amdgcn_s_setprio(2);
    LATE_LANE();
    v2u xq[5], dq[5], gr2, f3;
    const unsigned f2o = WS_F2 + (unsigned)((4 * rg + (a2 >> 1)) * 16 + 4 * wi + (ln_ & 3)) * 128;
    unsigned vm[4];
#pragma unroll
    for (int m = 0; m < 4; ++m) vm[m] = vo + ((m + wi) & 3) * 49152;
    v2u f2; v4u ysh;
    {
      f32x4 sacc[4];
#pragma unroll
      for (int i = 0; i < 4; ++i) sacc[i] = f32x4{0.f, 0.f, 0.f, 0.f};
      unsigned kb = CVS + KAO - wi * 128 - 3584 - 64, ubb = CVS + UBO - wi * 128 - 64; PIN(kb); PIN(ubb);
      LAS char* ka = L + kb; LAS char* ub = L + ubb;
      const bool late = RFL(lw(L, W_SB)) < 16;
      if (late) { waitv<0>(); arrive_flag<4>(L, W_F2C, lane, ws, WS_F2 + (unsigned)wid * 128, tlo, thi); }
      lds_wait_ge(L, W_SB, 16);
      if (par) conv_unit_shared<1>(ka + 64, ub + 64, sacc); else conv_unit_shared<0>(ka + 64, ub + 64, sacc);
      if (!late) { waitv<0>(); arrive_flag<4>(L, W_F2C, lane, ws, WS_F2 + (unsigned)wid * 128, tlo, thi); }
      ST(14);
      WL(4); WL(5);
      if (par) conv_unit_shared<1>(ka, ub, sacc); else conv_unit_shared<0>(ka, ub, sacc);
#pragma unroll
      for (int q = 0; q < 4; ++q) *(LAS f32x4*)(L + MXCH + (q * 4 + wi) * 1024 + vo) = sacc[q];
      lds_arrive(L, W_MB, lane);
    {
      const char* xb2 = x + (size_t)row2 * 1280 + cb2 * 160; const unsigned xo2 = OROW * 1280 + cg * 8;
      ld64o<0>(xq[0], xo2, xb2); ld64o<32>(xq[1], xo2, xb2); ld64o<64>(xq[2], xo2, xb2); ld64o<96>(xq[3], xo2, xb2); ld64o<128>(xq[4], xo2, xb2);
      const char* db = pDskip + cb2 * 160;
      ld64o<0>(dq[0], cg * 8, db); ld64o<32>(dq[1], cg * 8, db); ld64o<64>(dq[2], cg * 8, db); ld64o<96>(dq[3], cg * 8, db); ld64o<128>(dq[4], cg * 8, db);
      ld64c(gr2, WS_STATS + (row2 + OROW) * 8, ws);
    }
      lds_wait_ge(L, W_MB, 4);
      LAS char* p = L + MXCH + wi * 4096 + vo;
      const f32x4 s = add4(add4(*(LAS f32x4*)p, *(LAS f32x4*)(p + 1024)), add4(*(LAS f32x4*)(p + 2048), *(LAS f32x4*)(p + 3072)));
      ld64c(f2, f2o, ws);
      const unsigned y0 = pk2(s.x, s.y), y1 = pk2(s.z, s.w);
      *(LAS v2u*)(L + MXCH + wi * 4096 + YOFF) = v2u{y0, y1};
      FENCE();
      ysh = *(LAS v4u*)p;
    }
    ST(15);
    waitv<0>(); PIN(f2);
    f2 = spin64<false>(f2, f2o, ws, tlo, thi);
    lds_set(L, W_F2OK + wi * 4, 1);
    ST(16);
    AL(0); AL(1);
    if (ln_ < 32) st128c(vo, ws + WS_Y + (size_t)chS * 4096 + (2 * wi + ((wi & 1) ^ par)) * 512, ysh);
    f32x4 acc2[4][5];
    PIN(gr2);
    const float rs2 = __builtin_amdgcn_rcpf(bf2f(gr2.x)), m2 = -bfhi(gr2.x) * rs2;
#pragma unroll
    for (int n = 0; n < 5; ++n) {
      PIN(xq[n]); PIN(dq[n]);
      const f32x2 x0 = unpk(xq[n].x), x1 = unpk(xq[n].y), d0 = unpk(dq[n].x), d1 = unpk(dq[n].y);
      acc2[0][n] = f32x4{ffma(d0.x, ffma(x0.x, rs2, m2), x0.x), ffma(d0.y, ffma(x0.y, rs2, m2), x0.y),
                         ffma(d1.x, ffma(x1.x, rs2, m2), x1.x), ffma(d1.y, ffma(x1.y, rs2, m2), x1.y)};
      PIN(acc2[0][n]);
    }
    __builtin_amdgcn_s_setprio(0);
    ST(17);
    waitv<0>();
    arrive_flag<8>(L, W_F3C, lane, ws, WS_F3 + (unsigned)(xcd * 32 + j) * 128, tlo, thi);
    g2_step<1>(acc2, WF[0], AF[0], [&](int i) { if (i >= 3 && (i & 1)) AL1(2, (i - 3) >> 1); });
    g2_step<4>(acc2, WF[1], AF[1], [&](int i) { if (i >= 2) AL1(3 + ((i - 2) >> 2), (i - 2) & 3); });
    waitv<8>(); g2_step<4>(acc2, WF[2], AF[2], [&](int i) { if (i < 4) AL1(5, i); });
    ST(20);
    waitv<8>(); g2_step<4>(acc2, WF[3], AF[3], NOLD);
    waitv<4>(); g2_step<4>(acc2, WF[4], AF[4], NOLD);
    lds_wait_ge(L, W_F3C, 8);
    const unsigned f3o = WS_F3 + (unsigned)(cb2 * 32 + 8 * wi + (ln_ & 7)) * 128;
    ld64c(f3, f3o, ws);
    waitv<1>(); g2_step<4>(acc2, WF[5], AF[5], NOLD);
    ST(22);
#pragma unroll
    for (int m = 1; m < 4; ++m)
#pragma unroll
      for (int n = 0; n < 5; ++n) *(LAS f32x4*)(L + FM + wi * 15360 + (m - 1) * 5120 + n * 1024 + vo) = acc2[m][n];
    waitv<0>(); PIN(f3);
    f3 = spin64<false>(f3, f3o, ws, tlo, thi);
    lds_arrive(L, W_F3ALL, lane);
    lds_wait_ge(L, W_F3ALL, 4);
    ST(21);
    v4u yv[3];
    {
      const char* yb = ws + WS_Y + (size_t)cb2 * 80 * 4096 + row2 * 2;
      const unsigned yo = (unsigned)(ln_ >> 1) * 4096 + (ln_ & 1) * 16;
      ld128c<0>(yv[0], yo, yb); ld128c<0>(yv[1], yo, yb + 32 * 4096); ld128c<0>(yv[2], yo & 0xffffu, yb + 64 * 4096);
    }
#pragma unroll
    for (int n = 0; n < 5; ++n) {
#pragma unroll
      for (int d = 1; d < 4; ++d) acc2[0][n] = add4(acc2[0][n], *(LAS f32x4*)(L + FM + ((wi - d) & 3) * 15360 + (d - 1) * 5120 + n * 1024 + vo));
      *(LAS f32x4*)(L + FM + 61440 + wi * 5120 + n * 1024 + vo) = acc2[0][n];
    }
    waitv<0>(); PIN(yv[0]); PIN(yv[1]); PIN(yv[2]);
    LAS char* ys = L + YST + wi * 2560;
    *(LAS v4u*)(ys + vo) = yv[0]; *(LAS v4u*)(ys + 1024 + vo) = yv[1];
    if (ln_ < 32) *(LAS v4u*)(ys + 2048 + vo) = yv[2];
    lds_set(L, W_F2OK + wi * 4, 2);
    STEND();
    asm volatile("97:");
    __builtin_amdgcn_endpgm();
  }

  const bool odd = wi & 1;
  const int kch = chA + (wi >> 1);
  const unsigned vo = lane * 16;
  v4u WF[6][5], AF[6][4];
  waitv<6>();
  lds_arrive(L, W_ST, lane);
  waitv<0>();
  arrive_flag8(L, W_F1C, ws, WS_F1 + (unsigned)(lane & 7) * 2048 + wid * 8, tlo, thi);
  ST(8);
  auto reduce = [&](int c, int i) {
    LAS char* pp = L + KG_SCR + (c < 2 ? 4 * c : 3) * KG_WSTR + i * 16;
    const f32x4 s = add4(*(LAS f32x4*)pp, *(LAS f32x4*)(pp + (c < 2 ? 2 : 4) * KG_WSTR));
    *(LAS f32x4*)(L + KTAB + c * KP_STRIDE + i * 16) = s;
    return s;
  };
  lds_wait_ge(L, W_KG, 4);
  if (ts < 161) { st128c((unsigned)ts * 16, ws + WS_KP + (size_t)wid * KP_STRIDE, __builtin_bit_cast(v4u, reduce(2, ts))); reduce(0, ts); }
  if (ts >= 95) reduce(1, ts - 95);
  if (ts < 3) *(LAS float*)(L + KTAB + ts * KP_STRIDE - 4) = 0.f;
  lds_arrive(L, W_SB, lane);
  waitv<0>();
  arrive_flag(L, W_FKC, lane, ws, WS_FK + (unsigned)wid * 64, tlo, thi);
  ST(9);
  __builtin_amdgcn_s_setprio(0);
  lds_wait_min<4>(L, W_RDY, F1STAGE, lane);
  const unsigned f1o = WS_F1 + (unsigned)xcd * 2048 + ts * 8;
  v2u f1own; ld64c(f1own, f1o, ws);
  waitv<0>(); PIN(f1own);
  f1own = spin64<false>(f1own, f1o, ws, tlo, thi);
  v4u uu[3]; unsigned ulo[3];
#pragma unroll
  for (int r = 0; r < 3; ++r) {
    const int e = 64 * r + lane, tb = 64 * wi + e / 3, c = e % 3;
    ld128c<0>(uu[r], tb * 80 + c * 16, ws + WS_XNB + (size_t)(16 * xcd + (j >> 1)) * 20480 + par * 32);
    ulo[r] = (c == 2 - 2 * par ? CVS : CVA + (c - par) * CV_CH) + tb * 16;
  }
  if (b == 0) arrive_flag(L, W_F1ALL, lane, ws, WS_E, tlo, thi);
  else lds_arrive(L, W_F1ALL, lane);
  ST(4);
  const unsigned fko = WS_FK + (unsigned)(wid ^ 1) * 64;
  v2u fk; ld64c(fk, fko, ws);
  waitv<0>(); PIN(fk); PIN(uu[0]); PIN(uu[1]); PIN(uu[2]);
  fk = spin64<false>(fk, fko, ws, tlo, thi);
  v4u kp; ld128c<0>(kp, (unsigned)(ts < 161 ? ts : 160) * 16, ws + WS_KP + (size_t)(wid ^ 1) * KP_STRIDE);
  lds_wait_ge(L, W_SB, 8);
  waitv<0>(); PIN(kp);
  if (ts < 161) { LAS f32x4* tp = (LAS f32x4*)(L + KTAB + 2 * KP_STRIDE + ts * 16); *tp = add4(*tp, __builtin_bit_cast(f32x4, kp)); }
  lds_arrive(L, W_SB, lane);
  lds_wait_ge(L, W_F1ALL, 4);
  WL(0);
  lds_wait_min<4>(L, W_RDY, 9, lane);
  put_k(L, kix, ts, 0, CVA);
  WL(1);
  lds_wait_min<4>(L, W_RDY, 10, lane);
  put_k(L, kix, ts, 1, CVA + CV_CH);
  if (ZCOND) { *(LAS v4u*)(L + CVA + ZOFF) = v4u{0u, 0u, 0u, 0u}; *(LAS v4u*)(L + CVA + CV_CH + ZOFF) = v4u{0u, 0u, 0u, 0u}; }
  ST(7);
  lds_wait_min<4>(L, W_RDY, 11, lane);
  ST(11);
  put_u(L + ulo[0], uu[0], ts < 3); put_u(L + ulo[1], uu[1], false); put_u(L + ulo[2], uu[2], false);
  if (ZCOND) *(LAS v4u*)(L + CVS + ZOFF) = v4u{0u, 0u, 0u, 0u};
  lds_wait_ge(L, W_SB, 12);
  put_k(L, kix, ts, 2, CVS);
  lds_arrive(L, W_SB, lane);
  WL(2); WL(3);
  ST(5);
  lds_wait_ge(L, W_SB, 16);
  lds_wait_ge(L, W_RDY + wi * 4, 12);
  ST(13);
  __builtin_amdgcn_s_setprio(SPRIO2);
  LATE_LANE();
  f32x4 cacc[8];
#pragma unroll
  for (int i = 0; i < 8; ++i) cacc[i] = f32x4{0.f, 0.f, 0.f, 0.f};
  {
    const unsigned o1 = CVA + (wi >> 1) * CV_CH - (odd ? 4 * 64 : 0);
    unsigned kb = KAO + o1 - 3584 - 192, ubb = UBO + o1 - 192; PIN(kb); PIN(ubb);
    LAS char* ka = L + kb; LAS char* ub = L + ubb;
    v4u A[8], B[8];
#define LA(i, u) (*(LAS v4u*)(ka + 512 * (7 - (i)) + 64 * (3 - (u))))
#define LB(i, u) (*(LAS v4u*)(ub + 512 * (i) + 64 * (3 - (u))))
    A[7] = LA(7, 0); B[0] = LB(0, 0); A[0] = LA(0, 0); B[7] = LB(7, 0); A[6] = LA(6, 0); B[6] = LB(6, 0); A[1] = LA(1, 0); B[1] = LB(1, 0);
    A[5] = LA(5, 0); B[5] = LB(5, 0); A[2] = LA(2, 0); B[2] = LB(2, 0); A[4] = LA(4, 0); B[4] = LB(4, 0); A[3] = LA(3, 0); B[3] = LB(3, 0);
    SCHED();
#pragma unroll
    for (int u = 0; u < 4; ++u) {
      v4u An[2], Bn[2];
#pragma unroll
      for (int sd = 7; sd >= 0; --sd) {
#pragma unroll
        for (int o = 0; o <= sd; ++o) {
          if (sd + o < 8) cacc[sd + o] = mfma(A[sd], B[o], cacc[sd + o]);
          if (o != sd && sd + o < 8) cacc[sd + o] = mfma(A[o], B[sd], cacc[sd + o]);
        }
        SCHED();
        if (u < 3) {
          if (sd >= 2) { A[sd] = LA(sd, u + 1); B[sd] = LB(sd, u + 1); }
          if (sd == 6 || sd == 5) { An[6 - sd] = LA(6 - sd, u + 1); Bn[6 - sd] = LB(6 - sd, u + 1); }
        }
        SCHED();
      }
      if (u < 3) { A[0] = An[0]; A[1] = An[1]; B[0] = Bn[0]; B[1] = Bn[1]; }
    }
#undef LA
#undef LB
  }
  ST(10);
  WL(4); WL(5);
  unsigned vm[4];
  {
    LAS char* px = L + SXCH + wi * 4096 + vo;
    if (odd) {
#pragma unroll
      for (int i = 0; i < 4; ++i) { *(LAS f32x4*)(px + i * 1024) = cacc[i]; cacc[i] = cacc[4 + i]; }
    } else {
#pragma unroll
      for (int i = 0; i < 4; ++i) *(LAS f32x4*)(px + i * 1024) = cacc[4 + i];
    }
    lds_arrive(L, W_SX + (wi >> 1) * 4, lane);
#pragma unroll
    for (int m = 0; m < 4; ++m) vm[m] = vo + ((m + wi) & 3) * 49152;
    lds_wait_ge(L, W_SX + (wi >> 1) * 4, 2);
    px = L + SXCH + (wi ^ 1) * 4096 + vo;
    v2u yo[4];
#pragma unroll
    for (int i = 0; i < 4; ++i) { const f32x4 s = add4(cacc[i], *(LAS f32x4*)(px + i * 1024)); yo[i] = v2u{pk2(s.x, s.y), pk2(s.z, s.w)}; }
    char* yb = ws + WS_Y + (size_t)kch * 4096 + (odd ? 2048 : 0);
    LAS char* yl = L + SXCH + (wi ^ 1) * 4096;
#pragma unroll
    for (int i = 0; i < 4; ++i) *(LAS v2u*)(yl + i * 512 + YOFF) = yo[i];
    FENCE();
    st128c(vo, yb, *(LAS v4u*)(yl + vo)); st128c(vo, yb + 1024, *(LAS v4u*)(yl + 1024 + vo));
  }
  ST(14);
  lds_wait_ge(L, W_F2OK + wi * 4, 1);
  ST(16);
  AL(0); AL(1);
  ST(17);
  f32x4 acc2[4][5];
  waitv<0>();
  arrive_flag<8>(L, W_F3C, lane, ws, WS_F3 + (unsigned)(xcd * 32 + j) * 128, tlo, thi);
  __builtin_amdgcn_s_setprio(0);
  waitv<4>(); g2_step<0>(acc2, WF[0], AF[0], [&](int i) { if (i >= 3 && (i & 1)) AL1(2, (i - 3) >> 1); });
  waitv<4>(); g2_step<4>(acc2, WF[1], AF[1], [&](int i) { if (i >= 2) AL1(3 + ((i - 2) >> 2), (i - 2) & 3); });
  waitv<8>(); g2_step<4>(acc2, WF[2], AF[2], [&](int i) { if (i < 4) AL1(5, i); });
  ST(20);
  waitv<8>(); g2_step<4>(acc2, WF[3], AF[3], NOLD);
  lds_wait_ge(L, W_F3C, 8);
  waitv<0>();
#pragma unroll
  for (int m = 0; m < 4; ++m) { PIN(AF[4][m]); PIN(AF[5][m]); }
#pragma unroll
  for (int n = 0; n < 5; ++n) { PIN(WF[4][n]); PIN(WF[5][n]); }
#pragma unroll
  for (int mm = 1; mm < 5; ++mm) {
    const int m = mm & 3;
#pragma unroll
    for (int n = 0; n < 5; ++n) {
      acc2[m][n] = mfma(WF[4][n], AF[4][m], acc2[m][n]);
      acc2[m][n] = mfma(WF[5][n], AF[5][m], acc2[m][n]);
      if (mm > 1) *(LAS f32x4*)(L + FS + wi * 15360 + (mm - 2) * 5120 + n * 1024 + vo) = acc2[mm - 1][n];
      SCHED();
    }
  }
  lds_arrive(L, W_FS, lane);
  ST(22);
  lds_wait_ge(L, W_FS, 4);
#pragma unroll
  for (int d = 1; d < 4; ++d)
#pragma unroll
    for (int n = 0; n < 5; ++n) acc2[0][n] += *(LAS f32x4*)(L + FS + ((wi - d) & 3) * 15360 + (d - 1) * 5120 + n * 1024 + vo);
  ST(23);
  lds_wait_ge(L, W_F2OK + wi * 4, 2);
  ST(24);
  {
    char* ob = out + (size_t)row2 * 1280 + cb2 * 160; const unsigned oo = OROW * 1280 + cg * 8;
    v2u o[5];
#pragma unroll
    for (int n = 0; n < 5; ++n) {
      const v2u t = trread(L + YST + wi * 2560 + n * 512 + ln_ * 8);
      const f32x4 s = acc2[0][n] + *(LAS f32x4*)(L + FM + 61440 + wi * 5120 + n * 1024 + vo);
      const f32x2 s0 = s.xy + unpk(t.x), s1 = s.zw + unpk(t.y);
      o[n] = v2u{pk2(s0.x, s0.y), pk2(s1.x, s1.y)};
    }
    st64o<0>(oo, ob, o[0]); st64o<32>(oo, ob, o[1]); st64o<64>(oo, ob, o[2]); st64o<96>(oo, ob, o[3]); st64o<128>(oo, ob, o[4]);
  }
  STEND();
  asm volatile("99:");
}

extern "C" void kernel_launch(void* const* d_in, const int* in_sizes, int n_in, void* d_out, int out_size, void* d_ws, size_t ws_size,
                              hipStream_t stream) {
  fused_s4_mlp<<<256, 512, 0, stream>>>((const char*)d_in[0], (const char*)d_in[1], (const char*)d_in[2], (const char*)d_in[3],
      (const char*)d_in[4], (const char*)d_in[5], (const char*)d_in[6], (const char*)d_in[7], (const char*)d_in[8], (const char*)d_in[10],
      (const char*)d_in[11], (const char*)d_in[12], (char*)d_out, (char*)d_ws);
}
```

```cpp
#include <hip/hip_runtime.h>
#include <stdint.h>

typedef unsigned short u16;
typedef unsigned long long u64;
typedef unsigned v4u __attribute__((ext_vector_type(4)));
typedef unsigned v2u __attribute__((ext_vector_type(2)));
typedef float f32x4 __attribute__((ext_vector_type(4)));
typedef float f32x2 __attribute__((ext_vector_type(2)));
typedef short bf16x8 __attribute__((ext_vector_type(8)));
typedef short s16x4 __attribute__((ext_vector_type(4)));
typedef __bf16 bf16x2_t __attribute__((ext_vector_type(2)));
#define LAS __attribute__((address_space(3)))
#define GAS __attribute__((address_space(1)))
#define DI __device__ __forceinline__

constexpr unsigned WS_E = 0, WS_TMO = 64, WS_F1 = 131072, WS_STATS = 16384, WS_F2 = 32768, WS_F3 = 65536, WS_FK = 98304, WS_STAMP = 262144;
constexpr unsigned WS_XNB = 1u << 20, WS_W2T = 4u << 20, WS_Y = 7u << 20, WS_ACT = 10u << 20, WS_KP = 17u << 20;
constexpr unsigned KP_STRIDE = 2624;
constexpr unsigned SPIN_CAP = 400000;

constexpr unsigned W_F1C = 0, W_F2C = 4, W_F3C = 8, W_KG = 12, W_MB = 16, W_SB = 20, W_SX = 24, W_FM = 32, W_FS = 36, W_F3ALL = 40, W_F1ALL = 44,
                   W_F2OK = 48, W_RDY = 64, W_TAG = 80, W_FKC = 96, W_ST = 100;
constexpr unsigned KTAB = 272;
constexpr unsigned CSP = 8192;
constexpr unsigned CVA = 10240, CV_CH = 27008, CV_USTR = 4640, CV_KOFF = 18560, CV_KSTR = 4224;
constexpr unsigned KG_SCR = CVA, KG_WSTR = 4352, LN_ROWS = KG_SCR + 8 * KG_WSTR, LN_PITCH = 1344, W2P = LN_ROWS + 10752;
constexpr unsigned G1_RING = 64512, G1_STAGE = 28672, G1_BOFF = 16384;
constexpr unsigned CVS = G1_RING, SXCH = CVS + 27136, MXCH = SXCH + 16384;
constexpr unsigned CSF = G1_RING + 3 * G1_STAGE, YST = CSF + 3072;
constexpr unsigned FM = 256, FS = FM + 81920;
static_assert(FS + 61440 <= CSF && CVA + 2 * CV_CH <= G1_RING && W2P + 8192 <= G1_RING && YST + 10240 <= 163840 && MXCH + 16384 <= CSF, "lds map");

#define KPRIO 1
#define ROT2 0
#define F1STAGE 5
#define SPRIO2 1
#define F1GAP 16
#define F2GAP 8
#define SPRIO 1
#define ST(i) do {} while (0)
#define STEND() do {} while (0)
#define STARG
#define STPASS

DI float bf2f(unsigned v) { return __builtin_bit_cast(float, v << 16); }
DI float bfhi(unsigned v) { return __builtin_bit_cast(float, v & 0xffff0000u); }
DI unsigned pk2(float lo, float hi) { bf16x2_t r; r.x = (__bf16)lo; r.y = (__bf16)hi; return __builtin_bit_cast(unsigned, r); }
DI float rbf(float f) { return bf2f(pk2(f, f) & 0xffffu); }
DI f32x4 mfma(v4u a, v4u b, f32x4 c) {
  return __builtin_amdgcn_mfma_f32_16x16x32_bf16(__builtin_bit_cast(bf16x8, a), __builtin_bit_cast(bf16x8, b), c, 0, 0, 0);
}
DI v2u trread(LAS char* p) { return __builtin_bit_cast(v2u, __builtin_amdgcn_ds_read_tr16_b64_v4i16((LAS s16x4*)p)); }
template <int N> DI void waitv() { asm volatile("s_waitcnt vmcnt(%0)" ::"i"(N) : "memory"); }
DI void bar() { asm volatile("s_waitcnt lgkmcnt(0)\n\ts_barrier" ::: "memory"); }
#define PIN(x) asm volatile("" : "+v"(x))
#define FENCE() asm volatile("" ::: "memory")
#define SCHED() __builtin_amdgcn_sched_barrier(0)
#define RFL(x) __builtin_amdgcn_readfirstlane(x)

DI void ld16(unsigned& d, unsigned vo, const void* sb) { asm volatile("global_load_ushort %0, %1, %2" : "=v"(d) : "v"(vo), "s"(sb) : "memory"); }
template <int O> DI void ld32(unsigned& d, unsigned vo, const void* sb) { asm volatile("global_load_dword %0, %1, %2 offset:%3" : "=v"(d) : "v"(vo), "s"(sb), "i"(O) : "memory"); }
DI void ld32c(unsigned& d, unsigned vo, const void* sb) { asm volatile("global_load_dword %0, %1, %2 sc1" : "=v"(d) : "v"(vo), "s"(sb) : "memory"); }
DI void ld64(v2u& d, unsigned vo, const void* sb) { asm volatile("global_load_dwordx2 %0, %1, %2" : "=v"(d) : "v"(vo), "s"(sb) : "memory"); }
DI void ld128(v4u& d, unsigned vo, const void* sb) { asm volatile("global_load_dwordx4 %0, %1, %2" : "=v"(d) : "v"(vo), "s"(sb) : "memory"); }
DI void ld64c(v2u& d, unsigned vo, const void* sb) { asm volatile("global_load_dwordx2 %0, %1, %2 sc1" : "=v"(d) : "v"(vo), "s"(sb) : "memory"); }
template <int O> DI void ld128c(v4u& d, unsigned vo, const void* sb) { asm volatile("global_load_dwordx4 %0, %1, %2 offset:%3 sc1" : "=v"(d) : "v"(vo), "s"(sb), "i"(O) : "memory"); }
DI void st64c(unsigned vo, void* sb, v2u d) { asm volatile("global_store_dwordx2 %0, %1, %2 sc1" ::"v"(vo), "v"(d), "s"(sb) : "memory"); }
template <int O> DI void ld64o(v2u& d, unsigned vo, const void* sb) { asm volatile("global_load_dwordx2 %0, %1, %2 offset:%3" : "=v"(d) : "v"(vo), "s"(sb), "i"(O) : "memory"); }
template <int O> DI void st64o(unsigned vo, void* sb, v2u d) { asm volatile("global_store_dwordx2 %0, %1, %2 offset:%3" ::"v"(vo), "v"(d), "s"(sb), "i"(O) : "memory"); }
template <int O> DI void st64co(unsigned vo, void* sb, v2u d) { asm volatile("global_store_dwordx2 %0, %1, %2 offset:%3 sc1" ::"v"(vo), "v"(d), "s"(sb), "i"(O) : "memory"); }
DI void st128c(unsigned vo, void* sb, v4u d) { asm volatile("global_store_dwordx4 %0, %1, %2 sc1\n\ts_nop 1" ::"v"(vo), "v"(d), "s"(sb) : "memory"); }
DI void st64u(unsigned vo, void* sb, v2u d) { asm volatile("global_store_dwordx2 %0, %1, %2 sc1 ; CNT" ::"v"(vo), "v"(d), "s"(sb) : "memory"); }
DI void st128u(unsigned vo, void* sb, v4u d) { asm volatile("global_store_dwordx4 %0, %1, %2 sc1 ; CNT\n\ts_nop 1" ::"v"(vo), "v"(d), "s"(sb) : "memory"); }
DI void dma16(unsigned vo, const void* sb, unsigned lds) {
  asm volatile("s_mov_b32 m0, %2\n\ts_nop 0\n\tglobal_load_lds_dwordx4 %0, %1" ::"v"(vo), "s"(sb), "s"(lds) : "memory");
}
template <int O> DI void dma16o(unsigned vo, const void* sb) {
  asm volatile("global_load_lds_dwordx4 %0, %1 offset:%2" ::"v"(vo), "s"(sb), "i"(O) : "memory");
}
template <bool HIONLY> DI v2u spin64(v2u cur, unsigned vo, char* ws, unsigned tlo, unsigned thi) {
  if (__builtin_expect(__builtin_amdgcn_ballot_w64(HIONLY ? (cur.y != tlo) : (cur.x != tlo || cur.y != thi)) != 0ull, 0)) {
#pragma nounroll
    for (unsigned spins = 0; spins < SPIN_CAP; ++spins) {
      __builtin_amdgcn_s_sleep(6);
      asm volatile("global_load_dwordx2 %0, %1, %2 sc1\n\ts_waitcnt vmcnt(0)" : "=&v"(cur) : "v"(vo), "s"(ws) : "memory");
      if (__builtin_amdgcn_ballot_w64(HIONLY ? (cur.y != tlo) : (cur.x != tlo || cur.y != thi)) == 0ull) break;
    }
  }
  return cur;
}
DI unsigned lw(LAS char* L, unsigned off) { return *(volatile LAS unsigned*)(L + off); }
DI void lds_wait_at(unsigned a, unsigned target) {
  unsigned v, cnt; u64 t;
  asm volatile("s_mov_b32 %1, %5\n"
               "1:\n\tds_read_b32 %0, %3\n\ts_sub_u32 %1, %1, 1\n\ts_waitcnt lgkmcnt(0)\n\tv_cmp_gt_u32 vcc, %4, %0\n\t"
               "s_cselect_b64 %2, 0, vcc\n\ts_cmp_lg_u64 %2, 0\n\ts_cbranch_scc1 1b"
               : "=&v"(v), "=&s"(cnt), "=&s"(t) : "v"(a), "s"(target), "i"(4 * SPIN_CAP) : "vcc", "scc", "memory");
}
DI void lds_wait_ge(LAS char* L, unsigned off, unsigned target) { lds_wait_at((unsigned)(size_t)L + off, target); }
template <int N> DI void lds_wait_min(LAS char* L, unsigned off, unsigned target, int lane) {
  lds_wait_at((unsigned)(size_t)L + off + (lane & (N - 1)) * 4, target);
}
DI void lds_set(LAS char* L, unsigned off, unsigned v) { FENCE(); *(volatile LAS unsigned*)(L + off) = v; FENCE(); }
DI void lds_arrive(LAS char* L, unsigned off, int) {
  asm volatile("s_mov_b64 exec, 1\n\tds_add_u32 %0, %1\n\ts_mov_b64 exec, -1" ::"v"((unsigned)(size_t)L + off), "v"(1u) : "memory");
}
template <unsigned N = 4> DI void arrive_flag(LAS char* L, unsigned off, int, char* ws, unsigned fo, unsigned tlo, unsigned thi) {
  unsigned old;
  asm volatile("s_mov_b64 exec, 1\n\tds_add_rtn_u32 %0, %1, %2\n\ts_waitcnt lgkmcnt(0)\n\tv_cmp_eq_u32 vcc, %6, %0\n\t"
               "s_mov_b64 exec, vcc\n\tglobal_store_dwordx2 %3, %4, %5 sc1\n\ts_mov_b64 exec, -1"
               : "=&v"(old) : "v"((unsigned)(size_t)L + off), "v"(1u), "v"(fo), "v"(v2u{tlo, thi}), "s"(ws), "i"(N - 1) : "vcc", "memory");
}
DI void arrive_flag8(LAS char* L, unsigned off, char* ws, unsigned fo, unsigned tlo, unsigned thi) {
  unsigned old;
  asm volatile("s_mov_b64 exec, 1\n\tds_add_rtn_u32 %0, %1, %2\n\ts_waitcnt lgkmcnt(0)\n\tv_cmp_eq_u32 vcc, 3, %0\n\t"
               "s_mul_i32 exec_lo, vcc_lo, 0xff\n\tglobal_store_dwordx2 %3, %4, %5 sc1\n\ts_mov_b64 exec, -1"
               : "=&v"(old) : "v"((unsigned)(size_t)L + off), "v"(1u), "v"(fo), "v"(v2u{tlo, thi}), "s"(ws) : "vcc", "memory");
}
typedef float f32x16 __attribute__((ext_vector_type(16)));
DI f32x16 mfma32(v4u a, v4u b, f32x16 c) {
  return __builtin_amdgcn_mfma_f32_32x32x16_bf16(__builtin_bit_cast(bf16x8, a), __builtin_bit_cast(bf16x8, b), c, 0, 0, 0);
}
template <int C> DI float dpp_add(float v) {
  return v + __builtin_bit_cast(float, __builtin_amdgcn_update_dpp(0, __builtin_bit_cast(int, v), C, 0xf, 0xf, true));
}
DI float rl(float v, int l) { return __builtin_bit_cast(float, __builtin_amdgcn_readlane(__builtin_bit_cast(int, v), l)); }
DI float half_sum(float v, bool upper) {
  v = dpp_add<0xB1>(v); v = dpp_add<0x4E>(v); v = dpp_add<0x141>(v); v = dpp_add<0x140>(v);
  const float a = rl(v, 0) + rl(v, 16), c = rl(v, 32) + rl(v, 48);
  return upper ? c : a;
}
DI f32x2 unpk(unsigned v) { return f32x2{bf2f(v), bfhi(v)}; }
struct c2 { f32x2 r, i; };
DI c2 cmul(c2 a, c2 b) { return c2{__builtin_elementwise_fma(a.r, b.r, -(a.i * b.i)), __builtin_elementwise_fma(a.r, b.i, a.i * b.r)}; }
DI c2 csq(c2 v) { return c2{__builtin_elementwise_fma(v.r, v.r, -(v.i * v.i)), (v.r + v.r) * v.i}; }
DI c2 cexpk(f32x2 e1, f32x2 th, f32x2 tl, float k, float ks) {
  const f32x2 e = e1 * k, t = th * ks;
  const f32x2 mag = f32x2{__builtin_amdgcn_exp2f(e.x), __builtin_amdgcn_exp2f(e.y)};
  const f32x2 fr = __builtin_elementwise_fma(tl, f32x2{ks, ks}, f32x2{__builtin_amdgcn_fractf(t.x), __builtin_amdgcn_fractf(t.y)});
  return c2{f32x2{__builtin_amdgcn_cosf(fr.x), __builtin_amdgcn_cosf(fr.y)} * mag, f32x2{__builtin_amdgcn_sinf(fr.x), __builtin_amdgcn_sinf(fr.y)} * mag};
}
typedef _Float16 f16x8 __attribute__((ext_vector_type(8)));
typedef _Float16 f16x2 __attribute__((ext_vector_type(2)));
DI f32x4 mfmah(v4u a, v4u b, f32x4 c) {
  return __builtin_amdgcn_mfma_f32_16x16x32_f16(__builtin_bit_cast(f16x8, a), __builtin_bit_cast(f16x8, b), c, 0, 0, 0);
}
DI unsigned pkh1(float a, float b) { f16x2 r; r.x = (_Float16)a; r.y = (_Float16)b; return __builtin_bit_cast(unsigned, r); }
DI v2u pkh(c2 v) { return v2u{pkh1(v.r.x, v.i.x), pkh1(v.r.y, v.i.y)}; }
DI v2u pkb(c2 v) { return v2u{pk2(v.r.x, v.i.x), pk2(v.r.y, v.i.y)}; }
#define HMUL(o, i, w) "v_pk_mul_f16 " o ", " i ", " w " op_sel:[1,1] op_sel_hi:[1,0] neg_lo:[0,1]\n\t"
#define HFMA(o, i, w) "v_pk_fma_f16 " o ", " i ", " w ", " o " op_sel_hi:[0,1,1]\n\t"
constexpr int KROW = 528;
template <int R0, int R1> DI void h2(v2u& o0, v2u& o1, v2u i0, v2u i1, v2u w0, v2u w1) {
  unsigned a, b, c, d;
  asm volatile(HMUL("%0", "%4", "%8") HMUL("%1", "%5", "%9") HMUL("%2", "%6", "%10") HMUL("%3", "%7", "%11")
               HFMA("%0", "%4", "%8") HFMA("%1", "%5", "%9") HFMA("%2", "%6", "%10") HFMA("%3", "%7", "%11")
               "ds_write_addtid_b32 %0 offset:%12\n\tds_write_addtid_b32 %1 offset:%13\n\tds_write_addtid_b32 %2 offset:%14\n\tds_write_addtid_b32 %3 offset:%15"
               : "=&v"(a), "=&v"(b), "=&v"(c), "=&v"(d)
               : "v"(i0.x), "v"(i0.y), "v"(i1.x), "v"(i1.y), "v"(w0.x), "v"(w0.y), "v"(w1.x), "v"(w1.y),
                 "i"(R0 * KROW), "i"(R0 * KROW + 256), "i"(R1 * KROW), "i"(R1 * KROW + 256) : "memory");
  o0 = v2u{a, b}; o1 = v2u{c, d};
}
template <int R> DI void hput(v2u v) {
  asm volatile("ds_write_addtid_b32 %0 offset:%2\n\tds_write_addtid_b32 %1 offset:%3" ::"v"(v.x), "v"(v.y), "i"(R * KROW), "i"(R * KROW + 256) : "memory");
}
template <int C> DI unsigned dpp_maxu(unsigned v) { return max(v, (unsigned)__builtin_amdgcn_update_dpp(0, (int)v, C, 0xf, 0xf, true)); }
DI void rd8(v4u (&f)[8], LAS char* sr) {
#pragma unroll
  for (int k = 0; k < 8; ++k) f[k] = *(LAS v4u*)(sr + 64 * k);
}
template <bool H> DI void kmm(f32x4 (&ka)[2][2][2], int nh, const v4u (&UA)[2][8], const v4u (&vb)[8]) {
#pragma unroll
  for (int k = 0; k < 8; ++k)
#pragma unroll
    for (int m = 0; m < 2; ++m) {
      f32x4& a = ka[(k >> 1) & 1][m][nh];
      const f32x4 c = (k == 0 || k == 2) ? f32x4{0.f, 0.f, 0.f, 0.f} : a;
      a = H ? mfmah(UA[m][k], vb[k], c) : mfma(UA[m][k], vb[k], c);
    }
}
DI void kgen_slow(c2 a, c2 W1, c2 W256, c2 W8, LAS char* sr, f32x4 (&ka)[2][2][2]) {
  v4u UA[2][8], vb[8];
  c2 u[8]; u[0] = a;
#pragma unroll
  for (int i = 1; i < 8; ++i) u[i] = cmul(u[i - 1], W1);
  const c2 W512 = csq(W256), W1024 = csq(W512), r8 = cmul(a, W256), r10 = cmul(u[4], W256), r12 = cmul(a, W512), r14 = cmul(r8, W512), r16 = cmul(a, W1024);
  hput<0>(pkb(u[0])); hput<1>(pkb(u[1])); hput<2>(pkb(u[2])); hput<3>(pkb(u[3])); hput<4>(pkb(u[4])); hput<5>(pkb(u[5])); hput<6>(pkb(u[6])); hput<7>(pkb(u[7]));
  hput<8>(pkb(r8)); hput<9>(pkb(cmul(u[2], W256))); hput<10>(pkb(r10)); hput<11>(pkb(cmul(u[6], W256)));
  hput<12>(pkb(r12)); hput<13>(pkb(cmul(u[4], W512))); hput<14>(pkb(r14)); hput<15>(pkb(cmul(r10, W512)));
  rd8(UA[0], sr);
  hput<0>(pkb(r16)); hput<1>(pkb(cmul(r8, W1024))); hput<2>(pkb(cmul(r12, W1024))); hput<3>(pkb(cmul(r14, W1024))); hput<4>(pkb(cmul(r16, W1024)));
  rd8(UA[1], sr);
  c2 v = c2{f32x2{1.f, 1.f}, f32x2{0.f, 0.f}};
#pragma unroll
  for (int nh = 0; nh < 2; ++nh) {
#define VR(r) hput<r>(pkb(v)); v = cmul(v, W8);
    VR(0) VR(1) VR(2) VR(3) VR(4) VR(5) VR(6) VR(7) VR(8) VR(9) VR(10) VR(11) VR(12) VR(13) VR(14) VR(15)
#undef VR
    rd8(vb, sr);
    kmm<false>(ka, nh, UA, vb);
  }
}
DI void kgen(const unsigned (&pr)[7], bool dead, bool upper, unsigned sw, LAS char* sr, f32x4 (&ka)[2][2][2], float (&scale)[2]) {
  const float dt = rbf(__builtin_amdgcn_exp2f(bf2f(pr[6]) * 1.44269504088896341f));
  const f32x2 zr = unpk(pr[0]) * dt, zi = unpk(pr[1]) * dt, dbr = unpk(pr[2]) * dt, dbi = unpk(pr[3]) * dt;
  const f32x2 cr = unpk(dead ? 0u : pr[4]), ci = unpk(dead ? 0u : pr[5]);
  const c2 a = c2{__builtin_elementwise_fma(cr, dbr, -(ci * dbi)), -__builtin_elementwise_fma(cr, dbi, ci * dbr)};
  const f32x2 e1 = zr * 1.44269504088896341f, th = zi * 0.15915494309189535f;
  const f32x2 tl = __builtin_elementwise_fma(zi, f32x2{6.4206383e-9f, 6.4206383e-9f}, __builtin_elementwise_fma(zi, f32x2{0.15915494309189535f, 0.15915494309189535f}, -th));
  const c2 W1 = cexpk(e1, th, tl, 1.f, -1.f), W256 = cexpk(e1, th, tl, 256.f, -256.f), W8 = cexpk(e1, th, tl, 8.f, 8.f);
  asm volatile("s_mov_b32 m0, %0\n\ts_nop 0" ::"s"(sw) : "memory");
  if (__builtin_expect(__builtin_amdgcn_ballot_w64(!(zr.x <= 0.f && zr.y <= 0.f)) != 0ull, 0)) {
    kgen_slow(a, W1, W256, W8, sr, ka);
    scale[0] = scale[1] = 1.f;
    return;
  }
  unsigned am = max(max(__builtin_bit_cast(unsigned, a.r.x) & 0x7fffffffu, __builtin_bit_cast(unsigned, a.r.y) & 0x7fffffffu),
           max(__builtin_bit_cast(unsigned, a.i.x) & 0x7fffffffu, __builtin_bit_cast(unsigned, a.i.y) & 0x7fffffffu));
  am = dpp_maxu<0xB1>(am); am = dpp_maxu<0x4E>(am); am = dpp_maxu<0x141>(am); am = dpp_maxu<0x140>(am);
  const unsigned ex0 = min(max(max((unsigned)__builtin_amdgcn_readlane(am, 0), (unsigned)__builtin_amdgcn_readlane(am, 16)) >> 23, 2u), 252u);
  const unsigned ex1 = min(max(max((unsigned)__builtin_amdgcn_readlane(am, 32), (unsigned)__builtin_amdgcn_readlane(am, 48)) >> 23, 2u), 252u);
  scale[0] = __builtin_bit_cast(float, ex0 << 23); scale[1] = __builtin_bit_cast(float, ex1 << 23);
  const float down = __builtin_bit_cast(float, ((254u - (upper ? ex1 : ex0)) << 23));
  const c2 W2 = csq(W1), W512 = csq(W256), W16 = csq(W8), W4 = csq(W2), W1024 = csq(W512), W32 = csq(W16), W64 = csq(W32), W128 = csq(W64);
  const v2u u0 = pkh(c2{a.r * down, a.i * down}), h1 = pkh(W1), h2_ = pkh(W2), h4 = pkh(W4), h256 = pkh(W256), h512 = pkh(W512), h1024 = pkh(W1024);
  v2u u2, u4, u6, r8, r10, r12, r14, r16, t0, t1;
  v4u UA[2][8], vb[8];
  hput<0>(u0);
  h2<2, 4>(u2, u4, u0, u0, h2_, h4); h2<8, 12>(r8, r12, u0, u0, h256, h512); h2<1, 3>(t0, t1, u0, u2, h1, h1); h2<5, 6>(t0, u6, u4, u4, h1, h2_);
  h2<10, 14>(r10, r14, r8, r8, h4, h512); h2<7, 9>(t0, t1, u6, r8, h1, h2_); h2<11, 13>(t0, t1, r10, r12, h2_, h4);
  h2<15, 15>(r16, t1, u0, r14, h1024, h4);
  rd8(UA[0], sr);
  hput<0>(r16);
  h2<1, 2>(t0, t1, r8, r12, h1024, h1024); h2<3, 4>(t0, t1, r14, r16, h1024, h1024);
  rd8(UA[1], sr);
  v2u v[16];
  v[1] = pkh(W8); v[2] = pkh(W16); v[4] = pkh(W32); v[8] = pkh(W64);
  const v2u h128 = pkh(W128);
  hput<0>(v2u{0x3c00u, 0x3c00u}); hput<1>(v[1]); hput<2>(v[2]); hput<4>(v[4]); hput<8>(v[8]);
  h2<3, 5>(v[3], v[5], v[2], v[4], v[1], v[1]); h2<6, 9>(v[6], v[9], v[4], v[8], v[2], v[1]); h2<7, 10>(v[7], v[10], v[4], v[8], v[3], v[2]);
  h2<11, 12>(v[11], v[12], v[8], v[8], v[3], v[4]); h2<13, 14>(v[13], v[14], v[8], v[8], v[5], v[6]); h2<15, 15>(t0, v[15], v[1], v[8], h128, v[7]);
  rd8(vb, sr);
  hput<0>(h128); hput<1>(t0);
  h2<2, 3>(t0, t1, v[2], v[3], h128, h128); h2<4, 5>(t0, t1, v[4], v[5], h128, h128);
  kmm<true>(ka, 0, UA, vb);
  h2<6, 7>(t0, t1, v[6], v[7], h128, h128); h2<8, 9>(t0, t1, v[8], v[9], h128, h128); h2<10, 11>(t0, t1, v[10], v[11], h128, h128);
  h2<12, 13>(t0, t1, v[12], v[13], h128, h128); h2<14, 15>(t0, t1, v[14], v[15], h128, h128);
  rd8(vb, sr);
  SCHED();
  kmm<true>(ka, 1, UA, vb);
}
DI void kgen_store(LAS float* pt, const f32x4 (&kacc)[2][2], int lane) {
  const int l15 = lane & 15, g = lane >> 4;
  const int sh = g < 2 ? 3 : 4 - g;
  const int a0 = (l15 << sh) + (g < 2 ? 4 * g : 128 * g);
  const int a1 = g < 3 ? a0 + 2 : a0 + 64;
#pragma unroll
  for (int nh = 0; nh < 2; ++nh) {
    *(LAS f32x2*)(pt + a0 + ((16 * nh) << sh)) = kacc[0][nh].xy;
    *(LAS f32x2*)(pt + a1 + ((16 * nh) << sh)) = kacc[0][nh].zw;
  }
  if (g == 0) {
#pragma unroll
    for (int nh = 0; nh < 2; ++nh)
#pragma unroll
      for (int r = 0; r < 4; ++r) pt[512 + 32 * r + 16 * nh + l15] = kacc[1][nh][r];
  }
  if (lane == 16) { *(LAS f32x4*)(pt + 640) = f32x4{kacc[1][0][0], 0.f, 0.f, 0.f}; }
}

template <int PAR> DI void conv_unit_shared(LAS char* ka, LAS char* ub, f32x4 (&acc)[4]) {
  constexpr int T[4] = {PAR ? 1 : 0, PAR ? 2 : 3, PAR ? 5 : 4, PAR ? 6 : 7};
  v4u A[8], B[8];
#pragma unroll
  for (int i = 0; i <= T[3]; ++i) { A[i] = *(LAS v4u*)(ka + 512 * (7 - i)); B[i] = *(LAS v4u*)(ub + 512 * i); }
  SCHED();
#pragma unroll
  for (int q = 0; q < 4; ++q)
#pragma unroll
    for (int ip = 0; ip <= T[q]; ++ip) acc[q] = mfma(A[T[q] - ip], B[ip], acc[q]);
  SCHED();
}
DI float fadd(float a, float b) { float r; asm("v_add_f32 %0, %1, %2" : "=v"(r) : "v"(a), "v"(b)); return r; }
DI float ffma(float a, float b, float c) { float r; asm("v_fma_f32 %0, %1, %2, %3" : "=v"(r) : "v"(a), "v"(b), "v"(c)); return r; }
DI f32x4 add4(f32x4 a, f32x4 b) { return f32x4{fadd(a.x, b.x), fadd(a.y, b.y), fadd(a.z, b.z), fadd(a.w, b.w)}; }
DI f32x4 fma4(f32x4 a, float b, f32x4 c) { return f32x4{ffma(a.x, b, c.x), ffma(a.y, b, c.y), ffma(a.z, b, c.z), ffma(a.w, b, c.w)}; }
DI int rot12(int v) { return v >= 12 ? v - 12 : v; }
DI void w_loads(v4u (&WF)[5], const char* p, unsigned vo) {
  ld128c<0>(WF[0], vo, p); ld128c<1024>(WF[1], vo, p); ld128c<2048>(WF[2], vo, p); ld128c<3072>(WF[3], vo, p); ld128c<0>(WF[4], vo, p + 4096);
}
DI void a_loads(v4u (&AF)[4], const char* p, const unsigned (&vm)[4]) {
#pragma unroll
  for (int m = 0; m < 4; ++m) ld128c<0>(AF[m], vm[m], p);
}
template <int LIVE, class F> DI void g2_step(f32x4 (&acc)[4][5], v4u (&WF)[5], v4u (&AF)[4], F ld) {
#pragma unroll
  for (int m = 0; m < 4; ++m) PIN(AF[m]);
#pragma unroll
  for (int n = 0; n < 5; ++n) PIN(WF[n]);
#pragma unroll
  for (int e = 0; e < 20; ++e) {
    const int m = e / 5, n = e % 5;
    acc[m][n] = mfma(WF[n], AF[m], m >= LIVE ? f32x4{0.f, 0.f, 0.f, 0.f} : acc[m][n]);
    if (e & 1) { SCHED(); ld(e >> 1); SCHED(); }
  }
}
DI void make_kix(unsigned (&kix)[9], int ts) {
#pragma unroll
  for (int i = 0; i < 9; ++i) {
    const int lg = 2047 - 8 * ts - i;
    const int bits = (int)(pk2(__builtin_fmaxf((float)lg, 128.f), 0.f));
    const int ci = lg < bits - 0x4280 ? lg : bits - 0x4280;
    kix[i] = KTAB + 4 * ci;
    PIN(kix[i]);
  }
}
DI void put_k(LAS char* L, const unsigned (&kix)[9], int ts, int c, unsigned reg) {
  float kv[9];
#pragma unroll
  for (int i = 0; i < 9; ++i) kv[i] = *(LAS float*)(L + kix[i] + c * KP_STRIDE);
  LAS char* q = L + reg + CV_KOFF + ts * 16;
  *(LAS v4u*)(q + CV_KSTR) = v4u{pk2(kv[0], kv[1]), pk2(kv[2], kv[3]), pk2(kv[4], kv[5]), pk2(kv[6], kv[7])};
  *(LAS v4u*)q = v4u{pk2(kv[1], kv[2]), pk2(kv[3], kv[4]), pk2(kv[5], kv[6]), pk2(kv[7], kv[8])};
}
DI void put_u(LAS char* q, v4u u, bool first) {
  *(LAS v4u*)(q + 3 * CV_USTR + 384) = u;
  *(LAS v2u*)(q + 1 * CV_USTR + 392) = v2u{u.x, u.y}; *(LAS v2u*)(q + 1 * CV_USTR + 400) = v2u{u.z, u.w};
  *(LAS unsigned*)(q + 2 * CV_USTR + 388) = u.x; *(LAS v2u*)(q + 2 * CV_USTR + 392) = v2u{u.y, u.z}; *(LAS unsigned*)(q + 2 * CV_USTR + 400) = u.w;
  *(LAS unsigned*)(q + 396) = u.x; *(LAS v2u*)(q + 400) = v2u{u.y, u.z}; *(LAS unsigned*)(q + 408) = u.w;
  if (first) {
    *(LAS v2u*)(q + 384) = v2u{0u, 0u}; *(LAS unsigned*)(q + 392) = 0u;
    *(LAS v2u*)(q + CV_USTR + 384) = v2u{0u, 0u}; *(LAS unsigned*)(q + 2 * CV_USTR + 384) = 0u;
  }
}

__global__ __launch_bounds__(512, 2) void fused_s4_mlp(const char* __restrict__ x, const char* __restrict__ pAre, const char* __restrict__ pAim,
    const char* __restrict__ pBre, const char* __restrict__ pBim, const char* __restrict__ pCre, const char* __restrict__ pCim,
    const char* __restrict__ plogdt, const char* __restrict__ pDskip, const char* __restrict__ w1, const char* __restrict__ b1,
    const char* __restrict__ w2, char* __restrict__ out, char* __restrict__ ws) {
  __shared__ __attribute__((aligned(16))) char smem[163840];
  LAS char* L = (LAS char*)smem;
  const unsigned lds0 = (unsigned)(size_t)L;
  const char* pc0; unsigned codelen;
  asm volatile("s_getpc_b64 %0\n98:\n\ts_mov_b32 %1, 97f-98b\n\ts_max_u32 %1, %1, 99f-98b" : "=s"(pc0), "=&s"(codelen) :: "scc");
  const int b = blockIdx.x, lane = threadIdx.x & 63;
  const int w = RFL(threadIdx.x >> 6), wi = w & 3;
  {
    const unsigned co = (unsigned)(4 * (16 * w + (lane & 15)) + ((b >> 3) & 3)) * 128;
    asm volatile("s_mov_b32 m0, %2\n\ts_nop 0\n\tglobal_load_lds_dword %0, %1" ::"v"((co < codelen ? co : codelen) & ~3u), "s"(pc0), "s"(lds0 + YST + w * 256) : "memory");
  }
  const int xcd = b & 7, j = b >> 3, rg = xcd >> 1, chf = xcd & 1;
  const int ct1 = 8 * chf + (j & 7), rb1 = 4 * rg + (j >> 3);
  const int wid = rb1 * 16 + ct1;
  const int a2 = 4 * chf + (j >> 3), cb2 = j & 7;
  const int l15 = lane & 15, g = lane >> 4, qq = (lane >> 2) & 3, p4 = lane & 3, l31 = lane & 31, h = lane >> 5;
  const int par = j & 1, cbase = 80 * xcd + 5 * (j >> 1);
  const int chA = cbase + 3 * par, chS = cbase + 2;
  const int ts = 64 * wi + lane;
  ST(0);
  unsigned pr[7]; v4u xa, xb;
  {
    const bool odd = wi & 1, shl = odd && h, mw = w >= 4;
    const int kch = chA + (wi >> 1);
    unsigned p0 = (kch * 192 + (wi & 1) * 128 + 2 * lane) * 2, p1 = (chS * 192 + par * 96 + (wi >> 1) * 48 + 2 * min(l31, 23)) * 2, x0 = h * 1280 + l31 * 8;
    PIN(p0); PIN(p1); PIN(x0);
    const unsigned po = shl ? p1 : p0, mm = mw ? ~0u : 0u;
    const unsigned xo = (x0 + l31 * 8) & ~mm, vo = (po & mm) | ((x0 + 1024) & ~mm), v1 = po & mm;
    asm volatile("" ::"s"(x), "s"(pAre), "s"(pAim), "s"(pBre), "s"(pBim), "s"(pCre), "s"(pCim), "s"(plogdt), "s"(ws));
    const char* xrow = x + (size_t)(8 * wid + 2 * wi) * 1280;
    ld16(pr[6], (unsigned)(shl ? chS : kch) * 2, plogdt);
    ld32<0>(pr[0], vo, mw ? pAre : xrow); ld32<0>(pr[1], vo, mw ? pAim : xrow + 4);
    ld32c(pr[2], v1, mw ? pBre : ws); ld32c(pr[3], v1, mw ? pBim : ws + 4);
    ld32<0>(pr[4], po, pCre); ld32<0>(pr[5], po, pCim);
    ld128(xa, xo, xrow); ld128(xb, xo, xrow + 512);
  }
  const int row2 = 512 * rg + 64 * a2 + 16 * wi;
  const char* w2b = ws + WS_W2T + (size_t)(cb2 * 48 + 12 * wi) * 5120;
  const char* actb = ws + WS_ACT + (size_t)((32 * rg + 4 * a2) * 48 + 12 * wi) * 1024;
  const int r2 = ((a2 ^ (w >> 2)) & 1) * 6;
#define WL(s) w_loads(WF[s], w2b + rot12(r2 + (s)) * 5120, vo)
#define AL(s) a_loads(AF[s], actb + rot12(r2 + (s)) * 1024, vm)
#define AL1(s, m) ld128c<0>(AF[s][m], vm[m], actb + rot12(r2 + (s)) * 1024)
#define WL1(s, n) ld128c<0>(WF[s][n], vo, w2b + rot12(r2 + (s)) * 5120 + (n) * 1024)
#define NOLD [&](int) {}
#define LATE_LANE() int ln_ = lane; PIN(ln_); const int c15 = ln_ & 15, cg = ln_ >> 4
#define KAO (CV_KOFF + (c15 & 1) * CV_KSTR + 2 * (2072 - 8 * (c15 >> 1) + 8 * cg))
#define UBO ((c15 & 3) * CV_USTR + 2 * (64 * (c15 >> 2) + 8 * cg + 224))
#define YOFF ((unsigned)((c15 >> 2) * 128 + cg * 32 + (c15 & 3) * 8))
#define OROW ((unsigned)(2 * (c15 >> 2) + (c15 & 1) + 8 * ((c15 >> 1) & 1)))
#define ZOFF (lane < 24 ? wi * CV_USTR + lane * 16 : CV_KOFF + wi * CV_KSTR + 4096 + (lane - 24) * 16)
#define ZCOND (lane < 24 || (lane < 31 && wi < 2))

  unsigned voA[4], voB[3];
#pragma unroll
  for (int i = 0; i < 4; ++i) voA[i] = (8 * i + (lane >> 3)) * 1280 + (((lane & 7) ^ (4 * (i & 1) + (lane >> 4))) * 16) - 1024 * i;
#pragma unroll
  for (int i = 0; i < 3; ++i) { const int c = 64 * i + lane; voB[i] = (c / 12) * 3072 + (c % 12) * 16 - 1024 * i; }
  const char* g1a = x + (size_t)(rb1 * 128 + 32 * wi) * 1280;
  const char* g1b = w1 + ct1 * 192 + wi * 49152;
  const unsigned la = lds0 + G1_RING + wi * 4096, lb = lds0 + G1_RING + G1_BOFF + wi * 3072;
  const int r1 = ((j & 7) + (5 * (j >> 3)) / 2 + 5 * chf + rg) % 10;
  auto slab = [&](int s) { const int v = s + r1; return v >= 10 ? v - 10 : v; };
  auto issue = [&](int s) {
    const char* pa = g1a + slab(s) * 128; const char* pb = g1b + (size_t)slab(s) * 64 * 3072;
    dma16(voA[0], pa, la + s * G1_STAGE); dma16o<1024>(voA[1], pa); dma16o<2048>(voA[2], pa); dma16o<3072>(voA[3], pa);
    dma16(voB[0], pb, lb + s * G1_STAGE); dma16o<1024>(voB[1], pb); dma16o<2048>(voB[2], pb);
  };
  unsigned tlo = 0, thi = 0;
  unsigned kix[9];
  const int pu = b % 240, pks = pu / 5, pcg = pu % 5;
  if (w >= 4) {
    bar();
    make_kix(kix, ts);
    waitv<0>();
  } else {
    if (w == 0) *(LAS unsigned*)(L + lane * 4) = 0u;
    bar();
#pragma unroll
    for (int i = 0; i < 2; ++i)
      dma16((unsigned)((lane >> 1) * 1280 + (lane & 1) * 16), w2 + (size_t)pks * 32 * 1280 + (128 * pcg + 32 * wi + 16 * i) * 2, lds0 + W2P + wi * 2048 + i * 1024);
    issue(0);
#pragma unroll
    for (int i = 0; i < 9; ++i) kix[i] = 0;
    waitv<9>();
  }
  asm volatile("; AUDITCLR");
  PIN(xa); PIN(xb);
#pragma unroll
  for (int i = 0; i < 7; ++i) PIN(pr[i]);
  ST(1);
  int role0 = w; asm volatile("" : "+s"(role0));
  if (role0 >= 4) {
    __builtin_amdgcn_s_setprio(KPRIO);
    const bool odd = wi & 1;
    f32x4 ka[2][2][2]; float scale[2];
    kgen(pr, odd && lane >= 56, h, lds0 + KG_SCR + wi * 2 * KG_WSTR, L + KG_SCR + wi * 2 * KG_WSTR + l15 * KROW + g * 16, ka, scale);
#pragma unroll
    for (int i = 0; i < 4; ++i) {
      ka[1][i >> 1][i & 1] *= scale[1];
      ka[0][i >> 1][i & 1] = ka[0][i >> 1][i & 1] * scale[0] + (odd ? f32x4{0.f, 0.f, 0.f, 0.f} : ka[1][i >> 1][i & 1]);
    }
    kgen_store((LAS float*)(L + KG_SCR + wi * 2 * KG_WSTR), ka[0], lane);
    if (odd) kgen_store((LAS float*)(L + KG_SCR + wi * 2 * KG_WSTR + KG_WSTR), ka[1], lane);
    lds_arrive(L, W_KG, lane);
  } else {
    const v2u xc = v2u{pr[0], pr[1]}, E = v2u{pr[2], pr[3]};
    ST(6);
  const u64 tag64 = (((u64)RFL(E.y) << 32) | (unsigned)RFL(E.x)) + 1ull;
  tlo = (unsigned)tag64; thi = (unsigned)(tag64 >> 32);
  if (wi == 0) { lds_set(L, W_TAG, tlo); lds_set(L, W_TAG + 4, thi); lds_set(L, W_TAG + 8, 1); }
  {
    f32x2 v[10] = {unpk(xa.x), unpk(xa.y), unpk(xa.z), unpk(xa.w), unpk(xb.x), unpk(xb.y), unpk(xb.z), unpk(xb.w), unpk(xc.x), unpk(xc.y)};
    f32x2 s2 = ((v[0] + v[1]) + (v[2] + v[3])) + ((v[4] + v[5]) + (v[6] + v[7])) + (v[8] + v[9]);
    const float mean = half_sum(s2.x + s2.y, h) * (1.0f / 640.0f);
    f32x2 q2 = f32x2{0.f, 0.f};
#pragma unroll
    for (int i = 0; i < 10; ++i) { const f32x2 d = v[i] - mean; q2 = __builtin_elementwise_fma(d, d, q2); }
    const float var = rbf(half_sum(q2.x + q2.y, h) * (1.0f / 640.0f));
    const float m = rbf(mean), sd = rbf(__builtin_amdgcn_sqrtf(rbf(var + 1e-5f))), rs = __builtin_amdgcn_rcpf(sd);
    unsigned o[10];
#pragma unroll
    for (int i = 0; i < 10; ++i) { const f32x2 t = (v[i] - m) * rs; o[i] = pk2(t.x, t.y); }
    LAS char* rp = L + LN_ROWS + (2 * wi + h) * LN_PITCH;
    *(LAS v4u*)(rp + l31 * 16) = v4u{o[0], o[1], o[2], o[3]};
    *(LAS v4u*)(rp + 512 + l31 * 16) = v4u{o[4], o[5], o[6], o[7]};
    *(LAS v2u*)(rp + 1024 + l31 * 8) = v2u{o[8], o[9]};
    issue(1);
    waitv<7>();
    lds_arrive(L, W_ST, lane);
    issue(2);
    if (l31 == 0) st64c(WS_STATS + (unsigned)(8 * wid + 2 * wi + h) * 8, ws,
                        v2u{(__builtin_bit_cast(unsigned, m) & 0xffff0000u) | (__builtin_bit_cast(unsigned, sd) >> 16), tlo});
  }
#pragma unroll
  for (int i = 0; i < 2; ++i) {
    LAS char* sp = L + W2P + wi * 2048 + i * 1024 + (16 * (g >> 1) + 4 * (g & 1) + qq) * 32 + 8 * p4;
    const v2u lo = trread(sp), hi = trread(sp + 256);
    const int ntg = 8 * pcg + 2 * wi + i;
    st128c((unsigned)lane * 16, ws + WS_W2T + (size_t)(((ntg / 5) * 48 + pks) * 5 + ntg % 5) * 1024, v4u{lo.x, lo.y, hi.x, hi.y});
  }
  lds_wait_ge(L, W_ST, 4);
  {
    LAS char* sp = L + LN_ROWS + qq * LN_PITCH + (160 * wi + 16 * g) * 2 + 8 * p4;
#pragma unroll
    for (int i = 0; i < 3; ++i) {
      const v2u lo = trread(sp + 128 * i), hi = trread(sp + 128 * i + 4 * LN_PITCH);
      const unsigned ch = 64 * i + lane;
      if (i < 2 || lane < 32) st128c((ch / 5) * 20480 + (ch % 5) * 16, ws + WS_XNB + (size_t)wi * 32 * 20480 + wid * 80, v4u{lo.x, lo.y, hi.x, hi.y});
    }
  }
  }
  asm volatile("; AUDITCLR");
  ST(2);
  int role = w; asm volatile("" : "+s"(role));
  if (role < 4) {
    int lane = __builtin_amdgcn_mbcnt_hi(~0u, __builtin_amdgcn_mbcnt_lo(~0u, 0u)); PIN(lane);
    const int l15 = lane & 15, g = lane >> 4, qq = (lane >> 2) & 3, p4 = lane & 3, l31 = lane & 31, h = lane >> 5;
    const unsigned vo = lane * 16;
    f32x16 acc1[3]; f32x4 csa[3];
#pragma unroll
    for (int n = 0; n < 3; ++n) {
      csa[n] = f32x4{0.f, 0.f, 0.f, 0.f};
#pragma unroll
      for (int i = 0; i < 16; ++i) acc1[n][i] = 0.f;
    }
    const int xrow = (l31 & 17) + 2 * (l15 >> 2) + 8 * ((l15 >> 1) & 1);
    unsigned xo[4], wo[4];
#pragma unroll
    for (int k = 0; k < 4; ++k) {
      const int kr = (k + wi) & 3;
      xo[k] = G1_RING + (32 * wi + xrow) * 128 + (((2 * kr + h) ^ ((xrow >> 1) & 7)) * 16);
      wo[k] = G1_RING + G1_BOFF + (16 * kr + 8 * h + qq) * 192 + 32 * (g & 1) + 8 * p4;
      PIN(xo[k]); PIN(wo[k]);
    }
    const unsigned sel1 = ((l15 ^ g) & 1) ? 0u : 0x3f803f80u;
    const v4u sel = v4u{sel1, sel1, sel1, sel1};
    v4u X[4], W[4][3];
    auto rdw = [&](int s, int k, int n) {
      LAS char* p = L + wo[k] + (s % 3) * G1_STAGE + 64 * n; const v2u lo = trread(p), hi = trread(p + 768); W[k][n] = v4u{lo.x, lo.y, hi.x, hi.y};
    };
    auto rdx = [&](int s, int k) { X[k] = *(LAS v4u*)(L + xo[k] + (s % 3) * G1_STAGE); };
#define MM(k, n) acc1[n] = mfma32(W[k][n], X[k], acc1[n])
#define CS(n) csa[n] = mfma(W[0][n], sel, csa[n])
    v2u gran; unsigned bb[3];
    v4u WF[6][5], AF[6][4];
    const unsigned go = WS_STATS + (unsigned)(rb1 * 128 + 32 * wi + xrow) * 8;
    auto wl1 = [&](int s, int e0) { const int e = 7 * (s - 7) + e0; if (s >= 7 && e0 < 7 && e < 20) ld128c<0>(WF[e / 5][e % 5], vo, w2b + rot12(r2 + e / 5) * 5120 + (e % 5) * 1024); };
    ST(3);
    rdx(0, 0); rdw(0, 0, 0); rdw(0, 0, 1); rdw(0, 0, 2); rdx(0, 1); rdw(0, 1, 0); rdw(0, 1, 1); rdw(0, 1, 2);
#pragma unroll
    for (int s = 0; s < 10; ++s) {
      const bool refill = s < 7;
      const char* pa = g1a + slab(s + 3) * 128; const char* pb = g1b + (size_t)slab(s + 3) * 64 * 3072;
      SCHED();
      MM(0, 0); rdx(s, 2); rdw(s, 2, 0); SCHED();
      MM(0, 1); rdw(s, 2, 1); rdw(s, 2, 2); SCHED();
      MM(0, 2); rdx(s, 3); rdw(s, 3, 0); SCHED();
      CS(0); rdw(s, 3, 1); SCHED();
      CS(1); rdw(s, 3, 2); SCHED();
      CS(2);
      if (s == 0) { waitv<0>(); arrive_flag8(L, W_F1C, ws, WS_F1 + (unsigned)(lane & 7) * 2048 + wid * 8, tlo, thi); } else if (s == 1) {} else if (s < 7) waitv<7>(); else if (s == 7) waitv<11>(); else if (s == 8) waitv<11>();
      if (s == 9 && l15 < 2) {
#pragma unroll
        for (int n = 0; n < 3; ++n) *(LAS f32x4*)(L + CSP + wi * 384 + (32 * n + 16 * l15 + 4 * g) * 4) = csa[n];
      }
      lds_set(L, W_RDY + wi * 4, s + 2);
      SCHED();
      MM(1, 0);
      if (refill) { asm volatile("s_waitcnt lgkmcnt(7)" ::: "memory"); dma16(voA[0], pa, la + (s % 3) * G1_STAGE); }
      wl1(s, 0);

      SCHED();
      MM(1, 1); if (refill) dma16o<1024>(voA[1], pa); wl1(s, 1);
      unsigned fl = 0;
      if (s < 9) fl = lw(L, W_RDY + p4 * 4);
      SCHED();
      MM(1, 2); if (refill) dma16o<2048>(voA[2], pa); wl1(s, 2); SCHED();
      MM(2, 0); if (refill) dma16o<3072>(voA[3], pa); wl1(s, 3);
      SCHED();
      if (s == 9) {
        MM(2, 1); wl1(s, 4); SCHED();
        lds_wait_min<4>(L, W_RDY, 11, lane);
        float cs[3];
#pragma unroll
        for (int n = 0; n < 3; ++n) { LAS float* p = (LAS float*)(L + CSP) + 32 * n + l31; cs[n] = fadd(fadd(p[0], p[96]), fadd(p[192], p[288])); }
        MM(2, 2); wl1(s, 5); SCHED();
        waitv<20>(); PIN(gran); PIN(bb[0]); PIN(bb[1]); PIN(bb[2]);
        MM(3, 0); MM(3, 1); MM(3, 2);
        const unsigned keep = h ? 0u : ~0u, nm = (gran.x >> 16) ^ 0x8000u;
        const v4u xe = v4u{(nm | (nm << 16)) & keep, (nm | (gran.x << 16)) & keep, 0u, 0u};
#pragma unroll
        for (int n = 0; n < 3; ++n) {
          const float c0 = bfhi(__builtin_bit_cast(unsigned, cs[n])), r0 = cs[n] - c0, c1 = bfhi(__builtin_bit_cast(unsigned, r0)), r1 = r0 - c1;
          const v4u we = v4u{(__builtin_bit_cast(unsigned, c0) >> 16) | __builtin_bit_cast(unsigned, c1), (__builtin_bit_cast(unsigned, r1) >> 16) | (bb[n] << 16), 0u, 0u};
          acc1[n] = mfma32(we, xe, acc1[n]);
        }
        SCHED();
        break;
      }
      if (__builtin_expect(__builtin_amdgcn_ballot_w64(fl < (unsigned)(s + 2)) != 0ull, 0)) lds_wait_min<4>(L, W_RDY, s + 2, lane);
      FENCE();
      SCHED();
      rdx(s + 1, 0); rdw(s + 1, 0, 0); SCHED();
      MM(2, 1); rdw(s + 1, 0, 1); rdw(s + 1, 0, 2); if (refill) dma16(voB[0], pb, lb + (s % 3) * G1_STAGE); wl1(s, 4); SCHED();
      MM(2, 2); rdx(s + 1, 1); rdw(s + 1, 1, 0); if (refill) dma16o<1024>(voB[1], pb); wl1(s, 5); SCHED();
      MM(3, 0); rdw(s + 1, 1, 1); if (refill) dma16o<2048>(voB[2], pb); wl1(s, 6); SCHED();
      MM(3, 1); rdw(s + 1, 1, 2); wl1(s, 7); SCHED();
      MM(3, 2); wl1(s, 8);
      if (s == 6) { lds_wait_ge(L, W_F1ALL, 4); ld64c(gran, go, ws); ld16(bb[0], l31 * 2, b1 + ct1 * 192); ld16(bb[1], l31 * 2, b1 + ct1 * 192 + 64); ld16(bb[2], l31 * 2, b1 + ct1 * 192 + 128); }
      SCHED();
      if (s == 0) ST(4); if (s == 3) ST(5); if (s == 6) ST(7);
    }
#undef MM
#undef CS
    ST(11);
    {
      const float rs = __builtin_amdgcn_rcpf(bf2f(gran.x)) * 1.44269504088896341f;
      char* ab = ws + WS_ACT + (size_t)((rb1 * 8 + 2 * wi) * 48 + 3 * ct1) * 1024;
      const unsigned ao = (unsigned)((l31 >> 4) * 49152 + h * 256 + l15 * 16);
#pragma unroll
      for (int q = 0; q < 6; ++q) {
        unsigned pk[4];
#pragma unroll
        for (int e = 0; e < 4; ++e) {
          const f32x2 h2 = f32x2{acc1[q >> 1][8 * (q & 1) + 2 * e], acc1[q >> 1][8 * (q & 1) + 2 * e + 1]} * rs;
          const f32x2 s1 = f32x2{__builtin_amdgcn_exp2f(h2.x), __builtin_amdgcn_exp2f(h2.y)} + 1.0f;
          const f32x2 d = __builtin_elementwise_fma(s1, s1, f32x2{1.f, 1.f});
          const f32x2 rr = f32x2{__builtin_amdgcn_rcpf(d.x), __builtin_amdgcn_rcpf(d.y)};
          const f32x2 o = h2 * __builtin_elementwise_fma(rr, f32x2{-1.38629436111989062f, -1.38629436111989062f},
                                                         f32x2{0.69314718055994531f, 0.69314718055994531f});
          pk[e] = pk2(o.x, o.y);
        }
        st128c(ao + q * 512, ab, v4u{pk[0], pk[1], pk[2], pk[3]});
      }
    }
    lds_set(L, W_RDY + wi * 4, 12);
    ST(13);
    __builtin_amdgcn_s_setprio(2);
    LATE_LANE();
    v2u xq[5], dq[5], gr2, f3;
    const unsigned f2o = WS_F2 + (unsigned)((4 * rg + (a2 >> 1)) * 16 + 4 * wi + (ln_ & 3)) * 128;
    unsigned vm[4];
#pragma unroll
    for (int m = 0; m < 4; ++m) vm[m] = vo + ((m + wi) & 3) * 49152;
    v2u f2; v4u ysh;
    {
      f32x4 sacc[4];
#pragma unroll
      for (int i = 0; i < 4; ++i) sacc[i] = f32x4{0.f, 0.f, 0.f, 0.f};
      unsigned kb = CVS + KAO - wi * 128 - 3584 - 64, ubb = CVS + UBO - wi * 128 - 64; PIN(kb); PIN(ubb);
      LAS char* ka = L + kb; LAS char* ub = L + ubb;
      const bool late = RFL(lw(L, W_SB)) < 12;
      if (late) { waitv<0>(); arrive_flag<4>(L, W_F2C, lane, ws, WS_F2 + (unsigned)wid * 128, tlo, thi); }
      lds_wait_ge(L, W_SB, 12);
      if (par) conv_unit_shared<1>(ka + 64, ub + 64, sacc); else conv_unit_shared<0>(ka + 64, ub + 64, sacc);
      if (!late) { waitv<0>(); arrive_flag<4>(L, W_F2C, lane, ws, WS_F2 + (unsigned)wid * 128, tlo, thi); }
      ST(14);
      WL(4); WL(5);
      if (par) conv_unit_shared<1>(ka, ub, sacc); else conv_unit_shared<0>(ka, ub, sacc);
#pragma unroll
      for (int q = 0; q < 4; ++q) *(LAS f32x4*)(L + MXCH + (q * 4 + wi) * 1024 + vo) = sacc[q];
      lds_arrive(L, W_MB, lane);
    {
      const char* xb2 = x + (size_t)row2 * 1280 + cb2 * 160; const unsigned xo2 = OROW * 1280 + cg * 8;
      ld64o<0>(xq[0], xo2, xb2); ld64o<32>(xq[1], xo2, xb2); ld64o<64>(xq[2], xo2, xb2); ld64o<96>(xq[3], xo2, xb2); ld64o<128>(xq[4], xo2, xb2);
      const char* db = pDskip + cb2 * 160;
      ld64o<0>(dq[0], cg * 8, db); ld64o<32>(dq[1], cg * 8, db); ld64o<64>(dq[2], cg * 8, db); ld64o<96>(dq[3], cg * 8, db); ld64o<128>(dq[4], cg * 8, db);
      ld64c(gr2, WS_STATS + (row2 + OROW) * 8, ws);
    }
      lds_wait_ge(L, W_MB, 4);
      LAS char* p = L + MXCH + wi * 4096 + vo;
      const f32x4 s = add4(add4(*(LAS f32x4*)p, *(LAS f32x4*)(p + 1024)), add4(*(LAS f32x4*)(p + 2048), *(LAS f32x4*)(p + 3072)));
      ld64c(f2, f2o, ws);
      const unsigned y0 = pk2(s.x, s.y), y1 = pk2(s.z, s.w);
      *(LAS v2u*)(L + MXCH + wi * 4096 + YOFF) = v2u{y0, y1};
      FENCE();
      ysh = *(LAS v4u*)p;
    }
    ST(15);
    waitv<0>(); PIN(f2);
    f2 = spin64<false>(f2, f2o, ws, tlo, thi);
    lds_set(L, W_F2OK + wi * 4, 1);
    ST(16);
    AL(0); AL(1);
    if (ln_ < 32) st128c(vo, ws + WS_Y + (size_t)chS * 4096 + (2 * wi + ((wi & 1) ^ par)) * 512, ysh);
    f32x4 acc2[4][5];
    PIN(gr2);
    const float rs2 = __builtin_amdgcn_rcpf(bf2f(gr2.x)), m2 = -bfhi(gr2.x) * rs2;
#pragma unroll
    for (int n = 0; n < 5; ++n) {
      PIN(xq[n]); PIN(dq[n]);
      const f32x2 x0 = unpk(xq[n].x), x1 = unpk(xq[n].y), d0 = unpk(dq[n].x), d1 = unpk(dq[n].y);
      acc2[0][n] = f32x4{ffma(d0.x, ffma(x0.x, rs2, m2), x0.x), ffma(d0.y, ffma(x0.y, rs2, m2), x0.y),
                         ffma(d1.x, ffma(x1.x, rs2, m2), x1.x), ffma(d1.y, ffma(x1.y, rs2, m2), x1.y)};
      PIN(acc2[0][n]);
    }
    __builtin_amdgcn_s_setprio(0);
    ST(17);
    waitv<0>();
    arrive_flag<8>(L, W_F3C, lane, ws, WS_F3 + (unsigned)(xcd * 32 + j) * 128, tlo, thi);
    g2_step<1>(acc2, WF[0], AF[0], [&](int i) { if (i >= 3 && (i & 1)) AL1(2, (i - 3) >> 1); });
    g2_step<4>(acc2, WF[1], AF[1], [&](int i) { if (i >= 2) AL1(3 + ((i - 2) >> 2), (i - 2) & 3); });
    waitv<8>(); g2_step<4>(acc2, WF[2], AF[2], [&](int i) { if (i < 4) AL1(5, i); });
    ST(20);
    waitv<8>(); g2_step<4>(acc2, WF[3], AF[3], NOLD);
    waitv<4>(); g2_step<4>(acc2, WF[4], AF[4], NOLD);
    lds_wait_ge(L, W_F3C, 8);
    const unsigned f3o = WS_F3 + (unsigned)(cb2 * 32 + 8 * wi + (ln_ & 7)) * 128;
    ld64c(f3, f3o, ws);
    waitv<1>(); g2_step<4>(acc2, WF[5], AF[5], NOLD);
    ST(22);
#pragma unroll
    for (int m = 1; m < 4; ++m)
#pragma unroll
      for (int n = 0; n < 5; ++n) *(LAS f32x4*)(L + FM + wi * 15360 + (m - 1) * 5120 + n * 1024 + vo) = acc2[m][n];
    waitv<0>(); PIN(f3);
    f3 = spin64<false>(f3, f3o, ws, tlo, thi);
    lds_arrive(L, W_F3ALL, lane);
    lds_wait_ge(L, W_F3ALL, 4);
    ST(21);
    v4u yv[3];
    {
      const char* yb = ws + WS_Y + (size_t)cb2 * 80 * 4096 + row2 * 2;
      const unsigned yo = (unsigned)(ln_ >> 1) * 4096 + (ln_ & 1) * 16;
      ld128c<0>(yv[0], yo, yb); ld128c<0>(yv[1], yo, yb + 32 * 4096); ld128c<0>(yv[2], yo & 0xffffu, yb + 64 * 4096);
    }
#pragma unroll
    for (int n = 0; n < 5; ++n) {
#pragma unroll
      for (int d = 1; d < 4; ++d) acc2[0][n] = add4(acc2[0][n], *(LAS f32x4*)(L + FM + ((wi - d) & 3) * 15360 + (d - 1) * 5120 + n * 1024 + vo));
      *(LAS f32x4*)(L + FM + 61440 + wi * 5120 + n * 1024 + vo) = acc2[0][n];
    }
    waitv<0>(); PIN(yv[0]); PIN(yv[1]); PIN(yv[2]);
    LAS char* ys = L + YST + wi * 2560;
    *(LAS v4u*)(ys + vo) = yv[0]; *(LAS v4u*)(ys + 1024 + vo) = yv[1];
    if (ln_ < 32) *(LAS v4u*)(ys + 2048 + vo) = yv[2];
    lds_set(L, W_F2OK + wi * 4, 2);
    STEND();
    asm volatile("97:");
    __builtin_amdgcn_endpgm();
  }

  const bool odd = wi & 1;
  const int kch = chA + (wi >> 1);
  const unsigned vo = lane * 16;
  v4u WF[6][5], AF[6][4];
  lds_wait_ge(L, W_TAG + 8, 1);
  tlo = RFL(lw(L, W_TAG)); thi = RFL(lw(L, W_TAG + 4));
  auto reduce = [&](int c, int i) {
    LAS char* pp = L + KG_SCR + (c < 2 ? 4 * c : 3) * KG_WSTR + i * 16;
    const f32x4 s = add4(*(LAS f32x4*)pp, *(LAS f32x4*)(pp + (c < 2 ? 2 : 4) * KG_WSTR));
    *(LAS f32x4*)(L + KTAB + c * KP_STRIDE + i * 16) = s;
    return s;
  };
  lds_wait_ge(L, W_KG, 4);
  if (ts < 161) { st128c((unsigned)ts * 16, ws + WS_KP + (size_t)wid * KP_STRIDE, __builtin_bit_cast(v4u, reduce(2, ts))); reduce(0, ts); }
  if (ts >= 95) reduce(1, ts - 95);
  if (ts < 3) *(LAS float*)(L + KTAB + ts * KP_STRIDE - 4) = 0.f;
  lds_arrive(L, W_SB, lane);
  waitv<0>();
  arrive_flag(L, W_FKC, lane, ws, WS_FK + (unsigned)wid * 64, tlo, thi);
  ST(9);
  __builtin_amdgcn_s_setprio(0);
  lds_wait_min<4>(L, W_RDY, F1STAGE, lane);
  const unsigned f1o = WS_F1 + (unsigned)xcd * 2048 + ts * 8;
  v2u f1own; ld64c(f1own, f1o, ws);
  waitv<0>(); PIN(f1own);
  f1own = spin64<false>(f1own, f1o, ws, tlo, thi);
  v4u uu[3]; unsigned ulo[3];
#pragma unroll
  for (int r = 0; r < 3; ++r) {
    const int e = 64 * r + lane, tb = 64 * wi + e / 3, c = e % 3;
    ld128c<0>(uu[r], tb * 80 + c * 16, ws + WS_XNB + (size_t)(16 * xcd + (j >> 1)) * 20480 + par * 32);
    ulo[r] = (c == 2 - 2 * par ? CVS : CVA + (c - par) * CV_CH) + tb * 16;
  }
  if (b == 0) arrive_flag(L, W_F1ALL, lane, ws, WS_E, tlo, thi);
  else lds_arrive(L, W_F1ALL, lane);
  ST(4);
  const unsigned fko = WS_FK + (unsigned)(wid ^ 1) * 64;
  v2u fk; ld64c(fk, fko, ws);
  waitv<0>(); PIN(fk); PIN(uu[0]); PIN(uu[1]); PIN(uu[2]);
  fk = spin64<false>(fk, fko, ws, tlo, thi);
  v4u kp; ld128c<0>(kp, (unsigned)(ts < 161 ? ts : 160) * 16, ws + WS_KP + (size_t)(wid ^ 1) * KP_STRIDE);
  lds_wait_ge(L, W_SB, 4);
  waitv<0>(); PIN(kp);
  if (ts < 161) { LAS f32x4* tp = (LAS f32x4*)(L + KTAB + 2 * KP_STRIDE + ts * 16); *tp = add4(*tp, __builtin_bit_cast(f32x4, kp)); }
  lds_arrive(L, W_SB, lane);
  lds_wait_ge(L, W_F1ALL, 4);
  WL(0);
  lds_wait_min<4>(L, W_RDY, 9, lane);
  put_k(L, kix, ts, 0, CVA);
  WL(1);
  lds_wait_min<4>(L, W_RDY, 10, lane);
  put_k(L, kix, ts, 1, CVA + CV_CH);
  if (ZCOND) { *(LAS v4u*)(L + CVA + ZOFF) = v4u{0u, 0u, 0u, 0u}; *(LAS v4u*)(L + CVA + CV_CH + ZOFF) = v4u{0u, 0u, 0u, 0u}; }
  ST(7);
  lds_wait_min<4>(L, W_RDY, 11, lane);
  ST(11);
  put_u(L + ulo[0], uu[0], ts < 3); put_u(L + ulo[1], uu[1], false); put_u(L + ulo[2], uu[2], false);
  if (ZCOND) *(LAS v4u*)(L + CVS + ZOFF) = v4u{0u, 0u, 0u, 0u};
  lds_wait_ge(L, W_SB, 8);
  put_k(L, kix, ts, 2, CVS);
  lds_arrive(L, W_SB, lane);
  WL(2); WL(3);
  ST(5);
  lds_wait_ge(L, W_SB, 12);
  lds_wait_ge(L, W_RDY + wi * 4, 12);
  ST(13);
  __builtin_amdgcn_s_setprio(SPRIO2);
  LATE_LANE();
  f32x4 cacc[8];
#pragma unroll
  for (int i = 0; i < 8; ++i) cacc[i] = f32x4{0.f, 0.f, 0.f, 0.f};
  {
    const unsigned o1 = CVA + (wi >> 1) * CV_CH - (odd ? 4 * 64 : 0);
    unsigned kb = KAO + o1 - 3584 - 192, ubb = UBO + o1 - 192; PIN(kb); PIN(ubb);
    LAS char* ka = L + kb; LAS char* ub = L + ubb;
    v4u A[8], B[8];
#define LA(i, u) (*(LAS v4u*)(ka + 512 * (7 - (i)) + 64 * (3 - (u))))
#define LB(i, u) (*(LAS v4u*)(ub + 512 * (i) + 64 * (3 - (u))))
    A[7] = LA(7, 0); B[0] = LB(0, 0); A[0] = LA(0, 0); B[7] = LB(7, 0); A[6] = LA(6, 0); B[6] = LB(6, 0); A[1] = LA(1, 0); B[1] = LB(1, 0);
    A[5] = LA(5, 0); B[5] = LB(5, 0); A[2] = LA(2, 0); B[2] = LB(2, 0); A[4] = LA(4, 0); B[4] = LB(4, 0); A[3] = LA(3, 0); B[3] = LB(3, 0);
    SCHED();
#pragma unroll
    for (int u = 0; u < 4; ++u) {
      v4u An[2], Bn[2];
#pragma unroll
      for (int sd = 7; sd >= 0; --sd) {
#pragma unroll
        for (int o = 0; o <= sd; ++o) {
          if (sd + o < 8) cacc[sd + o] = mfma(A[sd], B[o], cacc[sd + o]);
          if (o != sd && sd + o < 8) cacc[sd + o] = mfma(A[o], B[sd], cacc[sd + o]);
        }
        SCHED();
        if (u < 3) {
          if (sd >= 2) { A[sd] = LA(sd, u + 1); B[sd] = LB(sd, u + 1); }
          if (sd == 6 || sd == 5) { An[6 - sd] = LA(6 - sd, u + 1); Bn[6 - sd] = LB(6 - sd, u + 1); }
        }
        SCHED();
      }
      if (u < 3) { A[0] = An[0]; A[1] = An[1]; B[0] = Bn[0]; B[1] = Bn[1]; }
    }
#undef LA
#undef LB
  }
  ST(10);
  unsigned vm[4];
  {
    LAS char* px = L + SXCH + wi * 4096 + vo;
    if (odd) {
#pragma unroll
      for (int i = 0; i < 4; ++i) { *(LAS f32x4*)(px + i * 1024) = cacc[i]; cacc[i] = cacc[4 + i]; }
    } else {
#pragma unroll
      for (int i = 0; i < 4; ++i) *(LAS f32x4*)(px + i * 1024) = cacc[4 + i];
    }
    lds_arrive(L, W_SX + (wi >> 1) * 4, lane);
    WL(4); WL(5);
#pragma unroll
    for (int m = 0; m < 4; ++m) vm[m] = vo + ((m + wi) & 3) * 49152;
    lds_wait_ge(L, W_SX + (wi >> 1) * 4, 2);
    px = L + SXCH + (wi ^ 1) * 4096 + vo;
    v2u yo[4];
#pragma unroll
    for (int i = 0; i < 4; ++i) { const f32x4 s = add4(cacc[i], *(LAS f32x4*)(px + i * 1024)); yo[i] = v2u{pk2(s.x, s.y), pk2(s.z, s.w)}; }
    char* yb = ws + WS_Y + (size_t)kch * 4096 + (odd ? 2048 : 0);
    LAS char* yl = L + SXCH + (wi ^ 1) * 4096;
#pragma unroll
    for (int i = 0; i < 4; ++i) *(LAS v2u*)(yl + i * 512 + YOFF) = yo[i];
    FENCE();
    st128c(vo, yb, *(LAS v4u*)(yl + vo)); st128c(vo, yb + 1024, *(LAS v4u*)(yl + 1024 + vo));
  }
  ST(14);
  lds_wait_ge(L, W_F2OK + wi * 4, 1);
  ST(16);
  AL(0); AL(1);
  ST(17);
  f32x4 acc2[4][5];
  waitv<0>();
  arrive_flag<8>(L, W_F3C, lane, ws, WS_F3 + (unsigned)(xcd * 32 + j) * 128, tlo, thi);
  __builtin_amdgcn_s_setprio(0);
  waitv<4>(); g2_step<0>(acc2, WF[0], AF[0], [&](int i) { if (i >= 3 && (i & 1)) AL1(2, (i - 3) >> 1); });
  waitv<4>(); g2_step<4>(acc2, WF[1], AF[1], [&](int i) { if (i >= 2) AL1(3 + ((i - 2) >> 2), (i - 2) & 3); });
  waitv<8>(); g2_step<4>(acc2, WF[2], AF[2], [&](int i) { if (i < 4) AL1(5, i); });
  ST(20);
  waitv<8>(); g2_step<4>(acc2, WF[3], AF[3], NOLD);
  lds_wait_ge(L, W_F3C, 8);
  waitv<0>();
#pragma unroll
  for (int m = 0; m < 4; ++m) { PIN(AF[4][m]); PIN(AF[5][m]); }
#pragma unroll
  for (int n = 0; n < 5; ++n) { PIN(WF[4][n]); PIN(WF[5][n]); }
#pragma unroll
  for (int mm = 1; mm < 5; ++mm) {
    const int m = mm & 3;
#pragma unroll
    for (int n = 0; n < 5; ++n) {
      acc2[m][n] = mfma(WF[4][n], AF[4][m], acc2[m][n]);
      acc2[m][n] = mfma(WF[5][n], AF[5][m], acc2[m][n]);
      if (mm > 1) *(LAS f32x4*)(L + FS + wi * 15360 + (mm - 2) * 5120 + n * 1024 + vo) = acc2[mm - 1][n];
      SCHED();
    }
  }
  lds_arrive(L, W_FS, lane);
  ST(22);
  lds_wait_ge(L, W_FS, 4);
#pragma unroll
  for (int d = 1; d < 4; ++d)
#pragma unroll
    for (int n = 0; n < 5; ++n) acc2[0][n] += *(LAS f32x4*)(L + FS + ((wi - d) & 3) * 15360 + (d - 1) * 5120 + n * 1024 + vo);
  ST(23);
  lds_wait_ge(L, W_F2OK + wi * 4, 2);
  ST(24);
  {
    char* ob = out + (size_t)row2 * 1280 + cb2 * 160; const unsigned oo = OROW * 1280 + cg * 8;
    v2u o[5];
#pragma unroll
    for (int n = 0; n < 5; ++n) {
      const v2u t = trread(L + YST + wi * 2560 + n * 512 + ln_ * 8);
      const f32x4 s = acc2[0][n] + *(LAS f32x4*)(L + FM + 61440 + wi * 5120 + n * 1024 + vo);
      const f32x2 s0 = s.xy + unpk(t.x), s1 = s.zw + unpk(t.y);
      o[n] = v2u{pk2(s0.x, s0.y), pk2(s1.x, s1.y)};
    }
    st64o<0>(oo, ob, o[0]); st64o<32>(oo, ob, o[1]); st64o<64>(oo, ob, o[2]); st64o<96>(oo, ob, o[3]); st64o<128>(oo, ob, o[4]);
  }
  STEND();
  asm volatile("99:");
}

extern "C" void kernel_launch(void* const* d_in, const int* in_sizes, int n_in, void* d_out, int out_size, void* d_ws, size_t ws_size,
                              hipStream_t stream) {
  fused_s4_mlp<<<256, 512, 0, stream>>>((const char*)d_in[0], (const char*)d_in[1], (const char*)d_in[2], (const char*)d_in[3],
      (const char*)d_in[4], (const char*)d_in[5], (const char*)d_in[6], (const char*)d_in[7], (const char*)d_in[8], (const char*)d_in[10],
      (const char*)d_in[11], (const char*)d_in[12], (char*)d_out, (char*)d_ws);
}
```

```cpp
#include <hip/hip_runtime.h>
#include <stdint.h>

typedef unsigned short u16;
typedef unsigned long long u64;
typedef unsigned v4u __attribute__((ext_vector_type(4)));
typedef unsigned v2u __attribute__((ext_vector_type(2)));
typedef float f32x4 __attribute__((ext_vector_type(4)));
typedef float f32x2 __attribute__((ext_vector_type(2)));
typedef short bf16x8 __attribute__((ext_vector_type(8)));
typedef short s16x4 __attribute__((ext_vector_type(4)));
typedef __bf16 bf16x2_t __attribute__((ext_vector_type(2)));
#define LAS __attribute__((address_space(3)))
#define GAS __attribute__((address_space(1)))
#define DI __device__ __forceinline__

constexpr unsigned WS_E = 0, WS_TMO = 64, WS_F1 = 131072, WS_STATS = 16384, WS_F2 = 32768, WS_F3 = 65536, WS_FK = 98304, WS_STAMP = 262144;
constexpr unsigned WS_XNB = 1u << 20, WS_W2T = 4u << 20, WS_Y = 7u << 20, WS_ACT = 10u << 20, WS_KP = 17u << 20;
constexpr unsigned KP_STRIDE = 2624;
constexpr unsigned SPIN_CAP = 400000;

constexpr unsigned W_F1C = 0, W_F2C = 4, W_F3C = 8, W_KG = 12, W_MB = 16, W_SB = 20, W_SX = 24, W_FM = 32, W_FS = 36, W_F3ALL = 40, W_F1ALL = 44,
                   W_F2OK = 48, W_RDY = 64, W_TAG = 80, W_FKC = 96, W_ST = 100;
constexpr unsigned KTAB = 272;
constexpr unsigned CSP = 8192;
constexpr unsigned CVA = 10240, CV_CH = 27008, CV_USTR = 4640, CV_KOFF = 18560, CV_KSTR = 4224;
constexpr unsigned KG_SCR = CVA, KG_WSTR = 4352, LN_ROWS = KG_SCR + 8 * KG_WSTR, LN_PITCH = 1344, W2P = LN_ROWS + 10752;
constexpr unsigned G1_RING = 64512, G1_STAGE = 28672, G1_BOFF = 16384;
constexpr unsigned CVS = G1_RING, SXCH = CVS + 27136, MXCH = SXCH + 16384;
constexpr unsigned CSF = G1_RING + 3 * G1_STAGE, YST = CSF + 3072;
constexpr unsigned FM = 256, FS = FM + 81920;
static_assert(FS + 61440 <= CSF && CVA + 2 * CV_CH <= G1_RING && W2P + 8192 <= G1_RING && YST + 10240 <= 163840 && MXCH + 16384 <= CSF, "lds map");

#define KPRIO 1
#define ROT2 0
#define F1STAGE 5
#define SPRIO2 1
#define F1GAP 16
#define F2GAP 8
#define SPRIO 1
#define ST(i) do {} while (0)
#define STEND() do {} while (0)
#define STARG
#define STPASS

DI float bf2f(unsigned v) { return __builtin_bit_cast(float, v << 16); }
DI float bfhi(unsigned v) { return __builtin_bit_cast(float, v & 0xffff0000u); }
DI unsigned pk2(float lo, float hi) { bf16x2_t r; r.x = (__bf16)lo; r.y = (__bf16)hi; return __builtin_bit_cast(unsigned, r); }
DI float rbf(float f) { return bf2f(pk2(f, f) & 0xffffu); }
DI f32x4 mfma(v4u a, v4u b, f32x4 c) {
  return __builtin_amdgcn_mfma_f32_16x16x32_bf16(__builtin_bit_cast(bf16x8, a), __builtin_bit_cast(bf16x8, b), c, 0, 0, 0);
}
DI v2u trread(LAS char* p) { return __builtin_bit_cast(v2u, __builtin_amdgcn_ds_read_tr16_b64_v4i16((LAS s16x4*)p)); }
template <int N> DI void waitv() { asm volatile("s_waitcnt vmcnt(%0)" ::"i"(N) : "memory"); }
DI void bar() { asm volatile("s_waitcnt lgkmcnt(0)\n\ts_barrier" ::: "memory"); }
#define PIN(x) asm volatile("" : "+v"(x))
#define FENCE() asm volatile("" ::: "memory")
#define SCHED() __builtin_amdgcn_sched_barrier(0)
#define RFL(x) __builtin_amdgcn_readfirstlane(x)

DI void ld16(unsigned& d, unsigned vo, const void* sb) { asm volatile("global_load_ushort %0, %1, %2" : "=v"(d) : "v"(vo), "s"(sb) : "memory"); }
template <int O> DI void ld32(unsigned& d, unsigned vo, const void* sb) { asm volatile("global_load_dword %0, %1, %2 offset:%3" : "=v"(d) : "v"(vo), "s"(sb), "i"(O) : "memory"); }
DI void ld32c(unsigned& d, unsigned vo, const void* sb) { asm volatile("global_load_dword %0, %1, %2 sc1" : "=v"(d) : "v"(vo), "s"(sb) : "memory"); }
DI void ld64(v2u& d, unsigned vo, const void* sb) { asm volatile("global_load_dwordx2 %0, %1, %2" : "=v"(d) : "v"(vo), "s"(sb) : "memory"); }
DI void ld128(v4u& d, unsigned vo, const void* sb) { asm volatile("global_load_dwordx4 %0, %1, %2" : "=v"(d) : "v"(vo), "s"(sb) : "memory"); }
DI void ld64c(v2u& d, unsigned vo, const void* sb) { asm volatile("global_load_dwordx2 %0, %1, %2 sc1" : "=v"(d) : "v"(vo), "s"(sb) : "memory"); }
template <int O> DI void ld128c(v4u& d, unsigned vo, const void* sb) { asm volatile("global_load_dwordx4 %0, %1, %2 offset:%3 sc1" : "=v"(d) : "v"(vo), "s"(sb), "i"(O) : "memory"); }
DI void st64c(unsigned vo, void* sb, v2u d) { asm volatile("global_store_dwordx2 %0, %1, %2 sc1" ::"v"(vo), "v"(d), "s"(sb) : "memory"); }
template <int O> DI void ld64o(v2u& d, unsigned vo, const void* sb) { asm volatile("global_load_dwordx2 %0, %1, %2 offset:%3" : "=v"(d) : "v"(vo), "s"(sb), "i"(O) : "memory"); }
template <int O> DI void st64o(unsigned vo, void* sb, v2u d) { asm volatile("global_store_dwordx2 %0, %1, %2 offset:%3" ::"v"(vo), "v"(d), "s"(sb), "i"(O) : "memory"); }
template <int O> DI void st64co(unsigned vo, void* sb, v2u d) { asm volatile("global_store_dwordx2 %0, %1, %2 offset:%3 sc1" ::"v"(vo), "v"(d), "s"(sb), "i"(O) : "memory"); }
DI void st128c(unsigned vo, void* sb, v4u d) { asm volatile("global_store_dwordx4 %0, %1, %2 sc1\n\ts_nop 1" ::"v"(vo), "v"(d), "s"(sb) : "memory"); }
DI void st64u(unsigned vo, void* sb, v2u d) { asm volatile("global_store_dwordx2 %0, %1, %2 sc1 ; CNT" ::"v"(vo), "v"(d), "s"(sb) : "memory"); }
DI void st128u(unsigned vo, void* sb, v4u d) { asm volatile("global_store_dwordx4 %0, %1, %2 sc1 ; CNT\n\ts_nop 1" ::"v"(vo), "v"(d), "s"(sb) : "memory"); }
DI void dma16(unsigned vo, const void* sb, unsigned lds) {
  asm volatile("s_mov_b32 m0, %2\n\ts_nop 0\n\tglobal_load_lds_dwordx4 %0, %1" ::"v"(vo), "s"(sb), "s"(lds) : "memory");
}
template <int O> DI void dma16o(unsigned vo, const void* sb) {
  asm volatile("global_load_lds_dwordx4 %0, %1 offset:%2" ::"v"(vo), "s"(sb), "i"(O) : "memory");
}
template <bool HIONLY> DI v2u spin64(v2u cur, unsigned vo, char* ws, unsigned tlo, unsigned thi) {
  if (__builtin_expect(__builtin_amdgcn_ballot_w64(HIONLY ? (cur.y != tlo) : (cur.x != tlo || cur.y != thi)) != 0ull, 0)) {
#pragma nounroll
    for (unsigned spins = 0; spins < SPIN_CAP; ++spins) {
      __builtin_amdgcn_s_sleep(6);
      asm volatile("global_load_dwordx2 %0, %1, %2 sc1\n\ts_waitcnt vmcnt(0)" : "=&v"(cur) : "v"(vo), "s"(ws) : "memory");
      if (__builtin_amdgcn_ballot_w64(HIONLY ? (cur.y != tlo) : (cur.x != tlo || cur.y != thi)) == 0ull) break;
    }
  }
  return cur;
}
DI unsigned lw(LAS char* L, unsigned off) { return *(volatile LAS unsigned*)(L + off); }
DI void lds_wait_at(unsigned a, unsigned target) {
  unsigned v, cnt; u64 t;
  asm volatile("s_mov_b32 %1, %5\n"
               "1:\n\tds_read_b32 %0, %3\n\ts_sub_u32 %1, %1, 1\n\ts_waitcnt lgkmcnt(0)\n\tv_cmp_gt_u32 vcc, %4, %0\n\t"
               "s_cselect_b64 %2, 0, vcc\n\ts_cmp_lg_u64 %2, 0\n\ts_cbranch_scc1 1b"
               : "=&v"(v), "=&s"(cnt), "=&s"(t) : "v"(a), "s"(target), "i"(4 * SPIN_CAP) : "vcc", "scc", "memory");
}
DI void lds_wait_ge(LAS char* L, unsigned off, unsigned target) { lds_wait_at((unsigned)(size_t)L + off, target); }
template <int N> DI void lds_wait_min(LAS char* L, unsigned off, unsigned target, int lane) {
  lds_wait_at((unsigned)(size_t)L + off + (lane & (N - 1)) * 4, target);
}
DI void lds_set(LAS char* L, unsigned off, unsigned v) { FENCE(); *(volatile LAS unsigned*)(L + off) = v; FENCE(); }
DI void lds_arrive(LAS char* L, unsigned off, int) {
  asm volatile("s_mov_b64 exec, 1\n\tds_add_u32 %0, %1\n\ts_mov_b64 exec, -1" ::"v"((unsigned)(size_t)L + off), "v"(1u) : "memory");
}
template <unsigned N = 4> DI void arrive_flag(LAS char* L, unsigned off, int, char* ws, unsigned fo, unsigned tlo, unsigned thi) {
  unsigned old;
  asm volatile("s_mov_b64 exec, 1\n\tds_add_rtn_u32 %0, %1, %2\n\ts_waitcnt lgkmcnt(0)\n\tv_cmp_eq_u32 vcc, %6, %0\n\t"
               "s_mov_b64 exec, vcc\n\tglobal_store_dwordx2 %3, %4, %5 sc1\n\ts_mov_b64 exec, -1"
               : "=&v"(old) : "v"((unsigned)(size_t)L + off), "v"(1u), "v"(fo), "v"(v2u{tlo, thi}), "s"(ws), "i"(N - 1) : "vcc", "memory");
}
DI void arrive_flag8(LAS char* L, unsigned off, char* ws, unsigned fo, unsigned tlo, unsigned thi) {
  unsigned old;
  asm volatile("s_mov_b64 exec, 1\n\tds_add_rtn_u32 %0, %1, %2\n\ts_waitcnt lgkmcnt(0)\n\tv_cmp_eq_u32 vcc, 3, %0\n\t"
               "s_mul_i32 exec_lo, vcc_lo, 0xff\n\tglobal_store_dwordx2 %3, %4, %5 sc1\n\ts_mov_b64 exec, -1"
               : "=&v"(old) : "v"((unsigned)(size_t)L + off), "v"(1u), "v"(fo), "v"(v2u{tlo, thi}), "s"(ws) : "vcc", "memory");
}
typedef float f32x16 __attribute__((ext_vector_type(16)));
DI f32x16 mfma32(v4u a, v4u b, f32x16 c) {
  return __builtin_amdgcn_mfma_f32_32x32x16_bf16(__builtin_bit_cast(bf16x8, a), __builtin_bit_cast(bf16x8, b), c, 0, 0, 0);
}
template <int C> DI float dpp_add(float v) {
  return v + __builtin_bit_cast(float, __builtin_amdgcn_update_dpp(0, __builtin_bit_cast(int, v), C, 0xf, 0xf, true));
}
DI float rl(float v, int l) { return __builtin_bit_cast(float, __builtin_amdgcn_readlane(__builtin_bit_cast(int, v), l)); }
DI float half_sum(float v, bool upper) {
  v = dpp_add<0xB1>(v); v = dpp_add<0x4E>(v); v = dpp_add<0x141>(v); v = dpp_add<0x140>(v);
  const float a = rl(v, 0) + rl(v, 16), c = rl(v, 32) + rl(v, 48);
  return upper ? c : a;
}
DI f32x2 unpk(unsigned v) { return f32x2{bf2f(v), bfhi(v)}; }
struct c2 { f32x2 r, i; };
DI c2 cmul(c2 a, c2 b) { return c2{__builtin_elementwise_fma(a.r, b.r, -(a.i * b.i)), __builtin_elementwise_fma(a.r, b.i, a.i * b.r)}; }
DI c2 csq(c2 v) { return c2{__builtin_elementwise_fma(v.r, v.r, -(v.i * v.i)), (v.r + v.r) * v.i}; }
DI c2 cexpk(f32x2 e1, f32x2 th, f32x2 tl, float k, float ks) {
  const f32x2 e = e1 * k, t = th * ks;
  const f32x2 mag = f32x2{__builtin_amdgcn_exp2f(e.x), __builtin_amdgcn_exp2f(e.y)};
  const f32x2 fr = __builtin_elementwise_fma(tl, f32x2{ks, ks}, f32x2{__builtin_amdgcn_fractf(t.x), __builtin_amdgcn_fractf(t.y)});
  return c2{f32x2{__builtin_amdgcn_cosf(fr.x), __builtin_amdgcn_cosf(fr.y)} * mag, f32x2{__builtin_amdgcn_sinf(fr.x), __builtin_amdgcn_sinf(fr.y)} * mag};
}
typedef _Float16 f16x8 __attribute__((ext_vector_type(8)));
typedef _Float16 f16x2 __attribute__((ext_vector_type(2)));
DI f32x4 mfmah(v4u a, v4u b, f32x4 c) {
  return __builtin_amdgcn_mfma_f32_16x16x32_f16(__builtin_bit_cast(f16x8, a), __builtin_bit_cast(f16x8, b), c, 0, 0, 0);
}
DI unsigned pkh1(float a, float b) { f16x2 r; r.x = (_Float16)a; r.y = (_Float16)b; return __builtin_bit_cast(unsigned, r); }
DI v2u pkh(c2 v) { return v2u{pkh1(v.r.x, v.i.x), pkh1(v.r.y, v.i.y)}; }
DI v2u pkb(c2 v) { return v2u{pk2(v.r.x, v.i.x), pk2(v.r.y, v.i.y)}; }
#define HMUL(o, i, w) "v_pk_mul_f16 " o ", " i ", " w " op_sel:[1,1] op_sel_hi:[1,0] neg_lo:[0,1]\n\t"
#define HFMA(o, i, w) "v_pk_fma_f16 " o ", " i ", " w ", " o " op_sel_hi:[0,1,1]\n\t"
constexpr int KROW = 528;
template <int R0, int R1> DI void h2(v2u& o0, v2u& o1, v2u i0, v2u i1, v2u w0, v2u w1) {
  unsigned a, b, c, d;
  asm volatile(HMUL("%0", "%4", "%8") HMUL("%1", "%5", "%9") HMUL("%2", "%6", "%10") HMUL("%3", "%7", "%11")
               HFMA("%0", "%4", "%8") HFMA("%1", "%5", "%9") HFMA("%2", "%6", "%10") HFMA("%3", "%7", "%11")
               "ds_write_addtid_b32 %0 offset:%12\n\tds_write_addtid_b32 %1 offset:%13\n\tds_write_addtid_b32 %2 offset:%14\n\tds_write_addtid_b32 %3 offset:%15"
               : "=&v"(a), "=&v"(b), "=&v"(c), "=&v"(d)
               : "v"(i0.x), "v"(i0.y), "v"(i1.x), "v"(i1.y), "v"(w0.x), "v"(w0.y), "v"(w1.x), "v"(w1.y),
                 "i"(R0 * KROW), "i"(R0 * KROW + 256), "i"(R1 * KROW), "i"(R1 * KROW + 256) : "memory");
  o0 = v2u{a, b}; o1 = v2u{c, d};
}
template <int R> DI void hput(v2u v) {
  asm volatile("ds_write_addtid_b32 %0 offset:%2\n\tds_write_addtid_b32 %1 offset:%3" ::"v"(v.x), "v"(v.y), "i"(R * KROW), "i"(R * KROW + 256) : "memory");
}
template <int C> DI unsigned dpp_maxu(unsigned v) { return max(v, (unsigned)__builtin_amdgcn_update_dpp(0, (int)v, C, 0xf, 0xf, true)); }
DI void rd8(v4u (&f)[8], LAS char* sr) {
#pragma unroll
  for (int k = 0; k < 8; ++k) f[k] = *(LAS v4u*)(sr + 64 * k);
}
template <bool H> DI void kmm(f32x4 (&ka)[2][2][2], int nh, const v4u (&UA)[2][8], const v4u (&vb)[8]) {
#pragma unroll
  for (int k = 0; k < 8; ++k)
#pragma unroll
    for (int m = 0; m < 2; ++m) {
      f32x4& a = ka[(k >> 1) & 1][m][nh];
      const f32x4 c = (k == 0 || k == 2) ? f32x4{0.f, 0.f, 0.f, 0.f} : a;
      a = H ? mfmah(UA[m][k], vb[k], c) : mfma(UA[m][k], vb[k], c);
    }
}
DI void kgen_slow(c2 a, c2 W1, c2 W256, c2 W8, LAS char* sr, f32x4 (&ka)[2][2][2]) {
  v4u UA[2][8], vb[8];
  c2 u[8]; u[0] = a;
#pragma unroll
  for (int i = 1; i < 8; ++i) u[i] = cmul(u[i - 1], W1);
  const c2 W512 = csq(W256), W1024 = csq(W512), r8 = cmul(a, W256), r10 = cmul(u[4], W256), r12 = cmul(a, W512), r14 = cmul(r8, W512), r16 = cmul(a, W1024);
  hput<0>(pkb(u[0])); hput<1>(pkb(u[1])); hput<2>(pkb(u[2])); hput<3>(pkb(u[3])); hput<4>(pkb(u[4])); hput<5>(pkb(u[5])); hput<6>(pkb(u[6])); hput<7>(pkb(u[7]));
  hput<8>(pkb(r8)); hput<9>(pkb(cmul(u[2], W256))); hput<10>(pkb(r10)); hput<11>(pkb(cmul(u[6], W256)));
  hput<12>(pkb(r12)); hput<13>(pkb(cmul(u[4], W512))); hput<14>(pkb(r14)); hput<15>(pkb(cmul(r10, W512)));
  rd8(UA[0], sr);
  hput<0>(pkb(r16)); hput<1>(pkb(cmul(r8, W1024))); hput<2>(pkb(cmul(r12, W1024))); hput<3>(pkb(cmul(r14, W1024))); hput<4>(pkb(cmul(r16, W1024)));
  rd8(UA[1], sr);
  c2 v = c2{f32x2{1.f, 1.f}, f32x2{0.f, 0.f}};
#pragma unroll
  for (int nh = 0; nh < 2; ++nh) {
#define VR(r) hput<r>(pkb(v)); v = cmul(v, W8);
    VR(0) VR(1) VR(2) VR(3) VR(4) VR(5) VR(6) VR(7) VR(8) VR(9) VR(10) VR(11) VR(12) VR(13) VR(14) VR(15)
#undef VR
    rd8(vb, sr);
    kmm<false>(ka, nh, UA, vb);
  }
}
DI void kgen(const unsigned (&pr)[7], bool dead, bool upper, unsigned sw, LAS char* sr, f32x4 (&ka)[2][2][2], float (&scale)[2]) {
  const float dt = rbf(__builtin_amdgcn_exp2f(bf2f(pr[6]) * 1.44269504088896341f));
  const f32x2 zr = unpk(pr[0]) * dt, zi = unpk(pr[1]) * dt, dbr = unpk(pr[2]) * dt, dbi = unpk(pr[3]) * dt;
  const f32x2 cr = unpk(dead ? 0u : pr[4]), ci = unpk(dead ? 0u : pr[5]);
  const c2 a = c2{__builtin_elementwise_fma(cr, dbr, -(ci * dbi)), -__builtin_elementwise_fma(cr, dbi, ci * dbr)};
  const f32x2 e1 = zr * 1.44269504088896341f, th = zi * 0.15915494309189535f;
  const f32x2 tl = __builtin_elementwise_fma(zi, f32x2{6.4206383e-9f, 6.4206383e-9f}, __builtin_elementwise_fma(zi, f32x2{0.15915494309189535f, 0.15915494309189535f}, -th));
  const c2 W1 = cexpk(e1, th, tl, 1.f, -1.f), W256 = cexpk(e1, th, tl, 256.f, -256.f), W8 = cexpk(e1, th, tl, 8.f, 8.f);
  asm volatile("s_mov_b32 m0, %0\n\ts_nop 0" ::"s"(sw) : "memory");
  if (__builtin_expect(__builtin_amdgcn_ballot_w64(!(zr.x <= 0.f && zr.y <= 0.f)) != 0ull, 0)) {
    kgen_slow(a, W1, W256, W8, sr, ka);
    scale[0] = scale[1] = 1.f;
    return;
  }
  unsigned am = max(max(__builtin_bit_cast(unsigned, a.r.x) & 0x7fffffffu, __builtin_bit_cast(unsigned, a.r.y) & 0x7fffffffu),
           max(__builtin_bit_cast(unsigned, a.i.x) & 0x7fffffffu, __builtin_bit_cast(unsigned, a.i.y) & 0x7fffffffu));
  am = dpp_maxu<0xB1>(am); am = dpp_maxu<0x4E>(am); am = dpp_maxu<0x141>(am); am = dpp_maxu<0x140>(am);
  const unsigned ex0 = min(max(max((unsigned)__builtin_amdgcn_readlane(am, 0), (unsigned)__builtin_amdgcn_readlane(am, 16)) >> 23, 2u), 252u);
  const unsigned ex1 = min(max(max((unsigned)__builtin_amdgcn_readlane(am, 32), (unsigned)__builtin_amdgcn_readlane(am, 48)) >> 23, 2u), 252u);
  scale[0] = __builtin_bit_cast(float, ex0 << 23); scale[1] = __builtin_bit_cast(float, ex1 << 23);
  const float down = __builtin_bit_cast(float, ((254u - (upper ? ex1 : ex0)) << 23));
  const c2 W2 = csq(W1), W512 = csq(W256), W16 = csq(W8), W4 = csq(W2), W1024 = csq(W512), W32 = csq(W16), W64 = csq(W32), W128 = csq(W64);
  const v2u u0 = pkh(c2{a.r * down, a.i * down}), h1 = pkh(W1), h2_ = pkh(W2), h4 = pkh(W4), h256 = pkh(W256), h512 = pkh(W512), h1024 = pkh(W1024);
  v2u u2, u4, u6, r8, r10, r12, r14, r16, t0, t1;
  v4u UA[2][8], vb[8];
  hput<0>(u0);
  h2<2, 4>(u2, u4, u0, u0, h2_, h4); h2<8, 12>(r8, r12, u0, u0, h256, h512); h2<1, 3>(t0, t1, u0, u2, h1, h1); h2<5, 6>(t0, u6, u4, u4, h1, h2_);
  h2<10, 14>(r10, r14, r8, r8, h4, h512); h2<7, 9>(t0, t1, u6, r8, h1, h2_); h2<11, 13>(t0, t1, r10, r12, h2_, h4);
  h2<15, 15>(r16, t1, u0, r14, h1024, h4);
  rd8(UA[0], sr);
  hput<0>(r16);
  h2<1, 2>(t0, t1, r8, r12, h1024, h1024); h2<3, 4>(t0, t1, r14, r16, h1024, h1024);
  rd8(UA[1], sr);
  v2u v[16];
  v[1] = pkh(W8); v[2] = pkh(W16); v[4] = pkh(W32); v[8] = pkh(W64);
  const v2u h128 = pkh(W128);
  hput<0>(v2u{0x3c00u, 0x3c00u}); hput<1>(v[1]); hput<2>(v[2]); hput<4>(v[4]); hput<8>(v[8]);
  h2<3, 5>(v[3], v[5], v[2], v[4], v[1], v[1]); h2<6, 9>(v[6], v[9], v[4], v[8], v[2], v[1]); h2<7, 10>(v[7], v[10], v[4], v[8], v[3], v[2]);
  h2<11, 12>(v[11], v[12], v[8], v[8], v[3], v[4]); h2<13, 14>(v[13], v[14], v[8], v[8], v[5], v[6]); h2<15, 15>(t0, v[15], v[1], v[8], h128, v[7]);
  rd8(vb, sr);
  hput<0>(h128); hput<1>(t0);
  h2<2, 3>(t0, t1, v[2], v[3], h128, h128); h2<4, 5>(t0, t1, v[4], v[5], h128, h128);
  kmm<true>(ka, 0, UA, vb);
  h2<6, 7>(t0, t1, v[6], v[7], h128, h128); h2<8, 9>(t0, t1, v[8], v[9], h128, h128); h2<10, 11>(t0, t1, v[10], v[11], h128, h128);
  h2<12, 13>(t0, t1, v[12], v[13], h128, h128); h2<14, 15>(t0, t1, v[14], v[15], h128, h128);
  rd8(vb, sr);
  SCHED();
  kmm<true>(ka, 1, UA, vb);
}
DI void kgen_store(LAS float* pt, const f32x4 (&kacc)[2][2], int lane) {
  const int l15 = lane & 15, g = lane >> 4;
  const int sh = g < 2 ? 3 : 4 - g;
  const int a0 = (l15 << sh) + (g < 2 ? 4 * g : 128 * g);
  const int a1 = g < 3 ? a0 + 2 : a0 + 64;
#pragma unroll
  for (int nh = 0; nh < 2; ++nh) {
    *(LAS f32x2*)(pt + a0 + ((16 * nh) << sh)) = kacc[0][nh].xy;
    *(LAS f32x2*)(pt + a1 + ((16 * nh) << sh)) = kacc[0][nh].zw;
  }
  if (g == 0) {
#pragma unroll
    for (int nh = 0; nh < 2; ++nh)
#pragma unroll
      for (int r = 0; r < 4; ++r) pt[512 + 32 * r + 16 * nh + l15] = kacc[1][nh][r];
  }
  if (lane == 16) { *(LAS f32x4*)(pt + 640) = f32x4{kacc[1][0][0], 0.f, 0.f, 0.f}; }
}

template <int PAR> DI void conv_unit_shared(LAS char* ka, LAS char* ub, f32x4 (&acc)[4]) {
  constexpr int T[4] = {PAR ? 1 : 0, PAR ? 2 : 3, PAR ? 5 : 4, PAR ? 6 : 7};
  v4u A[8], B[8];
#pragma unroll
  for (int i = 0; i <= T[3]; ++i) { A[i] = *(LAS v4u*)(ka + 512 * (7 - i)); B[i] = *(LAS v4u*)(ub + 512 * i); }
  SCHED();
#pragma unroll
  for (int q = 0; q < 4; ++q)
#pragma unroll
    for (int ip = 0; ip <= T[q]; ++ip) acc[q] = mfma(A[T[q] - ip], B[ip], acc[q]);
  SCHED();
}
DI float fadd(float a, float b) { float r; asm("v_add_f32 %0, %1, %2" : "=v"(r) : "v"(a), "v"(b)); return r; }
DI float ffma(float a, float b, float c) { float r; asm("v_fma_f32 %0, %1, %2, %3" : "=v"(r) : "v"(a), "v"(b), "v"(c)); return r; }
DI f32x4 add4(f32x4 a, f32x4 b) { return f32x4{fadd(a.x, b.x), fadd(a.y, b.y), fadd(a.z, b.z), fadd(a.w, b.w)}; }
DI f32x4 fma4(f32x4 a, float b, f32x4 c) { return f32x4{ffma(a.x, b, c.x), ffma(a.y, b, c.y), ffma(a.z, b, c.z), ffma(a.w, b, c.w)}; }
DI int rot12(int v) { return v >= 12 ? v - 12 : v; }
DI void w_loads(v4u (&WF)[5], const char* p, unsigned vo) {
  ld128c<0>(WF[0], vo, p); ld128c<1024>(WF[1], vo, p); ld128c<2048>(WF[2], vo, p); ld128c<3072>(WF[3], vo, p); ld128c<0>(WF[4], vo, p + 4096);
}
DI void a_loads(v4u (&AF)[4], const char* p, const unsigned (&vm)[4]) {
#pragma unroll
  for (int m = 0; m < 4; ++m) ld128c<0>(AF[m], vm[m], p);
}
template <int LIVE, class F> DI void g2_step(f32x4 (&acc)[4][5], v4u (&WF)[5], v4u (&AF)[4], F ld) {
#pragma unroll
  for (int m = 0; m < 4; ++m) PIN(AF[m]);
#pragma unroll
  for (int n = 0; n < 5; ++n) PIN(WF[n]);
#pragma unroll
  for (int e = 0; e < 20; ++e) {
    const int m = e / 5, n = e % 5;
    acc[m][n] = mfma(WF[n], AF[m], m >= LIVE ? f32x4{0.f, 0.f, 0.f, 0.f} : acc[m][n]);
    if (e & 1) { SCHED(); ld(e >> 1); SCHED(); }
  }
}
DI void make_kix(unsigned (&kix)[9], int ts) {
#pragma unroll
  for (int i = 0; i < 9; ++i) {
    const int lg = 2047 - 8 * ts - i;
    const int bits = (int)(pk2(__builtin_fmaxf((float)lg, 128.f), 0.f));
    const int ci = lg < bits - 0x4280 ? lg : bits - 0x4280;
    kix[i] = KTAB + 4 * ci;
    PIN(kix[i]);
  }
}
DI void put_k(LAS char* L, const unsigned (&kix)[9], int ts, int c, unsigned reg) {
  float kv[9];
#pragma unroll
  for (int i = 0; i < 9; ++i) kv[i] = *(LAS float*)(L + kix[i] + c * KP_STRIDE);
  LAS char* q = L + reg + CV_KOFF + ts * 16;
  *(LAS v4u*)(q + CV_KSTR) = v4u{pk2(kv[0], kv[1]), pk2(kv[2], kv[3]), pk2(kv[4], kv[5]), pk2(kv[6], kv[7])};
  *(LAS v4u*)q = v4u{pk2(kv[1], kv[2]), pk2(kv[3], kv[4]), pk2(kv[5], kv[6]), pk2(kv[7], kv[8])};
}
DI void put_u(LAS char* q, v4u u, bool first) {
  *(LAS v4u*)(q + 3 * CV_USTR + 384) = u;
  *(LAS v2u*)(q + 1 * CV_USTR + 392) = v2u{u.x, u.y}; *(LAS v2u*)(q + 1 * CV_USTR + 400) = v2u{u.z, u.w};
  *(LAS unsigned*)(q + 2 * CV_USTR + 388) = u.x; *(LAS v2u*)(q + 2 * CV_USTR + 392) = v2u{u.y, u.z}; *(LAS unsigned*)(q + 2 * CV_USTR + 400) = u.w;
  *(LAS unsigned*)(q + 396) = u.x; *(LAS v2u*)(q + 400) = v2u{u.y, u.z}; *(LAS unsigned*)(q + 408) = u.w;
  if (first) {
    *(LAS v2u*)(q + 384) = v2u{0u, 0u}; *(LAS unsigned*)(q + 392) = 0u;
    *(LAS v2u*)(q + CV_USTR + 384) = v2u{0u, 0u}; *(LAS unsigned*)(q + 2 * CV_USTR + 384) = 0u;
  }
}

__global__ __launch_bounds__(512, 2) void fused_s4_mlp(const char* __restrict__ x, const char* __restrict__ pAre, const char* __restrict__ pAim,
    const char* __restrict__ pBre, const char* __restrict__ pBim, const char* __restrict__ pCre, const char* __restrict__ pCim,
    const char* __restrict__ plogdt, const char* __restrict__ pDskip, const char* __restrict__ w1, const char* __restrict__ b1,
    const char* __restrict__ w2, char* __restrict__ out, char* __restrict__ ws) {
  __shared__ __attribute__((aligned(16))) char smem[163840];
  LAS char* L = (LAS char*)smem;
  const unsigned lds0 = (unsigned)(size_t)L;
  const char* pc0; unsigned codelen;
  asm volatile("s_getpc_b64 %0\n98:\n\ts_mov_b32 %1, 97f-98b\n\ts_max_u32 %1, %1, 99f-98b" : "=s"(pc0), "=&s"(codelen) :: "scc");
  const int b = blockIdx.x, lane = threadIdx.x & 63;
  const int w = RFL(threadIdx.x >> 6), wi = w & 3;
  {
    const unsigned co = (unsigned)(4 * (16 * w + (lane & 15)) + ((b >> 3) & 3)) * 128;
    asm volatile("s_mov_b32 m0, %2\n\ts_nop 0\n\tglobal_load_lds_dword %0, %1" ::"v"((co < codelen ? co : codelen) & ~3u), "s"(pc0), "s"(lds0 + YST + w * 256) : "memory");
  }
  const int xcd = b & 7, j = b >> 3, rg = xcd >> 1, chf = xcd & 1;
  const int ct1 = 8 * chf + (j & 7), rb1 = 4 * rg + (j >> 3);
  const int wid = rb1 * 16 + ct1;
  const int a2 = 4 * chf + (j >> 3), cb2 = j & 7;
  const int l15 = lane & 15, g = lane >> 4, qq = (lane >> 2) & 3, p4 = lane & 3, l31 = lane & 31, h = lane >> 5;
  const int par = j & 1, cbase = 80 * xcd + 5 * (j >> 1);
  const int chA = cbase + 3 * par, chS = cbase + 2;
  const int ts = 64 * wi + lane;
  ST(0);
  unsigned pr[7]; v4u xa, xb;
  {
    const bool odd = wi & 1, shl = odd && h, mw = w >= 4;
    const int kch = chA + (wi >> 1);
    unsigned p0 = (kch * 192 + (wi & 1) * 128 + 2 * lane) * 2, p1 = (chS * 192 + par * 96 + (wi >> 1) * 48 + 2 * min(l31, 23)) * 2, x0 = h * 1280 + l31 * 8;
    PIN(p0); PIN(p1); PIN(x0);
    const unsigned po = shl ? p1 : p0, mm = mw ? ~0u : 0u;
    const unsigned xo = (x0 + l31 * 8) & ~mm, vo = (po & mm) | ((x0 + 1024) & ~mm), v1 = po & mm;
    asm volatile("" ::"s"(x), "s"(pAre), "s"(pAim), "s"(pBre), "s"(pBim), "s"(pCre), "s"(pCim), "s"(plogdt), "s"(ws));
    const char* xrow = x + (size_t)(8 * wid + 2 * wi) * 1280;
    ld16(pr[6], (unsigned)(shl ? chS : kch) * 2, plogdt);
    ld32<0>(pr[0], vo, mw ? pAre : xrow); ld32<0>(pr[1], vo, mw ? pAim : xrow + 4);
    ld32c(pr[2], v1, mw ? pBre : ws); ld32c(pr[3], v1, mw ? pBim : ws + 4);
    ld32<0>(pr[4], po, pCre); ld32<0>(pr[5], po, pCim);
    ld128(xa, xo, xrow); ld128(xb, xo, xrow + 512);
  }
  const int row2 = 512 * rg + 64 * a2 + 16 * wi;
  const char* w2b = ws + WS_W2T + (size_t)(cb2 * 48 + 12 * wi) * 5120;
  const char* actb = ws + WS_ACT + (size_t)((32 * rg + 4 * a2) * 48 + 12 * wi) * 1024;
  const int r2 = ((a2 ^ (w >> 2)) & 1) * 6;
#define WL(s) w_loads(WF[s], w2b + rot12(r2 + (s)) * 5120, vo)
#define AL(s) a_loads(AF[s], actb + rot12(r2 + (s)) * 1024, vm)
#define AL1(s, m) ld128c<0>(AF[s][m], vm[m], actb + rot12(r2 + (s)) * 1024)
#define WL1(s, n) ld128c<0>(WF[s][n], vo, w2b + rot12(r2 + (s)) * 5120 + (n) * 1024)
#define NOLD [&](int) {}
#define LATE_LANE() int ln_ = lane; PIN(ln_); const int c15 = ln_ & 15, cg = ln_ >> 4
#define KAO (CV_KOFF + (c15 & 1) * CV_KSTR + 2 * (2072 - 8 * (c15 >> 1) + 8 * cg))
#define UBO ((c15 & 3) * CV_USTR + 2 * (64 * (c15 >> 2) + 8 * cg + 224))
#define YOFF ((unsigned)((c15 >> 2) * 128 + cg * 32 + (c15 & 3) * 8))
#define OROW ((unsigned)(2 * (c15 >> 2) + (c15 & 1) + 8 * ((c15 >> 1) & 1)))
#define ZOFF (lane < 24 ? wi * CV_USTR + lane * 16 : CV_KOFF + wi * CV_KSTR + 4096 + (lane - 24) * 16)
#define ZCOND (lane < 24 || (lane < 31 && wi < 2))

  unsigned voA[4], voB[3];
#pragma unroll
  for (int i = 0; i < 4; ++i) voA[i] = (8 * i + (lane >> 3)) * 1280 + (((lane & 7) ^ (4 * (i & 1) + (lane >> 4))) * 16) - 1024 * i;
#pragma unroll
  for (int i = 0; i < 3; ++i) { const int c = 64 * i + lane; voB[i] = (c / 12) * 3072 + (c % 12) * 16 - 1024 * i; }
  const char* g1a = x + (size_t)(rb1 * 128 + 32 * wi) * 1280;
  const char* g1b = w1 + ct1 * 192 + wi * 49152;
  const unsigned la = lds0 + G1_RING + wi * 4096, lb = lds0 + G1_RING + G1_BOFF + wi * 3072;
  const int r1 = ((j & 7) + (5 * (j >> 3)) / 2 + 5 * chf + rg) % 10;
  auto slab = [&](int s) { const int v = s + r1; return v >= 10 ? v - 10 : v; };
  auto issue = [&](int s) {
    const char* pa = g1a + slab(s) * 128; const char* pb = g1b + (size_t)slab(s) * 64 * 3072;
    dma16(voA[0], pa, la + s * G1_STAGE); dma16o<1024>(voA[1], pa); dma16o<2048>(voA[2], pa); dma16o<3072>(voA[3], pa);
    dma16(voB[0], pb, lb + s * G1_STAGE); dma16o<1024>(voB[1], pb); dma16o<2048>(voB[2], pb);
  };
  unsigned tlo = 0, thi = 0;
  unsigned kix[9];
  const int pu = b % 240, pks = pu / 5, pcg = pu % 5;
  if (w >= 4) {
    bar();
    make_kix(kix, ts);
    waitv<0>();
  } else {
    if (w == 0) *(LAS unsigned*)(L + lane * 4) = 0u;
    bar();
#pragma unroll
    for (int i = 0; i < 2; ++i)
      dma16((unsigned)((lane >> 1) * 1280 + (lane & 1) * 16), w2 + (size_t)pks * 32 * 1280 + (128 * pcg + 32 * wi + 16 * i) * 2, lds0 + W2P + wi * 2048 + i * 1024);
    issue(0); issue(1);
#pragma unroll
    for (int i = 0; i < 9; ++i) kix[i] = 0;
    waitv<14>();
  }
  asm volatile("; AUDITCLR");
  PIN(xa); PIN(xb);
#pragma unroll
  for (int i = 0; i < 7; ++i) PIN(pr[i]);
  ST(1);
  int role0 = w; asm volatile("" : "+s"(role0));
  if (role0 >= 4) {
    __builtin_amdgcn_s_setprio(KPRIO);
    const bool odd = wi & 1;
    f32x4 ka[2][2][2]; float scale[2];
    kgen(pr, odd && lane >= 56, h, lds0 + KG_SCR + wi * 2 * KG_WSTR, L + KG_SCR + wi * 2 * KG_WSTR + l15 * KROW + g * 16, ka, scale);
#pragma unroll
    for (int i = 0; i < 4; ++i) {
      ka[1][i >> 1][i & 1] *= scale[1];
      ka[0][i >> 1][i & 1] = ka[0][i >> 1][i & 1] * scale[0] + (odd ? f32x4{0.f, 0.f, 0.f, 0.f} : ka[1][i >> 1][i & 1]);
    }
    kgen_store((LAS float*)(L + KG_SCR + wi * 2 * KG_WSTR), ka[0], lane);
    if (odd) kgen_store((LAS float*)(L + KG_SCR + wi * 2 * KG_WSTR + KG_WSTR), ka[1], lane);
    lds_arrive(L, W_KG, lane);
  } else {
    const v2u xc = v2u{pr[0], pr[1]}, E = v2u{pr[2], pr[3]};
    ST(6);
  const u64 tag64 = (((u64)RFL(E.y) << 32) | (unsigned)RFL(E.x)) + 1ull;
  tlo = (unsigned)tag64; thi = (unsigned)(tag64 >> 32);
  if (wi == 0) { lds_set(L, W_TAG, tlo); lds_set(L, W_TAG + 4, thi); lds_set(L, W_TAG + 8, 1); }
  {
    f32x2 v[10] = {unpk(xa.x), unpk(xa.y), unpk(xa.z), unpk(xa.w), unpk(xb.x), unpk(xb.y), unpk(xb.z), unpk(xb.w), unpk(xc.x), unpk(xc.y)};
    f32x2 s2 = ((v[0] + v[1]) + (v[2] + v[3])) + ((v[4] + v[5]) + (v[6] + v[7])) + (v[8] + v[9]);
    const float mean = half_sum(s2.x + s2.y, h) * (1.0f / 640.0f);
    f32x2 q2 = f32x2{0.f, 0.f};
#pragma unroll
    for (int i = 0; i < 10; ++i) { const f32x2 d = v[i] - mean; q2 = __builtin_elementwise_fma(d, d, q2); }
    const float var = rbf(half_sum(q2.x + q2.y, h) * (1.0f / 640.0f));
    const float m = rbf(mean), sd = rbf(__builtin_amdgcn_sqrtf(rbf(var + 1e-5f))), rs = __builtin_amdgcn_rcpf(sd);
    unsigned o[10];
#pragma unroll
    for (int i = 0; i < 10; ++i) { const f32x2 t = (v[i] - m) * rs; o[i] = pk2(t.x, t.y); }
    LAS char* rp = L + LN_ROWS + (2 * wi + h) * LN_PITCH;
    *(LAS v4u*)(rp + l31 * 16) = v4u{o[0], o[1], o[2], o[3]};
    *(LAS v4u*)(rp + 512 + l31 * 16) = v4u{o[4], o[5], o[6], o[7]};
    *(LAS v2u*)(rp + 1024 + l31 * 8) = v2u{o[8], o[9]};
    waitv<0>();
    lds_arrive(L, W_ST, lane);
    issue(2);
    if (l31 == 0) st64c(WS_STATS + (unsigned)(8 * wid + 2 * wi + h) * 8, ws,
                        v2u{(__builtin_bit_cast(unsigned, m) & 0xffff0000u) | (__builtin_bit_cast(unsigned, sd) >> 16), tlo});
  }
#pragma unroll
  for (int i = 0; i < 2; ++i) {
    LAS char* sp = L + W2P + wi * 2048 + i * 1024 + (16 * (g >> 1) + 4 * (g & 1) + qq) * 32 + 8 * p4;
    const v2u lo = trread(sp), hi = trread(sp + 256);
    const int ntg = 8 * pcg + 2 * wi + i;
    st128c((unsigned)lane * 16, ws + WS_W2T + (size_t)(((ntg / 5) * 48 + pks) * 5 + ntg % 5) * 1024, v4u{lo.x, lo.y, hi.x, hi.y});
  }
  lds_wait_ge(L, W_ST, 4);
  {
    LAS char* sp = L + LN_ROWS + qq * LN_PITCH + (160 * wi + 16 * g) * 2 + 8 * p4;
#pragma unroll
    for (int i = 0; i < 3; ++i) {
      const v2u lo = trread(sp + 128 * i), hi = trread(sp + 128 * i + 4 * LN_PITCH);
      const unsigned ch = 64 * i + lane;
      if (i < 2 || lane < 32) st128c((ch / 5) * 20480 + (ch % 5) * 16, ws + WS_XNB + (size_t)wi * 32 * 20480 + wid * 80, v4u{lo.x, lo.y, hi.x, hi.y});
    }
  }
  }
  asm volatile("; AUDITCLR");
  ST(2);
  int role = w; asm volatile("" : "+s"(role));
  if (role < 4) {
    int lane = __builtin_amdgcn_mbcnt_hi(~0u, __builtin_amdgcn_mbcnt_lo(~0u, 0u)); PIN(lane);
    const int l15 = lane & 15, g = lane >> 4, qq = (lane >> 2) & 3, p4 = lane & 3, l31 = lane & 31, h = lane >> 5;
    const unsigned vo = lane * 16;
    f32x16 acc1[3]; f32x4 csa[3];
#pragma unroll
    for (int n = 0; n < 3; ++n) {
      csa[n] = f32x4{0.f, 0.f, 0.f, 0.f};
#pragma unroll
      for (int i = 0; i < 16; ++i) acc1[n][i] = 0.f;
    }
    const int xrow = (l31 & 17) + 2 * (l15 >> 2) + 8 * ((l15 >> 1) & 1);
    unsigned xo[4], wo[4];
#pragma unroll
    for (int k = 0; k < 4; ++k) {
      const int kr = (k + wi) & 3;
      xo[k] = G1_RING + (32 * wi + xrow) * 128 + (((2 * kr + h) ^ ((xrow >> 1) & 7)) * 16);
      wo[k] = G1_RING + G1_BOFF + (16 * kr + 8 * h + qq) * 192 + 32 * (g & 1) + 8 * p4;
      PIN(xo[k]); PIN(wo[k]);
    }
    const unsigned sel1 = ((l15 ^ g) & 1) ? 0u : 0x3f803f80u;
    const v4u sel = v4u{sel1, sel1, sel1, sel1};
    v4u X[4], W[4][3];
    auto rdw = [&](int s, int k, int n) {
      LAS char* p = L + wo[k] + (s % 3) * G1_STAGE + 64 * n; const v2u lo = trread(p), hi = trread(p + 768); W[k][n] = v4u{lo.x, lo.y, hi.x, hi.y};
    };
    auto rdx = [&](int s, int k) { X[k] = *(LAS v4u*)(L + xo[k] + (s % 3) * G1_STAGE); };
#define MM(k, n) acc1[n] = mfma32(W[k][n], X[k], acc1[n])
#define CS(n) csa[n] = mfma(W[0][n], sel, csa[n])
    v2u gran; unsigned bb[3];
    v4u WF[6][5], AF[6][4];
    const unsigned go = WS_STATS + (unsigned)(rb1 * 128 + 32 * wi + xrow) * 8;
    auto wl1 = [&](int s, int e0) { const int e = 7 * (s - 7) + e0; if (s >= 7 && e0 < 7 && e < 20) ld128c<0>(WF[e / 5][e % 5], vo, w2b + rot12(r2 + e / 5) * 5120 + (e % 5) * 1024); };
    ST(3);
    rdx(0, 0); rdw(0, 0, 0); rdw(0, 0, 1); rdw(0, 0, 2); rdx(0, 1); rdw(0, 1, 0); rdw(0, 1, 1); rdw(0, 1, 2);
#pragma unroll
    for (int s = 0; s < 10; ++s) {
      const bool refill = s < 7;
      const char* pa = g1a + slab(s + 3) * 128; const char* pb = g1b + (size_t)slab(s + 3) * 64 * 3072;
      SCHED();
      MM(0, 0); rdx(s, 2); rdw(s, 2, 0); SCHED();
      MM(0, 1); rdw(s, 2, 1); rdw(s, 2, 2); SCHED();
      MM(0, 2); rdx(s, 3); rdw(s, 3, 0); SCHED();
      CS(0); rdw(s, 3, 1); SCHED();
      CS(1); rdw(s, 3, 2); SCHED();
      CS(2);
      if (s == 0) { waitv<0>(); arrive_flag8(L, W_F1C, ws, WS_F1 + (unsigned)(lane & 7) * 2048 + wid * 8, tlo, thi); } else if (s == 1) {} else if (s < 7) waitv<7>(); else if (s == 7) waitv<11>(); else if (s == 8) waitv<11>();
      if (s == 9 && l15 < 2) {
#pragma unroll
        for (int n = 0; n < 3; ++n) *(LAS f32x4*)(L + CSP + wi * 384 + (32 * n + 16 * l15 + 4 * g) * 4) = csa[n];
      }
      lds_set(L, W_RDY + wi * 4, s + 2);
      SCHED();
      MM(1, 0);
      if (refill) { asm volatile("s_waitcnt lgkmcnt(7)" ::: "memory"); dma16(voA[0], pa, la + (s % 3) * G1_STAGE); }
      wl1(s, 0);

      SCHED();
      MM(1, 1); if (refill) dma16o<1024>(voA[1], pa); wl1(s, 1);
      unsigned fl = 0;
      if (s < 9) fl = lw(L, W_RDY + p4 * 4);
      SCHED();
      MM(1, 2); if (refill) dma16o<2048>(voA[2], pa); wl1(s, 2); SCHED();
      MM(2, 0); if (refill) dma16o<3072>(voA[3], pa); wl1(s, 3);
      SCHED();
      if (s == 9) {
        MM(2, 1); wl1(s, 4); SCHED();
        lds_wait_min<4>(L, W_RDY, 11, lane);
        float cs[3];
#pragma unroll
        for (int n = 0; n < 3; ++n) { LAS float* p = (LAS float*)(L + CSP) + 32 * n + l31; cs[n] = fadd(fadd(p[0], p[96]), fadd(p[192], p[288])); }
        MM(2, 2); wl1(s, 5); SCHED();
        waitv<20>(); PIN(gran); PIN(bb[0]); PIN(bb[1]); PIN(bb[2]);
        MM(3, 0); MM(3, 1); MM(3, 2);
        const unsigned keep = h ? 0u : ~0u, nm = (gran.x >> 16) ^ 0x8000u;
        const v4u xe = v4u{(nm | (nm << 16)) & keep, (nm | (gran.x << 16)) & keep, 0u, 0u};
#pragma unroll
        for (int n = 0; n < 3; ++n) {
          const float c0 = bfhi(__builtin_bit_cast(unsigned, cs[n])), r0 = cs[n] - c0, c1 = bfhi(__builtin_bit_cast(unsigned, r0)), r1 = r0 - c1;
          const v4u we = v4u{(__builtin_bit_cast(unsigned, c0) >> 16) | __builtin_bit_cast(unsigned, c1), (__builtin_bit_cast(unsigned, r1) >> 16) | (bb[n] << 16), 0u, 0u};
          acc1[n] = mfma32(we, xe, acc1[n]);
        }
        SCHED();
        break;
      }
      if (__builtin_expect(__builtin_amdgcn_ballot_w64(fl < (unsigned)(s + 2)) != 0ull, 0)) lds_wait_min<4>(L, W_RDY, s + 2, lane);
      FENCE();
      SCHED();
      rdx(s + 1, 0); rdw(s + 1, 0, 0); SCHED();
      MM(2, 1); rdw(s + 1, 0, 1); rdw(s + 1, 0, 2); if (refill) dma16(voB[0], pb, lb + (s % 3) * G1_STAGE); wl1(s, 4); SCHED();
      MM(2, 2); rdx(s + 1, 1); rdw(s + 1, 1, 0); if (refill) dma16o<1024>(voB[1], pb); wl1(s, 5); SCHED();
      MM(3, 0); rdw(s + 1, 1, 1); if (refill) dma16o<2048>(voB[2], pb); wl1(s, 6); SCHED();
      MM(3, 1); rdw(s + 1, 1, 2); wl1(s, 7); SCHED();
      MM(3, 2); wl1(s, 8);
      if (s == 6) { lds_wait_ge(L, W_F1ALL, 4); ld64c(gran, go, ws); ld16(bb[0], l31 * 2, b1 + ct1 * 192); ld16(bb[1], l31 * 2, b1 + ct1 * 192 + 64); ld16(bb[2], l31 * 2, b1 + ct1 * 192 + 128); }
      SCHED();
      if (s == 0) ST(4); if (s == 3) ST(5); if (s == 6) ST(7);
    }
#undef MM
#undef CS
    ST(11);
    {
      const float rs = __builtin_amdgcn_rcpf(bf2f(gran.x)) * 1.44269504088896341f;
      char* ab = ws + WS_ACT + (size_t)((rb1 * 8 + 2 * wi) * 48 + 3 * ct1) * 1024;
      const unsigned ao = (unsigned)((l31 >> 4) * 49152 + h * 256 + l15 * 16);
#pragma unroll
      for (int q = 0; q < 6; ++q) {
        unsigned pk[4];
#pragma unroll
        for (int e = 0; e < 4; ++e) {
          const f32x2 h2 = f32x2{acc1[q >> 1][8 * (q & 1) + 2 * e], acc1[q >> 1][8 * (q & 1) + 2 * e + 1]} * rs;
          const f32x2 s1 = f32x2{__builtin_amdgcn_exp2f(h2.x), __builtin_amdgcn_exp2f(h2.y)} + 1.0f;
          const f32x2 d = __builtin_elementwise_fma(s1, s1, f32x2{1.f, 1.f});
          const f32x2 rr = f32x2{__builtin_amdgcn_rcpf(d.x), __builtin_amdgcn_rcpf(d.y)};
          const f32x2 o = h2 * __builtin_elementwise_fma(rr, f32x2{-1.38629436111989062f, -1.38629436111989062f},
                                                         f32x2{0.69314718055994531f, 0.69314718055994531f});
          pk[e] = pk2(o.x, o.y);
        }
        st128c(ao + q * 512, ab, v4u{pk[0], pk[1], pk[2], pk[3]});
      }
    }
    lds_set(L, W_RDY + wi * 4, 12);
    ST(13);
    __builtin_amdgcn_s_setprio(2);
    LATE_LANE();
    v2u xq[5], dq[5], gr2, f3;
    const unsigned f2o = WS_F2 + (unsigned)((4 * rg + (a2 >> 1)) * 16 + 4 * wi + (ln_ & 3)) * 128;
    unsigned vm[4];
#pragma unroll
    for (int m = 0; m < 4; ++m) vm[m] = vo + ((m + wi) & 3) * 49152;
    v2u f2; v4u ysh;
    {
      f32x4 sacc[4];
#pragma unroll
      for (int i = 0; i < 4; ++i) sacc[i] = f32x4{0.f, 0.f, 0.f, 0.f};
      unsigned kb = CVS + KAO - wi * 128 - 3584 - 64, ubb = CVS + UBO - wi * 128 - 64; PIN(kb); PIN(ubb);
      LAS char* ka = L + kb; LAS char* ub = L + ubb;
      const bool late = RFL(lw(L, W_SB)) < 12;
      if (late) { waitv<0>(); arrive_flag<4>(L, W_F2C, lane, ws, WS_F2 + (unsigned)wid * 128, tlo, thi); }
      lds_wait_ge(L, W_SB, 12);
      if (par) conv_unit_shared<1>(ka + 64, ub + 64, sacc); else conv_unit_shared<0>(ka + 64, ub + 64, sacc);
      if (!late) { waitv<0>(); arrive_flag<4>(L, W_F2C, lane, ws, WS_F2 + (unsigned)wid * 128, tlo, thi); }
      ST(14);
      WL(4); WL(5);
      if (par) conv_unit_shared<1>(ka, ub, sacc); else conv_unit_shared<0>(ka, ub, sacc);
#pragma unroll
      for (int q = 0; q < 4; ++q) *(LAS f32x4*)(L + MXCH + (q * 4 + wi) * 1024 + vo) = sacc[q];
      lds_arrive(L, W_MB, lane);
    {
      const char* xb2 = x + (size_t)row2 * 1280 + cb2 * 160; const unsigned xo2 = OROW * 1280 + cg * 8;
      ld64o<0>(xq[0], xo2, xb2); ld64o<32>(xq[1], xo2, xb2); ld64o<64>(xq[2], xo2, xb2); ld64o<96>(xq[3], xo2, xb2); ld64o<128>(xq[4], xo2, xb2);
      const char* db = pDskip + cb2 * 160;
      ld64o<0>(dq[0], cg * 8, db); ld64o<32>(dq[1], cg * 8, db); ld64o<64>(dq[2], cg * 8, db); ld64o<96>(dq[3], cg * 8, db); ld64o<128>(dq[4], cg * 8, db);
      ld64c(gr2, WS_STATS + (row2 + OROW) * 8, ws);
    }
      lds_wait_ge(L, W_MB, 4);
      LAS char* p = L + MXCH + wi * 4096 + vo;
      const f32x4 s = add4(add4(*(LAS f32x4*)p, *(LAS f32x4*)(p + 1024)), add4(*(LAS f32x4*)(p + 2048), *(LAS f32x4*)(p + 3072)));
      ld64c(f2, f2o, ws);
      const unsigned y0 = pk2(s.x, s.y), y1 = pk2(s.z, s.w);
      *(LAS v2u*)(L + MXCH + wi * 4096 + YOFF) = v2u{y0, y1};
      FENCE();
      ysh = *(LAS v4u*)p;
    }
    ST(15);
    waitv<0>(); PIN(f2);
    f2 = spin64<false>(f2, f2o, ws, tlo, thi);
    lds_set(L, W_F2OK + wi * 4, 1);
    ST(16);
    AL(0); AL(1);
    if (ln_ < 32) st128c(vo, ws + WS_Y + (size_t)chS * 4096 + (2 * wi + ((wi & 1) ^ par)) * 512, ysh);
    f32x4 acc2[4][5];
    PIN(gr2);
    const float rs2 = __builtin_amdgcn_rcpf(bf2f(gr2.x)), m2 = -bfhi(gr2.x) * rs2;
#pragma unroll
    for (int n = 0; n < 5; ++n) {
      PIN(xq[n]); PIN(dq[n]);
      const f32x2 x0 = unpk(xq[n].x), x1 = unpk(xq[n].y), d0 = unpk(dq[n].x), d1 = unpk(dq[n].y);
      acc2[0][n] = f32x4{ffma(d0.x, ffma(x0.x, rs2, m2), x0.x), ffma(d0.y, ffma(x0.y, rs2, m2), x0.y),
                         ffma(d1.x, ffma(x1.x, rs2, m2), x1.x), ffma(d1.y, ffma(x1.y, rs2, m2), x1.y)};
      PIN(acc2[0][n]);
    }
    __builtin_amdgcn_s_setprio(0);
    ST(17);
    waitv<0>();
    arrive_flag<8>(L, W_F3C, lane, ws, WS_F3 + (unsigned)(xcd * 32 + j) * 128, tlo, thi);
    g2_step<1>(acc2, WF[0], AF[0], [&](int i) { if (i >= 3 && (i & 1)) AL1(2, (i - 3) >> 1); });
    g2_step<4>(acc2, WF[1], AF[1], [&](int i) { if (i >= 2) AL1(3 + ((i - 2) >> 2), (i - 2) & 3); });
    waitv<8>(); g2_step<4>(acc2, WF[2], AF[2], [&](int i) { if (i < 4) AL1(5, i); });
    ST(20);
    waitv<8>(); g2_step<4>(acc2, WF[3], AF[3], NOLD);
    waitv<4>(); g2_step<4>(acc2, WF[4], AF[4], NOLD);
    lds_wait_ge(L, W_F3C, 8);
    const unsigned f3o = WS_F3 + (unsigned)(cb2 * 32 + 8 * wi + (ln_ & 7)) * 128;
    ld64c(f3, f3o, ws);
    waitv<1>(); g2_step<4>(acc2, WF[5], AF[5], NOLD);
    ST(22);
#pragma unroll
    for (int m = 1; m < 4; ++m)
#pragma unroll
      for (int n = 0; n < 5; ++n) *(LAS f32x4*)(L + FM + wi * 15360 + (m - 1) * 5120 + n * 1024 + vo) = acc2[m][n];
    waitv<0>(); PIN(f3);
    f3 = spin64<false>(f3, f3o, ws, tlo, thi);
    lds_arrive(L, W_F3ALL, lane);
    lds_wait_ge(L, W_F3ALL, 4);
    ST(21);
    v4u yv[3];
    {
      const char* yb = ws + WS_Y + (size_t)cb2 * 80 * 4096 + row2 * 2;
      const unsigned yo = (unsigned)(ln_ >> 1) * 4096 + (ln_ & 1) * 16;
      ld128c<0>(yv[0], yo, yb); ld128c<0>(yv[1], yo, yb + 32 * 4096); ld128c<0>(yv[2], yo & 0xffffu, yb + 64 * 4096);
    }
#pragma unroll
    for (int n = 0; n < 5; ++n) {
#pragma unroll
      for (int d = 1; d < 4; ++d) acc2[0][n] = add4(acc2[0][n], *(LAS f32x4*)(L + FM + ((wi - d) & 3) * 15360 + (d - 1) * 5120 + n * 1024 + vo));
      *(LAS f32x4*)(L + FM + 61440 + wi * 5120 + n * 1024 + vo) = acc2[0][n];
    }
    waitv<0>(); PIN(yv[0]); PIN(yv[1]); PIN(yv[2]);
    LAS char* ys = L + YST + wi * 2560;
    *(LAS v4u*)(ys + vo) = yv[0]; *(LAS v4u*)(ys + 1024 + vo) = yv[1];
    if (ln_ < 32) *(LAS v4u*)(ys + 2048 + vo) = yv[2];
    lds_set(L, W_F2OK + wi * 4, 2);
    STEND();
    asm volatile("97:");
    __builtin_amdgcn_endpgm();
  }

  const bool odd = wi & 1;
  const int kch = chA + (wi >> 1);
  const unsigned vo = lane * 16;
  v4u WF[6][5], AF[6][4];
  lds_wait_ge(L, W_TAG + 8, 1);
  tlo = RFL(lw(L, W_TAG)); thi = RFL(lw(L, W_TAG + 4));
  auto reduce = [&](int c, int i) {
    LAS char* pp = L + KG_SCR + (c < 2 ? 4 * c : 3) * KG_WSTR + i * 16;
    const f32x4 s = add4(*(LAS f32x4*)pp, *(LAS f32x4*)(pp + (c < 2 ? 2 : 4) * KG_WSTR));
    *(LAS f32x4*)(L + KTAB + c * KP_STRIDE + i * 16) = s;
    return s;
  };
  lds_wait_ge(L, W_KG, 4);
  if (ts < 161) { st128c((unsigned)ts * 16, ws + WS_KP + (size_t)wid * KP_STRIDE, __builtin_bit_cast(v4u, reduce(2, ts))); reduce(0, ts); }
  if (ts >= 95) reduce(1, ts - 95);
  if (ts < 3) *(LAS float*)(L + KTAB + ts * KP_STRIDE - 4) = 0.f;
  lds_arrive(L, W_SB, lane);
  waitv<0>();
  arrive_flag(L, W_FKC, lane, ws, WS_FK + (unsigned)wid * 64, tlo, thi);
  ST(9);
  __builtin_amdgcn_s_setprio(0);
  lds_wait_min<4>(L, W_RDY, F1STAGE, lane);
  const unsigned f1o = WS_F1 + (unsigned)xcd * 2048 + ts * 8;
  v2u f1own; ld64c(f1own, f1o, ws);
  waitv<0>(); PIN(f1own);
  f1own = spin64<false>(f1own, f1o, ws, tlo, thi);
  v4u uu[3]; unsigned ulo[3];
#pragma unroll
  for (int r = 0; r < 3; ++r) {
    const int e = 64 * r + lane, tb = 64 * wi + e / 3, c = e % 3;
    ld128c<0>(uu[r], tb * 80 + c * 16, ws + WS_XNB + (size_t)(16 * xcd + (j >> 1)) * 20480 + par * 32);
    ulo[r] = (c == 2 - 2 * par ? CVS : CVA + (c - par) * CV_CH) + tb * 16;
  }
  if (b == 0) arrive_flag(L, W_F1ALL, lane, ws, WS_E, tlo, thi);
  else lds_arrive(L, W_F1ALL, lane);
  ST(4);
  const unsigned fko = WS_FK + (unsigned)(wid ^ 1) * 64;
  v2u fk; ld64c(fk, fko, ws);
  waitv<0>(); PIN(fk); PIN(uu[0]); PIN(uu[1]); PIN(uu[2]);
  fk = spin64<false>(fk, fko, ws, tlo, thi);
  v4u kp; ld128c<0>(kp, (unsigned)(ts < 161 ? ts : 160) * 16, ws + WS_KP + (size_t)(wid ^ 1) * KP_STRIDE);
  lds_wait_ge(L, W_SB, 4);
  waitv<0>(); PIN(kp);
  if (ts < 161) { LAS f32x4* tp = (LAS f32x4*)(L + KTAB + 2 * KP_STRIDE + ts * 16); *tp = add4(*tp, __builtin_bit_cast(f32x4, kp)); }
  lds_arrive(L, W_SB, lane);
  lds_wait_ge(L, W_F1ALL, 4);
  WL(0);
  lds_wait_min<4>(L, W_RDY, 9, lane);
  put_k(L, kix, ts, 0, CVA);
  WL(1);
  lds_wait_min<4>(L, W_RDY, 10, lane);
  put_k(L, kix, ts, 1, CVA + CV_CH);
  if (ZCOND) { *(LAS v4u*)(L + CVA + ZOFF) = v4u{0u, 0u, 0u, 0u}; *(LAS v4u*)(L + CVA + CV_CH + ZOFF) = v4u{0u, 0u, 0u, 0u}; }
  ST(7);
  lds_wait_min<4>(L, W_RDY, 11, lane);
  ST(11);
  put_u(L + ulo[0], uu[0], ts < 3); put_u(L + ulo[1], uu[1], false); put_u(L + ulo[2], uu[2], false);
  if (ZCOND) *(LAS v4u*)(L + CVS + ZOFF) = v4u{0u, 0u, 0u, 0u};
  lds_wait_ge(L, W_SB, 8);
  put_k(L, kix, ts, 2, CVS);
  lds_arrive(L, W_SB, lane);
  WL(2); WL(3);
  ST(5);
  lds_wait_ge(L, W_SB, 12);
  lds_wait_ge(L, W_RDY + wi * 4, 12);
  ST(13);
  __builtin_amdgcn_s_setprio(SPRIO2);
  LATE_LANE();
  f32x4 cacc[8];
#pragma unroll
  for (int i = 0; i < 8; ++i) cacc[i] = f32x4{0.f, 0.f, 0.f, 0.f};
  {
    const unsigned o1 = CVA + (wi >> 1) * CV_CH - (odd ? 4 * 64 : 0);
    unsigned kb = KAO + o1 - 3584 - 192, ubb = UBO + o1 - 192; PIN(kb); PIN(ubb);
    LAS char* ka = L + kb; LAS char* ub = L + ubb;
    v4u A[8], B[8];
#define LA(i, u) (*(LAS v4u*)(ka + 512 * (7 - (i)) + 64 * (3 - (u))))
#define LB(i, u) (*(LAS v4u*)(ub + 512 * (i) + 64 * (3 - (u))))
    A[7] = LA(7, 0); B[0] = LB(0, 0); A[0] = LA(0, 0); B[7] = LB(7, 0); A[6] = LA(6, 0); B[6] = LB(6, 0); A[1] = LA(1, 0); B[1] = LB(1, 0);
    A[5] = LA(5, 0); B[5] = LB(5, 0); A[2] = LA(2, 0); B[2] = LB(2, 0); A[4] = LA(4, 0); B[4] = LB(4, 0); A[3] = LA(3, 0); B[3] = LB(3, 0);
    SCHED();
#pragma unroll
    for (int u = 0; u < 4; ++u) {
      v4u An[2], Bn[2];
#pragma unroll
      for (int sd = 7; sd >= 0; --sd) {
#pragma unroll
        for (int o = 0; o <= sd; ++o) {
          if (sd + o < 8) cacc[sd + o] = mfma(A[sd], B[o], cacc[sd + o]);
          if (o != sd && sd + o < 8) cacc[sd + o] = mfma(A[o], B[sd], cacc[sd + o]);
        }
        SCHED();
        if (u < 3) {
          if (sd >= 2) { A[sd] = LA(sd, u + 1); B[sd] = LB(sd, u + 1); }
          if (sd == 6 || sd == 5) { An[6 - sd] = LA(6 - sd, u + 1); Bn[6 - sd] = LB(6 - sd, u + 1); }
        }
        SCHED();
      }
      if (u < 3) { A[0] = An[0]; A[1] = An[1]; B[0] = Bn[0]; B[1] = Bn[1]; }
    }
#undef LA
#undef LB
  }
  ST(10);
  unsigned vm[4];
  {
    LAS char* px = L + SXCH + wi * 4096 + vo;
    if (odd) {
#pragma unroll
      for (int i = 0; i < 4; ++i) { *(LAS f32x4*)(px + i * 1024) = cacc[i]; cacc[i] = cacc[4 + i]; }
    } else {
#pragma unroll
      for (int i = 0; i < 4; ++i) *(LAS f32x4*)(px + i * 1024) = cacc[4 + i];
    }
    lds_arrive(L, W_SX + (wi >> 1) * 4, lane);
    WL(4); WL(5);
#pragma unroll
    for (int m = 0; m < 4; ++m) vm[m] = vo + ((m + wi) & 3) * 49152;
    lds_wait_ge(L, W_SX + (wi >> 1) * 4, 2);
    px = L + SXCH + (wi ^ 1) * 4096 + vo;
    v2u yo[4];
#pragma unroll
    for (int i = 0; i < 4; ++i) { const f32x4 s = add4(cacc[i], *(LAS f32x4*)(px + i * 1024)); yo[i] = v2u{pk2(s.x, s.y), pk2(s.z, s.w)}; }
    char* yb = ws + WS_Y + (size_t)kch * 4096 + (odd ? 2048 : 0);
    LAS char* yl = L + SXCH + (wi ^ 1) * 4096;
#pragma unroll
    for (int i = 0; i < 4; ++i) *(LAS v2u*)(yl + i * 512 + YOFF) = yo[i];
    FENCE();
    st128c(vo, yb, *(LAS v4u*)(yl + vo)); st128c(vo, yb + 1024, *(LAS v4u*)(yl + 1024 + vo));
  }
  ST(14);
  lds_wait_ge(L, W_F2OK + wi * 4, 1);
  ST(16);
  AL(0); AL(1);
  ST(17);
  f32x4 acc2[4][5];
  waitv<0>();
  arrive_flag<8>(L, W_F3C, lane, ws, WS_F3 + (unsigned)(xcd * 32 + j) * 128, tlo, thi);
  __builtin_amdgcn_s_setprio(0);
  waitv<4>(); g2_step<0>(acc2, WF[0], AF[0], [&](int i) { if (i >= 3 && (i & 1)) AL1(2, (i - 3) >> 1); });
  waitv<4>(); g2_step<4>(acc2, WF[1], AF[1], [&](int i) { if (i >= 2) AL1(3 + ((i - 2) >> 2), (i - 2) & 3); });
  waitv<8>(); g2_step<4>(acc2, WF[2], AF[2], [&](int i) { if (i < 4) AL1(5, i); });
  ST(20);
  waitv<8>(); g2_step<4>(acc2, WF[3], AF[3], NOLD);
  lds_wait_ge(L, W_F3C, 8);
  waitv<0>();
#pragma unroll
  for (int m = 0; m < 4; ++m) { PIN(AF[4][m]); PIN(AF[5][m]); }
#pragma unroll
  for (int n = 0; n < 5; ++n) { PIN(WF[4][n]); PIN(WF[5][n]); }
#pragma unroll
  for (int mm = 1; mm < 5; ++mm) {
    const int m = mm & 3;
#pragma unroll
    for (int n = 0; n < 5; ++n) {
      acc2[m][n] = mfma(WF[4][n], AF[4][m], acc2[m][n]);
      acc2[m][n] = mfma(WF[5][n], AF[5][m], acc2[m][n]);
      if (mm > 1) *(LAS f32x4*)(L + FS + wi * 15360 + (mm - 2) * 5120 + n * 1024 + vo) = acc2[mm - 1][n];
      SCHED();
    }
  }
  lds_arrive(L, W_FS, lane);
  ST(22);
  lds_wait_ge(L, W_FS, 4);
#pragma unroll
  for (int d = 1; d < 4; ++d)
#pragma unroll
    for (int n = 0; n < 5; ++n) acc2[0][n] += *(LAS f32x4*)(L + FS + ((wi - d) & 3) * 15360 + (d - 1) * 5120 + n * 1024 + vo);
  ST(23);
  lds_wait_ge(L, W_F2OK + wi * 4, 2);
  ST(24);
  {
    char* ob = out + (size_t)row2 * 1280 + cb2 * 160; const unsigned oo = OROW * 1280 + cg * 8;
    v2u o[5];
#pragma unroll
    for (int n = 0; n < 5; ++n) {
      const v2u t = trread(L + YST + wi * 2560 + n * 512 + ln_ * 8);
      const f32x4 s = acc2[0][n] + *(LAS f32x4*)(L + FM + 61440 + wi * 5120 + n * 1024 + vo);
      const f32x2 s0 = s.xy + unpk(t.x), s1 = s.zw + unpk(t.y);
      o[n] = v2u{pk2(s0.x, s0.y), pk2(s1.x, s1.y)};
    }
    st64o<0>(oo, ob, o[0]); st64o<32>(oo, ob, o[1]); st64o<64>(oo, ob, o[2]); st64o<96>(oo, ob, o[3]); st64o<128>(oo, ob, o[4]);
  }
  STEND();
  asm volatile("99:");
}

extern "C" void kernel_launch(void* const* d_in, const int* in_sizes, int n_in, void* d_out, int out_size, void* d_ws, size_t ws_size,
                              hipStream_t stream) {
  fused_s4_mlp<<<256, 512, 0, stream>>>((const char*)d_in[0], (const char*)d_in[1], (const char*)d_in[2], (const char*)d_in[3],
      (const char*)d_in[4], (const char*)d_in[5], (const char*)d_in[6], (const char*)d_in[7], (const char*)d_in[8], (const char*)d_in[10],
      (const char*)d_in[11], (const char*)d_in[12], (char*)d_out, (char*)d_ws);
}
```
